```python
import torch

def _softmax(x, axis=-1):
    return torch.softmax(x, dim=axis)
def _gelu(x):
    return torch.nn.functional.gelu(x, approximate="tanh")
def _silu(x):
    return torch.nn.functional.silu(x)
def _sigmoid(x):
    return torch.sigmoid(x)
def _layernorm(x, axis=-1, eps=1e-5):
    m = x.mean(dim=axis, keepdim=True)
    v = x.var(dim=axis, keepdim=True, unbiased=False)
    return (x - m) / torch.sqrt(v + eps)
def _scatter_add(tgt, idx, src, axis=0):
    return tgt.clone().index_add_(axis, idx, src)
def _segment_sum(src, seg, n):
    out = torch.zeros((n,) + src.shape[1:], dtype=src.dtype)
    return out.index_add_(0, seg, src)
def _pad(x, pw):
    flat = tuple(v for pair in reversed(pw) for v in pair)
    return torch.nn.functional.pad(x, flat)

kernel_name = 'concat_heads__gqa_block_cached__gqa_block_cached__near_ridge'

def setup_inputs():

    _w = (lambda a: (lambda t: t.to(torch.bfloat16) if t.is_floating_point() else t)(torch.as_tensor(a)))
    def _A_setup():
        B, Sq, D = 32, 32, 512
        Hq, Hkv, Sk, Dh = 32, 8, 512, 128
        assert Hq % Hkv == 0, (Hq, Hkv)
        return {
            "x": _w((lambda *s: torch.randn(*s, dtype=torch.bfloat16) * 0.8)(B, Sq, D)),
            "k_cache": _w((lambda *s: torch.randn(*s, dtype=torch.bfloat16) * 0.8)(B, Hkv, Sk, Dh)),
            "v_cache": _w((lambda *s: torch.randn(*s, dtype=torch.bfloat16) * 0.05)(B, Hkv, Sk, Dh)),
            "wq": _w((lambda *s: torch.randn(*s, dtype=torch.bfloat16) * 0.8)(D, Hq * Dh) * (D ** -0.5)),
            "wo": _w((lambda *s: torch.randn(*s, dtype=torch.bfloat16) * 0.05)(Hq * Dh, D) * ((Hq * Dh) ** -0.5)),
        }
    _A_in = _A_setup()

    def _B_setup():
        B, Sq, D = 32, 32, 512
        Hq, Hkv, Sk, Dh = 32, 8, 512, 128
        assert Hq % Hkv == 0, (Hq, Hkv)
        return {
            "x": _w((lambda *s: torch.randn(*s, dtype=torch.bfloat16) * 0.8)(B, Sq, D)),
            "k_cache": _w((lambda *s: torch.randn(*s, dtype=torch.bfloat16) * 0.8)(B, Hkv, Sk, Dh)),
            "v_cache": _w((lambda *s: torch.randn(*s, dtype=torch.bfloat16) * 0.05)(B, Hkv, Sk, Dh)),
            "wq": _w((lambda *s: torch.randn(*s, dtype=torch.bfloat16) * 0.8)(D, Hq * Dh) * (D ** -0.5)),
            "wo": _w((lambda *s: torch.randn(*s, dtype=torch.bfloat16) * 0.05)(Hq * Dh, D) * ((Hq * Dh) ** -0.5)),
        }
    _B_in = _B_setup()

    return {
        "A_x": _A_in["x"],
        "A_k_cache": _A_in["k_cache"],
        "A_v_cache": _A_in["v_cache"],
        "A_wq": _A_in["wq"],
        "A_wo": _A_in["wo"],
        "B_x": _B_in["x"],
        "B_k_cache": _B_in["k_cache"],
        "B_v_cache": _B_in["v_cache"],
        "B_wq": _B_in["wq"],
        "B_wo": _B_in["wo"],
    }

def reference(A_x, A_k_cache, A_v_cache, A_wq, A_wo, B_x, B_k_cache, B_v_cache, B_wq, B_wo):
    def _A_ref(x, k_cache, v_cache, wq, wo):
        B, Sq, D = x.shape
        Hq, Dh = 32, 128
        Hkv = k_cache.shape[1]
        rep = Hq // Hkv
        q = (lambda x, *d: x.permute(*d))((x @ wq).reshape(B, Sq, Hq, Dh), 0, 2, 1, 3)
        k = (lambda x, n, axis: torch.repeat_interleave(x, n, dim=axis))(k_cache, rep, axis=1)
        v = (lambda x, n, axis: torch.repeat_interleave(x, n, dim=axis))(v_cache, rep, axis=1)
        scores = torch.einsum("bhqd,bhkd->bhqk", q, k) * (Dh ** -0.5)
        p = _softmax(scores, axis=-1)
        o = torch.einsum("bhqk,bhkd->bhqd", p, v)
        return (lambda x, *d: x.permute(*d))(o, 0, 2, 1, 3).reshape(B, Sq, Hq * Dh) @ wo

    def _B_ref(x, k_cache, v_cache, wq, wo):
        B, Sq, D = x.shape
        Hq, Dh = 32, 128
        Hkv = k_cache.shape[1]
        rep = Hq // Hkv
        q = (lambda x, *d: x.permute(*d))((x @ wq).reshape(B, Sq, Hq, Dh), 0, 2, 1, 3)
        k = (lambda x, n, axis: torch.repeat_interleave(x, n, dim=axis))(k_cache, rep, axis=1)
        v = (lambda x, n, axis: torch.repeat_interleave(x, n, dim=axis))(v_cache, rep, axis=1)
        scores = torch.einsum("bhqd,bhkd->bhqk", q, k) * (Dh ** -0.5)
        p = _softmax(scores, axis=-1)
        o = torch.einsum("bhqk,bhkd->bhqd", p, v)
        return (lambda x, *d: x.permute(*d))(o, 0, 2, 1, 3).reshape(B, Sq, Hq * Dh) @ wo

    a = _A_ref(A_x, A_k_cache, A_v_cache, A_wq, A_wo)
    b = _B_ref(B_x, B_k_cache, B_v_cache, B_wq, B_wo)
    return torch.concatenate([a, b], axis=-1)
```

```cpp
#include <hip/hip_runtime.h>
#include <hip/hip_bf16.h>
typedef float f32x16 __attribute__((ext_vector_type(16)));
typedef float f32x4 __attribute__((ext_vector_type(4)));
typedef float f32x2 __attribute__((ext_vector_type(2)));
typedef __bf16 bf16x8 __attribute__((ext_vector_type(8)));
typedef __bf16 bf16x2 __attribute__((ext_vector_type(2)));
typedef short s16x4 __attribute__((ext_vector_type(4)));
typedef unsigned u32x4 __attribute__((ext_vector_type(4)));
typedef unsigned u32x2 __attribute__((ext_vector_type(2)));
#define DI __device__ __forceinline__
#define KV_AUX 2
#define W_NT 0
DI unsigned cvtpk(float lo, float hi) { f32x2 v = {lo, hi}; bf16x2 b = __builtin_convertvector(v, bf16x2); return __builtin_bit_cast(unsigned, b); }
DI bf16x8 asbf(u32x4 v) { return __builtin_bit_cast(bf16x8, v); }
DI u32x4 ldw(const char* p) { return W_NT ? __builtin_nontemporal_load((const u32x4*)p) : *(const u32x4*)p; }
DI void dma16(const char* g, char* l, int aux) {
  if (aux == 2) __builtin_amdgcn_global_load_lds((const __attribute__((address_space(1))) void*)g, (__attribute__((address_space(3))) void*)l, 16, 0, 2);
  else __builtin_amdgcn_global_load_lds((const __attribute__((address_space(1))) void*)g, (__attribute__((address_space(3))) void*)l, 16, 0, 0);
}
DI void proj_step(f32x4 (&acc)[4][8], const u32x4 (&raw)[8], const bf16x8 (&bf)[4]) {
#pragma unroll
  for (int c = 0; c < 4; ++c) {
    u32x4 f0, f1;
#pragma unroll
    for (int jp = 0; jp < 4; ++jp) {
      f0[jp] = __builtin_amdgcn_perm(raw[2 * jp + 1][c], raw[2 * jp][c], 0x05040100u);
      f1[jp] = __builtin_amdgcn_perm(raw[2 * jp + 1][c], raw[2 * jp][c], 0x07060302u);
    }
#pragma unroll
    for (int mb = 0; mb < 4; ++mb) {
      acc[mb][2 * c] = __builtin_amdgcn_mfma_f32_16x16x32_bf16(asbf(f0), bf[mb], acc[mb][2 * c], 0, 0, 0);
      acc[mb][2 * c + 1] = __builtin_amdgcn_mfma_f32_16x16x32_bf16(asbf(f1), bf[mb], acc[mb][2 * c + 1], 0, 0, 0);
    }
  }
}

__global__ __launch_bounds__(256, 1) void fused_k(const char* __restrict__ xA, const char* __restrict__ kA, const char* __restrict__ vA, const char* __restrict__ wqA, const char* __restrict__ woA,
                                                  const char* __restrict__ xB, const char* __restrict__ kB, const char* __restrict__ vB, const char* __restrict__ wqB, const char* __restrict__ woB,
                                                  char* __restrict__ slab) {
  extern __shared__ __attribute__((aligned(16))) char lds[];
  const int tid = threadIdx.x, l = tid & 63;
  const int w = __builtin_amdgcn_readfirstlane(tid >> 6);
  const int wg = blockIdx.x, xcd = wg & 7, br = xcd & 1, kvh = 2 * (xcd >> 1) + ((wg >> 3) & 1), bp = wg >> 4;
  const char* x = (br ? xB : xA) + (size_t)bp * 65536;
  const char* K = br ? kB : kA; const char* V = br ? vB : vA;
  const char* wq = (br ? wqB : wqA) + kvh * 1024 + w * 256;
  const char* wo = (br ? woB : woA) + (size_t)kvh * 512 * 1024 + w * 256;
  const int rot = (bp + 4 * (kvh & 1)) & 7;
  const int i16 = l & 15, g = l >> 4;
  char* XL = lds + 98304;
  {
    const int r = l >> 3, pos = l & 7;
#pragma unroll
    for (int n = 0; n < 16; ++n) {
      const int blk = n * 4 + w, p = blk >> 3, rb = blk & 7;
      const char* src = x + (rb * 8 + r) * 1024 + ((p + rot) & 7) * 128 + ((pos ^ (r & 6)) << 4);
      dma16(src, XL + blk * 1024, 0);
    }
  }
  auto kv_dma = [&](const char* tile, char* slot) {
    const int sub = l >> 5, r = (l >> 2) & 7, p = l & 3;
#pragma unroll
    for (int q = 0; q < 4; ++q) {
      const int R = 2 * w + (q >> 1), u = q & 1;
      const int ch = 4 * (2 * u + sub) + (p ^ ((2 * R + (r >> 2)) & 3));
      dma16(tile + (R * 8 + r) * 256 + ch * 16, slot + R * 2048 + u * 1024, KV_AUX);
    }
  };
  auto ktile = [&](int t) { return K + ((size_t)((bp * 2 + (t >> 3)) * 8 + kvh) * 512 + (t & 7) * 64) * 256; };
  auto vtile = [&](int t) { return V + ((size_t)((bp * 2 + (t >> 3)) * 8 + kvh) * 512 + (t & 7) * 64) * 256; };
  f32x4 acc[4][8];
#pragma unroll
  for (int a = 0; a < 4; ++a)
#pragma unroll
    for (int b = 0; b < 8; ++b) acc[a][b] = f32x4{0, 0, 0, 0};
  const char* wql = wq + (size_t)g * 8 * 8192 + i16 * 16;
  u32x4 raw[2][8];
  auto ldq = [&](int s, u32x4 (&d)[8]) { const char* p = wql + (size_t)((s + 2 * rot) & 15) * 32 * 8192;
#pragma unroll
    for (int j = 0; j < 8; ++j) d[j] = ldw(p + j * 8192); };
  ldq(0, raw[0]); ldq(1, raw[1]);
  asm volatile("s_waitcnt vmcnt(16)" ::: "memory");
  __builtin_amdgcn_s_barrier();
  const int xb = (i16 >> 3) * 1024 + (i16 & 7) * 128 + ((g ^ (i16 & 6)) << 4);
#pragma unroll
  for (int s = 0; s < 16; ++s) {
    bf16x8 bf[4];
#pragma unroll
    for (int mb = 0; mb < 4; ++mb) bf[mb] = *(const bf16x8*)(XL + ((xb ^ ((s & 1) * 64)) + (s >> 1) * 8192 + mb * 2048));
    proj_step(acc, raw[s & 1], bf);
    if (s + 2 < 16) ldq(s + 2, raw[s & 1]);
  }
  kv_dma(ktile(0), lds + 0); kv_dma(vtile(0), lds + 16384);
  const float C = 0.088388347648318440f * 1.4426950408889634f;
  bf16x8 qf[2][8];
#pragma unroll
  for (int b = 0; b < 2; ++b)
#pragma unroll
    for (int r = 0; r < 4; ++r) {
      u32x4 X, Y;
#pragma unroll
      for (int jp = 0; jp < 4; ++jp) {
        unsigned a = cvtpk(acc[2 * b][2 * jp][r] * C, acc[2 * b][2 * jp + 1][r] * C);
        unsigned c = cvtpk(acc[2 * b + 1][2 * jp][r] * C, acc[2 * b + 1][2 * jp + 1][r] * C);
        auto sw = __builtin_amdgcn_permlane16_swap(a, c, false, false);
        X[jp] = sw[0]; Y[jp] = sw[1];
      }
      qf[b][r] = asbf(X); qf[b][4 + r] = asbf(Y);
    }
  const int key = l & 31, hi = l >> 5;
  int kb_[4];
#pragma unroll
  for (int r = 0; r < 4; ++r) kb_[r] = 2048 * (key >> 3) + 1024 * hi + 64 * (key & 7) + 16 * (r ^ ((key >> 2) & 3));
  const int blk = (l >> 4) & 1, qq = (l & 15) >> 2, pp = l & 3;
  int vb_[2];
#pragma unroll
  for (int t = 0; t < 2; ++t) vb_[t] = 2048 * t + 64 * (4 * hi + qq) + 16 * ((2 * blk + (pp >> 1)) ^ (2 * t + hi)) + 8 * (pp & 1);
  char* OL = lds + 98304;
#pragma unroll 1
  for (int b = 0; b < 2; ++b) {
    f32x16 o[4];
#pragma unroll
    for (int d = 0; d < 4; ++d) o[d] = f32x16{};
    float m = -__builtin_inff(), lsum = 0.f;
    bf16x8 q[8];
#pragma unroll
    for (int s = 0; s < 8; ++s) q[s] = b ? qf[1][s] : qf[0][s];
#pragma unroll 1
    for (int kt = 0; kt < 8; ++kt) {
      const int t = b * 8 + kt;
      asm volatile("s_waitcnt vmcnt(0)" ::: "memory");
      __builtin_amdgcn_s_barrier();
      char* Ks = lds + (t & 1) * 32768; char* Vs = Ks + 16384;
      if (t + 1 < 16) { kv_dma(ktile(t + 1), lds + ((t + 1) & 1) * 32768); kv_dma(vtile(t + 1), lds + ((t + 1) & 1) * 32768 + 16384); }
      f32x16 s0 = {}, s1 = {};
#pragma unroll
      for (int s = 0; s < 8; ++s) {
        bf16x8 k0 = *(const bf16x8*)(Ks + kb_[s & 3] + 512 * (s >> 2));
        bf16x8 k1 = *(const bf16x8*)(Ks + kb_[s & 3] + 512 * (s >> 2) + 8192);
        s0 = __builtin_amdgcn_mfma_f32_32x32x16_bf16(k0, q[s], s0, 0, 0, 0);
        s1 = __builtin_amdgcn_mfma_f32_32x32x16_bf16(k1, q[s], s1, 0, 0, 0);
      }
      float pm = s0[0];
#pragma unroll
      for (int r = 1; r < 16; ++r) pm = fmaxf(pm, s0[r]);
#pragma unroll
      for (int r = 0; r < 16; ++r) pm = fmaxf(pm, s1[r]);
      { auto rr = __builtin_amdgcn_permlane32_swap(__float_as_uint(pm), __float_as_uint(pm), false, false); pm = fmaxf(__uint_as_float(rr[0]), __uint_as_float(rr[1])); }
      const float mn = fmaxf(m, pm), alpha = __builtin_amdgcn_exp2f(m - mn); m = mn;
      float ps = 0.f;
#pragma unroll
      for (int r = 0; r < 16; ++r) { s0[r] = __builtin_amdgcn_exp2f(s0[r] - mn); s1[r] = __builtin_amdgcn_exp2f(s1[r] - mn); ps += s0[r] + s1[r]; }
      lsum = lsum * alpha + ps;
#pragma unroll
      for (int d = 0; d < 4; ++d)
#pragma unroll
        for (int r = 0; r < 16; ++r) o[d][r] *= alpha;
      bf16x8 p[4];
#pragma unroll
      for (int ks = 0; ks < 4; ++ks) { u32x4 u;
#pragma unroll
        for (int e = 0; e < 4; ++e) u[e] = (ks < 2) ? cvtpk(s0[8 * ks + 2 * e], s0[8 * ks + 2 * e + 1]) : cvtpk(s1[8 * (ks - 2) + 2 * e], s1[8 * (ks - 2) + 2 * e + 1]);
        p[ks] = asbf(u); }
#pragma unroll
      for (int ks = 0; ks < 4; ++ks)
#pragma unroll
        for (int d = 0; d < 4; ++d) {
          typedef __attribute__((address_space(3))) s16x4* lp;
          s16x4 lo = __builtin_amdgcn_ds_read_tr16_b64_v4i16((lp)(Vs + vb_[0] + 4096 * ks + 512 * d));
          s16x4 hi4 = __builtin_amdgcn_ds_read_tr16_b64_v4i16((lp)(Vs + vb_[1] + 4096 * ks + 512 * d));
          typedef short s16x8 __attribute__((ext_vector_type(8)));
          s16x8 vf = __builtin_shufflevector(lo, hi4, 0, 1, 2, 3, 4, 5, 6, 7);
          o[d] = __builtin_amdgcn_mfma_f32_32x32x16_bf16(__builtin_bit_cast(bf16x8, vf), p[ks], o[d], 0, 0, 0);
        }
      asm volatile("s_waitcnt lgkmcnt(0)" ::: "memory");
    }
    { auto rr = __builtin_amdgcn_permlane32_swap(__float_as_uint(lsum), __float_as_uint(lsum), false, false); lsum = __uint_as_float(rr[0]) + __uint_as_float(rr[1]); }
    const float il = 1.f / lsum;
#pragma unroll
    for (int d = 0; d < 4; ++d)
#pragma unroll
      for (int u = 0; u < 2; ++u) { u32x4 pc;
#pragma unroll
        for (int e = 0; e < 4; ++e) pc[e] = cvtpk(o[d][8 * u + 2 * e] * il, o[d][8 * u + 2 * e + 1] * il);
        *(u32x4*)(OL + (4 * w + d) * 4096 + (2 * b + (key >> 4)) * 1024 + ((2 * u + hi) * 16 + (key & 15)) * 16) = pc; }
  }
  const char* wol = wo + ((g >> 1) * 16 + (g & 1) * 4) * 1024 + i16 * 16;
  auto ldo = [&](int s, u32x4 (&d)[8]) { const char* p = wol + (size_t)((s + 2 * rot) & 15) * 32 * 1024;
#pragma unroll
    for (int j = 0; j < 8; ++j) d[j] = ldw(p + (8 * (j >> 2) + (j & 3)) * 1024); };
  ldo(0, raw[0]); ldo(1, raw[1]);
#pragma unroll
  for (int a = 0; a < 4; ++a)
#pragma unroll
    for (int b = 0; b < 8; ++b) acc[a][b] = f32x4{0, 0, 0, 0};
  asm volatile("s_waitcnt lgkmcnt(0)" ::: "memory");
  __builtin_amdgcn_s_barrier();
#pragma unroll
  for (int s = 0; s < 16; ++s) {
    bf16x8 bf[4];
    const char* ob = OL + ((s + 2 * rot) & 15) * 4096 + l * 16;
#pragma unroll
    for (int mb = 0; mb < 4; ++mb) bf[mb] = *(const bf16x8*)(ob + mb * 1024);
    proj_step(acc, raw[s & 1], bf);
    if (s + 2 < 16) ldo(s + 2, raw[s & 1]);
  }
  char* sl = slab + (size_t)((br * 16 + bp) * 8 + kvh) * 65536 + w * 16384 + l * 16;
#pragma unroll
  for (int mb = 0; mb < 4; ++mb)
#pragma unroll
    for (int r = 0; r < 4; ++r) { u32x4 pc;
#pragma unroll
      for (int e = 0; e < 4; ++e) pc[e] = cvtpk(acc[mb][2 * e][r], acc[mb][2 * e + 1][r]);
      *(u32x4*)(sl + mb * 4096 + r * 1024) = pc; }
}

__global__ __launch_bounds__(256) void reduce_k(const char* __restrict__ slab, char* __restrict__ out) {
  const int tid = threadIdx.x, l = tid & 63, v = tid >> 6;
  const int grp = blockIdx.x >> 3, j = blockIdx.x & 7;
  const int br = grp >> 4, bp = grp & 15;
  const int w = j >> 1, mb = 2 * (j & 1) + (v >> 1);
#pragma unroll
  for (int rr = 0; rr < 2; ++rr) {
    const int r = 2 * (v & 1) + rr;
    const char* p = slab + (size_t)grp * 8 * 65536 + w * 16384 + mb * 4096 + r * 1024 + l * 16;
    u32x4 d[8];
#pragma unroll
    for (int s = 0; s < 8; ++s) d[s] = *(const u32x4*)(p + (size_t)s * 65536);
    float a[8] = {};
#pragma unroll
    for (int s = 0; s < 8; ++s)
#pragma unroll
      for (int e = 0; e < 4; ++e) { a[2 * e] += __uint_as_float(d[s][e] << 16); a[2 * e + 1] += __uint_as_float(d[s][e] & 0xffff0000u); }
    u32x4 o = {cvtpk(a[0], a[1]), cvtpk(a[2], a[3]), cvtpk(a[4], a[5]), cvtpk(a[6], a[7])};
    const int row = bp * 64 + mb * 16 + (l & 15), col = br * 512 + w * 128 + (l >> 4) * 32 + r * 8;
    *(u32x4*)(out + ((size_t)row * 1024 + col) * 2) = o;
  }
}

extern "C" void kernel_launch(void* const* d_in, const int* in_sizes, int n_in, void* d_out, int out_size, void* d_ws, size_t ws_size, hipStream_t stream) {
  (void)hipFuncSetAttribute((const void*)fused_k, hipFuncAttributeMaxDynamicSharedMemorySize, 163840);
  fused_k<<<256, 256, 163840, stream>>>((const char*)d_in[0], (const char*)d_in[1], (const char*)d_in[2], (const char*)d_in[3], (const char*)d_in[4],
                                        (const char*)d_in[5], (const char*)d_in[6], (const char*)d_in[7], (const char*)d_in[8], (const char*)d_in[9], (char*)d_ws);
  reduce_k<<<256, 256, 0, stream>>>((const char*)d_ws, (char*)d_out);
}
```

```cpp
#include <hip/hip_runtime.h>

__global__ __launch_bounds__(256, 1) void fused_k(const char* xA, const char* kA, const char* vA, const char* wqA, const char* woA,
                                                  const char* xB, const char* kB, const char* vB, const char* wqB, const char* woB,
                                                  char* ws, char* out) {
  const unsigned long long ka = (unsigned long long)__builtin_amdgcn_kernarg_segment_ptr();
  const int w = __builtin_amdgcn_readfirstlane(threadIdx.x >> 6);
  asm volatile(
".Lbeg:\n"
"s_and_b32 s54, %1, 1\n"
"s_mul_i32 s4, s54, 40\n"
"s_mov_b64 s[14:15], %0\n"
"s_add_u32 s14, s14, s4\n"
"s_addc_u32 s15, s15, 0\n"
"s_load_dwordx8 s[4:11], s[14:15], 0x0\n"
"s_load_dwordx2 s[12:13], s[14:15], 0x20\n"
"s_load_dwordx4 s[16:19], %0, 0x50\n"
"v_mbcnt_lo_u32_b32 v0, -1, 0\n"
"v_mbcnt_hi_u32_b32 v0, -1, v0\n"
"s_mov_b32 s48, %2\n"
"s_lshl_b32 s60, %2, 12\n"
"s_lshr_b32 s52, %1, 3\n"
"s_lshr_b32 s51, %1, 4\n"
"s_and_b32 s53, %1, 6\n"
"s_and_b32 s80, s52, 1\n"
"s_or_b32 s53, s53, s80\n"
"s_lshl_b32 s80, s80, 2\n"
"s_lshr_b32 s49, %1, 5\n"
"s_add_u32 s49, s49, s80\n"
"s_and_b32 s80, %1, 6\n"
"s_add_u32 s49, s49, s80\n"
"s_and_b32 s49, s49, 7\n"
"s_mul_i32 s50, %1, 0\n"
"s_mul_i32 s80, s52, 1\n"
"s_add_u32 s50, s50, s80\n"
"s_lshr_b32 s80, %1, 6\n"
"s_add_u32 s50, s50, s80\n"
"s_and_b32 s50, s50, 7\n"
"s_mov_b32 s61, 21802\n"
"s_bitcmp0_b32 s61, s51\n"
"s_cbranch_scc0 .LtypeB\n"
"s_getpc_b64 s[100:101]\n"
".LApfb:\n"
"s_sub_u32 s100, s100, .LApfb-.Lbeg\n"
"s_subb_u32 s101, s101, 0\n"
"s_lshl_b32 s82, s52, 2\n"
"s_add_u32 s82, s82, s48\n"
"s_lshl_b32 s82, s82, 10\n"
"v_lshlrev_b32 v26, 7, v0\n"
"v_add_u32 v26, s82, v26\n"
"v_min_u32 v26, .Lend-.Lbeg-4, v26\n"
"s_mov_b64 exec, 255\n"
"global_load_dword v25, v26, s[100:101]\n"
"s_mov_b64 exec, -1\n"
"v_lshrrev_b32 v16, 3, v0\n"
"v_and_b32 v17, 6, v16\n"
"v_and_b32 v18, 7, v0\n"
"v_xor_b32 v17, v17, v18\n"
"v_lshlrev_b32 v17, 4, v17\n"
"v_lshl_add_u32 v1, v16, 10, v17\n"
"v_lshrrev_b32 v16, 4, v0\n"
"v_and_b32 v17, 15, v0\n"
"v_lshlrev_b32 v18, 4, v17\n"
"v_lshl_add_u32 v8, v16, 16, v18\n"
"v_add_u32 v9, 8192, v8\n"
"v_add_u32 v10, 16384, v8\n"
"v_add_u32 v11, 24576, v8\n"
"v_add_u32 v12, 32768, v8\n"
"v_add_u32 v13, 40960, v8\n"
"v_add_u32 v14, 49152, v8\n"
"v_add_u32 v15, 57344, v8\n"
"v_and_b32 v18, 6, v17\n"
"v_xor_b32 v18, v18, v16\n"
"v_lshlrev_b32 v18, 4, v18\n"
"v_and_b32 v19, 7, v17\n"
"v_lshl_add_u32 v18, v19, 7, v18\n"
"v_lshrrev_b32 v19, 3, v17\n"
"v_lshl_add_u32 v18, v19, 10, v18\n"
"v_add_u32 v6, 131072, v18\n"
"v_xor_b32 v7, 64, v6\n"
"v_bfe_u32 v18, v0, 2, 3\n"
"v_lshrrev_b32 v19, 5, v0\n"
"v_and_b32 v20, 3, v0\n"
"v_lshrrev_b32 v21, 2, v18\n"
"v_lshlrev_b32 v21, 1, v21\n"
"v_xor_b32 v20, v20, v21\n"
"v_lshlrev_b32 v18, 8, v18\n"
"v_lshl_add_u32 v18, v19, 6, v18\n"
"v_lshl_add_u32 v22, v20, 4, v18\n"
"v_xor_b32 v20, 1, v20\n"
"v_lshl_add_u32 v23, v20, 4, v18\n"
"v_add_u32 v2, 1024, v22\n"
"v_add_u32 v3, 128, v22\n"
"v_add_u32 v4, 1024, v23\n"
"v_add_u32 v5, 128, v23\n"
"s_add_u32 s80, s50, 0\n"
"s_and_b32 s80, s80, 7\n"
"s_lshl_b32 s80, s80, 14\n"
"s_add_u32 s62, s80, s60\n"
"s_add_u32 s80, s50, 1\n"
"s_and_b32 s80, s80, 7\n"
"s_lshl_b32 s80, s80, 14\n"
"s_add_u32 s63, s80, s60\n"
"s_add_u32 s80, s50, 2\n"
"s_and_b32 s80, s80, 7\n"
"s_lshl_b32 s80, s80, 14\n"
"s_add_u32 s64, s80, s60\n"
"s_add_u32 s80, s50, 3\n"
"s_and_b32 s80, s80, 7\n"
"s_lshl_b32 s80, s80, 14\n"
"s_add_u32 s65, s80, s60\n"
"s_add_u32 s80, s50, 4\n"
"s_and_b32 s80, s80, 7\n"
"s_lshl_b32 s80, s80, 14\n"
"s_add_u32 s66, s80, s60\n"
"s_add_u32 s80, s50, 5\n"
"s_and_b32 s80, s80, 7\n"
"s_lshl_b32 s80, s80, 14\n"
"s_add_u32 s67, s80, s60\n"
"s_add_u32 s80, s50, 6\n"
"s_and_b32 s80, s80, 7\n"
"s_lshl_b32 s80, s80, 14\n"
"s_add_u32 s68, s80, s60\n"
"s_add_u32 s80, s50, 7\n"
"s_and_b32 s80, s80, 7\n"
"s_lshl_b32 s80, s80, 14\n"
"s_add_u32 s69, s80, s60\n"
"s_mov_b32 s56, 0x05040100\n"
"s_mov_b32 s57, 0x07060302\n"
"s_mov_b32 s58, 0x3e0293ee\n"
"s_mov_b32 s59, 0x41000000\n"
"s_mov_b32 s78, 0xffff0000\n"
"s_mov_b32 s79, 0xffff0000\n"
"s_mov_b32 s61, 0\n"
"s_getpc_b64 s[72:73]\n"
".LApc0:\n"
"s_add_u32 s72, s72, .Lrare0-.LApc0\n"
"s_addc_u32 s73, s73, 0\n"
"s_getpc_b64 s[74:75]\n"
".LApc1:\n"
"s_add_u32 s74, s74, .Lrare1-.LApc1\n"
"s_addc_u32 s75, s75, 0\n"
"s_waitcnt lgkmcnt(0)\n"
"s_lshl_b32 s83, s51, 16\n"
"s_add_u32 s20, s4, s83\n"
"s_addc_u32 s21, s5, 0\n"
"s_and_b32 s21, s21, 65535\n"
"s_mov_b32 s22, 65536\n"
"s_mov_b32 s23, 131072\n"
"s_add_u32 s80, s49, 0\n"
"s_and_b32 s80, s80, 7\n"
"s_lshl_b32 s80, s80, 7\n"
"s_lshl_b32 s81, s60, 2\n"
"s_add_u32 s80, s80, s81\n"
"s_add_u32 s81, s80, 8192\n"
"s_lshr_b32 s82, s60, 1\n"
"s_add_u32 m0, s82, 131072\n"
"s_nop 0\n"
"buffer_load_dwordx4 v1, s[20:23], s80 offen offset:0 lds\n"
"s_add_u32 m0, s82, 132096\n"
"s_nop 0\n"
"buffer_load_dwordx4 v1, s[20:23], s81 offen offset:0 lds\n"
"s_add_u32 s80, s49, 1\n"
"s_and_b32 s80, s80, 7\n"
"s_lshl_b32 s80, s80, 7\n"
"s_lshl_b32 s81, s60, 2\n"
"s_add_u32 s80, s80, s81\n"
"s_add_u32 s81, s80, 8192\n"
"s_lshr_b32 s82, s60, 1\n"
"s_add_u32 m0, s82, 139264\n"
"s_nop 0\n"
"buffer_load_dwordx4 v1, s[20:23], s80 offen offset:0 lds\n"
"s_add_u32 m0, s82, 140288\n"
"s_nop 0\n"
"buffer_load_dwordx4 v1, s[20:23], s81 offen offset:0 lds\n"
"s_lshl_b32 s83, s53, 10\n"
"s_lshl_b32 s76, s48, 8\n"
"s_add_u32 s83, s83, s76\n"
"s_sub_u32 s77, 4194304, s83\n"
"s_add_u32 s24, s10, s83\n"
"s_addc_u32 s25, s11, 0\n"
"s_and_b32 s25, s25, 65535\n"
"s_mov_b32 s26, s77\n"
"s_mov_b32 s27, 131072\n"
"s_add_u32 s84, s49, 0\n"
"s_and_b32 s84, s84, 7\n"
"s_lshl_b32 s84, s84, 19\n"
"buffer_load_dwordx4 v[32:35], v8, s[24:27], s84 offen offset:0\n"
"buffer_load_dwordx4 v[36:39], v9, s[24:27], s84 offen offset:0\n"
"buffer_load_dwordx4 v[40:43], v10, s[24:27], s84 offen offset:0\n"
"buffer_load_dwordx4 v[44:47], v11, s[24:27], s84 offen offset:0\n"
"buffer_load_dwordx4 v[48:51], v12, s[24:27], s84 offen offset:0\n"
"buffer_load_dwordx4 v[52:55], v13, s[24:27], s84 offen offset:0\n"
"buffer_load_dwordx4 v[56:59], v14, s[24:27], s84 offen offset:0\n"
"buffer_load_dwordx4 v[60:63], v15, s[24:27], s84 offen offset:0\n"
"s_sub_u32 s6, s6, 1024\n"
"s_subb_u32 s7, s7, 0\n"
"s_sub_u32 s8, s8, 1024\n"
"s_subb_u32 s9, s9, 0\n"
"s_lshl_b32 s83, s51, 4\n"
"s_add_u32 s83, s83, s53\n"
"s_lshl_b32 s83, s83, 17\n"
"s_add_u32 s28, s6, s83\n"
"s_addc_u32 s29, s7, 0\n"
"s_and_b32 s29, s29, 65535\n"
"s_mov_b32 s30, 1180672\n"
"s_mov_b32 s31, 131072\n"
"s_add_u32 s32, s8, s83\n"
"s_addc_u32 s33, s9, 0\n"
"s_and_b32 s33, s33, 65535\n"
"s_mov_b32 s34, 1180672\n"
"s_mov_b32 s35, 131072\n"
"s_lshl_b32 s83, s53, 19\n"
"s_lshl_b32 s76, s48, 8\n"
"s_add_u32 s83, s83, s76\n"
"s_sub_u32 s77, 524288, s76\n"
"s_add_u32 s36, s12, s83\n"
"s_addc_u32 s37, s13, 0\n"
"s_and_b32 s37, s37, 65535\n"
"s_mov_b32 s38, s77\n"
"s_mov_b32 s39, 131072\n"
"s_lshl_b32 s83, s54, 4\n"
"s_add_u32 s83, s83, s51\n"
"s_mul_i32 s83, s83, 561152\n"
"s_add_u32 s40, s16, s83\n"
"s_addc_u32 s41, s17, 0\n"
"s_and_b32 s41, s41, 65535\n"
"s_mov_b32 s42, 561152\n"
"s_mov_b32 s43, 131072\n"
"s_lshl_b32 s83, s51, 17\n"
"s_lshl_b32 s76, s54, 10\n"
"s_add_u32 s83, s83, s76\n"
"s_sub_u32 s77, 131072, s76\n"
"s_add_u32 s44, s18, s83\n"
"s_addc_u32 s45, s19, 0\n"
"s_and_b32 s45, s45, 65535\n"
"s_mov_b32 s46, s77\n"
"s_mov_b32 s47, 131072\n"
"s_add_u32 s4, s28, 1048576\n"
"s_addc_u32 s5, s29, 0\n"
"s_mov_b32 s6, 132096\n"
"s_mov_b32 s7, 131072\n"
"s_add_u32 s8, s32, 1048576\n"
"s_addc_u32 s9, s33, 0\n"
"s_mov_b32 s10, 132096\n"
"s_mov_b32 s11, 131072\n"
"s_add_u32 s85, s49, 0\n"
"s_and_b32 s85, s85, 7\n"
"s_lshl_b32 s85, s85, 19\n"
"s_add_u32 s85, s85, 262144\n"
"buffer_load_dwordx4 v[64:67], v8, s[24:27], s85 offen offset:0\n"
"buffer_load_dwordx4 v[68:71], v9, s[24:27], s85 offen offset:0\n"
"buffer_load_dwordx4 v[72:75], v10, s[24:27], s85 offen offset:0\n"
"buffer_load_dwordx4 v[76:79], v11, s[24:27], s85 offen offset:0\n"
"buffer_load_dwordx4 v[80:83], v12, s[24:27], s85 offen offset:0\n"
"buffer_load_dwordx4 v[84:87], v13, s[24:27], s85 offen offset:0\n"
"buffer_load_dwordx4 v[88:91], v14, s[24:27], s85 offen offset:0\n"
"buffer_load_dwordx4 v[92:95], v15, s[24:27], s85 offen offset:0\n"
"s_waitcnt vmcnt(16)\n"
"s_barrier\n"
"ds_read_b128 a[0:3], v6 offset:0\n"
"ds_read_b128 a[4:7], v6 offset:2048\n"
"ds_read_b128 a[8:11], v6 offset:4096\n"
"ds_read_b128 a[12:15], v6 offset:6144\n"
"s_waitcnt vmcnt(8)\n"
"v_perm_b32 v16, v36, v32, s56\n"
"v_perm_b32 v17, v44, v40, s56\n"
"v_perm_b32 v18, v52, v48, s56\n"
"v_perm_b32 v19, v60, v56, s56\n"
"v_perm_b32 v20, v36, v32, s57\n"
"v_perm_b32 v21, v44, v40, s57\n"
"v_perm_b32 v22, v52, v48, s57\n"
"v_perm_b32 v23, v60, v56, s57\n"
"s_waitcnt lgkmcnt(3)\n"
"v_mfma_f32_16x16x32_bf16 v[128:131], v[16:19], a[0:3], 0\n"
"v_perm_b32 v24, v37, v33, s56\n"
"v_perm_b32 v25, v45, v41, s56\n"
"v_mfma_f32_16x16x32_bf16 v[132:135], v[20:23], a[0:3], 0\n"
"s_waitcnt lgkmcnt(2)\n"
"v_perm_b32 v26, v53, v49, s56\n"
"v_perm_b32 v27, v61, v57, s56\n"
"v_mfma_f32_16x16x32_bf16 v[160:163], v[16:19], a[4:7], 0\n"
"v_perm_b32 v28, v37, v33, s57\n"
"v_perm_b32 v29, v45, v41, s57\n"
"v_mfma_f32_16x16x32_bf16 v[164:167], v[20:23], a[4:7], 0\n"
"s_waitcnt lgkmcnt(0)\n"
"v_perm_b32 v30, v53, v49, s57\n"
"v_perm_b32 v31, v61, v57, s57\n"
"v_mfma_f32_16x16x32_bf16 v[192:195], v[16:19], a[8:11], 0\n"
"s_add_u32 s84, s49, 1\n"
"s_and_b32 s84, s84, 7\n"
"v_mfma_f32_16x16x32_bf16 v[196:199], v[20:23], a[8:11], 0\n"
"s_lshl_b32 s84, s84, 19\n"
"v_mfma_f32_16x16x32_bf16 v[224:227], v[16:19], a[12:15], 0\n"
"ds_read_b128 a[16:19], v7 offset:0\n"
"buffer_load_dwordx4 v[96:99], v8, s[24:27], s84 offen offset:0\n"
"v_mfma_f32_16x16x32_bf16 v[228:231], v[20:23], a[12:15], 0\n"
"ds_read_b128 a[20:23], v7 offset:2048\n"
"v_perm_b32 v16, v38, v34, s56\n"
"v_mfma_f32_16x16x32_bf16 v[136:139], v[24:27], a[0:3], 0\n"
"ds_read_b128 a[24:27], v7 offset:4096\n"
"v_perm_b32 v17, v46, v42, s56\n"
"v_mfma_f32_16x16x32_bf16 v[140:143], v[28:31], a[0:3], 0\n"
"ds_read_b128 a[28:31], v7 offset:6144\n"
"v_perm_b32 v18, v54, v50, s56\n"
"v_mfma_f32_16x16x32_bf16 v[168:171], v[24:27], a[4:7], 0\n"
"buffer_load_dwordx4 v[100:103], v9, s[24:27], s84 offen offset:0\n"
"v_perm_b32 v19, v62, v58, s56\n"
"v_mfma_f32_16x16x32_bf16 v[172:175], v[28:31], a[4:7], 0\n"
"v_perm_b32 v20, v38, v34, s57\n"
"v_perm_b32 v21, v46, v42, s57\n"
"v_mfma_f32_16x16x32_bf16 v[200:203], v[24:27], a[8:11], 0\n"
"v_perm_b32 v22, v54, v50, s57\n"
"v_perm_b32 v23, v62, v58, s57\n"
"v_mfma_f32_16x16x32_bf16 v[204:207], v[28:31], a[8:11], 0\n"
"v_mfma_f32_16x16x32_bf16 v[232:235], v[24:27], a[12:15], 0\n"
"buffer_load_dwordx4 v[104:107], v10, s[24:27], s84 offen offset:0\n"
"v_perm_b32 v24, v39, v35, s56\n"
"v_mfma_f32_16x16x32_bf16 v[236:239], v[28:31], a[12:15], 0\n"
"v_perm_b32 v25, v47, v43, s56\n"
"v_perm_b32 v26, v55, v51, s56\n"
"v_mfma_f32_16x16x32_bf16 v[144:147], v[16:19], a[0:3], 0\n"
"v_perm_b32 v27, v63, v59, s56\n"
"v_perm_b32 v28, v39, v35, s57\n"
"v_mfma_f32_16x16x32_bf16 v[148:151], v[20:23], a[0:3], 0\n"
"v_perm_b32 v29, v47, v43, s57\n"
"v_perm_b32 v30, v55, v51, s57\n"
"v_mfma_f32_16x16x32_bf16 v[176:179], v[16:19], a[4:7], 0\n"
"buffer_load_dwordx4 v[108:111], v11, s[24:27], s84 offen offset:0\n"
"v_perm_b32 v31, v63, v59, s57\n"
"v_mfma_f32_16x16x32_bf16 v[180:183], v[20:23], a[4:7], 0\n"
"buffer_load_dwordx4 v[112:115], v12, s[24:27], s84 offen offset:0\n"
"buffer_load_dwordx4 v[116:119], v13, s[24:27], s84 offen offset:0\n"
"buffer_load_dwordx4 v[120:123], v14, s[24:27], s84 offen offset:0\n"
"buffer_load_dwordx4 v[124:127], v15, s[24:27], s84 offen offset:0\n"
"v_mfma_f32_16x16x32_bf16 v[208:211], v[16:19], a[8:11], 0\n"
"s_add_u32 s80, s49, 2\n"
"s_and_b32 s80, s80, 7\n"
"v_mfma_f32_16x16x32_bf16 v[212:215], v[20:23], a[8:11], 0\n"
"s_lshl_b32 s80, s80, 7\n"
"s_lshl_b32 s81, s60, 2\n"
"v_mfma_f32_16x16x32_bf16 v[240:243], v[16:19], a[12:15], 0\n"
"s_waitcnt vmcnt(8)\n"
"v_perm_b32 v16, v68, v64, s56\n"
"v_perm_b32 v17, v76, v72, s56\n"
"v_mfma_f32_16x16x32_bf16 v[244:247], v[20:23], a[12:15], 0\n"
"v_perm_b32 v18, v84, v80, s56\n"
"v_perm_b32 v19, v92, v88, s56\n"
"v_mfma_f32_16x16x32_bf16 v[152:155], v[24:27], a[0:3], 0\n"
"v_perm_b32 v20, v68, v64, s57\n"
"v_perm_b32 v21, v76, v72, s57\n"
"v_mfma_f32_16x16x32_bf16 v[156:159], v[28:31], a[0:3], 0\n"
"v_perm_b32 v22, v84, v80, s57\n"
"v_perm_b32 v23, v92, v88, s57\n"
"v_mfma_f32_16x16x32_bf16 v[184:187], v[24:27], a[4:7], 0\n"
"s_waitcnt lgkmcnt(0)\n"
"s_add_u32 s80, s80, s81\n"
"s_add_u32 s81, s80, 8192\n"
"v_mfma_f32_16x16x32_bf16 v[188:191], v[28:31], a[4:7], 0\n"
"s_lshr_b32 s82, s60, 1\n"
"s_add_u32 m0, s82, 147456\n"
"v_mfma_f32_16x16x32_bf16 v[216:219], v[24:27], a[8:11], 0\n"
"buffer_load_dwordx4 v1, s[20:23], s80 offen offset:0 lds\n"
"s_add_u32 m0, s82, 148480\n"
"v_mfma_f32_16x16x32_bf16 v[220:223], v[28:31], a[8:11], 0\n"
"v_mfma_f32_16x16x32_bf16 v[248:251], v[24:27], a[12:15], 0\n"
"v_perm_b32 v24, v69, v65, s56\n"
"v_perm_b32 v25, v77, v73, s56\n"
"v_mfma_f32_16x16x32_bf16 v[252:255], v[28:31], a[12:15], 0\n"
"v_perm_b32 v26, v85, v81, s56\n"
"v_perm_b32 v27, v93, v89, s56\n"
"v_mfma_f32_16x16x32_bf16 v[128:131], v[16:19], a[16:19], v[128:131]\n"
"buffer_load_dwordx4 v1, s[20:23], s81 offen offset:0 lds\n"
"v_perm_b32 v28, v69, v65, s57\n"
"v_mfma_f32_16x16x32_bf16 v[132:135], v[20:23], a[16:19], v[132:135]\n"
"v_perm_b32 v29, v77, v73, s57\n"
"v_perm_b32 v30, v85, v81, s57\n"
"v_mfma_f32_16x16x32_bf16 v[160:163], v[16:19], a[20:23], v[160:163]\n"
"v_perm_b32 v31, v93, v89, s57\n"
"s_add_u32 s80, s49, 3\n"
"v_mfma_f32_16x16x32_bf16 v[164:167], v[20:23], a[20:23], v[164:167]\n"
"s_and_b32 s80, s80, 7\n"
"s_lshl_b32 s80, s80, 7\n"
"v_mfma_f32_16x16x32_bf16 v[192:195], v[16:19], a[24:27], v[192:195]\n"
"s_lshl_b32 s81, s60, 2\n"
"s_add_u32 s80, s80, s81\n"
"v_mfma_f32_16x16x32_bf16 v[196:199], v[20:23], a[24:27], v[196:199]\n"
"s_add_u32 s81, s80, 8192\n"
"s_lshr_b32 s82, s60, 1\n"
"v_mfma_f32_16x16x32_bf16 v[224:227], v[16:19], a[28:31], v[224:227]\n"
"ds_read_b128 a[0:3], v6 offset:8192\n"
"v_perm_b32 v16, v70, v66, s56\n"
"v_mfma_f32_16x16x32_bf16 v[228:231], v[20:23], a[28:31], v[228:231]\n"
"ds_read_b128 a[4:7], v6 offset:10240\n"
"v_perm_b32 v17, v78, v74, s56\n"
"v_mfma_f32_16x16x32_bf16 v[136:139], v[24:27], a[16:19], v[136:139]\n"
"ds_read_b128 a[8:11], v6 offset:12288\n"
"v_perm_b32 v18, v86, v82, s56\n"
"v_mfma_f32_16x16x32_bf16 v[140:143], v[28:31], a[16:19], v[140:143]\n"
"ds_read_b128 a[12:15], v6 offset:14336\n"
"v_perm_b32 v19, v94, v90, s56\n"
"v_mfma_f32_16x16x32_bf16 v[168:171], v[24:27], a[20:23], v[168:171]\n"
"v_perm_b32 v20, v70, v66, s57\n"
"v_perm_b32 v21, v78, v74, s57\n"
"v_mfma_f32_16x16x32_bf16 v[172:175], v[28:31], a[20:23], v[172:175]\n"
"v_perm_b32 v22, v86, v82, s57\n"
"v_perm_b32 v23, v94, v90, s57\n"
"v_mfma_f32_16x16x32_bf16 v[200:203], v[24:27], a[24:27], v[200:203]\n"
"s_add_u32 m0, s82, 155648\n"
"v_mfma_f32_16x16x32_bf16 v[204:207], v[28:31], a[24:27], v[204:207]\n"
"buffer_load_dwordx4 v1, s[20:23], s80 offen offset:0 lds\n"
"s_add_u32 m0, s82, 156672\n"
"v_mfma_f32_16x16x32_bf16 v[232:235], v[24:27], a[28:31], v[232:235]\n"
"v_perm_b32 v24, v71, v67, s56\n"
"v_perm_b32 v25, v79, v75, s56\n"
"v_mfma_f32_16x16x32_bf16 v[236:239], v[28:31], a[28:31], v[236:239]\n"
"v_perm_b32 v26, v87, v83, s56\n"
"v_perm_b32 v27, v95, v91, s56\n"
"v_mfma_f32_16x16x32_bf16 v[144:147], v[16:19], a[16:19], v[144:147]\n"
"v_perm_b32 v28, v71, v67, s57\n"
"v_perm_b32 v29, v79, v75, s57\n"
"v_mfma_f32_16x16x32_bf16 v[148:151], v[20:23], a[16:19], v[148:151]\n"
"buffer_load_dwordx4 v1, s[20:23], s81 offen offset:0 lds\n"
"v_perm_b32 v30, v87, v83, s57\n"
"v_mfma_f32_16x16x32_bf16 v[176:179], v[16:19], a[20:23], v[176:179]\n"
"v_perm_b32 v31, v95, v91, s57\n"
"s_add_u32 s85, s49, 1\n"
"v_mfma_f32_16x16x32_bf16 v[180:183], v[20:23], a[20:23], v[180:183]\n"
"s_and_b32 s85, s85, 7\n"
"s_lshl_b32 s85, s85, 19\n"
"v_mfma_f32_16x16x32_bf16 v[208:211], v[16:19], a[24:27], v[208:211]\n"
"s_add_u32 s85, s85, 262144\n"
"v_mfma_f32_16x16x32_bf16 v[212:215], v[20:23], a[24:27], v[212:215]\n"
"buffer_load_dwordx4 v[32:35], v8, s[24:27], s85 offen offset:0\n"
"v_mfma_f32_16x16x32_bf16 v[240:243], v[16:19], a[28:31], v[240:243]\n"
"s_waitcnt vmcnt(5)\n"
"v_perm_b32 v16, v100, v96, s56\n"
"v_perm_b32 v17, v108, v104, s56\n"
"v_mfma_f32_16x16x32_bf16 v[244:247], v[20:23], a[28:31], v[244:247]\n"
"v_perm_b32 v18, v116, v112, s56\n"
"v_perm_b32 v19, v124, v120, s56\n"
"v_mfma_f32_16x16x32_bf16 v[152:155], v[24:27], a[16:19], v[152:155]\n"
"v_perm_b32 v20, v100, v96, s57\n"
"v_perm_b32 v21, v108, v104, s57\n"
"buffer_load_dwordx4 v[36:39], v9, s[24:27], s85 offen offset:0\n"
"buffer_load_dwordx4 v[40:43], v10, s[24:27], s85 offen offset:0\n"
"buffer_load_dwordx4 v[44:47], v11, s[24:27], s85 offen offset:0\n"
"buffer_load_dwordx4 v[48:51], v12, s[24:27], s85 offen offset:0\n"
"buffer_load_dwordx4 v[52:55], v13, s[24:27], s85 offen offset:0\n"
"buffer_load_dwordx4 v[56:59], v14, s[24:27], s85 offen offset:0\n"
"buffer_load_dwordx4 v[60:63], v15, s[24:27], s85 offen offset:0\n"
"v_mfma_f32_16x16x32_bf16 v[156:159], v[28:31], a[16:19], v[156:159]\n"
"v_perm_b32 v22, v116, v112, s57\n"
"v_perm_b32 v23, v124, v120, s57\n"
"v_mfma_f32_16x16x32_bf16 v[184:187], v[24:27], a[20:23], v[184:187]\n"
"s_waitcnt lgkmcnt(0)\n"
"v_mfma_f32_16x16x32_bf16 v[188:191], v[28:31], a[20:23], v[188:191]\n"
"s_add_u32 s84, s49, 2\n"
"s_and_b32 s84, s84, 7\n"
"v_mfma_f32_16x16x32_bf16 v[216:219], v[24:27], a[24:27], v[216:219]\n"
"s_lshl_b32 s84, s84, 19\n"
"v_mfma_f32_16x16x32_bf16 v[220:223], v[28:31], a[24:27], v[220:223]\n"
"buffer_load_dwordx4 v[64:67], v8, s[24:27], s84 offen offset:0\n"
"v_mfma_f32_16x16x32_bf16 v[248:251], v[24:27], a[28:31], v[248:251]\n"
"v_perm_b32 v24, v101, v97, s56\n"
"v_perm_b32 v25, v109, v105, s56\n"
"v_mfma_f32_16x16x32_bf16 v[252:255], v[28:31], a[28:31], v[252:255]\n"
"v_perm_b32 v26, v117, v113, s56\n"
"v_perm_b32 v27, v125, v121, s56\n"
"v_mfma_f32_16x16x32_bf16 v[128:131], v[16:19], a[0:3], v[128:131]\n"
"v_perm_b32 v28, v101, v97, s57\n"
"v_perm_b32 v29, v109, v105, s57\n"
"v_mfma_f32_16x16x32_bf16 v[132:135], v[20:23], a[0:3], v[132:135]\n"
"buffer_load_dwordx4 v[68:71], v9, s[24:27], s84 offen offset:0\n"
"v_perm_b32 v30, v117, v113, s57\n"
"v_mfma_f32_16x16x32_bf16 v[160:163], v[16:19], a[4:7], v[160:163]\n"
"v_perm_b32 v31, v125, v121, s57\n"
"v_mfma_f32_16x16x32_bf16 v[164:167], v[20:23], a[4:7], v[164:167]\n"
"v_mfma_f32_16x16x32_bf16 v[192:195], v[16:19], a[8:11], v[192:195]\n"
"v_mfma_f32_16x16x32_bf16 v[196:199], v[20:23], a[8:11], v[196:199]\n"
"buffer_load_dwordx4 v[72:75], v10, s[24:27], s84 offen offset:0\n"
"v_mfma_f32_16x16x32_bf16 v[224:227], v[16:19], a[12:15], v[224:227]\n"
"ds_read_b128 a[16:19], v7 offset:8192\n"
"v_perm_b32 v16, v102, v98, s56\n"
"v_mfma_f32_16x16x32_bf16 v[228:231], v[20:23], a[12:15], v[228:231]\n"
"ds_read_b128 a[20:23], v7 offset:10240\n"
"v_perm_b32 v17, v110, v106, s56\n"
"v_mfma_f32_16x16x32_bf16 v[136:139], v[24:27], a[0:3], v[136:139]\n"
"ds_read_b128 a[24:27], v7 offset:12288\n"
"v_perm_b32 v18, v118, v114, s56\n"
"v_mfma_f32_16x16x32_bf16 v[140:143], v[28:31], a[0:3], v[140:143]\n"
"ds_read_b128 a[28:31], v7 offset:14336\n"
"buffer_load_dwordx4 v[76:79], v11, s[24:27], s84 offen offset:0\n"
"v_mfma_f32_16x16x32_bf16 v[168:171], v[24:27], a[4:7], v[168:171]\n"
"v_perm_b32 v19, v126, v122, s56\n"
"v_perm_b32 v20, v102, v98, s57\n"
"v_mfma_f32_16x16x32_bf16 v[172:175], v[28:31], a[4:7], v[172:175]\n"
"v_perm_b32 v21, v110, v106, s57\n"
"v_perm_b32 v22, v118, v114, s57\n"
"v_mfma_f32_16x16x32_bf16 v[200:203], v[24:27], a[8:11], v[200:203]\n"
"v_perm_b32 v23, v126, v122, s57\n"
"v_mfma_f32_16x16x32_bf16 v[204:207], v[28:31], a[8:11], v[204:207]\n"
"buffer_load_dwordx4 v[80:83], v12, s[24:27], s84 offen offset:0\n"
"v_mfma_f32_16x16x32_bf16 v[232:235], v[24:27], a[12:15], v[232:235]\n"
"v_perm_b32 v24, v103, v99, s56\n"
"v_perm_b32 v25, v111, v107, s56\n"
"v_mfma_f32_16x16x32_bf16 v[236:239], v[28:31], a[12:15], v[236:239]\n"
"v_perm_b32 v26, v119, v115, s56\n"
"v_perm_b32 v27, v127, v123, s56\n"
"v_mfma_f32_16x16x32_bf16 v[144:147], v[16:19], a[0:3], v[144:147]\n"
"v_perm_b32 v28, v103, v99, s57\n"
"v_perm_b32 v29, v111, v107, s57\n"
"v_mfma_f32_16x16x32_bf16 v[148:151], v[20:23], a[0:3], v[148:151]\n"
"buffer_load_dwordx4 v[84:87], v13, s[24:27], s84 offen offset:0\n"
"v_perm_b32 v30, v119, v115, s57\n"
"v_mfma_f32_16x16x32_bf16 v[176:179], v[16:19], a[4:7], v[176:179]\n"
"v_perm_b32 v31, v127, v123, s57\n"
"v_mfma_f32_16x16x32_bf16 v[180:183], v[20:23], a[4:7], v[180:183]\n"
"v_mfma_f32_16x16x32_bf16 v[208:211], v[16:19], a[8:11], v[208:211]\n"
"v_mfma_f32_16x16x32_bf16 v[212:215], v[20:23], a[8:11], v[212:215]\n"
"buffer_load_dwordx4 v[88:91], v14, s[24:27], s84 offen offset:0\n"
"v_mfma_f32_16x16x32_bf16 v[240:243], v[16:19], a[12:15], v[240:243]\n"
"s_waitcnt vmcnt(7)\n"
"v_perm_b32 v16, v36, v32, s56\n"
"v_perm_b32 v17, v44, v40, s56\n"
"v_mfma_f32_16x16x32_bf16 v[244:247], v[20:23], a[12:15], v[244:247]\n"
"v_perm_b32 v18, v52, v48, s56\n"
"v_perm_b32 v19, v60, v56, s56\n"
"v_mfma_f32_16x16x32_bf16 v[152:155], v[24:27], a[0:3], v[152:155]\n"
"v_perm_b32 v20, v36, v32, s57\n"
"v_perm_b32 v21, v44, v40, s57\n"
"buffer_load_dwordx4 v[92:95], v15, s[24:27], s84 offen offset:0\n"
"v_mfma_f32_16x16x32_bf16 v[156:159], v[28:31], a[0:3], v[156:159]\n"
"v_perm_b32 v22, v52, v48, s57\n"
"v_perm_b32 v23, v60, v56, s57\n"
"v_mfma_f32_16x16x32_bf16 v[184:187], v[24:27], a[4:7], v[184:187]\n"
"s_waitcnt lgkmcnt(0)\n"
"v_mfma_f32_16x16x32_bf16 v[188:191], v[28:31], a[4:7], v[188:191]\n"
"s_add_u32 s85, s49, 2\n"
"s_and_b32 s85, s85, 7\n"
"v_mfma_f32_16x16x32_bf16 v[216:219], v[24:27], a[8:11], v[216:219]\n"
"s_lshl_b32 s85, s85, 19\n"
"s_add_u32 s85, s85, 262144\n"
"v_mfma_f32_16x16x32_bf16 v[220:223], v[28:31], a[8:11], v[220:223]\n"
"buffer_load_dwordx4 v[96:99], v8, s[24:27], s85 offen offset:0\n"
"v_mfma_f32_16x16x32_bf16 v[248:251], v[24:27], a[12:15], v[248:251]\n"
"v_perm_b32 v24, v37, v33, s56\n"
"v_perm_b32 v25, v45, v41, s56\n"
"v_mfma_f32_16x16x32_bf16 v[252:255], v[28:31], a[12:15], v[252:255]\n"
"v_perm_b32 v26, v53, v49, s56\n"
"v_perm_b32 v27, v61, v57, s56\n"
"v_mfma_f32_16x16x32_bf16 v[128:131], v[16:19], a[16:19], v[128:131]\n"
"v_perm_b32 v28, v37, v33, s57\n"
"v_perm_b32 v29, v45, v41, s57\n"
"v_mfma_f32_16x16x32_bf16 v[132:135], v[20:23], a[16:19], v[132:135]\n"
"buffer_load_dwordx4 v[100:103], v9, s[24:27], s85 offen offset:0\n"
"v_perm_b32 v30, v53, v49, s57\n"
"v_mfma_f32_16x16x32_bf16 v[160:163], v[16:19], a[20:23], v[160:163]\n"
"v_perm_b32 v31, v61, v57, s57\n"
"v_mfma_f32_16x16x32_bf16 v[164:167], v[20:23], a[20:23], v[164:167]\n"
"v_mfma_f32_16x16x32_bf16 v[192:195], v[16:19], a[24:27], v[192:195]\n"
"buffer_load_dwordx4 v[104:107], v10, s[24:27], s85 offen offset:0\n"
"buffer_load_dwordx4 v[108:111], v11, s[24:27], s85 offen offset:0\n"
"buffer_load_dwordx4 v[112:115], v12, s[24:27], s85 offen offset:0\n"
"buffer_load_dwordx4 v[116:119], v13, s[24:27], s85 offen offset:0\n"
"buffer_load_dwordx4 v[120:123], v14, s[24:27], s85 offen offset:0\n"
"buffer_load_dwordx4 v[124:127], v15, s[24:27], s85 offen offset:0\n"
"s_barrier\n"
"v_mfma_f32_16x16x32_bf16 v[196:199], v[20:23], a[24:27], v[196:199]\n"
"v_mfma_f32_16x16x32_bf16 v[224:227], v[16:19], a[28:31], v[224:227]\n"
"ds_read_b128 a[0:3], v6 offset:16384\n"
"v_perm_b32 v16, v38, v34, s56\n"
"v_mfma_f32_16x16x32_bf16 v[228:231], v[20:23], a[28:31], v[228:231]\n"
"ds_read_b128 a[4:7], v6 offset:18432\n"
"v_perm_b32 v17, v46, v42, s56\n"
"v_mfma_f32_16x16x32_bf16 v[136:139], v[24:27], a[16:19], v[136:139]\n"
"ds_read_b128 a[8:11], v6 offset:20480\n"
"v_perm_b32 v18, v54, v50, s56\n"
"v_mfma_f32_16x16x32_bf16 v[140:143], v[28:31], a[16:19], v[140:143]\n"
"ds_read_b128 a[12:15], v6 offset:22528\n"
"v_perm_b32 v19, v62, v58, s56\n"
"v_mfma_f32_16x16x32_bf16 v[168:171], v[24:27], a[20:23], v[168:171]\n"
"v_perm_b32 v20, v38, v34, s57\n"
"v_perm_b32 v21, v46, v42, s57\n"
"v_mfma_f32_16x16x32_bf16 v[172:175], v[28:31], a[20:23], v[172:175]\n"
"v_perm_b32 v22, v54, v50, s57\n"
"v_perm_b32 v23, v62, v58, s57\n"
"v_mfma_f32_16x16x32_bf16 v[200:203], v[24:27], a[24:27], v[200:203]\n"
"s_add_u32 s80, s49, 4\n"
"s_and_b32 s80, s80, 7\n"
"v_mfma_f32_16x16x32_bf16 v[204:207], v[28:31], a[24:27], v[204:207]\n"
"s_lshl_b32 s80, s80, 7\n"
"s_lshl_b32 s81, s60, 2\n"
"v_mfma_f32_16x16x32_bf16 v[232:235], v[24:27], a[28:31], v[232:235]\n"
"v_perm_b32 v24, v39, v35, s56\n"
"v_perm_b32 v25, v47, v43, s56\n"
"v_mfma_f32_16x16x32_bf16 v[236:239], v[28:31], a[28:31], v[236:239]\n"
"v_perm_b32 v26, v55, v51, s56\n"
"v_perm_b32 v27, v63, v59, s56\n"
"v_mfma_f32_16x16x32_bf16 v[144:147], v[16:19], a[16:19], v[144:147]\n"
"v_perm_b32 v28, v39, v35, s57\n"
"v_perm_b32 v29, v47, v43, s57\n"
"v_mfma_f32_16x16x32_bf16 v[148:151], v[20:23], a[16:19], v[148:151]\n"
"v_perm_b32 v30, v55, v51, s57\n"
"v_perm_b32 v31, v63, v59, s57\n"
"v_mfma_f32_16x16x32_bf16 v[176:179], v[16:19], a[20:23], v[176:179]\n"
"s_add_u32 s80, s80, s81\n"
"s_add_u32 s81, s80, 8192\n"
"v_mfma_f32_16x16x32_bf16 v[180:183], v[20:23], a[20:23], v[180:183]\n"
"s_lshr_b32 s82, s60, 1\n"
"s_add_u32 m0, s82, 131072\n"
"v_mfma_f32_16x16x32_bf16 v[208:211], v[16:19], a[24:27], v[208:211]\n"
"buffer_load_dwordx4 v1, s[20:23], s80 offen offset:0 lds\n"
"s_add_u32 m0, s82, 132096\n"
"v_mfma_f32_16x16x32_bf16 v[212:215], v[20:23], a[24:27], v[212:215]\n"
"v_mfma_f32_16x16x32_bf16 v[240:243], v[16:19], a[28:31], v[240:243]\n"
"s_waitcnt vmcnt(9)\n"
"v_perm_b32 v16, v68, v64, s56\n"
"v_perm_b32 v17, v76, v72, s56\n"
"v_mfma_f32_16x16x32_bf16 v[244:247], v[20:23], a[28:31], v[244:247]\n"
"v_perm_b32 v18, v84, v80, s56\n"
"v_perm_b32 v19, v92, v88, s56\n"
"v_mfma_f32_16x16x32_bf16 v[152:155], v[24:27], a[16:19], v[152:155]\n"
"buffer_load_dwordx4 v1, s[20:23], s81 offen offset:0 lds\n"
"v_perm_b32 v20, v68, v64, s57\n"
"v_mfma_f32_16x16x32_bf16 v[156:159], v[28:31], a[16:19], v[156:159]\n"
"v_perm_b32 v21, v76, v72, s57\n"
"v_perm_b32 v22, v84, v80, s57\n"
"v_mfma_f32_16x16x32_bf16 v[184:187], v[24:27], a[20:23], v[184:187]\n"
"s_waitcnt lgkmcnt(0)\n"
"v_perm_b32 v23, v92, v88, s57\n"
"s_add_u32 s80, s49, 5\n"
"v_mfma_f32_16x16x32_bf16 v[188:191], v[28:31], a[20:23], v[188:191]\n"
"s_and_b32 s80, s80, 7\n"
"s_lshl_b32 s80, s80, 7\n"
"v_mfma_f32_16x16x32_bf16 v[216:219], v[24:27], a[24:27], v[216:219]\n"
"s_lshl_b32 s81, s60, 2\n"
"s_add_u32 s80, s80, s81\n"
"v_mfma_f32_16x16x32_bf16 v[220:223], v[28:31], a[24:27], v[220:223]\n"
"s_add_u32 s81, s80, 8192\n"
"s_lshr_b32 s82, s60, 1\n"
"v_mfma_f32_16x16x32_bf16 v[248:251], v[24:27], a[28:31], v[248:251]\n"
"v_perm_b32 v24, v69, v65, s56\n"
"v_perm_b32 v25, v77, v73, s56\n"
"v_mfma_f32_16x16x32_bf16 v[252:255], v[28:31], a[28:31], v[252:255]\n"
"v_perm_b32 v26, v85, v81, s56\n"
"v_perm_b32 v27, v93, v89, s56\n"
"v_mfma_f32_16x16x32_bf16 v[128:131], v[16:19], a[0:3], v[128:131]\n"
"v_perm_b32 v28, v69, v65, s57\n"
"v_perm_b32 v29, v77, v73, s57\n"
"v_mfma_f32_16x16x32_bf16 v[132:135], v[20:23], a[0:3], v[132:135]\n"
"v_perm_b32 v30, v85, v81, s57\n"
"v_perm_b32 v31, v93, v89, s57\n"
"v_mfma_f32_16x16x32_bf16 v[160:163], v[16:19], a[4:7], v[160:163]\n"
"s_add_u32 m0, s82, 139264\n"
"v_mfma_f32_16x16x32_bf16 v[164:167], v[20:23], a[4:7], v[164:167]\n"
"buffer_load_dwordx4 v1, s[20:23], s80 offen offset:0 lds\n"
"s_add_u32 m0, s82, 140288\n"
"v_mfma_f32_16x16x32_bf16 v[192:195], v[16:19], a[8:11], v[192:195]\n"
"v_mfma_f32_16x16x32_bf16 v[196:199], v[20:23], a[8:11], v[196:199]\n"
"v_mfma_f32_16x16x32_bf16 v[224:227], v[16:19], a[12:15], v[224:227]\n"
"ds_read_b128 a[16:19], v7 offset:16384\n"
"v_perm_b32 v16, v70, v66, s56\n"
"v_mfma_f32_16x16x32_bf16 v[228:231], v[20:23], a[12:15], v[228:231]\n"
"ds_read_b128 a[20:23], v7 offset:18432\n"
"buffer_load_dwordx4 v1, s[20:23], s81 offen offset:0 lds\n"
"v_mfma_f32_16x16x32_bf16 v[136:139], v[24:27], a[0:3], v[136:139]\n"
"ds_read_b128 a[24:27], v7 offset:20480\n"
"v_perm_b32 v17, v78, v74, s56\n"
"v_mfma_f32_16x16x32_bf16 v[140:143], v[28:31], a[0:3], v[140:143]\n"
"ds_read_b128 a[28:31], v7 offset:22528\n"
"v_perm_b32 v18, v86, v82, s56\n"
"v_mfma_f32_16x16x32_bf16 v[168:171], v[24:27], a[4:7], v[168:171]\n"
"v_perm_b32 v19, v94, v90, s56\n"
"v_perm_b32 v20, v70, v66, s57\n"
"v_mfma_f32_16x16x32_bf16 v[172:175], v[28:31], a[4:7], v[172:175]\n"
"v_perm_b32 v21, v78, v74, s57\n"
"v_perm_b32 v22, v86, v82, s57\n"
"v_mfma_f32_16x16x32_bf16 v[200:203], v[24:27], a[8:11], v[200:203]\n"
"v_perm_b32 v23, v94, v90, s57\n"
"s_add_u32 s84, s49, 3\n"
"v_mfma_f32_16x16x32_bf16 v[204:207], v[28:31], a[8:11], v[204:207]\n"
"s_and_b32 s84, s84, 7\n"
"s_lshl_b32 s84, s84, 19\n"
"v_mfma_f32_16x16x32_bf16 v[232:235], v[24:27], a[12:15], v[232:235]\n"
"buffer_load_dwordx4 v[32:35], v8, s[24:27], s84 offen offset:0\n"
"v_perm_b32 v24, v71, v67, s56\n"
"v_mfma_f32_16x16x32_bf16 v[236:239], v[28:31], a[12:15], v[236:239]\n"
"v_perm_b32 v25, v79, v75, s56\n"
"v_perm_b32 v26, v87, v83, s56\n"
"v_mfma_f32_16x16x32_bf16 v[144:147], v[16:19], a[0:3], v[144:147]\n"
"v_perm_b32 v27, v95, v91, s56\n"
"v_perm_b32 v28, v71, v67, s57\n"
"v_mfma_f32_16x16x32_bf16 v[148:151], v[20:23], a[0:3], v[148:151]\n"
"v_perm_b32 v29, v79, v75, s57\n"
"v_perm_b32 v30, v87, v83, s57\n"
"v_mfma_f32_16x16x32_bf16 v[176:179], v[16:19], a[4:7], v[176:179]\n"
"buffer_load_dwordx4 v[36:39], v9, s[24:27], s84 offen offset:0\n"
"v_perm_b32 v31, v95, v91, s57\n"
"v_mfma_f32_16x16x32_bf16 v[180:183], v[20:23], a[4:7], v[180:183]\n"
"v_mfma_f32_16x16x32_bf16 v[208:211], v[16:19], a[8:11], v[208:211]\n"
"v_mfma_f32_16x16x32_bf16 v[212:215], v[20:23], a[8:11], v[212:215]\n"
"v_mfma_f32_16x16x32_bf16 v[240:243], v[16:19], a[12:15], v[240:243]\n"
"s_waitcnt vmcnt(6)\n"
"buffer_load_dwordx4 v[40:43], v10, s[24:27], s84 offen offset:0\n"
"v_perm_b32 v16, v100, v96, s56\n"
"v_mfma_f32_16x16x32_bf16 v[244:247], v[20:23], a[12:15], v[244:247]\n"
"v_perm_b32 v17, v108, v104, s56\n"
"v_perm_b32 v18, v116, v112, s56\n"
"v_mfma_f32_16x16x32_bf16 v[152:155], v[24:27], a[0:3], v[152:155]\n"
"v_perm_b32 v19, v124, v120, s56\n"
"v_perm_b32 v20, v100, v96, s57\n"
"buffer_load_dwordx4 v[44:47], v11, s[24:27], s84 offen offset:0\n"
"buffer_load_dwordx4 v[48:51], v12, s[24:27], s84 offen offset:0\n"
"buffer_load_dwordx4 v[52:55], v13, s[24:27], s84 offen offset:0\n"
"buffer_load_dwordx4 v[56:59], v14, s[24:27], s84 offen offset:0\n"
"buffer_load_dwordx4 v[60:63], v15, s[24:27], s84 offen offset:0\n"
"v_mfma_f32_16x16x32_bf16 v[156:159], v[28:31], a[0:3], v[156:159]\n"
"v_perm_b32 v21, v108, v104, s57\n"
"v_perm_b32 v22, v116, v112, s57\n"
"v_mfma_f32_16x16x32_bf16 v[184:187], v[24:27], a[4:7], v[184:187]\n"
"s_waitcnt lgkmcnt(0)\n"
"v_perm_b32 v23, v124, v120, s57\n"
"v_mfma_f32_16x16x32_bf16 v[188:191], v[28:31], a[4:7], v[188:191]\n"
"s_add_u32 s85, s49, 3\n"
"s_and_b32 s85, s85, 7\n"
"v_mfma_f32_16x16x32_bf16 v[216:219], v[24:27], a[8:11], v[216:219]\n"
"s_lshl_b32 s85, s85, 19\n"
"s_add_u32 s85, s85, 262144\n"
"v_mfma_f32_16x16x32_bf16 v[220:223], v[28:31], a[8:11], v[220:223]\n"
"buffer_load_dwordx4 v[64:67], v8, s[24:27], s85 offen offset:0\n"
"v_mfma_f32_16x16x32_bf16 v[248:251], v[24:27], a[12:15], v[248:251]\n"
"v_perm_b32 v24, v101, v97, s56\n"
"v_perm_b32 v25, v109, v105, s56\n"
"v_mfma_f32_16x16x32_bf16 v[252:255], v[28:31], a[12:15], v[252:255]\n"
"v_perm_b32 v26, v117, v113, s56\n"
"v_perm_b32 v27, v125, v121, s56\n"
"v_mfma_f32_16x16x32_bf16 v[128:131], v[16:19], a[16:19], v[128:131]\n"
"v_perm_b32 v28, v101, v97, s57\n"
"v_perm_b32 v29, v109, v105, s57\n"
"v_mfma_f32_16x16x32_bf16 v[132:135], v[20:23], a[16:19], v[132:135]\n"
"buffer_load_dwordx4 v[68:71], v9, s[24:27], s85 offen offset:0\n"
"v_perm_b32 v30, v117, v113, s57\n"
"v_mfma_f32_16x16x32_bf16 v[160:163], v[16:19], a[20:23], v[160:163]\n"
"v_perm_b32 v31, v125, v121, s57\n"
"v_mfma_f32_16x16x32_bf16 v[164:167], v[20:23], a[20:23], v[164:167]\n"
"v_mfma_f32_16x16x32_bf16 v[192:195], v[16:19], a[24:27], v[192:195]\n"
"v_mfma_f32_16x16x32_bf16 v[196:199], v[20:23], a[24:27], v[196:199]\n"
"buffer_load_dwordx4 v[72:75], v10, s[24:27], s85 offen offset:0\n"
"v_mfma_f32_16x16x32_bf16 v[224:227], v[16:19], a[28:31], v[224:227]\n"
"ds_read_b128 a[0:3], v6 offset:24576\n"
"v_perm_b32 v16, v102, v98, s56\n"
"v_mfma_f32_16x16x32_bf16 v[228:231], v[20:23], a[28:31], v[228:231]\n"
"ds_read_b128 a[4:7], v6 offset:26624\n"
"v_perm_b32 v17, v110, v106, s56\n"
"v_mfma_f32_16x16x32_bf16 v[136:139], v[24:27], a[16:19], v[136:139]\n"
"ds_read_b128 a[8:11], v6 offset:28672\n"
"v_perm_b32 v18, v118, v114, s56\n"
"v_mfma_f32_16x16x32_bf16 v[140:143], v[28:31], a[16:19], v[140:143]\n"
"ds_read_b128 a[12:15], v6 offset:30720\n"
"buffer_load_dwordx4 v[76:79], v11, s[24:27], s85 offen offset:0\n"
"v_mfma_f32_16x16x32_bf16 v[168:171], v[24:27], a[20:23], v[168:171]\n"
"v_perm_b32 v19, v126, v122, s56\n"
"v_perm_b32 v20, v102, v98, s57\n"
"v_mfma_f32_16x16x32_bf16 v[172:175], v[28:31], a[20:23], v[172:175]\n"
"v_perm_b32 v21, v110, v106, s57\n"
"v_perm_b32 v22, v118, v114, s57\n"
"v_mfma_f32_16x16x32_bf16 v[200:203], v[24:27], a[24:27], v[200:203]\n"
"v_perm_b32 v23, v126, v122, s57\n"
"v_mfma_f32_16x16x32_bf16 v[204:207], v[28:31], a[24:27], v[204:207]\n"
"buffer_load_dwordx4 v[80:83], v12, s[24:27], s85 offen offset:0\n"
"v_mfma_f32_16x16x32_bf16 v[232:235], v[24:27], a[28:31], v[232:235]\n"
"v_perm_b32 v24, v103, v99, s56\n"
"v_perm_b32 v25, v111, v107, s56\n"
"v_mfma_f32_16x16x32_bf16 v[236:239], v[28:31], a[28:31], v[236:239]\n"
"v_perm_b32 v26, v119, v115, s56\n"
"v_perm_b32 v27, v127, v123, s56\n"
"v_mfma_f32_16x16x32_bf16 v[144:147], v[16:19], a[16:19], v[144:147]\n"
"v_perm_b32 v28, v103, v99, s57\n"
"v_perm_b32 v29, v111, v107, s57\n"
"v_mfma_f32_16x16x32_bf16 v[148:151], v[20:23], a[16:19], v[148:151]\n"
"buffer_load_dwordx4 v[84:87], v13, s[24:27], s85 offen offset:0\n"
"v_perm_b32 v30, v119, v115, s57\n"
"v_mfma_f32_16x16x32_bf16 v[176:179], v[16:19], a[20:23], v[176:179]\n"
"v_perm_b32 v31, v127, v123, s57\n"
"v_mfma_f32_16x16x32_bf16 v[180:183], v[20:23], a[20:23], v[180:183]\n"
"v_mfma_f32_16x16x32_bf16 v[208:211], v[16:19], a[24:27], v[208:211]\n"
"v_mfma_f32_16x16x32_bf16 v[212:215], v[20:23], a[24:27], v[212:215]\n"
"buffer_load_dwordx4 v[88:91], v14, s[24:27], s85 offen offset:0\n"
"v_mfma_f32_16x16x32_bf16 v[240:243], v[16:19], a[28:31], v[240:243]\n"
"s_waitcnt vmcnt(7)\n"
"v_perm_b32 v16, v36, v32, s56\n"
"v_perm_b32 v17, v44, v40, s56\n"
"v_mfma_f32_16x16x32_bf16 v[244:247], v[20:23], a[28:31], v[244:247]\n"
"v_perm_b32 v18, v52, v48, s56\n"
"v_perm_b32 v19, v60, v56, s56\n"
"v_mfma_f32_16x16x32_bf16 v[152:155], v[24:27], a[16:19], v[152:155]\n"
"v_perm_b32 v20, v36, v32, s57\n"
"v_perm_b32 v21, v44, v40, s57\n"
"buffer_load_dwordx4 v[92:95], v15, s[24:27], s85 offen offset:0\n"
"v_mfma_f32_16x16x32_bf16 v[156:159], v[28:31], a[16:19], v[156:159]\n"
"v_perm_b32 v22, v52, v48, s57\n"
"v_perm_b32 v23, v60, v56, s57\n"
"v_mfma_f32_16x16x32_bf16 v[184:187], v[24:27], a[20:23], v[184:187]\n"
"s_waitcnt lgkmcnt(0)\n"
"v_mfma_f32_16x16x32_bf16 v[188:191], v[28:31], a[20:23], v[188:191]\n"
"s_add_u32 s84, s49, 4\n"
"s_and_b32 s84, s84, 7\n"
"v_mfma_f32_16x16x32_bf16 v[216:219], v[24:27], a[24:27], v[216:219]\n"
"s_lshl_b32 s84, s84, 19\n"
"v_mfma_f32_16x16x32_bf16 v[220:223], v[28:31], a[24:27], v[220:223]\n"
"buffer_load_dwordx4 v[96:99], v8, s[24:27], s84 offen offset:0\n"
"v_mfma_f32_16x16x32_bf16 v[248:251], v[24:27], a[28:31], v[248:251]\n"
"v_perm_b32 v24, v37, v33, s56\n"
"v_perm_b32 v25, v45, v41, s56\n"
"v_mfma_f32_16x16x32_bf16 v[252:255], v[28:31], a[28:31], v[252:255]\n"
"v_perm_b32 v26, v53, v49, s56\n"
"v_perm_b32 v27, v61, v57, s56\n"
"v_mfma_f32_16x16x32_bf16 v[128:131], v[16:19], a[0:3], v[128:131]\n"
"v_perm_b32 v28, v37, v33, s57\n"
"v_perm_b32 v29, v45, v41, s57\n"
"v_mfma_f32_16x16x32_bf16 v[132:135], v[20:23], a[0:3], v[132:135]\n"
"buffer_load_dwordx4 v[100:103], v9, s[24:27], s84 offen offset:0\n"
"v_perm_b32 v30, v53, v49, s57\n"
"v_mfma_f32_16x16x32_bf16 v[160:163], v[16:19], a[4:7], v[160:163]\n"
"v_perm_b32 v31, v61, v57, s57\n"
"v_mfma_f32_16x16x32_bf16 v[164:167], v[20:23], a[4:7], v[164:167]\n"
"v_mfma_f32_16x16x32_bf16 v[192:195], v[16:19], a[8:11], v[192:195]\n"
"v_mfma_f32_16x16x32_bf16 v[196:199], v[20:23], a[8:11], v[196:199]\n"
"buffer_load_dwordx4 v[104:107], v10, s[24:27], s84 offen offset:0\n"
"v_mfma_f32_16x16x32_bf16 v[224:227], v[16:19], a[12:15], v[224:227]\n"
"ds_read_b128 a[16:19], v7 offset:24576\n"
"v_perm_b32 v16, v38, v34, s56\n"
"v_mfma_f32_16x16x32_bf16 v[228:231], v[20:23], a[12:15], v[228:231]\n"
"ds_read_b128 a[20:23], v7 offset:26624\n"
"v_perm_b32 v17, v46, v42, s56\n"
"v_mfma_f32_16x16x32_bf16 v[136:139], v[24:27], a[0:3], v[136:139]\n"
"ds_read_b128 a[24:27], v7 offset:28672\n"
"v_perm_b32 v18, v54, v50, s56\n"
"v_mfma_f32_16x16x32_bf16 v[140:143], v[28:31], a[0:3], v[140:143]\n"
"ds_read_b128 a[28:31], v7 offset:30720\n"
"buffer_load_dwordx4 v[108:111], v11, s[24:27], s84 offen offset:0\n"
"v_mfma_f32_16x16x32_bf16 v[168:171], v[24:27], a[4:7], v[168:171]\n"
"v_perm_b32 v19, v62, v58, s56\n"
"v_perm_b32 v20, v38, v34, s57\n"
"v_mfma_f32_16x16x32_bf16 v[172:175], v[28:31], a[4:7], v[172:175]\n"
"v_perm_b32 v21, v46, v42, s57\n"
"v_perm_b32 v22, v54, v50, s57\n"
"v_mfma_f32_16x16x32_bf16 v[200:203], v[24:27], a[8:11], v[200:203]\n"
"v_perm_b32 v23, v62, v58, s57\n"
"v_mfma_f32_16x16x32_bf16 v[204:207], v[28:31], a[8:11], v[204:207]\n"
"buffer_load_dwordx4 v[112:115], v12, s[24:27], s84 offen offset:0\n"
"v_mfma_f32_16x16x32_bf16 v[232:235], v[24:27], a[12:15], v[232:235]\n"
"v_perm_b32 v24, v39, v35, s56\n"
"v_perm_b32 v25, v47, v43, s56\n"
"v_mfma_f32_16x16x32_bf16 v[236:239], v[28:31], a[12:15], v[236:239]\n"
"v_perm_b32 v26, v55, v51, s56\n"
"v_perm_b32 v27, v63, v59, s56\n"
"v_mfma_f32_16x16x32_bf16 v[144:147], v[16:19], a[0:3], v[144:147]\n"
"v_perm_b32 v28, v39, v35, s57\n"
"v_perm_b32 v29, v47, v43, s57\n"
"v_mfma_f32_16x16x32_bf16 v[148:151], v[20:23], a[0:3], v[148:151]\n"
"buffer_load_dwordx4 v[116:119], v13, s[24:27], s84 offen offset:0\n"
"v_perm_b32 v30, v55, v51, s57\n"
"v_mfma_f32_16x16x32_bf16 v[176:179], v[16:19], a[4:7], v[176:179]\n"
"v_perm_b32 v31, v63, v59, s57\n"
"v_mfma_f32_16x16x32_bf16 v[180:183], v[20:23], a[4:7], v[180:183]\n"
"v_mfma_f32_16x16x32_bf16 v[208:211], v[16:19], a[8:11], v[208:211]\n"
"v_mfma_f32_16x16x32_bf16 v[212:215], v[20:23], a[8:11], v[212:215]\n"
"buffer_load_dwordx4 v[120:123], v14, s[24:27], s84 offen offset:0\n"
"v_mfma_f32_16x16x32_bf16 v[240:243], v[16:19], a[12:15], v[240:243]\n"
"s_waitcnt vmcnt(7)\n"
"v_perm_b32 v16, v68, v64, s56\n"
"v_perm_b32 v17, v76, v72, s56\n"
"v_mfma_f32_16x16x32_bf16 v[244:247], v[20:23], a[12:15], v[244:247]\n"
"v_perm_b32 v18, v84, v80, s56\n"
"v_perm_b32 v19, v92, v88, s56\n"
"v_mfma_f32_16x16x32_bf16 v[152:155], v[24:27], a[0:3], v[152:155]\n"
"v_perm_b32 v20, v68, v64, s57\n"
"v_perm_b32 v21, v76, v72, s57\n"
"buffer_load_dwordx4 v[124:127], v15, s[24:27], s84 offen offset:0\n"
"v_mfma_f32_16x16x32_bf16 v[156:159], v[28:31], a[0:3], v[156:159]\n"
"v_perm_b32 v22, v84, v80, s57\n"
"v_perm_b32 v23, v92, v88, s57\n"
"v_mfma_f32_16x16x32_bf16 v[184:187], v[24:27], a[4:7], v[184:187]\n"
"s_waitcnt lgkmcnt(0)\n"
"v_mfma_f32_16x16x32_bf16 v[188:191], v[28:31], a[4:7], v[188:191]\n"
"s_add_u32 s85, s49, 4\n"
"s_and_b32 s85, s85, 7\n"
"v_mfma_f32_16x16x32_bf16 v[216:219], v[24:27], a[8:11], v[216:219]\n"
"s_lshl_b32 s85, s85, 19\n"
"s_add_u32 s85, s85, 262144\n"
"v_mfma_f32_16x16x32_bf16 v[220:223], v[28:31], a[8:11], v[220:223]\n"
"buffer_load_dwordx4 v[32:35], v8, s[24:27], s85 offen offset:0\n"
"v_mfma_f32_16x16x32_bf16 v[248:251], v[24:27], a[12:15], v[248:251]\n"
"v_perm_b32 v24, v69, v65, s56\n"
"v_perm_b32 v25, v77, v73, s56\n"
"v_mfma_f32_16x16x32_bf16 v[252:255], v[28:31], a[12:15], v[252:255]\n"
"v_perm_b32 v26, v85, v81, s56\n"
"v_perm_b32 v27, v93, v89, s56\n"
"v_mfma_f32_16x16x32_bf16 v[128:131], v[16:19], a[16:19], v[128:131]\n"
"v_perm_b32 v28, v69, v65, s57\n"
"v_perm_b32 v29, v77, v73, s57\n"
"v_mfma_f32_16x16x32_bf16 v[132:135], v[20:23], a[16:19], v[132:135]\n"
"buffer_load_dwordx4 v[36:39], v9, s[24:27], s85 offen offset:0\n"
"v_perm_b32 v30, v85, v81, s57\n"
"v_mfma_f32_16x16x32_bf16 v[160:163], v[16:19], a[20:23], v[160:163]\n"
"v_perm_b32 v31, v93, v89, s57\n"
"v_mfma_f32_16x16x32_bf16 v[164:167], v[20:23], a[20:23], v[164:167]\n"
"v_mfma_f32_16x16x32_bf16 v[192:195], v[16:19], a[24:27], v[192:195]\n"
"buffer_load_dwordx4 v[40:43], v10, s[24:27], s85 offen offset:0\n"
"buffer_load_dwordx4 v[44:47], v11, s[24:27], s85 offen offset:0\n"
"buffer_load_dwordx4 v[48:51], v12, s[24:27], s85 offen offset:0\n"
"buffer_load_dwordx4 v[52:55], v13, s[24:27], s85 offen offset:0\n"
"buffer_load_dwordx4 v[56:59], v14, s[24:27], s85 offen offset:0\n"
"buffer_load_dwordx4 v[60:63], v15, s[24:27], s85 offen offset:0\n"
"s_barrier\n"
"v_mfma_f32_16x16x32_bf16 v[196:199], v[20:23], a[24:27], v[196:199]\n"
"v_mfma_f32_16x16x32_bf16 v[224:227], v[16:19], a[28:31], v[224:227]\n"
"ds_read_b128 a[0:3], v6 offset:0\n"
"v_perm_b32 v16, v70, v66, s56\n"
"v_mfma_f32_16x16x32_bf16 v[228:231], v[20:23], a[28:31], v[228:231]\n"
"ds_read_b128 a[4:7], v6 offset:2048\n"
"v_perm_b32 v17, v78, v74, s56\n"
"v_mfma_f32_16x16x32_bf16 v[136:139], v[24:27], a[16:19], v[136:139]\n"
"ds_read_b128 a[8:11], v6 offset:4096\n"
"v_perm_b32 v18, v86, v82, s56\n"
"v_mfma_f32_16x16x32_bf16 v[140:143], v[28:31], a[16:19], v[140:143]\n"
"ds_read_b128 a[12:15], v6 offset:6144\n"
"v_perm_b32 v19, v94, v90, s56\n"
"v_mfma_f32_16x16x32_bf16 v[168:171], v[24:27], a[20:23], v[168:171]\n"
"v_perm_b32 v20, v70, v66, s57\n"
"v_perm_b32 v21, v78, v74, s57\n"
"v_mfma_f32_16x16x32_bf16 v[172:175], v[28:31], a[20:23], v[172:175]\n"
"v_perm_b32 v22, v86, v82, s57\n"
"v_perm_b32 v23, v94, v90, s57\n"
"v_mfma_f32_16x16x32_bf16 v[200:203], v[24:27], a[24:27], v[200:203]\n"
"s_add_u32 s80, s49, 6\n"
"s_and_b32 s80, s80, 7\n"
"v_mfma_f32_16x16x32_bf16 v[204:207], v[28:31], a[24:27], v[204:207]\n"
"s_lshl_b32 s80, s80, 7\n"
"s_lshl_b32 s81, s60, 2\n"
"v_mfma_f32_16x16x32_bf16 v[232:235], v[24:27], a[28:31], v[232:235]\n"
"v_perm_b32 v24, v71, v67, s56\n"
"v_perm_b32 v25, v79, v75, s56\n"
"v_mfma_f32_16x16x32_bf16 v[236:239], v[28:31], a[28:31], v[236:239]\n"
"v_perm_b32 v26, v87, v83, s56\n"
"v_perm_b32 v27, v95, v91, s56\n"
"v_mfma_f32_16x16x32_bf16 v[144:147], v[16:19], a[16:19], v[144:147]\n"
"v_perm_b32 v28, v71, v67, s57\n"
"v_perm_b32 v29, v79, v75, s57\n"
"v_mfma_f32_16x16x32_bf16 v[148:151], v[20:23], a[16:19], v[148:151]\n"
"v_perm_b32 v30, v87, v83, s57\n"
"v_perm_b32 v31, v95, v91, s57\n"
"v_mfma_f32_16x16x32_bf16 v[176:179], v[16:19], a[20:23], v[176:179]\n"
"s_add_u32 s80, s80, s81\n"
"s_add_u32 s81, s80, 8192\n"
"v_mfma_f32_16x16x32_bf16 v[180:183], v[20:23], a[20:23], v[180:183]\n"
"s_lshr_b32 s82, s60, 1\n"
"s_add_u32 m0, s82, 147456\n"
"v_mfma_f32_16x16x32_bf16 v[208:211], v[16:19], a[24:27], v[208:211]\n"
"buffer_load_dwordx4 v1, s[20:23], s80 offen offset:0 lds\n"
"s_add_u32 m0, s82, 148480\n"
"v_mfma_f32_16x16x32_bf16 v[212:215], v[20:23], a[24:27], v[212:215]\n"
"v_mfma_f32_16x16x32_bf16 v[240:243], v[16:19], a[28:31], v[240:243]\n"
"s_waitcnt vmcnt(9)\n"
"v_perm_b32 v16, v100, v96, s56\n"
"v_perm_b32 v17, v108, v104, s56\n"
"v_mfma_f32_16x16x32_bf16 v[244:247], v[20:23], a[28:31], v[244:247]\n"
"v_perm_b32 v18, v116, v112, s56\n"
"v_perm_b32 v19, v124, v120, s56\n"
"v_mfma_f32_16x16x32_bf16 v[152:155], v[24:27], a[16:19], v[152:155]\n"
"buffer_load_dwordx4 v1, s[20:23], s81 offen offset:0 lds\n"
"v_perm_b32 v20, v100, v96, s57\n"
"v_mfma_f32_16x16x32_bf16 v[156:159], v[28:31], a[16:19], v[156:159]\n"
"v_perm_b32 v21, v108, v104, s57\n"
"v_perm_b32 v22, v116, v112, s57\n"
"v_mfma_f32_16x16x32_bf16 v[184:187], v[24:27], a[20:23], v[184:187]\n"
"s_waitcnt lgkmcnt(0)\n"
"v_perm_b32 v23, v124, v120, s57\n"
"s_add_u32 s80, s49, 7\n"
"v_mfma_f32_16x16x32_bf16 v[188:191], v[28:31], a[20:23], v[188:191]\n"
"s_and_b32 s80, s80, 7\n"
"s_lshl_b32 s80, s80, 7\n"
"v_mfma_f32_16x16x32_bf16 v[216:219], v[24:27], a[24:27], v[216:219]\n"
"s_lshl_b32 s81, s60, 2\n"
"s_add_u32 s80, s80, s81\n"
"v_mfma_f32_16x16x32_bf16 v[220:223], v[28:31], a[24:27], v[220:223]\n"
"s_add_u32 s81, s80, 8192\n"
"s_lshr_b32 s82, s60, 1\n"
"v_mfma_f32_16x16x32_bf16 v[248:251], v[24:27], a[28:31], v[248:251]\n"
"v_perm_b32 v24, v101, v97, s56\n"
"v_perm_b32 v25, v109, v105, s56\n"
"v_mfma_f32_16x16x32_bf16 v[252:255], v[28:31], a[28:31], v[252:255]\n"
"v_perm_b32 v26, v117, v113, s56\n"
"v_perm_b32 v27, v125, v121, s56\n"
"v_mfma_f32_16x16x32_bf16 v[128:131], v[16:19], a[0:3], v[128:131]\n"
"v_perm_b32 v28, v101, v97, s57\n"
"v_perm_b32 v29, v109, v105, s57\n"
"v_mfma_f32_16x16x32_bf16 v[132:135], v[20:23], a[0:3], v[132:135]\n"
"v_perm_b32 v30, v117, v113, s57\n"
"v_perm_b32 v31, v125, v121, s57\n"
"v_mfma_f32_16x16x32_bf16 v[160:163], v[16:19], a[4:7], v[160:163]\n"
"s_add_u32 m0, s82, 155648\n"
"v_mfma_f32_16x16x32_bf16 v[164:167], v[20:23], a[4:7], v[164:167]\n"
"buffer_load_dwordx4 v1, s[20:23], s80 offen offset:0 lds\n"
"s_add_u32 m0, s82, 156672\n"
"v_mfma_f32_16x16x32_bf16 v[192:195], v[16:19], a[8:11], v[192:195]\n"
"v_mfma_f32_16x16x32_bf16 v[196:199], v[20:23], a[8:11], v[196:199]\n"
"v_mfma_f32_16x16x32_bf16 v[224:227], v[16:19], a[12:15], v[224:227]\n"
"ds_read_b128 a[16:19], v7 offset:0\n"
"v_perm_b32 v16, v102, v98, s56\n"
"v_mfma_f32_16x16x32_bf16 v[228:231], v[20:23], a[12:15], v[228:231]\n"
"ds_read_b128 a[20:23], v7 offset:2048\n"
"buffer_load_dwordx4 v1, s[20:23], s81 offen offset:0 lds\n"
"v_mfma_f32_16x16x32_bf16 v[136:139], v[24:27], a[0:3], v[136:139]\n"
"ds_read_b128 a[24:27], v7 offset:4096\n"
"v_perm_b32 v17, v110, v106, s56\n"
"v_mfma_f32_16x16x32_bf16 v[140:143], v[28:31], a[0:3], v[140:143]\n"
"ds_read_b128 a[28:31], v7 offset:6144\n"
"v_perm_b32 v18, v118, v114, s56\n"
"v_mfma_f32_16x16x32_bf16 v[168:171], v[24:27], a[4:7], v[168:171]\n"
"v_perm_b32 v19, v126, v122, s56\n"
"v_perm_b32 v20, v102, v98, s57\n"
"v_mfma_f32_16x16x32_bf16 v[172:175], v[28:31], a[4:7], v[172:175]\n"
"v_perm_b32 v21, v110, v106, s57\n"
"v_perm_b32 v22, v118, v114, s57\n"
"v_mfma_f32_16x16x32_bf16 v[200:203], v[24:27], a[8:11], v[200:203]\n"
"v_perm_b32 v23, v126, v122, s57\n"
"s_add_u32 s84, s49, 5\n"
"v_mfma_f32_16x16x32_bf16 v[204:207], v[28:31], a[8:11], v[204:207]\n"
"s_and_b32 s84, s84, 7\n"
"s_lshl_b32 s84, s84, 19\n"
"v_mfma_f32_16x16x32_bf16 v[232:235], v[24:27], a[12:15], v[232:235]\n"
"buffer_load_dwordx4 v[64:67], v8, s[24:27], s84 offen offset:0\n"
"v_perm_b32 v24, v103, v99, s56\n"
"v_mfma_f32_16x16x32_bf16 v[236:239], v[28:31], a[12:15], v[236:239]\n"
"v_perm_b32 v25, v111, v107, s56\n"
"v_perm_b32 v26, v119, v115, s56\n"
"v_mfma_f32_16x16x32_bf16 v[144:147], v[16:19], a[0:3], v[144:147]\n"
"v_perm_b32 v27, v127, v123, s56\n"
"v_perm_b32 v28, v103, v99, s57\n"
"v_mfma_f32_16x16x32_bf16 v[148:151], v[20:23], a[0:3], v[148:151]\n"
"v_perm_b32 v29, v111, v107, s57\n"
"v_perm_b32 v30, v119, v115, s57\n"
"v_mfma_f32_16x16x32_bf16 v[176:179], v[16:19], a[4:7], v[176:179]\n"
"buffer_load_dwordx4 v[68:71], v9, s[24:27], s84 offen offset:0\n"
"v_perm_b32 v31, v127, v123, s57\n"
"v_mfma_f32_16x16x32_bf16 v[180:183], v[20:23], a[4:7], v[180:183]\n"
"v_mfma_f32_16x16x32_bf16 v[208:211], v[16:19], a[8:11], v[208:211]\n"
"v_mfma_f32_16x16x32_bf16 v[212:215], v[20:23], a[8:11], v[212:215]\n"
"v_mfma_f32_16x16x32_bf16 v[240:243], v[16:19], a[12:15], v[240:243]\n"
"s_waitcnt vmcnt(6)\n"
"buffer_load_dwordx4 v[72:75], v10, s[24:27], s84 offen offset:0\n"
"v_perm_b32 v16, v36, v32, s56\n"
"v_mfma_f32_16x16x32_bf16 v[244:247], v[20:23], a[12:15], v[244:247]\n"
"v_perm_b32 v17, v44, v40, s56\n"
"v_perm_b32 v18, v52, v48, s56\n"
"v_mfma_f32_16x16x32_bf16 v[152:155], v[24:27], a[0:3], v[152:155]\n"
"v_perm_b32 v19, v60, v56, s56\n"
"v_perm_b32 v20, v36, v32, s57\n"
"buffer_load_dwordx4 v[76:79], v11, s[24:27], s84 offen offset:0\n"
"buffer_load_dwordx4 v[80:83], v12, s[24:27], s84 offen offset:0\n"
"buffer_load_dwordx4 v[84:87], v13, s[24:27], s84 offen offset:0\n"
"buffer_load_dwordx4 v[88:91], v14, s[24:27], s84 offen offset:0\n"
"buffer_load_dwordx4 v[92:95], v15, s[24:27], s84 offen offset:0\n"
"v_mfma_f32_16x16x32_bf16 v[156:159], v[28:31], a[0:3], v[156:159]\n"
"v_perm_b32 v21, v44, v40, s57\n"
"v_perm_b32 v22, v52, v48, s57\n"
"v_mfma_f32_16x16x32_bf16 v[184:187], v[24:27], a[4:7], v[184:187]\n"
"s_waitcnt lgkmcnt(0)\n"
"v_perm_b32 v23, v60, v56, s57\n"
"v_mfma_f32_16x16x32_bf16 v[188:191], v[28:31], a[4:7], v[188:191]\n"
"s_add_u32 s85, s49, 5\n"
"s_and_b32 s85, s85, 7\n"
"v_mfma_f32_16x16x32_bf16 v[216:219], v[24:27], a[8:11], v[216:219]\n"
"s_lshl_b32 s85, s85, 19\n"
"s_add_u32 s85, s85, 262144\n"
"v_mfma_f32_16x16x32_bf16 v[220:223], v[28:31], a[8:11], v[220:223]\n"
"buffer_load_dwordx4 v[96:99], v8, s[24:27], s85 offen offset:0\n"
"v_mfma_f32_16x16x32_bf16 v[248:251], v[24:27], a[12:15], v[248:251]\n"
"v_perm_b32 v24, v37, v33, s56\n"
"v_perm_b32 v25, v45, v41, s56\n"
"v_mfma_f32_16x16x32_bf16 v[252:255], v[28:31], a[12:15], v[252:255]\n"
"v_perm_b32 v26, v53, v49, s56\n"
"v_perm_b32 v27, v61, v57, s56\n"
"v_mfma_f32_16x16x32_bf16 v[128:131], v[16:19], a[16:19], v[128:131]\n"
"v_perm_b32 v28, v37, v33, s57\n"
"v_perm_b32 v29, v45, v41, s57\n"
"v_mfma_f32_16x16x32_bf16 v[132:135], v[20:23], a[16:19], v[132:135]\n"
"buffer_load_dwordx4 v[100:103], v9, s[24:27], s85 offen offset:0\n"
"v_perm_b32 v30, v53, v49, s57\n"
"v_mfma_f32_16x16x32_bf16 v[160:163], v[16:19], a[20:23], v[160:163]\n"
"v_perm_b32 v31, v61, v57, s57\n"
"v_mfma_f32_16x16x32_bf16 v[164:167], v[20:23], a[20:23], v[164:167]\n"
"v_mfma_f32_16x16x32_bf16 v[192:195], v[16:19], a[24:27], v[192:195]\n"
"v_mfma_f32_16x16x32_bf16 v[196:199], v[20:23], a[24:27], v[196:199]\n"
"buffer_load_dwordx4 v[104:107], v10, s[24:27], s85 offen offset:0\n"
"v_mfma_f32_16x16x32_bf16 v[224:227], v[16:19], a[28:31], v[224:227]\n"
"ds_read_b128 a[0:3], v6 offset:8192\n"
"v_perm_b32 v16, v38, v34, s56\n"
"v_mfma_f32_16x16x32_bf16 v[228:231], v[20:23], a[28:31], v[228:231]\n"
"ds_read_b128 a[4:7], v6 offset:10240\n"
"v_perm_b32 v17, v46, v42, s56\n"
"v_mfma_f32_16x16x32_bf16 v[136:139], v[24:27], a[16:19], v[136:139]\n"
"ds_read_b128 a[8:11], v6 offset:12288\n"
"v_perm_b32 v18, v54, v50, s56\n"
"v_mfma_f32_16x16x32_bf16 v[140:143], v[28:31], a[16:19], v[140:143]\n"
"ds_read_b128 a[12:15], v6 offset:14336\n"
"buffer_load_dwordx4 v[108:111], v11, s[24:27], s85 offen offset:0\n"
"v_mfma_f32_16x16x32_bf16 v[168:171], v[24:27], a[20:23], v[168:171]\n"
"v_perm_b32 v19, v62, v58, s56\n"
"v_perm_b32 v20, v38, v34, s57\n"
"v_mfma_f32_16x16x32_bf16 v[172:175], v[28:31], a[20:23], v[172:175]\n"
"v_perm_b32 v21, v46, v42, s57\n"
"v_perm_b32 v22, v54, v50, s57\n"
"v_mfma_f32_16x16x32_bf16 v[200:203], v[24:27], a[24:27], v[200:203]\n"
"v_perm_b32 v23, v62, v58, s57\n"
"v_mfma_f32_16x16x32_bf16 v[204:207], v[28:31], a[24:27], v[204:207]\n"
"buffer_load_dwordx4 v[112:115], v12, s[24:27], s85 offen offset:0\n"
"v_mfma_f32_16x16x32_bf16 v[232:235], v[24:27], a[28:31], v[232:235]\n"
"v_perm_b32 v24, v39, v35, s56\n"
"v_perm_b32 v25, v47, v43, s56\n"
"v_mfma_f32_16x16x32_bf16 v[236:239], v[28:31], a[28:31], v[236:239]\n"
"v_perm_b32 v26, v55, v51, s56\n"
"v_perm_b32 v27, v63, v59, s56\n"
"v_mfma_f32_16x16x32_bf16 v[144:147], v[16:19], a[16:19], v[144:147]\n"
"v_perm_b32 v28, v39, v35, s57\n"
"v_perm_b32 v29, v47, v43, s57\n"
"v_mfma_f32_16x16x32_bf16 v[148:151], v[20:23], a[16:19], v[148:151]\n"
"buffer_load_dwordx4 v[116:119], v13, s[24:27], s85 offen offset:0\n"
"v_perm_b32 v30, v55, v51, s57\n"
"v_mfma_f32_16x16x32_bf16 v[176:179], v[16:19], a[20:23], v[176:179]\n"
"v_perm_b32 v31, v63, v59, s57\n"
"v_mfma_f32_16x16x32_bf16 v[180:183], v[20:23], a[20:23], v[180:183]\n"
"v_mfma_f32_16x16x32_bf16 v[208:211], v[16:19], a[24:27], v[208:211]\n"
"v_mfma_f32_16x16x32_bf16 v[212:215], v[20:23], a[24:27], v[212:215]\n"
"buffer_load_dwordx4 v[120:123], v14, s[24:27], s85 offen offset:0\n"
"v_mfma_f32_16x16x32_bf16 v[240:243], v[16:19], a[28:31], v[240:243]\n"
"s_waitcnt vmcnt(7)\n"
"v_perm_b32 v16, v68, v64, s56\n"
"v_perm_b32 v17, v76, v72, s56\n"
"v_mfma_f32_16x16x32_bf16 v[244:247], v[20:23], a[28:31], v[244:247]\n"
"v_perm_b32 v18, v84, v80, s56\n"
"v_perm_b32 v19, v92, v88, s56\n"
"v_mfma_f32_16x16x32_bf16 v[152:155], v[24:27], a[16:19], v[152:155]\n"
"v_perm_b32 v20, v68, v64, s57\n"
"v_perm_b32 v21, v76, v72, s57\n"
"buffer_load_dwordx4 v[124:127], v15, s[24:27], s85 offen offset:0\n"
"v_mfma_f32_16x16x32_bf16 v[156:159], v[28:31], a[16:19], v[156:159]\n"
"v_perm_b32 v22, v84, v80, s57\n"
"v_perm_b32 v23, v92, v88, s57\n"
"v_mfma_f32_16x16x32_bf16 v[184:187], v[24:27], a[20:23], v[184:187]\n"
"s_waitcnt lgkmcnt(0)\n"
"v_mfma_f32_16x16x32_bf16 v[188:191], v[28:31], a[20:23], v[188:191]\n"
"s_add_u32 s84, s49, 6\n"
"s_and_b32 s84, s84, 7\n"
"v_mfma_f32_16x16x32_bf16 v[216:219], v[24:27], a[24:27], v[216:219]\n"
"s_lshl_b32 s84, s84, 19\n"
"v_mfma_f32_16x16x32_bf16 v[220:223], v[28:31], a[24:27], v[220:223]\n"
"buffer_load_dwordx4 v[32:35], v8, s[24:27], s84 offen offset:0\n"
"v_mfma_f32_16x16x32_bf16 v[248:251], v[24:27], a[28:31], v[248:251]\n"
"v_perm_b32 v24, v69, v65, s56\n"
"v_perm_b32 v25, v77, v73, s56\n"
"v_mfma_f32_16x16x32_bf16 v[252:255], v[28:31], a[28:31], v[252:255]\n"
"v_perm_b32 v26, v85, v81, s56\n"
"v_perm_b32 v27, v93, v89, s56\n"
"v_mfma_f32_16x16x32_bf16 v[128:131], v[16:19], a[0:3], v[128:131]\n"
"v_perm_b32 v28, v69, v65, s57\n"
"v_perm_b32 v29, v77, v73, s57\n"
"v_mfma_f32_16x16x32_bf16 v[132:135], v[20:23], a[0:3], v[132:135]\n"
"buffer_load_dwordx4 v[36:39], v9, s[24:27], s84 offen offset:0\n"
"v_perm_b32 v30, v85, v81, s57\n"
"v_mfma_f32_16x16x32_bf16 v[160:163], v[16:19], a[4:7], v[160:163]\n"
"v_perm_b32 v31, v93, v89, s57\n"
"v_mfma_f32_16x16x32_bf16 v[164:167], v[20:23], a[4:7], v[164:167]\n"
"v_mfma_f32_16x16x32_bf16 v[192:195], v[16:19], a[8:11], v[192:195]\n"
"v_mfma_f32_16x16x32_bf16 v[196:199], v[20:23], a[8:11], v[196:199]\n"
"buffer_load_dwordx4 v[40:43], v10, s[24:27], s84 offen offset:0\n"
"v_mfma_f32_16x16x32_bf16 v[224:227], v[16:19], a[12:15], v[224:227]\n"
"ds_read_b128 a[16:19], v7 offset:8192\n"
"v_perm_b32 v16, v70, v66, s56\n"
"v_mfma_f32_16x16x32_bf16 v[228:231], v[20:23], a[12:15], v[228:231]\n"
"ds_read_b128 a[20:23], v7 offset:10240\n"
"v_perm_b32 v17, v78, v74, s56\n"
"v_mfma_f32_16x16x32_bf16 v[136:139], v[24:27], a[0:3], v[136:139]\n"
"ds_read_b128 a[24:27], v7 offset:12288\n"
"v_perm_b32 v18, v86, v82, s56\n"
"v_mfma_f32_16x16x32_bf16 v[140:143], v[28:31], a[0:3], v[140:143]\n"
"ds_read_b128 a[28:31], v7 offset:14336\n"
"buffer_load_dwordx4 v[44:47], v11, s[24:27], s84 offen offset:0\n"
"v_mfma_f32_16x16x32_bf16 v[168:171], v[24:27], a[4:7], v[168:171]\n"
"v_perm_b32 v19, v94, v90, s56\n"
"v_perm_b32 v20, v70, v66, s57\n"
"v_mfma_f32_16x16x32_bf16 v[172:175], v[28:31], a[4:7], v[172:175]\n"
"v_perm_b32 v21, v78, v74, s57\n"
"v_perm_b32 v22, v86, v82, s57\n"
"v_mfma_f32_16x16x32_bf16 v[200:203], v[24:27], a[8:11], v[200:203]\n"
"v_perm_b32 v23, v94, v90, s57\n"
"v_mfma_f32_16x16x32_bf16 v[204:207], v[28:31], a[8:11], v[204:207]\n"
"buffer_load_dwordx4 v[48:51], v12, s[24:27], s84 offen offset:0\n"
"v_mfma_f32_16x16x32_bf16 v[232:235], v[24:27], a[12:15], v[232:235]\n"
"v_perm_b32 v24, v71, v67, s56\n"
"v_perm_b32 v25, v79, v75, s56\n"
"v_mfma_f32_16x16x32_bf16 v[236:239], v[28:31], a[12:15], v[236:239]\n"
"v_perm_b32 v26, v87, v83, s56\n"
"v_perm_b32 v27, v95, v91, s56\n"
"v_mfma_f32_16x16x32_bf16 v[144:147], v[16:19], a[0:3], v[144:147]\n"
"v_perm_b32 v28, v71, v67, s57\n"
"v_perm_b32 v29, v79, v75, s57\n"
"v_mfma_f32_16x16x32_bf16 v[148:151], v[20:23], a[0:3], v[148:151]\n"
"buffer_load_dwordx4 v[52:55], v13, s[24:27], s84 offen offset:0\n"
"v_perm_b32 v30, v87, v83, s57\n"
"v_mfma_f32_16x16x32_bf16 v[176:179], v[16:19], a[4:7], v[176:179]\n"
"v_perm_b32 v31, v95, v91, s57\n"
"v_mfma_f32_16x16x32_bf16 v[180:183], v[20:23], a[4:7], v[180:183]\n"
"v_mfma_f32_16x16x32_bf16 v[208:211], v[16:19], a[8:11], v[208:211]\n"
"v_mfma_f32_16x16x32_bf16 v[212:215], v[20:23], a[8:11], v[212:215]\n"
"buffer_load_dwordx4 v[56:59], v14, s[24:27], s84 offen offset:0\n"
"v_mfma_f32_16x16x32_bf16 v[240:243], v[16:19], a[12:15], v[240:243]\n"
"s_waitcnt vmcnt(7)\n"
"v_perm_b32 v16, v100, v96, s56\n"
"v_perm_b32 v17, v108, v104, s56\n"
"v_mfma_f32_16x16x32_bf16 v[244:247], v[20:23], a[12:15], v[244:247]\n"
"v_perm_b32 v18, v116, v112, s56\n"
"v_perm_b32 v19, v124, v120, s56\n"
"v_mfma_f32_16x16x32_bf16 v[152:155], v[24:27], a[0:3], v[152:155]\n"
"v_perm_b32 v20, v100, v96, s57\n"
"v_perm_b32 v21, v108, v104, s57\n"
"buffer_load_dwordx4 v[60:63], v15, s[24:27], s84 offen offset:0\n"
"v_mfma_f32_16x16x32_bf16 v[156:159], v[28:31], a[0:3], v[156:159]\n"
"v_perm_b32 v22, v116, v112, s57\n"
"v_perm_b32 v23, v124, v120, s57\n"
"v_mfma_f32_16x16x32_bf16 v[184:187], v[24:27], a[4:7], v[184:187]\n"
"s_waitcnt lgkmcnt(0)\n"
"v_mfma_f32_16x16x32_bf16 v[188:191], v[28:31], a[4:7], v[188:191]\n"
"s_add_u32 s85, s49, 6\n"
"s_and_b32 s85, s85, 7\n"
"v_mfma_f32_16x16x32_bf16 v[216:219], v[24:27], a[8:11], v[216:219]\n"
"s_lshl_b32 s85, s85, 19\n"
"s_add_u32 s85, s85, 262144\n"
"v_mfma_f32_16x16x32_bf16 v[220:223], v[28:31], a[8:11], v[220:223]\n"
"buffer_load_dwordx4 v[64:67], v8, s[24:27], s85 offen offset:0\n"
"v_mfma_f32_16x16x32_bf16 v[248:251], v[24:27], a[12:15], v[248:251]\n"
"v_perm_b32 v24, v101, v97, s56\n"
"v_perm_b32 v25, v109, v105, s56\n"
"v_mfma_f32_16x16x32_bf16 v[252:255], v[28:31], a[12:15], v[252:255]\n"
"v_perm_b32 v26, v117, v113, s56\n"
"v_perm_b32 v27, v125, v121, s56\n"
"v_mfma_f32_16x16x32_bf16 v[128:131], v[16:19], a[16:19], v[128:131]\n"
"v_perm_b32 v28, v101, v97, s57\n"
"v_perm_b32 v29, v109, v105, s57\n"
"v_mfma_f32_16x16x32_bf16 v[132:135], v[20:23], a[16:19], v[132:135]\n"
"buffer_load_dwordx4 v[68:71], v9, s[24:27], s85 offen offset:0\n"
"v_perm_b32 v30, v117, v113, s57\n"
"v_mfma_f32_16x16x32_bf16 v[160:163], v[16:19], a[20:23], v[160:163]\n"
"v_perm_b32 v31, v125, v121, s57\n"
"v_mfma_f32_16x16x32_bf16 v[164:167], v[20:23], a[20:23], v[164:167]\n"
"v_mfma_f32_16x16x32_bf16 v[192:195], v[16:19], a[24:27], v[192:195]\n"
"buffer_load_dwordx4 v[72:75], v10, s[24:27], s85 offen offset:0\n"
"buffer_load_dwordx4 v[76:79], v11, s[24:27], s85 offen offset:0\n"
"buffer_load_dwordx4 v[80:83], v12, s[24:27], s85 offen offset:0\n"
"buffer_load_dwordx4 v[84:87], v13, s[24:27], s85 offen offset:0\n"
"buffer_load_dwordx4 v[88:91], v14, s[24:27], s85 offen offset:0\n"
"buffer_load_dwordx4 v[92:95], v15, s[24:27], s85 offen offset:0\n"
"s_barrier\n"
"v_mfma_f32_16x16x32_bf16 v[196:199], v[20:23], a[24:27], v[196:199]\n"
"v_mfma_f32_16x16x32_bf16 v[224:227], v[16:19], a[28:31], v[224:227]\n"
"ds_read_b128 a[0:3], v6 offset:16384\n"
"v_perm_b32 v16, v102, v98, s56\n"
"v_mfma_f32_16x16x32_bf16 v[228:231], v[20:23], a[28:31], v[228:231]\n"
"ds_read_b128 a[4:7], v6 offset:18432\n"
"v_perm_b32 v17, v110, v106, s56\n"
"v_mfma_f32_16x16x32_bf16 v[136:139], v[24:27], a[16:19], v[136:139]\n"
"ds_read_b128 a[8:11], v6 offset:20480\n"
"v_perm_b32 v18, v118, v114, s56\n"
"v_mfma_f32_16x16x32_bf16 v[140:143], v[28:31], a[16:19], v[140:143]\n"
"ds_read_b128 a[12:15], v6 offset:22528\n"
"v_perm_b32 v19, v126, v122, s56\n"
"v_mfma_f32_16x16x32_bf16 v[168:171], v[24:27], a[20:23], v[168:171]\n"
"v_perm_b32 v20, v102, v98, s57\n"
"v_perm_b32 v21, v110, v106, s57\n"
"v_mfma_f32_16x16x32_bf16 v[172:175], v[28:31], a[20:23], v[172:175]\n"
"v_perm_b32 v22, v118, v114, s57\n"
"v_perm_b32 v23, v126, v122, s57\n"
"v_mfma_f32_16x16x32_bf16 v[200:203], v[24:27], a[24:27], v[200:203]\n"
"v_mfma_f32_16x16x32_bf16 v[204:207], v[28:31], a[24:27], v[204:207]\n"
"v_mfma_f32_16x16x32_bf16 v[232:235], v[24:27], a[28:31], v[232:235]\n"
"v_perm_b32 v24, v103, v99, s56\n"
"v_perm_b32 v25, v111, v107, s56\n"
"v_mfma_f32_16x16x32_bf16 v[236:239], v[28:31], a[28:31], v[236:239]\n"
"v_perm_b32 v26, v119, v115, s56\n"
"v_perm_b32 v27, v127, v123, s56\n"
"v_mfma_f32_16x16x32_bf16 v[144:147], v[16:19], a[16:19], v[144:147]\n"
"v_perm_b32 v28, v103, v99, s57\n"
"v_perm_b32 v29, v111, v107, s57\n"
"v_mfma_f32_16x16x32_bf16 v[148:151], v[20:23], a[16:19], v[148:151]\n"
"v_perm_b32 v30, v119, v115, s57\n"
"v_perm_b32 v31, v127, v123, s57\n"
"v_mfma_f32_16x16x32_bf16 v[176:179], v[16:19], a[20:23], v[176:179]\n"
"v_mfma_f32_16x16x32_bf16 v[180:183], v[20:23], a[20:23], v[180:183]\n"
"v_mfma_f32_16x16x32_bf16 v[208:211], v[16:19], a[24:27], v[208:211]\n"
"v_mfma_f32_16x16x32_bf16 v[212:215], v[20:23], a[24:27], v[212:215]\n"
"v_mfma_f32_16x16x32_bf16 v[240:243], v[16:19], a[28:31], v[240:243]\n"
"s_waitcnt vmcnt(8)\n"
"v_perm_b32 v16, v36, v32, s56\n"
"v_perm_b32 v17, v44, v40, s56\n"
"v_mfma_f32_16x16x32_bf16 v[244:247], v[20:23], a[28:31], v[244:247]\n"
"v_perm_b32 v18, v52, v48, s56\n"
"v_perm_b32 v19, v60, v56, s56\n"
"v_mfma_f32_16x16x32_bf16 v[152:155], v[24:27], a[16:19], v[152:155]\n"
"v_perm_b32 v20, v36, v32, s57\n"
"v_perm_b32 v21, v44, v40, s57\n"
"v_mfma_f32_16x16x32_bf16 v[156:159], v[28:31], a[16:19], v[156:159]\n"
"v_perm_b32 v22, v52, v48, s57\n"
"v_perm_b32 v23, v60, v56, s57\n"
"v_mfma_f32_16x16x32_bf16 v[184:187], v[24:27], a[20:23], v[184:187]\n"
"s_waitcnt lgkmcnt(0)\n"
"v_mfma_f32_16x16x32_bf16 v[188:191], v[28:31], a[20:23], v[188:191]\n"
"s_add_u32 s84, s49, 7\n"
"s_and_b32 s84, s84, 7\n"
"v_mfma_f32_16x16x32_bf16 v[216:219], v[24:27], a[24:27], v[216:219]\n"
"s_lshl_b32 s84, s84, 19\n"
"v_mfma_f32_16x16x32_bf16 v[220:223], v[28:31], a[24:27], v[220:223]\n"
"buffer_load_dwordx4 v[96:99], v8, s[24:27], s84 offen offset:0\n"
"v_mfma_f32_16x16x32_bf16 v[248:251], v[24:27], a[28:31], v[248:251]\n"
"v_perm_b32 v24, v37, v33, s56\n"
"v_perm_b32 v25, v45, v41, s56\n"
"v_mfma_f32_16x16x32_bf16 v[252:255], v[28:31], a[28:31], v[252:255]\n"
"v_perm_b32 v26, v53, v49, s56\n"
"v_perm_b32 v27, v61, v57, s56\n"
"v_mfma_f32_16x16x32_bf16 v[128:131], v[16:19], a[0:3], v[128:131]\n"
"v_perm_b32 v28, v37, v33, s57\n"
"v_perm_b32 v29, v45, v41, s57\n"
"v_mfma_f32_16x16x32_bf16 v[132:135], v[20:23], a[0:3], v[132:135]\n"
"buffer_load_dwordx4 v[100:103], v9, s[24:27], s84 offen offset:0\n"
"v_perm_b32 v30, v53, v49, s57\n"
"v_mfma_f32_16x16x32_bf16 v[160:163], v[16:19], a[4:7], v[160:163]\n"
"v_perm_b32 v31, v61, v57, s57\n"
"v_mfma_f32_16x16x32_bf16 v[164:167], v[20:23], a[4:7], v[164:167]\n"
"v_mfma_f32_16x16x32_bf16 v[192:195], v[16:19], a[8:11], v[192:195]\n"
"v_mfma_f32_16x16x32_bf16 v[196:199], v[20:23], a[8:11], v[196:199]\n"
"buffer_load_dwordx4 v[104:107], v10, s[24:27], s84 offen offset:0\n"
"v_mfma_f32_16x16x32_bf16 v[224:227], v[16:19], a[12:15], v[224:227]\n"
"ds_read_b128 a[16:19], v7 offset:16384\n"
"v_perm_b32 v16, v38, v34, s56\n"
"v_mfma_f32_16x16x32_bf16 v[228:231], v[20:23], a[12:15], v[228:231]\n"
"ds_read_b128 a[20:23], v7 offset:18432\n"
"v_perm_b32 v17, v46, v42, s56\n"
"v_mfma_f32_16x16x32_bf16 v[136:139], v[24:27], a[0:3], v[136:139]\n"
"ds_read_b128 a[24:27], v7 offset:20480\n"
"v_perm_b32 v18, v54, v50, s56\n"
"v_mfma_f32_16x16x32_bf16 v[140:143], v[28:31], a[0:3], v[140:143]\n"
"ds_read_b128 a[28:31], v7 offset:22528\n"
"buffer_load_dwordx4 v[108:111], v11, s[24:27], s84 offen offset:0\n"
"v_mfma_f32_16x16x32_bf16 v[168:171], v[24:27], a[4:7], v[168:171]\n"
"v_perm_b32 v19, v62, v58, s56\n"
"v_perm_b32 v20, v38, v34, s57\n"
"v_mfma_f32_16x16x32_bf16 v[172:175], v[28:31], a[4:7], v[172:175]\n"
"v_perm_b32 v21, v46, v42, s57\n"
"v_perm_b32 v22, v54, v50, s57\n"
"v_mfma_f32_16x16x32_bf16 v[200:203], v[24:27], a[8:11], v[200:203]\n"
"v_perm_b32 v23, v62, v58, s57\n"
"v_mfma_f32_16x16x32_bf16 v[204:207], v[28:31], a[8:11], v[204:207]\n"
"buffer_load_dwordx4 v[112:115], v12, s[24:27], s84 offen offset:0\n"
"v_mfma_f32_16x16x32_bf16 v[232:235], v[24:27], a[12:15], v[232:235]\n"
"v_perm_b32 v24, v39, v35, s56\n"
"v_perm_b32 v25, v47, v43, s56\n"
"v_mfma_f32_16x16x32_bf16 v[236:239], v[28:31], a[12:15], v[236:239]\n"
"v_perm_b32 v26, v55, v51, s56\n"
"v_perm_b32 v27, v63, v59, s56\n"
"v_mfma_f32_16x16x32_bf16 v[144:147], v[16:19], a[0:3], v[144:147]\n"
"v_perm_b32 v28, v39, v35, s57\n"
"v_perm_b32 v29, v47, v43, s57\n"
"v_mfma_f32_16x16x32_bf16 v[148:151], v[20:23], a[0:3], v[148:151]\n"
"buffer_load_dwordx4 v[116:119], v13, s[24:27], s84 offen offset:0\n"
"v_perm_b32 v30, v55, v51, s57\n"
"v_mfma_f32_16x16x32_bf16 v[176:179], v[16:19], a[4:7], v[176:179]\n"
"v_perm_b32 v31, v63, v59, s57\n"
"v_mfma_f32_16x16x32_bf16 v[180:183], v[20:23], a[4:7], v[180:183]\n"
"v_mfma_f32_16x16x32_bf16 v[208:211], v[16:19], a[8:11], v[208:211]\n"
"v_mfma_f32_16x16x32_bf16 v[212:215], v[20:23], a[8:11], v[212:215]\n"
"buffer_load_dwordx4 v[120:123], v14, s[24:27], s84 offen offset:0\n"
"v_mfma_f32_16x16x32_bf16 v[240:243], v[16:19], a[12:15], v[240:243]\n"
"s_waitcnt vmcnt(7)\n"
"v_perm_b32 v16, v68, v64, s56\n"
"v_perm_b32 v17, v76, v72, s56\n"
"v_mfma_f32_16x16x32_bf16 v[244:247], v[20:23], a[12:15], v[244:247]\n"
"v_perm_b32 v18, v84, v80, s56\n"
"v_perm_b32 v19, v92, v88, s56\n"
"v_mfma_f32_16x16x32_bf16 v[152:155], v[24:27], a[0:3], v[152:155]\n"
"v_perm_b32 v20, v68, v64, s57\n"
"v_perm_b32 v21, v76, v72, s57\n"
"buffer_load_dwordx4 v[124:127], v15, s[24:27], s84 offen offset:0\n"
"v_mfma_f32_16x16x32_bf16 v[156:159], v[28:31], a[0:3], v[156:159]\n"
"v_perm_b32 v22, v84, v80, s57\n"
"v_perm_b32 v23, v92, v88, s57\n"
"v_mfma_f32_16x16x32_bf16 v[184:187], v[24:27], a[4:7], v[184:187]\n"
"s_waitcnt lgkmcnt(0)\n"
"v_mfma_f32_16x16x32_bf16 v[188:191], v[28:31], a[4:7], v[188:191]\n"
"s_add_u32 s85, s49, 7\n"
"s_and_b32 s85, s85, 7\n"
"v_mfma_f32_16x16x32_bf16 v[216:219], v[24:27], a[8:11], v[216:219]\n"
"s_lshl_b32 s85, s85, 19\n"
"s_add_u32 s85, s85, 262144\n"
"v_mfma_f32_16x16x32_bf16 v[220:223], v[28:31], a[8:11], v[220:223]\n"
"buffer_load_dwordx4 v[32:35], v8, s[24:27], s85 offen offset:0\n"
"v_mfma_f32_16x16x32_bf16 v[248:251], v[24:27], a[12:15], v[248:251]\n"
"v_perm_b32 v24, v69, v65, s56\n"
"v_perm_b32 v25, v77, v73, s56\n"
"v_mfma_f32_16x16x32_bf16 v[252:255], v[28:31], a[12:15], v[252:255]\n"
"v_perm_b32 v26, v85, v81, s56\n"
"v_perm_b32 v27, v93, v89, s56\n"
"v_mfma_f32_16x16x32_bf16 v[128:131], v[16:19], a[16:19], v[128:131]\n"
"v_perm_b32 v28, v69, v65, s57\n"
"v_perm_b32 v29, v77, v73, s57\n"
"v_mfma_f32_16x16x32_bf16 v[132:135], v[20:23], a[16:19], v[132:135]\n"
"buffer_load_dwordx4 v[36:39], v9, s[24:27], s85 offen offset:0\n"
"v_perm_b32 v30, v85, v81, s57\n"
"v_mfma_f32_16x16x32_bf16 v[160:163], v[16:19], a[20:23], v[160:163]\n"
"v_perm_b32 v31, v93, v89, s57\n"
"v_mfma_f32_16x16x32_bf16 v[164:167], v[20:23], a[20:23], v[164:167]\n"
"v_mfma_f32_16x16x32_bf16 v[192:195], v[16:19], a[24:27], v[192:195]\n"
"v_mfma_f32_16x16x32_bf16 v[196:199], v[20:23], a[24:27], v[196:199]\n"
"buffer_load_dwordx4 v[40:43], v10, s[24:27], s85 offen offset:0\n"
"v_mfma_f32_16x16x32_bf16 v[224:227], v[16:19], a[28:31], v[224:227]\n"
"ds_read_b128 a[0:3], v6 offset:24576\n"
"v_perm_b32 v16, v70, v66, s56\n"
"v_mfma_f32_16x16x32_bf16 v[228:231], v[20:23], a[28:31], v[228:231]\n"
"ds_read_b128 a[4:7], v6 offset:26624\n"
"v_perm_b32 v17, v78, v74, s56\n"
"v_mfma_f32_16x16x32_bf16 v[136:139], v[24:27], a[16:19], v[136:139]\n"
"ds_read_b128 a[8:11], v6 offset:28672\n"
"v_perm_b32 v18, v86, v82, s56\n"
"v_mfma_f32_16x16x32_bf16 v[140:143], v[28:31], a[16:19], v[140:143]\n"
"ds_read_b128 a[12:15], v6 offset:30720\n"
"buffer_load_dwordx4 v[44:47], v11, s[24:27], s85 offen offset:0\n"
"v_mfma_f32_16x16x32_bf16 v[168:171], v[24:27], a[20:23], v[168:171]\n"
"v_perm_b32 v19, v94, v90, s56\n"
"v_perm_b32 v20, v70, v66, s57\n"
"v_mfma_f32_16x16x32_bf16 v[172:175], v[28:31], a[20:23], v[172:175]\n"
"v_perm_b32 v21, v78, v74, s57\n"
"v_perm_b32 v22, v86, v82, s57\n"
"v_mfma_f32_16x16x32_bf16 v[200:203], v[24:27], a[24:27], v[200:203]\n"
"v_perm_b32 v23, v94, v90, s57\n"
"v_mfma_f32_16x16x32_bf16 v[204:207], v[28:31], a[24:27], v[204:207]\n"
"buffer_load_dwordx4 v[48:51], v12, s[24:27], s85 offen offset:0\n"
"v_mfma_f32_16x16x32_bf16 v[232:235], v[24:27], a[28:31], v[232:235]\n"
"v_perm_b32 v24, v71, v67, s56\n"
"v_perm_b32 v25, v79, v75, s56\n"
"v_mfma_f32_16x16x32_bf16 v[236:239], v[28:31], a[28:31], v[236:239]\n"
"v_perm_b32 v26, v87, v83, s56\n"
"v_perm_b32 v27, v95, v91, s56\n"
"v_mfma_f32_16x16x32_bf16 v[144:147], v[16:19], a[16:19], v[144:147]\n"
"v_perm_b32 v28, v71, v67, s57\n"
"v_perm_b32 v29, v79, v75, s57\n"
"v_mfma_f32_16x16x32_bf16 v[148:151], v[20:23], a[16:19], v[148:151]\n"
"buffer_load_dwordx4 v[52:55], v13, s[24:27], s85 offen offset:0\n"
"v_perm_b32 v30, v87, v83, s57\n"
"v_mfma_f32_16x16x32_bf16 v[176:179], v[16:19], a[20:23], v[176:179]\n"
"v_perm_b32 v31, v95, v91, s57\n"
"v_mfma_f32_16x16x32_bf16 v[180:183], v[20:23], a[20:23], v[180:183]\n"
"v_mfma_f32_16x16x32_bf16 v[208:211], v[16:19], a[24:27], v[208:211]\n"
"v_mfma_f32_16x16x32_bf16 v[212:215], v[20:23], a[24:27], v[212:215]\n"
"buffer_load_dwordx4 v[56:59], v14, s[24:27], s85 offen offset:0\n"
"v_mfma_f32_16x16x32_bf16 v[240:243], v[16:19], a[28:31], v[240:243]\n"
"s_waitcnt vmcnt(7)\n"
"v_perm_b32 v16, v100, v96, s56\n"
"v_perm_b32 v17, v108, v104, s56\n"
"v_mfma_f32_16x16x32_bf16 v[244:247], v[20:23], a[28:31], v[244:247]\n"
"v_perm_b32 v18, v116, v112, s56\n"
"v_perm_b32 v19, v124, v120, s56\n"
"v_mfma_f32_16x16x32_bf16 v[152:155], v[24:27], a[16:19], v[152:155]\n"
"v_perm_b32 v20, v100, v96, s57\n"
"v_perm_b32 v21, v108, v104, s57\n"
"buffer_load_dwordx4 v[60:63], v15, s[24:27], s85 offen offset:0\n"
"v_mfma_f32_16x16x32_bf16 v[156:159], v[28:31], a[16:19], v[156:159]\n"
"v_perm_b32 v22, v116, v112, s57\n"
"v_perm_b32 v23, v124, v120, s57\n"
"v_mfma_f32_16x16x32_bf16 v[184:187], v[24:27], a[20:23], v[184:187]\n"
"s_waitcnt lgkmcnt(0)\n"
"s_add_u32 m0, s60, 0\n"
"v_mfma_f32_16x16x32_bf16 v[188:191], v[28:31], a[20:23], v[188:191]\n"
"v_mfma_f32_16x16x32_bf16 v[216:219], v[24:27], a[24:27], v[216:219]\n"
"buffer_load_dwordx4 v2, s[28:31], s62 offen offset:0 nt lds\n"
"v_mfma_f32_16x16x32_bf16 v[220:223], v[28:31], a[24:27], v[220:223]\n"
"v_mfma_f32_16x16x32_bf16 v[248:251], v[24:27], a[28:31], v[248:251]\n"
"v_perm_b32 v24, v101, v97, s56\n"
"v_perm_b32 v25, v109, v105, s56\n"
"v_mfma_f32_16x16x32_bf16 v[252:255], v[28:31], a[28:31], v[252:255]\n"
"v_perm_b32 v26, v117, v113, s56\n"
"v_perm_b32 v27, v125, v121, s56\n"
"v_mfma_f32_16x16x32_bf16 v[128:131], v[16:19], a[0:3], v[128:131]\n"
"buffer_load_dwordx4 v3, s[28:31], s62 offen offset:1024 nt lds\n"
"v_perm_b32 v28, v101, v97, s57\n"
"v_mfma_f32_16x16x32_bf16 v[132:135], v[20:23], a[0:3], v[132:135]\n"
"v_perm_b32 v29, v109, v105, s57\n"
"v_perm_b32 v30, v117, v113, s57\n"
"v_mfma_f32_16x16x32_bf16 v[160:163], v[16:19], a[4:7], v[160:163]\n"
"v_perm_b32 v31, v125, v121, s57\n"
"v_mfma_f32_16x16x32_bf16 v[164:167], v[20:23], a[4:7], v[164:167]\n"
"v_mfma_f32_16x16x32_bf16 v[192:195], v[16:19], a[8:11], v[192:195]\n"
"buffer_load_dwordx4 v4, s[28:31], s62 offen offset:2048 nt lds\n"
"v_mfma_f32_16x16x32_bf16 v[196:199], v[20:23], a[8:11], v[196:199]\n"
"v_mfma_f32_16x16x32_bf16 v[224:227], v[16:19], a[12:15], v[224:227]\n"
"ds_read_b128 a[16:19], v7 offset:24576\n"
"v_perm_b32 v16, v102, v98, s56\n"
"v_mfma_f32_16x16x32_bf16 v[228:231], v[20:23], a[12:15], v[228:231]\n"
"ds_read_b128 a[20:23], v7 offset:26624\n"
"v_perm_b32 v17, v110, v106, s56\n"
"v_mfma_f32_16x16x32_bf16 v[136:139], v[24:27], a[0:3], v[136:139]\n"
"ds_read_b128 a[24:27], v7 offset:28672\n"
"buffer_load_dwordx4 v5, s[28:31], s62 offen offset:3072 nt lds\n"
"v_mfma_f32_16x16x32_bf16 v[140:143], v[28:31], a[0:3], v[140:143]\n"
"ds_read_b128 a[28:31], v7 offset:30720\n"
"v_perm_b32 v18, v118, v114, s56\n"
"v_mfma_f32_16x16x32_bf16 v[168:171], v[24:27], a[4:7], v[168:171]\n"
"v_perm_b32 v19, v126, v122, s56\n"
"v_perm_b32 v20, v102, v98, s57\n"
"v_mfma_f32_16x16x32_bf16 v[172:175], v[28:31], a[4:7], v[172:175]\n"
"v_perm_b32 v21, v110, v106, s57\n"
"v_perm_b32 v22, v118, v114, s57\n"
"v_mfma_f32_16x16x32_bf16 v[200:203], v[24:27], a[8:11], v[200:203]\n"
"v_perm_b32 v23, v126, v122, s57\n"
"s_add_u32 m0, s60, 16384\n"
"v_mfma_f32_16x16x32_bf16 v[204:207], v[28:31], a[8:11], v[204:207]\n"
"buffer_load_dwordx4 v2, s[28:31], s63 offen offset:0 nt lds\n"
"v_mfma_f32_16x16x32_bf16 v[232:235], v[24:27], a[12:15], v[232:235]\n"
"v_perm_b32 v24, v103, v99, s56\n"
"v_perm_b32 v25, v111, v107, s56\n"
"v_mfma_f32_16x16x32_bf16 v[236:239], v[28:31], a[12:15], v[236:239]\n"
"v_perm_b32 v26, v119, v115, s56\n"
"v_perm_b32 v27, v127, v123, s56\n"
"v_mfma_f32_16x16x32_bf16 v[144:147], v[16:19], a[0:3], v[144:147]\n"
"v_perm_b32 v28, v103, v99, s57\n"
"v_perm_b32 v29, v111, v107, s57\n"
"v_mfma_f32_16x16x32_bf16 v[148:151], v[20:23], a[0:3], v[148:151]\n"
"buffer_load_dwordx4 v3, s[28:31], s63 offen offset:1024 nt lds\n"
"v_perm_b32 v30, v119, v115, s57\n"
"v_mfma_f32_16x16x32_bf16 v[176:179], v[16:19], a[4:7], v[176:179]\n"
"v_perm_b32 v31, v127, v123, s57\n"
"v_mfma_f32_16x16x32_bf16 v[180:183], v[20:23], a[4:7], v[180:183]\n"
"v_mfma_f32_16x16x32_bf16 v[208:211], v[16:19], a[8:11], v[208:211]\n"
"v_mfma_f32_16x16x32_bf16 v[212:215], v[20:23], a[8:11], v[212:215]\n"
"buffer_load_dwordx4 v4, s[28:31], s63 offen offset:2048 nt lds\n"
"v_mfma_f32_16x16x32_bf16 v[240:243], v[16:19], a[12:15], v[240:243]\n"
"s_waitcnt vmcnt(7)\n"
"v_perm_b32 v16, v36, v32, s56\n"
"v_perm_b32 v17, v44, v40, s56\n"
"v_mfma_f32_16x16x32_bf16 v[244:247], v[20:23], a[12:15], v[244:247]\n"
"v_perm_b32 v18, v52, v48, s56\n"
"v_perm_b32 v19, v60, v56, s56\n"
"v_mfma_f32_16x16x32_bf16 v[152:155], v[24:27], a[0:3], v[152:155]\n"
"v_perm_b32 v20, v36, v32, s57\n"
"v_perm_b32 v21, v44, v40, s57\n"
"v_mfma_f32_16x16x32_bf16 v[156:159], v[28:31], a[0:3], v[156:159]\n"
"buffer_load_dwordx4 v5, s[28:31], s63 offen offset:3072 nt lds\n"
"v_perm_b32 v22, v52, v48, s57\n"
"v_mfma_f32_16x16x32_bf16 v[184:187], v[24:27], a[4:7], v[184:187]\n"
"s_waitcnt lgkmcnt(0)\n"
"v_perm_b32 v23, v60, v56, s57\n"
"v_mfma_f32_16x16x32_bf16 v[188:191], v[28:31], a[4:7], v[188:191]\n"
"v_mfma_f32_16x16x32_bf16 v[216:219], v[24:27], a[8:11], v[216:219]\n"
"v_mfma_f32_16x16x32_bf16 v[220:223], v[28:31], a[8:11], v[220:223]\n"
"v_mfma_f32_16x16x32_bf16 v[248:251], v[24:27], a[12:15], v[248:251]\n"
"v_perm_b32 v24, v37, v33, s56\n"
"v_perm_b32 v25, v45, v41, s56\n"
"v_mfma_f32_16x16x32_bf16 v[252:255], v[28:31], a[12:15], v[252:255]\n"
"v_perm_b32 v26, v53, v49, s56\n"
"v_perm_b32 v27, v61, v57, s56\n"
"v_mfma_f32_16x16x32_bf16 v[128:131], v[16:19], a[16:19], v[128:131]\n"
"v_perm_b32 v28, v37, v33, s57\n"
"v_perm_b32 v29, v45, v41, s57\n"
"v_mfma_f32_16x16x32_bf16 v[132:135], v[20:23], a[16:19], v[132:135]\n"
"v_perm_b32 v30, v53, v49, s57\n"
"v_perm_b32 v31, v61, v57, s57\n"
"v_mfma_f32_16x16x32_bf16 v[160:163], v[16:19], a[20:23], v[160:163]\n"
"v_mfma_f32_16x16x32_bf16 v[164:167], v[20:23], a[20:23], v[164:167]\n"
"v_mfma_f32_16x16x32_bf16 v[192:195], v[16:19], a[24:27], v[192:195]\n"
"v_mfma_f32_16x16x32_bf16 v[196:199], v[20:23], a[24:27], v[196:199]\n"
"v_mfma_f32_16x16x32_bf16 v[224:227], v[16:19], a[28:31], v[224:227]\n"
"v_perm_b32 v16, v38, v34, s56\n"
"v_perm_b32 v17, v46, v42, s56\n"
"v_mfma_f32_16x16x32_bf16 v[228:231], v[20:23], a[28:31], v[228:231]\n"
"v_perm_b32 v18, v54, v50, s56\n"
"v_perm_b32 v19, v62, v58, s56\n"
"v_mfma_f32_16x16x32_bf16 v[136:139], v[24:27], a[16:19], v[136:139]\n"
"v_perm_b32 v20, v38, v34, s57\n"
"v_perm_b32 v21, v46, v42, s57\n"
"v_mfma_f32_16x16x32_bf16 v[140:143], v[28:31], a[16:19], v[140:143]\n"
"v_perm_b32 v22, v54, v50, s57\n"
"v_perm_b32 v23, v62, v58, s57\n"
"v_mfma_f32_16x16x32_bf16 v[168:171], v[24:27], a[20:23], v[168:171]\n"
"v_mfma_f32_16x16x32_bf16 v[172:175], v[28:31], a[20:23], v[172:175]\n"
"v_mfma_f32_16x16x32_bf16 v[200:203], v[24:27], a[24:27], v[200:203]\n"
"v_mfma_f32_16x16x32_bf16 v[204:207], v[28:31], a[24:27], v[204:207]\n"
"v_mfma_f32_16x16x32_bf16 v[232:235], v[24:27], a[28:31], v[232:235]\n"
"v_perm_b32 v24, v39, v35, s56\n"
"v_perm_b32 v25, v47, v43, s56\n"
"v_mfma_f32_16x16x32_bf16 v[236:239], v[28:31], a[28:31], v[236:239]\n"
"v_perm_b32 v26, v55, v51, s56\n"
"v_perm_b32 v27, v63, v59, s56\n"
"v_mfma_f32_16x16x32_bf16 v[144:147], v[16:19], a[16:19], v[144:147]\n"
"v_perm_b32 v28, v39, v35, s57\n"
"v_perm_b32 v29, v47, v43, s57\n"
"v_mfma_f32_16x16x32_bf16 v[148:151], v[20:23], a[16:19], v[148:151]\n"
"v_perm_b32 v30, v55, v51, s57\n"
"v_perm_b32 v31, v63, v59, s57\n"
"v_mfma_f32_16x16x32_bf16 v[176:179], v[16:19], a[20:23], v[176:179]\n"
"v_mfma_f32_16x16x32_bf16 v[180:183], v[20:23], a[20:23], v[180:183]\n"
"v_mfma_f32_16x16x32_bf16 v[208:211], v[16:19], a[24:27], v[208:211]\n"
"v_mfma_f32_16x16x32_bf16 v[212:215], v[20:23], a[24:27], v[212:215]\n"
"v_mfma_f32_16x16x32_bf16 v[240:243], v[16:19], a[28:31], v[240:243]\n"
"v_mfma_f32_16x16x32_bf16 v[244:247], v[20:23], a[28:31], v[244:247]\n"
"v_mfma_f32_16x16x32_bf16 v[152:155], v[24:27], a[16:19], v[152:155]\n"
"v_mfma_f32_16x16x32_bf16 v[156:159], v[28:31], a[16:19], v[156:159]\n"
"v_mfma_f32_16x16x32_bf16 v[184:187], v[24:27], a[20:23], v[184:187]\n"
"v_mfma_f32_16x16x32_bf16 v[188:191], v[28:31], a[20:23], v[188:191]\n"
"v_mfma_f32_16x16x32_bf16 v[216:219], v[24:27], a[24:27], v[216:219]\n"
"v_mfma_f32_16x16x32_bf16 v[220:223], v[28:31], a[24:27], v[220:223]\n"
"v_mfma_f32_16x16x32_bf16 v[248:251], v[24:27], a[28:31], v[248:251]\n"
"v_mfma_f32_16x16x32_bf16 v[252:255], v[28:31], a[28:31], v[252:255]\n"
"v_pk_mul_f32 v[128:129], v[128:129], s[58:59] op_sel_hi:[1,0]\n"
"v_pk_mul_f32 v[130:131], v[130:131], s[58:59] op_sel_hi:[1,0]\n"
"v_pk_mul_f32 v[160:161], v[160:161], s[58:59] op_sel_hi:[1,0]\n"
"v_pk_mul_f32 v[162:163], v[162:163], s[58:59] op_sel_hi:[1,0]\n"
"v_pk_mul_f32 v[132:133], v[132:133], s[58:59] op_sel_hi:[1,0]\n"
"v_pk_mul_f32 v[134:135], v[134:135], s[58:59] op_sel_hi:[1,0]\n"
"v_pk_mul_f32 v[164:165], v[164:165], s[58:59] op_sel_hi:[1,0]\n"
"v_pk_mul_f32 v[166:167], v[166:167], s[58:59] op_sel_hi:[1,0]\n"
"v_pk_mul_f32 v[136:137], v[136:137], s[58:59] op_sel_hi:[1,0]\n"
"v_pk_mul_f32 v[138:139], v[138:139], s[58:59] op_sel_hi:[1,0]\n"
"v_pk_mul_f32 v[168:169], v[168:169], s[58:59] op_sel_hi:[1,0]\n"
"v_pk_mul_f32 v[170:171], v[170:171], s[58:59] op_sel_hi:[1,0]\n"
"v_pk_mul_f32 v[140:141], v[140:141], s[58:59] op_sel_hi:[1,0]\n"
"v_pk_mul_f32 v[142:143], v[142:143], s[58:59] op_sel_hi:[1,0]\n"
"v_pk_mul_f32 v[172:173], v[172:173], s[58:59] op_sel_hi:[1,0]\n"
"v_pk_mul_f32 v[174:175], v[174:175], s[58:59] op_sel_hi:[1,0]\n"
"v_pk_mul_f32 v[144:145], v[144:145], s[58:59] op_sel_hi:[1,0]\n"
"v_pk_mul_f32 v[146:147], v[146:147], s[58:59] op_sel_hi:[1,0]\n"
"v_pk_mul_f32 v[176:177], v[176:177], s[58:59] op_sel_hi:[1,0]\n"
"v_pk_mul_f32 v[178:179], v[178:179], s[58:59] op_sel_hi:[1,0]\n"
"v_pk_mul_f32 v[148:149], v[148:149], s[58:59] op_sel_hi:[1,0]\n"
"v_pk_mul_f32 v[150:151], v[150:151], s[58:59] op_sel_hi:[1,0]\n"
"v_pk_mul_f32 v[180:181], v[180:181], s[58:59] op_sel_hi:[1,0]\n"
"v_pk_mul_f32 v[182:183], v[182:183], s[58:59] op_sel_hi:[1,0]\n"
"v_pk_mul_f32 v[152:153], v[152:153], s[58:59] op_sel_hi:[1,0]\n"
"v_pk_mul_f32 v[154:155], v[154:155], s[58:59] op_sel_hi:[1,0]\n"
"v_pk_mul_f32 v[184:185], v[184:185], s[58:59] op_sel_hi:[1,0]\n"
"v_pk_mul_f32 v[186:187], v[186:187], s[58:59] op_sel_hi:[1,0]\n"
"v_pk_mul_f32 v[156:157], v[156:157], s[58:59] op_sel_hi:[1,0]\n"
"v_pk_mul_f32 v[158:159], v[158:159], s[58:59] op_sel_hi:[1,0]\n"
"v_pk_mul_f32 v[188:189], v[188:189], s[58:59] op_sel_hi:[1,0]\n"
"v_pk_mul_f32 v[190:191], v[190:191], s[58:59] op_sel_hi:[1,0]\n"
"v_cvt_pk_bf16_f32 v32, v128, v132\n"
"v_cvt_pk_bf16_f32 v33, v136, v140\n"
"v_cvt_pk_bf16_f32 v34, v144, v148\n"
"v_cvt_pk_bf16_f32 v35, v152, v156\n"
"v_cvt_pk_bf16_f32 v36, v129, v133\n"
"v_cvt_pk_bf16_f32 v37, v137, v141\n"
"v_cvt_pk_bf16_f32 v38, v145, v149\n"
"v_cvt_pk_bf16_f32 v39, v153, v157\n"
"v_cvt_pk_bf16_f32 v40, v130, v134\n"
"v_cvt_pk_bf16_f32 v41, v138, v142\n"
"v_cvt_pk_bf16_f32 v42, v146, v150\n"
"v_cvt_pk_bf16_f32 v43, v154, v158\n"
"v_cvt_pk_bf16_f32 v44, v131, v135\n"
"v_cvt_pk_bf16_f32 v45, v139, v143\n"
"v_cvt_pk_bf16_f32 v46, v147, v151\n"
"v_cvt_pk_bf16_f32 v47, v155, v159\n"
"v_cvt_pk_bf16_f32 v48, v160, v164\n"
"v_cvt_pk_bf16_f32 v49, v168, v172\n"
"v_cvt_pk_bf16_f32 v50, v176, v180\n"
"v_cvt_pk_bf16_f32 v51, v184, v188\n"
"v_cvt_pk_bf16_f32 v52, v161, v165\n"
"v_cvt_pk_bf16_f32 v53, v169, v173\n"
"v_cvt_pk_bf16_f32 v54, v177, v181\n"
"v_cvt_pk_bf16_f32 v55, v185, v189\n"
"v_cvt_pk_bf16_f32 v56, v162, v166\n"
"v_cvt_pk_bf16_f32 v57, v170, v174\n"
"v_cvt_pk_bf16_f32 v58, v178, v182\n"
"v_cvt_pk_bf16_f32 v59, v186, v190\n"
"v_cvt_pk_bf16_f32 v60, v163, v167\n"
"v_cvt_pk_bf16_f32 v61, v171, v175\n"
"v_cvt_pk_bf16_f32 v62, v179, v183\n"
"v_cvt_pk_bf16_f32 v63, v187, v191\n"
"v_bfe_u32 v180, v0, 3, 1\n"
"v_lshrrev_b32 v181, 4, v0\n"
"v_and_b32 v182, 7, v0\n"
"v_bfe_u32 v183, v0, 2, 1\n"
"v_lshl_or_b32 v183, v183, 1, v180\n"
"v_lshlrev_b32 v180, 11, v180\n"
"v_lshl_add_u32 v180, v181, 9, v180\n"
"v_lshl_add_u32 v180, v182, 6, v180\n"
"v_xor_b32 v184, 0, v183\n"
"v_lshl_add_u32 v168, v184, 4, v180\n"
"v_xor_b32 v184, 1, v183\n"
"v_lshl_add_u32 v169, v184, 4, v180\n"
"v_xor_b32 v184, 2, v183\n"
"v_lshl_add_u32 v170, v184, 4, v180\n"
"v_xor_b32 v184, 3, v183\n"
"v_lshl_add_u32 v171, v184, 4, v180\n"
"s_waitcnt vmcnt(4)\n"
"s_barrier\n"
"ds_read_b128 a[64:67], v168 offset:0\n"
"ds_read_b128 a[68:71], v168 offset:4096\n"
"ds_read_b128 a[72:75], v168 offset:8192\n"
"ds_read_b128 a[76:79], v168 offset:12288\n"
"ds_read_b128 a[80:83], v169 offset:0\n"
"ds_read_b128 a[84:87], v169 offset:4096\n"
"ds_read_b128 a[88:91], v169 offset:8192\n"
"ds_read_b128 a[92:95], v169 offset:12288\n"
"s_add_u32 m0, s60, 32768\n"
"s_nop 0\n"
"buffer_load_dwordx4 v2, s[32:35], s62 offen offset:0 nt lds\n"
"buffer_load_dwordx4 v3, s[32:35], s62 offen offset:1024 nt lds\n"
"buffer_load_dwordx4 v4, s[32:35], s62 offen offset:2048 nt lds\n"
"buffer_load_dwordx4 v5, s[32:35], s62 offen offset:3072 nt lds\n"
"s_add_u32 m0, s60, 49152\n"
"s_nop 0\n"
"buffer_load_dwordx4 v2, s[28:31], s64 offen offset:0 nt lds\n"
"buffer_load_dwordx4 v3, s[28:31], s64 offen offset:1024 nt lds\n"
"buffer_load_dwordx4 v4, s[28:31], s64 offen offset:2048 nt lds\n"
"buffer_load_dwordx4 v5, s[28:31], s64 offen offset:3072 nt lds\n"
"s_waitcnt lgkmcnt(7)\n"
"v_mfma_f32_16x16x32_bf16 v[64:67], a[64:67], v[32:35], 0\n"
"s_waitcnt lgkmcnt(2)\n"
"v_lshrrev_b32 v180, 5, v0\n"
"v_bfe_u32 v182, v0, 4, 1\n"
"v_mfma_f32_16x16x32_bf16 v[72:75], a[68:71], v[32:35], 0\n"
"v_lshl_or_b32 v183, v182, 1, v180\n"
"v_bfe_u32 v184, v0, 1, 1\n"
"v_mfma_f32_16x16x32_bf16 v[80:83], a[72:75], v[32:35], 0\n"
"v_xor_b32 v183, v183, v184\n"
"v_lshlrev_b32 v180, 11, v180\n"
"v_mfma_f32_16x16x32_bf16 v[88:91], a[76:79], v[32:35], 0\n"
"v_lshl_add_u32 v180, v182, 8, v180\n"
"v_bfe_u32 v182, v0, 2, 2\n"
"v_mfma_f32_16x16x32_bf16 v[68:71], a[64:67], v[48:51], 0\n"
"ds_read_b128 a[64:67], v170 offset:0\n"
"v_lshl_add_u32 v180, v182, 6, v180\n"
"v_mfma_f32_16x16x32_bf16 v[76:79], a[68:71], v[48:51], 0\n"
"ds_read_b128 a[68:71], v170 offset:4096\n"
"v_and_b32 v182, 1, v0\n"
"v_mfma_f32_16x16x32_bf16 v[84:87], a[72:75], v[48:51], 0\n"
"ds_read_b128 a[72:75], v170 offset:8192\n"
"v_lshl_add_u32 v180, v182, 3, v180\n"
"v_mfma_f32_16x16x32_bf16 v[92:95], a[76:79], v[48:51], 0\n"
"ds_read_b128 a[76:79], v170 offset:12288\n"
"v_lshl_add_u32 v172, v183, 4, v180\n"
"v_mfma_f32_16x16x32_bf16 v[64:67], a[80:83], v[36:39], v[64:67]\n"
"s_add_u32 m0, s60, 65536\n"
"v_xor_b32 v173, 32, v172\n"
"v_mfma_f32_16x16x32_bf16 v[72:75], a[84:87], v[36:39], v[72:75]\n"
"s_waitcnt lgkmcnt(4)\n"
"buffer_load_dwordx4 v2, s[32:35], s63 offen offset:0 nt lds\n"
"v_lshlrev_b32 v12, 4, v0\n"
"v_mfma_f32_16x16x32_bf16 v[80:83], a[88:91], v[36:39], v[80:83]\n"
"v_add_u32 v174, s60, v12\n"
"v_add_u32 v175, s60, v174\n"
"v_mfma_f32_16x16x32_bf16 v[88:91], a[92:95], v[36:39], v[88:91]\n"
"v_and_b32 v182, 15, v0\n"
"v_lshlrev_b32 v182, 4, v182\n"
"v_mfma_f32_16x16x32_bf16 v[68:71], a[80:83], v[52:55], v[68:71]\n"
"ds_read_b128 a[80:83], v171 offset:0\n"
"v_lshl_add_u32 v13, v181, 12, v182\n"
"v_mfma_f32_16x16x32_bf16 v[76:79], a[84:87], v[52:55], v[76:79]\n"
"ds_read_b128 a[84:87], v171 offset:4096\n"
"buffer_load_dwordx4 v3, s[32:35], s63 offen offset:1024 nt lds\n"
"v_mfma_f32_16x16x32_bf16 v[84:87], a[88:91], v[52:55], v[84:87]\n"
"ds_read_b128 a[88:91], v171 offset:8192\n"
"v_add_u32 v14, 16384, v13\n"
"v_mfma_f32_16x16x32_bf16 v[92:95], a[92:95], v[52:55], v[92:95]\n"
"s_waitcnt lgkmcnt(3)\n"
"ds_read_b128 a[92:95], v171 offset:12288\n"
"v_mov_b32 v176, 0x3f803f80\n"
"v_mfma_f32_16x16x32_bf16 v[64:67], a[64:67], v[40:43], v[64:67]\n"
"v_mov_b32 v177, 0x3f803f80\n"
"v_mov_b32 v178, 0x3f803f80\n"
"v_mfma_f32_16x16x32_bf16 v[72:75], a[68:71], v[40:43], v[72:75]\n"
"buffer_load_dwordx4 v4, s[32:35], s63 offen offset:2048 nt lds\n"
"v_mov_b32 v179, 0x3f803f80\n"
"v_mfma_f32_16x16x32_bf16 v[80:83], a[72:75], v[40:43], v[80:83]\n"
"v_mul_f32 v192, s58, v192\n"
"v_mul_f32 v196, s58, v196\n"
"v_mfma_f32_16x16x32_bf16 v[88:91], a[76:79], v[40:43], v[88:91]\n"
"v_cvt_pk_bf16_f32 v16, v192, v196\n"
"v_mul_f32 v200, s58, v200\n"
"v_mfma_f32_16x16x32_bf16 v[68:71], a[64:67], v[56:59], v[68:71]\n"
"v_mul_f32 v204, s58, v204\n"
"v_cvt_pk_bf16_f32 v17, v200, v204\n"
"v_mfma_f32_16x16x32_bf16 v[76:79], a[68:71], v[56:59], v[76:79]\n"
"buffer_load_dwordx4 v5, s[32:35], s63 offen offset:3072 nt lds\n"
"v_mul_f32 v208, s58, v208\n"
"v_mfma_f32_16x16x32_bf16 v[84:87], a[72:75], v[56:59], v[84:87]\n"
"v_mul_f32 v212, s58, v212\n"
"v_cvt_pk_bf16_f32 v18, v208, v212\n"
"v_mfma_f32_16x16x32_bf16 v[92:95], a[76:79], v[56:59], v[92:95]\n"
"s_waitcnt lgkmcnt(0)\n"
"v_mul_f32 v216, s58, v216\n"
"v_mul_f32 v220, s58, v220\n"
"v_mfma_f32_16x16x32_bf16 v[64:67], a[80:83], v[44:47], v[64:67]\n"
"v_cvt_pk_bf16_f32 v19, v216, v220\n"
"v_mul_f32 v193, s58, v193\n"
"v_mfma_f32_16x16x32_bf16 v[72:75], a[84:87], v[44:47], v[72:75]\n"
"v_mul_f32 v197, s58, v197\n"
"v_cvt_pk_bf16_f32 v20, v193, v197\n"
"v_mfma_f32_16x16x32_bf16 v[80:83], a[88:91], v[44:47], v[80:83]\n"
"v_mul_f32 v201, s58, v201\n"
"v_mul_f32 v205, s58, v205\n"
"v_mfma_f32_16x16x32_bf16 v[88:91], a[92:95], v[44:47], v[88:91]\n"
"v_cvt_pk_bf16_f32 v21, v201, v205\n"
"v_mul_f32 v209, s58, v209\n"
"v_mfma_f32_16x16x32_bf16 v[68:71], a[80:83], v[60:63], v[68:71]\n"
"v_mul_f32 v213, s58, v213\n"
"v_cvt_pk_bf16_f32 v22, v209, v213\n"
"v_mfma_f32_16x16x32_bf16 v[76:79], a[84:87], v[60:63], v[76:79]\n"
"v_mul_f32 v217, s58, v217\n"
"v_mul_f32 v221, s58, v221\n"
"v_mfma_f32_16x16x32_bf16 v[84:87], a[88:91], v[60:63], v[84:87]\n"
"v_cvt_pk_bf16_f32 v23, v217, v221\n"
"v_mul_f32 v194, s58, v194\n"
"v_mfma_f32_16x16x32_bf16 v[92:95], a[92:95], v[60:63], v[92:95]\n"
"v_mul_f32 v198, s58, v198\n"
"v_cvt_pk_bf16_f32 v24, v194, v198\n"
"s_waitcnt vmcnt(8)\n"
"s_barrier\n"
".LAbar0:\n"
"ds_read_b128 a[64:67], v168 offset:16384\n"
"ds_read_b128 a[68:71], v168 offset:20480\n"
"ds_read_b128 a[72:75], v168 offset:24576\n"
"ds_read_b128 a[76:79], v168 offset:28672\n"
"ds_read_b128 a[80:83], v169 offset:16384\n"
"ds_read_b128 a[84:87], v169 offset:20480\n"
"ds_read_b128 a[88:91], v169 offset:24576\n"
"ds_read_b128 a[92:95], v169 offset:28672\n"
"s_add_u32 m0, s60, 0\n"
"s_nop 0\n"
"buffer_load_dwordx4 v2, s[28:31], s65 offen offset:0 nt lds\n"
"buffer_load_dwordx4 v3, s[28:31], s65 offen offset:1024 nt lds\n"
"buffer_load_dwordx4 v4, s[28:31], s65 offen offset:2048 nt lds\n"
"buffer_load_dwordx4 v5, s[28:31], s65 offen offset:3072 nt lds\n"
"s_add_u32 m0, s60, 81920\n"
"s_nop 0\n"
"buffer_load_dwordx4 v2, s[32:35], s64 offen offset:0 nt lds\n"
"buffer_load_dwordx4 v3, s[32:35], s64 offen offset:1024 nt lds\n"
"buffer_load_dwordx4 v4, s[32:35], s64 offen offset:2048 nt lds\n"
"buffer_load_dwordx4 v5, s[32:35], s64 offen offset:3072 nt lds\n"
"v_max3_f32 v188, v64, v65, v66\n"
"v_max3_f32 v189, v68, v69, v70\n"
"v_max3_f32 v188, v188, v67, v72\n"
"v_max3_f32 v189, v189, v71, v76\n"
"v_max3_f32 v188, v188, v73, v74\n"
"v_max3_f32 v189, v189, v77, v78\n"
"v_max3_f32 v188, v188, v75, v80\n"
"v_max3_f32 v189, v189, v79, v84\n"
"v_max3_f32 v188, v188, v81, v82\n"
"v_max3_f32 v189, v189, v85, v86\n"
"v_max3_f32 v188, v188, v83, v88\n"
"v_max3_f32 v189, v189, v87, v92\n"
"v_max3_f32 v188, v188, v89, v90\n"
"v_max3_f32 v189, v189, v93, v94\n"
"v_max_f32 v188, v188, v91\n"
"v_max_f32 v189, v189, v95\n"
"s_nop 0\n"
"s_nop 0\n"
"v_permlane32_swap_b32 v188, v189\n"
"v_max_f32 v188, v188, v189\n"
"v_mov_b32 v189, v188\n"
"s_nop 0\n"
"s_nop 0\n"
"v_permlane16_swap_b32 v188, v189\n"
"v_max_f32 v188, v188, v189\n"
"v_mov_b32 v190, v188\n"
"s_nop 0\n"
"s_nop 0\n"
"v_permlane32_swap_b32 v188, v190\n"
"v_sub_f32 v128, 0, v188\n"
"v_sub_f32 v129, 0, v188\n"
"v_sub_f32 v130, 0, v188\n"
"v_sub_f32 v131, 0, v188\n"
"v_sub_f32 v64, v64, v188\n"
"v_sub_f32 v65, v65, v188\n"
"v_sub_f32 v66, v66, v188\n"
"v_sub_f32 v67, v67, v188\n"
"v_sub_f32 v72, v72, v188\n"
"v_sub_f32 v73, v73, v188\n"
"v_sub_f32 v74, v74, v188\n"
"v_sub_f32 v75, v75, v188\n"
"v_sub_f32 v80, v80, v188\n"
"v_sub_f32 v81, v81, v188\n"
"v_sub_f32 v82, v82, v188\n"
"v_sub_f32 v83, v83, v188\n"
"v_sub_f32 v88, v88, v188\n"
"v_sub_f32 v89, v89, v188\n"
"v_sub_f32 v90, v90, v188\n"
"v_sub_f32 v91, v91, v188\n"
"v_sub_f32 v132, 0, v190\n"
"v_sub_f32 v133, 0, v190\n"
"v_sub_f32 v134, 0, v190\n"
"v_sub_f32 v135, 0, v190\n"
"v_sub_f32 v68, v68, v190\n"
"v_sub_f32 v69, v69, v190\n"
"v_sub_f32 v70, v70, v190\n"
"v_sub_f32 v71, v71, v190\n"
"v_sub_f32 v76, v76, v190\n"
"v_sub_f32 v77, v77, v190\n"
"v_sub_f32 v78, v78, v190\n"
"v_sub_f32 v79, v79, v190\n"
"v_sub_f32 v84, v84, v190\n"
"v_sub_f32 v85, v85, v190\n"
"v_sub_f32 v86, v86, v190\n"
"v_sub_f32 v87, v87, v190\n"
"v_sub_f32 v92, v92, v190\n"
"v_sub_f32 v93, v93, v190\n"
"v_sub_f32 v94, v94, v190\n"
"v_sub_f32 v95, v95, v190\n"
"s_waitcnt lgkmcnt(0)\n"
"v_mfma_f32_16x16x32_bf16 v[96:99], a[64:67], v[32:35], v[128:131]\n"
"v_exp_f32 v64, v64\n"
"v_mfma_f32_16x16x32_bf16 v[104:107], a[68:71], v[32:35], v[128:131]\n"
"v_exp_f32 v65, v65\n"
"v_mfma_f32_16x16x32_bf16 v[112:115], a[72:75], v[32:35], v[128:131]\n"
"v_exp_f32 v66, v66\n"
"v_mfma_f32_16x16x32_bf16 v[120:123], a[76:79], v[32:35], v[128:131]\n"
"v_exp_f32 v67, v67\n"
"v_mfma_f32_16x16x32_bf16 v[100:103], a[64:67], v[48:51], v[132:135]\n"
"ds_read_b128 a[64:67], v170 offset:16384\n"
"v_cvt_pk_bf16_f32 v144, v64, v65\n"
"v_mfma_f32_16x16x32_bf16 v[108:111], a[68:71], v[48:51], v[132:135]\n"
"ds_read_b128 a[68:71], v170 offset:20480\n"
"v_mul_f32 v202, s58, v202\n"
"v_exp_f32 v72, v72\n"
"v_mfma_f32_16x16x32_bf16 v[116:119], a[72:75], v[48:51], v[132:135]\n"
"ds_read_b128 a[72:75], v170 offset:24576\n"
"v_mul_f32 v206, s58, v206\n"
"v_mfma_f32_16x16x32_bf16 v[124:127], a[76:79], v[48:51], v[132:135]\n"
"ds_read_b128 a[76:79], v170 offset:28672\n"
"v_cvt_pk_bf16_f32 v25, v202, v206\n"
"v_exp_f32 v73, v73\n"
"v_mfma_f32_16x16x32_bf16 v[96:99], a[80:83], v[36:39], v[96:99]\n"
"s_add_u32 m0, s60, 98304\n"
"v_cvt_pk_bf16_f32 v145, v66, v67\n"
"v_exp_f32 v74, v74\n"
"v_mfma_f32_16x16x32_bf16 v[104:107], a[84:87], v[36:39], v[104:107]\n"
"buffer_load_dwordx4 v2, s[28:31], s66 offen offset:0 nt lds\n"
"v_mul_f32 v210, s58, v210\n"
"v_mfma_f32_16x16x32_bf16 v[112:115], a[88:91], v[36:39], v[112:115]\n"
"v_exp_f32 v75, v75\n"
"v_mfma_f32_16x16x32_bf16 v[120:123], a[92:95], v[36:39], v[120:123]\n"
"v_cvt_pk_bf16_f32 v146, v72, v73\n"
"v_mul_f32 v214, s58, v214\n"
"v_exp_f32 v68, v68\n"
"v_mfma_f32_16x16x32_bf16 v[100:103], a[80:83], v[52:55], v[100:103]\n"
"ds_read_b128 a[80:83], v171 offset:16384\n"
"v_cvt_pk_bf16_f32 v26, v210, v214\n"
"v_mfma_f32_16x16x32_bf16 v[108:111], a[84:87], v[52:55], v[108:111]\n"
"ds_read_b128 a[84:87], v171 offset:20480\n"
"buffer_load_dwordx4 v3, s[28:31], s66 offen offset:1024 nt lds\n"
"v_exp_f32 v69, v69\n"
"v_mfma_f32_16x16x32_bf16 v[116:119], a[88:91], v[52:55], v[116:119]\n"
"ds_read_b128 a[88:91], v171 offset:24576\n"
"v_cvt_pk_bf16_f32 v147, v74, v75\n"
"v_exp_f32 v70, v70\n"
"v_mfma_f32_16x16x32_bf16 v[124:127], a[92:95], v[52:55], v[124:127]\n"
"s_waitcnt lgkmcnt(3)\n"
"ds_read_b128 a[92:95], v171 offset:28672\n"
"v_mul_f32 v218, s58, v218\n"
"v_mfma_f32_16x16x32_bf16 v[96:99], a[64:67], v[40:43], v[96:99]\n"
"ds_read_b64_tr_b16 a[96:97], v172 offset:32768\n"
"v_mul_f32 v222, s58, v222\n"
"v_exp_f32 v71, v71\n"
"v_mfma_f32_16x16x32_bf16 v[104:107], a[68:71], v[40:43], v[104:107]\n"
"ds_read_b64_tr_b16 a[98:99], v172 offset:36864\n"
"buffer_load_dwordx4 v4, s[28:31], s66 offen offset:2048 nt lds\n"
"v_cvt_pk_bf16_f32 v148, v68, v69\n"
"v_mfma_f32_16x16x32_bf16 v[112:115], a[72:75], v[40:43], v[112:115]\n"
"ds_read_b64_tr_b16 a[100:101], v173 offset:32768\n"
"v_cvt_pk_bf16_f32 v27, v218, v222\n"
"v_exp_f32 v76, v76\n"
"v_mfma_f32_16x16x32_bf16 v[120:123], a[76:79], v[40:43], v[120:123]\n"
"ds_read_b64_tr_b16 a[102:103], v173 offset:36864\n"
"v_mul_f32 v195, s58, v195\n"
"v_exp_f32 v77, v77\n"
"v_mfma_f32_16x16x32_bf16 v[100:103], a[64:67], v[56:59], v[100:103]\n"
"ds_read_b64_tr_b16 a[104:105], v172 offset:33280\n"
"v_cvt_pk_bf16_f32 v149, v70, v71\n"
"v_exp_f32 v78, v78\n"
"v_mfma_f32_16x16x32_bf16 v[108:111], a[68:71], v[56:59], v[108:111]\n"
"ds_read_b64_tr_b16 a[106:107], v172 offset:37376\n"
"buffer_load_dwordx4 v5, s[28:31], s66 offen offset:3072 nt lds\n"
"v_exp_f32 v79, v79\n"
"v_mfma_f32_16x16x32_bf16 v[116:119], a[72:75], v[56:59], v[116:119]\n"
"ds_read_b64_tr_b16 a[108:109], v173 offset:33280\n"
"v_cvt_pk_bf16_f32 v150, v76, v77\n"
"v_mfma_f32_16x16x32_bf16 v[124:127], a[76:79], v[56:59], v[124:127]\n"
"s_waitcnt lgkmcnt(3)\n"
"ds_read_b64_tr_b16 a[110:111], v173 offset:37376\n"
"v_mul_f32 v199, s58, v199\n"
"v_exp_f32 v80, v80\n"
"v_mfma_f32_16x16x32_bf16 v[96:99], a[80:83], v[44:47], v[96:99]\n"
"ds_read_b64_tr_b16 a[112:113], v172 offset:33792\n"
"v_cvt_pk_bf16_f32 v28, v195, v199\n"
"v_exp_f32 v81, v81\n"
"v_mfma_f32_16x16x32_bf16 v[104:107], a[84:87], v[44:47], v[104:107]\n"
"ds_read_b64_tr_b16 a[114:115], v172 offset:37888\n"
"v_cvt_pk_bf16_f32 v151, v78, v79\n"
"v_mfma_f32_16x16x32_bf16 v[112:115], a[88:91], v[44:47], v[112:115]\n"
"ds_read_b64_tr_b16 a[116:117], v173 offset:33792\n"
"v_mul_f32 v203, s58, v203\n"
"v_exp_f32 v82, v82\n"
"v_mfma_f32_16x16x32_bf16 v[120:123], a[92:95], v[44:47], v[120:123]\n"
"ds_read_b64_tr_b16 a[118:119], v173 offset:37888\n"
"v_mul_f32 v207, s58, v207\n"
"v_exp_f32 v83, v83\n"
"v_mfma_f32_16x16x32_bf16 v[100:103], a[80:83], v[60:63], v[100:103]\n"
"ds_read_b64_tr_b16 a[120:121], v172 offset:34304\n"
"v_cvt_pk_bf16_f32 v152, v80, v81\n"
"v_mfma_f32_16x16x32_bf16 v[108:111], a[84:87], v[60:63], v[108:111]\n"
"ds_read_b64_tr_b16 a[122:123], v172 offset:38400\n"
"v_cvt_pk_bf16_f32 v29, v203, v207\n"
"v_exp_f32 v88, v88\n"
"v_mfma_f32_16x16x32_bf16 v[116:119], a[88:91], v[60:63], v[116:119]\n"
"ds_read_b64_tr_b16 a[124:125], v173 offset:34304\n"
"v_mul_f32 v211, s58, v211\n"
"v_exp_f32 v89, v89\n"
"v_mfma_f32_16x16x32_bf16 v[124:127], a[92:95], v[60:63], v[124:127]\n"
"ds_read_b64_tr_b16 a[126:127], v173 offset:38400\n"
"v_cvt_pk_bf16_f32 v153, v82, v83\n"
"s_waitcnt vmcnt(12)\n"
"s_barrier\n"
".LAbar1:\n"
"v_mfma_f32_16x16x32_bf16 a[0:3], a[96:99], v[144:147], 0\n"
"ds_read_b128 a[64:67], v168 offset:49152\n"
"s_add_u32 m0, s60, 16384\n"
"v_exp_f32 v90, v90\n"
"v_mfma_f32_16x16x32_bf16 a[8:11], a[100:103], v[144:147], 0\n"
"s_waitcnt lgkmcnt(3)\n"
"ds_read_b128 a[68:71], v168 offset:53248\n"
"buffer_load_dwordx4 v2, s[32:35], s65 offen offset:0 nt lds\n"
"v_mfma_f32_16x16x32_bf16 a[16:19], a[104:107], v[144:147], 0\n"
"ds_read_b128 a[72:75], v168 offset:57344\n"
"v_mul_f32 v215, s58, v215\n"
"v_exp_f32 v91, v91\n"
"v_mfma_f32_16x16x32_bf16 a[24:27], a[108:111], v[144:147], 0\n"
"ds_read_b128 a[76:79], v168 offset:61440\n"
"v_cvt_pk_bf16_f32 v154, v88, v89\n"
"v_mfma_f32_16x16x32_bf16 a[4:7], a[96:99], v[148:151], 0\n"
"ds_read_b64_tr_b16 a[96:97], v172 offset:40960\n"
"v_cvt_pk_bf16_f32 v30, v211, v215\n"
"v_exp_f32 v84, v84\n"
"v_mfma_f32_16x16x32_bf16 a[12:15], a[100:103], v[148:151], 0\n"
"ds_read_b64_tr_b16 a[98:99], v172 offset:45056\n"
"buffer_load_dwordx4 v3, s[32:35], s65 offen offset:1024 nt lds\n"
"v_mfma_f32_16x16x32_bf16 a[20:23], a[104:107], v[148:151], 0\n"
"ds_read_b64_tr_b16 a[100:101], v173 offset:40960\n"
"v_mul_f32 v219, s58, v219\n"
"v_exp_f32 v85, v85\n"
"v_mfma_f32_16x16x32_bf16 a[28:31], a[108:111], v[148:151], 0\n"
"ds_read_b64_tr_b16 a[102:103], v173 offset:45056\n"
"v_cvt_pk_bf16_f32 v155, v90, v91\n"
"v_mfma_f32_16x16x32_bf16 v[160:163], v[176:179], v[144:147], 0\n"
"ds_read_b64_tr_b16 a[104:105], v172 offset:41472\n"
"v_mul_f32 v223, s58, v223\n"
"v_exp_f32 v86, v86\n"
"v_mfma_f32_16x16x32_bf16 a[32:35], a[112:115], v[144:147], 0\n"
"ds_read_b64_tr_b16 a[106:107], v172 offset:45568\n"
"buffer_load_dwordx4 v4, s[32:35], s65 offen offset:2048 nt lds\n"
"v_mfma_f32_16x16x32_bf16 a[40:43], a[116:119], v[144:147], 0\n"
"ds_read_b64_tr_b16 a[108:109], v173 offset:41472\n"
"v_cvt_pk_bf16_f32 v31, v219, v223\n"
"v_exp_f32 v87, v87\n"
"v_mfma_f32_16x16x32_bf16 a[48:51], a[120:123], v[144:147], 0\n"
"s_waitcnt lgkmcnt(4)\n"
"ds_read_b64_tr_b16 a[110:111], v173 offset:45568\n"
"v_cvt_pk_bf16_f32 v156, v84, v85\n"
"v_mfma_f32_16x16x32_bf16 a[56:59], a[124:127], v[144:147], 0\n"
"v_exp_f32 v92, v92\n"
"v_mfma_f32_16x16x32_bf16 a[36:39], a[112:115], v[148:151], 0\n"
"ds_read_b64_tr_b16 a[112:113], v172 offset:41984\n"
"buffer_load_dwordx4 v5, s[32:35], s65 offen offset:3072 nt lds\n"
"v_exp_f32 v93, v93\n"
"v_mfma_f32_16x16x32_bf16 a[44:47], a[116:119], v[148:151], 0\n"
"ds_read_b64_tr_b16 a[114:115], v172 offset:46080\n"
"v_cvt_pk_bf16_f32 v157, v86, v87\n"
"v_mfma_f32_16x16x32_bf16 a[52:55], a[120:123], v[148:151], 0\n"
"ds_read_b64_tr_b16 a[116:117], v173 offset:41984\n"
"v_max3_f32 v188, v96, v97, v98\n"
"v_mfma_f32_16x16x32_bf16 a[60:63], a[124:127], v[148:151], 0\n"
"ds_read_b64_tr_b16 a[118:119], v173 offset:46080\n"
"v_max3_f32 v189, v100, v101, v102\n"
"v_exp_f32 v94, v94\n"
"v_mfma_f32_16x16x32_bf16 v[164:167], v[176:179], v[148:151], 0\n"
"ds_read_b64_tr_b16 a[120:121], v172 offset:42496\n"
"v_max3_f32 v188, v188, v99, v104\n"
"v_exp_f32 v95, v95\n"
"v_mfma_f32_16x16x32_bf16 a[0:3], a[96:99], v[152:155], a[0:3]\n"
"s_waitcnt lgkmcnt(4)\n"
"ds_read_b64_tr_b16 a[122:123], v172 offset:46592\n"
"v_cvt_pk_bf16_f32 v158, v92, v93\n"
"v_mfma_f32_16x16x32_bf16 a[8:11], a[100:103], v[152:155], a[8:11]\n"
"ds_read_b64_tr_b16 a[124:125], v173 offset:42496\n"
"v_cvt_pk_bf16_f32 v159, v94, v95\n"
"v_mfma_f32_16x16x32_bf16 a[16:19], a[104:107], v[152:155], a[16:19]\n"
"ds_read_b64_tr_b16 a[126:127], v173 offset:46592\n"
"v_add_u32 v172, 0x10000, v172\n"
"v_mfma_f32_16x16x32_bf16 a[24:27], a[108:111], v[152:155], a[24:27]\n"
"v_add_u32 v173, 0x10000, v173\n"
"v_max3_f32 v189, v189, v103, v108\n"
"v_mfma_f32_16x16x32_bf16 a[4:7], a[96:99], v[156:159], a[4:7]\n"
"ds_read_b128 a[80:83], v169 offset:49152\n"
"v_max3_f32 v188, v188, v105, v106\n"
"v_mfma_f32_16x16x32_bf16 a[12:15], a[100:103], v[156:159], a[12:15]\n"
"ds_read_b128 a[84:87], v169 offset:53248\n"
"v_max3_f32 v189, v189, v109, v110\n"
"v_mfma_f32_16x16x32_bf16 a[20:23], a[104:107], v[156:159], a[20:23]\n"
"ds_read_b128 a[88:91], v169 offset:57344\n"
"v_max3_f32 v188, v188, v107, v112\n"
"v_mfma_f32_16x16x32_bf16 a[28:31], a[108:111], v[156:159], a[28:31]\n"
"ds_read_b128 a[92:95], v169 offset:61440\n"
"v_max3_f32 v189, v189, v111, v116\n"
"v_mfma_f32_16x16x32_bf16 v[160:163], v[176:179], v[152:155], v[160:163]\n"
"s_waitcnt lgkmcnt(4)\n"
"ds_read_b64_tr_b16 a[96:97], v172 offset:0\n"
"v_max3_f32 v188, v188, v113, v114\n"
"v_mfma_f32_16x16x32_bf16 a[32:35], a[112:115], v[152:155], a[32:35]\n"
"ds_read_b64_tr_b16 a[98:99], v172 offset:4096\n"
"v_max3_f32 v189, v189, v117, v118\n"
"v_mfma_f32_16x16x32_bf16 a[40:43], a[116:119], v[152:155], a[40:43]\n"
"ds_read_b64_tr_b16 a[100:101], v173 offset:0\n"
"v_max3_f32 v188, v188, v115, v120\n"
"v_mfma_f32_16x16x32_bf16 a[48:51], a[120:123], v[152:155], a[48:51]\n"
"ds_read_b64_tr_b16 a[102:103], v173 offset:4096\n"
"v_max3_f32 v189, v189, v119, v124\n"
"v_mfma_f32_16x16x32_bf16 a[56:59], a[124:127], v[152:155], a[56:59]\n"
"ds_read_b64_tr_b16 a[104:105], v172 offset:512\n"
"v_max3_f32 v188, v188, v121, v122\n"
"v_mfma_f32_16x16x32_bf16 a[36:39], a[112:115], v[156:159], a[36:39]\n"
"ds_read_b64_tr_b16 a[106:107], v172 offset:4608\n"
"v_max3_f32 v189, v189, v125, v126\n"
"v_mfma_f32_16x16x32_bf16 a[44:47], a[116:119], v[156:159], a[44:47]\n"
"ds_read_b64_tr_b16 a[108:109], v173 offset:512\n"
"v_max3_f32 v190, v188, v123, v127\n"
"v_mfma_f32_16x16x32_bf16 a[52:55], a[120:123], v[156:159], a[52:55]\n"
"ds_read_b64_tr_b16 a[110:111], v173 offset:4608\n"
"v_max_f32 v190, v190, v189\n"
"v_mfma_f32_16x16x32_bf16 a[60:63], a[124:127], v[156:159], a[60:63]\n"
"v_cmp_lt_f32 vcc, s59, v190\n"
"s_add_u32 m0, s60, 114688\n"
"v_mfma_f32_16x16x32_bf16 v[164:167], v[176:179], v[156:159], v[164:167]\n"
"buffer_load_dwordx4 v2, s[28:31], s67 offen offset:0 nt lds\n"
"v_mov_b32 v192, v16\n"
"buffer_load_dwordx4 v3, s[28:31], s67 offen offset:1024 nt lds\n"
"buffer_load_dwordx4 v4, s[28:31], s67 offen offset:2048 nt lds\n"
"buffer_load_dwordx4 v5, s[28:31], s67 offen offset:3072 nt lds\n"
"v_and_b32 v184, 7, v0\n"
"s_lshl_b32 s80, s53, 2\n"
"s_add_u32 s80, s80, s48\n"
"s_lshl_b32 s80, s80, 7\n"
"s_add_u32 s80, s80, 524288\n"
"v_lshl_add_u32 v185, v184, 2, s80\n"
"buffer_load_dword v10, v185, s[40:43], 0 offen offset:0 sc1\n"
"s_lshl_b32 s80, s53, 2\n"
"s_add_u32 s80, s80, s48\n"
"s_lshl_b32 s80, s80, 7\n"
"s_add_u32 s80, s80, 524352\n"
"v_lshl_add_u32 v186, v184, 3, s80\n"
"buffer_load_dword v11, v186, s[40:43], 0 offen offset:4 sc1\n"
"s_cbranch_vccz .LAskip1\n"
"s_swappc_b64 s[70:71], s[74:75]\n"
".LAskip1:\n"
"v_mfma_f32_16x16x32_bf16 v[64:67], a[64:67], v[32:35], v[128:131]\n"
"v_exp_f32 v96, v96\n"
"v_mfma_f32_16x16x32_bf16 v[72:75], a[68:71], v[32:35], v[128:131]\n"
"v_exp_f32 v97, v97\n"
"v_mfma_f32_16x16x32_bf16 v[80:83], a[72:75], v[32:35], v[128:131]\n"
"v_exp_f32 v98, v98\n"
"v_mfma_f32_16x16x32_bf16 v[88:91], a[76:79], v[32:35], v[128:131]\n"
"v_exp_f32 v99, v99\n"
"v_mfma_f32_16x16x32_bf16 v[68:71], a[64:67], v[48:51], v[132:135]\n"
"ds_read_b128 a[64:67], v170 offset:49152\n"
"v_cvt_pk_bf16_f32 v144, v96, v97\n"
"v_mfma_f32_16x16x32_bf16 v[76:79], a[68:71], v[48:51], v[132:135]\n"
"ds_read_b128 a[68:71], v170 offset:53248\n"
"v_mov_b32 v193, v17\n"
"v_exp_f32 v104, v104\n"
"v_mfma_f32_16x16x32_bf16 v[84:87], a[72:75], v[48:51], v[132:135]\n"
"ds_read_b128 a[72:75], v170 offset:57344\n"
"v_mov_b32 v194, v18\n"
"v_mfma_f32_16x16x32_bf16 v[92:95], a[76:79], v[48:51], v[132:135]\n"
"s_waitcnt lgkmcnt(3)\n"
"ds_read_b128 a[76:79], v170 offset:61440\n"
"v_mov_b32 v195, v19\n"
"v_exp_f32 v105, v105\n"
"v_mfma_f32_16x16x32_bf16 v[64:67], a[80:83], v[36:39], v[64:67]\n"
"s_add_u32 m0, s60, 131072\n"
"v_cvt_pk_bf16_f32 v145, v98, v99\n"
"v_exp_f32 v106, v106\n"
"v_mfma_f32_16x16x32_bf16 v[72:75], a[84:87], v[36:39], v[72:75]\n"
"buffer_load_dwordx4 v2, s[32:35], s66 offen offset:0 nt lds\n"
"v_mov_b32 v196, v20\n"
"v_mfma_f32_16x16x32_bf16 v[80:83], a[88:91], v[36:39], v[80:83]\n"
"v_exp_f32 v107, v107\n"
"v_mfma_f32_16x16x32_bf16 v[88:91], a[92:95], v[36:39], v[88:91]\n"
"v_cvt_pk_bf16_f32 v146, v104, v105\n"
"v_mov_b32 v197, v21\n"
"v_exp_f32 v100, v100\n"
"v_mfma_f32_16x16x32_bf16 v[68:71], a[80:83], v[52:55], v[68:71]\n"
"ds_read_b128 a[80:83], v171 offset:49152\n"
"v_mov_b32 v198, v22\n"
"v_mfma_f32_16x16x32_bf16 v[76:79], a[84:87], v[52:55], v[76:79]\n"
"ds_read_b128 a[84:87], v171 offset:53248\n"
"buffer_load_dwordx4 v3, s[32:35], s66 offen offset:1024 nt lds\n"
"v_exp_f32 v101, v101\n"
"v_mfma_f32_16x16x32_bf16 v[84:87], a[88:91], v[52:55], v[84:87]\n"
"ds_read_b128 a[88:91], v171 offset:57344\n"
"v_cvt_pk_bf16_f32 v147, v106, v107\n"
"v_exp_f32 v102, v102\n"
"v_mfma_f32_16x16x32_bf16 v[92:95], a[92:95], v[52:55], v[92:95]\n"
"s_waitcnt lgkmcnt(3)\n"
"ds_read_b128 a[92:95], v171 offset:61440\n"
"v_mov_b32 v199, v23\n"
"v_mfma_f32_16x16x32_bf16 v[64:67], a[64:67], v[40:43], v[64:67]\n"
"ds_read_b64_tr_b16 a[112:113], v172 offset:1024\n"
"v_mov_b32 v200, v24\n"
"v_exp_f32 v103, v103\n"
"v_mfma_f32_16x16x32_bf16 v[72:75], a[68:71], v[40:43], v[72:75]\n"
"ds_read_b64_tr_b16 a[114:115], v172 offset:5120\n"
"buffer_load_dwordx4 v4, s[32:35], s66 offen offset:2048 nt lds\n"
"v_cvt_pk_bf16_f32 v148, v100, v101\n"
"v_mfma_f32_16x16x32_bf16 v[80:83], a[72:75], v[40:43], v[80:83]\n"
"ds_read_b64_tr_b16 a[116:117], v173 offset:1024\n"
"v_mov_b32 v201, v25\n"
"v_exp_f32 v108, v108\n"
"v_mfma_f32_16x16x32_bf16 v[88:91], a[76:79], v[40:43], v[88:91]\n"
"ds_read_b64_tr_b16 a[118:119], v173 offset:5120\n"
"v_mov_b32 v202, v26\n"
"v_exp_f32 v109, v109\n"
"v_mfma_f32_16x16x32_bf16 v[68:71], a[64:67], v[56:59], v[68:71]\n"
"ds_read_b64_tr_b16 a[120:121], v172 offset:1536\n"
"v_cvt_pk_bf16_f32 v149, v102, v103\n"
"v_exp_f32 v110, v110\n"
"v_mfma_f32_16x16x32_bf16 v[76:79], a[68:71], v[56:59], v[76:79]\n"
"ds_read_b64_tr_b16 a[122:123], v172 offset:5632\n"
"buffer_load_dwordx4 v5, s[32:35], s66 offen offset:3072 nt lds\n"
"v_exp_f32 v111, v111\n"
"v_mfma_f32_16x16x32_bf16 v[84:87], a[72:75], v[56:59], v[84:87]\n"
"ds_read_b64_tr_b16 a[124:125], v173 offset:1536\n"
"v_cvt_pk_bf16_f32 v150, v108, v109\n"
"v_mfma_f32_16x16x32_bf16 v[92:95], a[76:79], v[56:59], v[92:95]\n"
"s_waitcnt lgkmcnt(3)\n"
"ds_read_b64_tr_b16 a[126:127], v173 offset:5632\n"
"v_mov_b32 v203, v27\n"
"v_exp_f32 v112, v112\n"
"v_mfma_f32_16x16x32_bf16 v[64:67], a[80:83], v[44:47], v[64:67]\n"
"v_exp_f32 v113, v113\n"
"v_mfma_f32_16x16x32_bf16 v[72:75], a[84:87], v[44:47], v[72:75]\n"
"v_cvt_pk_bf16_f32 v151, v110, v111\n"
"v_mov_b32 v204, v28\n"
"v_mfma_f32_16x16x32_bf16 v[80:83], a[88:91], v[44:47], v[80:83]\n"
"v_exp_f32 v114, v114\n"
"v_mfma_f32_16x16x32_bf16 v[88:91], a[92:95], v[44:47], v[88:91]\n"
"v_exp_f32 v115, v115\n"
"v_mfma_f32_16x16x32_bf16 v[68:71], a[80:83], v[60:63], v[68:71]\n"
"v_cvt_pk_bf16_f32 v152, v112, v113\n"
"v_mov_b32 v205, v29\n"
"v_mfma_f32_16x16x32_bf16 v[76:79], a[84:87], v[60:63], v[76:79]\n"
"v_exp_f32 v120, v120\n"
"v_mfma_f32_16x16x32_bf16 v[84:87], a[88:91], v[60:63], v[84:87]\n"
"v_exp_f32 v121, v121\n"
"v_mfma_f32_16x16x32_bf16 v[92:95], a[92:95], v[60:63], v[92:95]\n"
"v_cvt_pk_bf16_f32 v153, v114, v115\n"
"v_mov_b32 v206, v30\n"
"s_waitcnt vmcnt(18)\n"
"s_barrier\n"
"s_sleep 2\n"
".LAbar2:\n"
"v_mfma_f32_16x16x32_bf16 a[0:3], a[96:99], v[144:147], a[0:3]\n"
"ds_read_b128 a[64:67], v168 offset:0\n"
"s_add_u32 m0, s60, 32768\n"
"v_exp_f32 v122, v122\n"
"v_mfma_f32_16x16x32_bf16 a[8:11], a[100:103], v[144:147], a[8:11]\n"
"ds_read_b128 a[68:71], v168 offset:4096\n"
"buffer_load_dwordx4 v2, s[28:31], s68 offen offset:0 nt lds\n"
"v_mfma_f32_16x16x32_bf16 a[16:19], a[104:107], v[144:147], a[16:19]\n"
"ds_read_b128 a[72:75], v168 offset:8192\n"
"v_mov_b32 v207, v31\n"
"v_exp_f32 v123, v123\n"
"v_mfma_f32_16x16x32_bf16 a[24:27], a[108:111], v[144:147], a[24:27]\n"
"ds_read_b128 a[76:79], v168 offset:12288\n"
"v_cvt_pk_bf16_f32 v154, v120, v121\n"
"v_mfma_f32_16x16x32_bf16 a[4:7], a[96:99], v[148:151], a[4:7]\n"
"ds_read_b64_tr_b16 a[96:97], v172 offset:8192\n"
"v_mul_f32 v224, s58, v224\n"
"v_exp_f32 v116, v116\n"
"v_mfma_f32_16x16x32_bf16 a[12:15], a[100:103], v[148:151], a[12:15]\n"
"ds_read_b64_tr_b16 a[98:99], v172 offset:12288\n"
"buffer_load_dwordx4 v3, s[28:31], s68 offen offset:1024 nt lds\n"
"v_mfma_f32_16x16x32_bf16 a[20:23], a[104:107], v[148:151], a[20:23]\n"
"ds_read_b64_tr_b16 a[100:101], v173 offset:8192\n"
"v_mul_f32 v228, s58, v228\n"
"v_exp_f32 v117, v117\n"
"v_mfma_f32_16x16x32_bf16 a[28:31], a[108:111], v[148:151], a[28:31]\n"
"ds_read_b64_tr_b16 a[102:103], v173 offset:12288\n"
"v_cvt_pk_bf16_f32 v155, v122, v123\n"
"v_mfma_f32_16x16x32_bf16 v[160:163], v[176:179], v[144:147], v[160:163]\n"
"ds_read_b64_tr_b16 a[104:105], v172 offset:8704\n"
"v_cvt_pk_bf16_f32 v208, v224, v228\n"
"v_exp_f32 v118, v118\n"
"v_mfma_f32_16x16x32_bf16 a[32:35], a[112:115], v[144:147], a[32:35]\n"
"ds_read_b64_tr_b16 a[106:107], v172 offset:12800\n"
"buffer_load_dwordx4 v4, s[28:31], s68 offen offset:2048 nt lds\n"
"v_mfma_f32_16x16x32_bf16 a[40:43], a[116:119], v[144:147], a[40:43]\n"
"s_waitcnt lgkmcnt(4)\n"
"ds_read_b64_tr_b16 a[108:109], v173 offset:8704\n"
"v_mul_f32 v232, s58, v232\n"
"v_exp_f32 v119, v119\n"
"v_mfma_f32_16x16x32_bf16 a[48:51], a[120:123], v[144:147], a[48:51]\n"
"ds_read_b64_tr_b16 a[110:111], v173 offset:12800\n"
"v_cvt_pk_bf16_f32 v156, v116, v117\n"
"v_mfma_f32_16x16x32_bf16 a[56:59], a[124:127], v[144:147], a[56:59]\n"
"v_exp_f32 v124, v124\n"
"v_mfma_f32_16x16x32_bf16 a[36:39], a[112:115], v[148:151], a[36:39]\n"
"ds_read_b64_tr_b16 a[112:113], v172 offset:9216\n"
"buffer_load_dwordx4 v5, s[28:31], s68 offen offset:3072 nt lds\n"
"v_exp_f32 v125, v125\n"
"v_mfma_f32_16x16x32_bf16 a[44:47], a[116:119], v[148:151], a[44:47]\n"
"ds_read_b64_tr_b16 a[114:115], v172 offset:13312\n"
"v_cvt_pk_bf16_f32 v157, v118, v119\n"
"v_mfma_f32_16x16x32_bf16 a[52:55], a[120:123], v[148:151], a[52:55]\n"
"ds_read_b64_tr_b16 a[116:117], v173 offset:9216\n"
"v_max3_f32 v188, v64, v65, v66\n"
"v_mfma_f32_16x16x32_bf16 a[60:63], a[124:127], v[148:151], a[60:63]\n"
"ds_read_b64_tr_b16 a[118:119], v173 offset:13312\n"
"v_max3_f32 v189, v68, v69, v70\n"
"v_exp_f32 v126, v126\n"
"v_mfma_f32_16x16x32_bf16 v[164:167], v[176:179], v[148:151], v[164:167]\n"
"ds_read_b64_tr_b16 a[120:121], v172 offset:9728\n"
"v_max3_f32 v188, v188, v67, v72\n"
"v_exp_f32 v127, v127\n"
"v_mfma_f32_16x16x32_bf16 a[0:3], a[96:99], v[152:155], a[0:3]\n"
"s_waitcnt lgkmcnt(4)\n"
"ds_read_b64_tr_b16 a[122:123], v172 offset:13824\n"
"v_cvt_pk_bf16_f32 v158, v124, v125\n"
"v_mfma_f32_16x16x32_bf16 a[8:11], a[100:103], v[152:155], a[8:11]\n"
"ds_read_b64_tr_b16 a[124:125], v173 offset:9728\n"
"v_cvt_pk_bf16_f32 v159, v126, v127\n"
"v_mfma_f32_16x16x32_bf16 a[16:19], a[104:107], v[152:155], a[16:19]\n"
"ds_read_b64_tr_b16 a[126:127], v173 offset:13824\n"
"v_max3_f32 v189, v189, v71, v76\n"
"v_mfma_f32_16x16x32_bf16 a[24:27], a[108:111], v[152:155], a[24:27]\n"
"v_max3_f32 v188, v188, v73, v74\n"
"v_max3_f32 v189, v189, v77, v78\n"
"v_mfma_f32_16x16x32_bf16 a[4:7], a[96:99], v[156:159], a[4:7]\n"
"ds_read_b128 a[80:83], v169 offset:0\n"
"v_max3_f32 v188, v188, v75, v80\n"
"v_mfma_f32_16x16x32_bf16 a[12:15], a[100:103], v[156:159], a[12:15]\n"
"ds_read_b128 a[84:87], v169 offset:4096\n"
"v_max3_f32 v189, v189, v79, v84\n"
"v_mfma_f32_16x16x32_bf16 a[20:23], a[104:107], v[156:159], a[20:23]\n"
"ds_read_b128 a[88:91], v169 offset:8192\n"
"v_max3_f32 v188, v188, v81, v82\n"
"v_mfma_f32_16x16x32_bf16 a[28:31], a[108:111], v[156:159], a[28:31]\n"
"ds_read_b128 a[92:95], v169 offset:12288\n"
"v_max3_f32 v189, v189, v85, v86\n"
"v_mfma_f32_16x16x32_bf16 v[160:163], v[176:179], v[152:155], v[160:163]\n"
"s_waitcnt lgkmcnt(4)\n"
"ds_read_b64_tr_b16 a[96:97], v172 offset:16384\n"
"v_max3_f32 v188, v188, v83, v88\n"
"v_mfma_f32_16x16x32_bf16 a[32:35], a[112:115], v[152:155], a[32:35]\n"
"ds_read_b64_tr_b16 a[98:99], v172 offset:20480\n"
"v_max3_f32 v189, v189, v87, v92\n"
"v_mfma_f32_16x16x32_bf16 a[40:43], a[116:119], v[152:155], a[40:43]\n"
"ds_read_b64_tr_b16 a[100:101], v173 offset:16384\n"
"v_max3_f32 v188, v188, v89, v90\n"
"v_mfma_f32_16x16x32_bf16 a[48:51], a[120:123], v[152:155], a[48:51]\n"
"ds_read_b64_tr_b16 a[102:103], v173 offset:20480\n"
"v_max3_f32 v189, v189, v93, v94\n"
"v_mfma_f32_16x16x32_bf16 a[56:59], a[124:127], v[152:155], a[56:59]\n"
"ds_read_b64_tr_b16 a[104:105], v172 offset:16896\n"
"v_max3_f32 v190, v188, v91, v95\n"
"v_mfma_f32_16x16x32_bf16 a[36:39], a[112:115], v[156:159], a[36:39]\n"
"ds_read_b64_tr_b16 a[106:107], v172 offset:20992\n"
"v_max_f32 v190, v190, v189\n"
"v_mfma_f32_16x16x32_bf16 a[44:47], a[116:119], v[156:159], a[44:47]\n"
"ds_read_b64_tr_b16 a[108:109], v173 offset:16896\n"
"v_cmp_lt_f32 vcc, s59, v190\n"
"v_mfma_f32_16x16x32_bf16 a[52:55], a[120:123], v[156:159], a[52:55]\n"
"ds_read_b64_tr_b16 a[110:111], v173 offset:20992\n"
"s_add_u32 m0, s60, 49152\n"
"v_mfma_f32_16x16x32_bf16 a[60:63], a[124:127], v[156:159], a[60:63]\n"
"buffer_load_dwordx4 v2, s[32:35], s67 offen offset:0 nt lds\n"
"v_mul_f32 v236, s58, v236\n"
"v_mfma_f32_16x16x32_bf16 v[164:167], v[176:179], v[156:159], v[164:167]\n"
"v_cvt_pk_bf16_f32 v209, v232, v236\n"
"v_mul_f32 v240, s58, v240\n"
"buffer_load_dwordx4 v3, s[32:35], s67 offen offset:1024 nt lds\n"
"buffer_load_dwordx4 v4, s[32:35], s67 offen offset:2048 nt lds\n"
"buffer_load_dwordx4 v5, s[32:35], s67 offen offset:3072 nt lds\n"
"s_cbranch_vccz .LAskip2\n"
"s_swappc_b64 s[70:71], s[72:73]\n"
".LAskip2:\n"
"v_mfma_f32_16x16x32_bf16 v[96:99], a[64:67], v[32:35], v[128:131]\n"
"v_exp_f32 v64, v64\n"
"v_mfma_f32_16x16x32_bf16 v[104:107], a[68:71], v[32:35], v[128:131]\n"
"v_exp_f32 v65, v65\n"
"v_mfma_f32_16x16x32_bf16 v[112:115], a[72:75], v[32:35], v[128:131]\n"
"v_exp_f32 v66, v66\n"
"v_mfma_f32_16x16x32_bf16 v[120:123], a[76:79], v[32:35], v[128:131]\n"
"v_exp_f32 v67, v67\n"
"v_mfma_f32_16x16x32_bf16 v[100:103], a[64:67], v[48:51], v[132:135]\n"
"ds_read_b128 a[64:67], v170 offset:0\n"
"v_cvt_pk_bf16_f32 v144, v64, v65\n"
"v_mfma_f32_16x16x32_bf16 v[108:111], a[68:71], v[48:51], v[132:135]\n"
"ds_read_b128 a[68:71], v170 offset:4096\n"
"v_mul_f32 v244, s58, v244\n"
"v_exp_f32 v72, v72\n"
"v_mfma_f32_16x16x32_bf16 v[116:119], a[72:75], v[48:51], v[132:135]\n"
"ds_read_b128 a[72:75], v170 offset:8192\n"
"v_cvt_pk_bf16_f32 v210, v240, v244\n"
"v_mfma_f32_16x16x32_bf16 v[124:127], a[76:79], v[48:51], v[132:135]\n"
"s_waitcnt lgkmcnt(3)\n"
"ds_read_b128 a[76:79], v170 offset:12288\n"
"v_mul_f32 v248, s58, v248\n"
"v_exp_f32 v73, v73\n"
"v_mfma_f32_16x16x32_bf16 v[96:99], a[80:83], v[36:39], v[96:99]\n"
"s_add_u32 m0, s60, 147456\n"
"v_cvt_pk_bf16_f32 v145, v66, v67\n"
"v_exp_f32 v74, v74\n"
"v_mfma_f32_16x16x32_bf16 v[104:107], a[84:87], v[36:39], v[104:107]\n"
"buffer_load_dwordx4 v2, s[28:31], s69 offen offset:0 nt lds\n"
"v_mul_f32 v252, s58, v252\n"
"v_mfma_f32_16x16x32_bf16 v[112:115], a[88:91], v[36:39], v[112:115]\n"
"v_exp_f32 v75, v75\n"
"v_mfma_f32_16x16x32_bf16 v[120:123], a[92:95], v[36:39], v[120:123]\n"
"v_cvt_pk_bf16_f32 v146, v72, v73\n"
"v_cvt_pk_bf16_f32 v211, v248, v252\n"
"v_exp_f32 v68, v68\n"
"v_mfma_f32_16x16x32_bf16 v[100:103], a[80:83], v[52:55], v[100:103]\n"
"ds_read_b128 a[80:83], v171 offset:0\n"
"v_mul_f32 v225, s58, v225\n"
"v_mfma_f32_16x16x32_bf16 v[108:111], a[84:87], v[52:55], v[108:111]\n"
"ds_read_b128 a[84:87], v171 offset:4096\n"
"buffer_load_dwordx4 v3, s[28:31], s69 offen offset:1024 nt lds\n"
"v_exp_f32 v69, v69\n"
"v_mfma_f32_16x16x32_bf16 v[116:119], a[88:91], v[52:55], v[116:119]\n"
"ds_read_b128 a[88:91], v171 offset:8192\n"
"v_cvt_pk_bf16_f32 v147, v74, v75\n"
"v_exp_f32 v70, v70\n"
"v_mfma_f32_16x16x32_bf16 v[124:127], a[92:95], v[52:55], v[124:127]\n"
"s_waitcnt lgkmcnt(3)\n"
"ds_read_b128 a[92:95], v171 offset:12288\n"
"v_mul_f32 v229, s58, v229\n"
"v_mfma_f32_16x16x32_bf16 v[96:99], a[64:67], v[40:43], v[96:99]\n"
"ds_read_b64_tr_b16 a[112:113], v172 offset:17408\n"
"v_add_u32 v168, 0x10000, v168\n"
"v_exp_f32 v71, v71\n"
"v_mfma_f32_16x16x32_bf16 v[104:107], a[68:71], v[40:43], v[104:107]\n"
"ds_read_b64_tr_b16 a[114:115], v172 offset:21504\n"
"buffer_load_dwordx4 v4, s[28:31], s69 offen offset:2048 nt lds\n"
"v_cvt_pk_bf16_f32 v148, v68, v69\n"
"v_mfma_f32_16x16x32_bf16 v[112:115], a[72:75], v[40:43], v[112:115]\n"
"ds_read_b64_tr_b16 a[116:117], v173 offset:17408\n"
"v_add_u32 v169, 0x10000, v169\n"
"v_exp_f32 v76, v76\n"
"v_mfma_f32_16x16x32_bf16 v[120:123], a[76:79], v[40:43], v[120:123]\n"
"ds_read_b64_tr_b16 a[118:119], v173 offset:21504\n"
"v_add_u32 v170, 0x10000, v170\n"
"v_exp_f32 v77, v77\n"
"v_mfma_f32_16x16x32_bf16 v[100:103], a[64:67], v[56:59], v[100:103]\n"
"ds_read_b64_tr_b16 a[120:121], v172 offset:17920\n"
"v_add_u32 v171, 0x10000, v171\n"
"v_cvt_pk_bf16_f32 v149, v70, v71\n"
"v_mfma_f32_16x16x32_bf16 v[108:111], a[68:71], v[56:59], v[108:111]\n"
"ds_read_b64_tr_b16 a[122:123], v172 offset:22016\n"
"buffer_load_dwordx4 v5, s[28:31], s69 offen offset:3072 nt lds\n"
"v_exp_f32 v78, v78\n"
"v_mfma_f32_16x16x32_bf16 v[116:119], a[72:75], v[56:59], v[116:119]\n"
"ds_read_b64_tr_b16 a[124:125], v173 offset:17920\n"
"v_cvt_pk_bf16_f32 v212, v225, v229\n"
"v_exp_f32 v79, v79\n"
"v_mfma_f32_16x16x32_bf16 v[124:127], a[76:79], v[56:59], v[124:127]\n"
"s_waitcnt lgkmcnt(3)\n"
"ds_read_b64_tr_b16 a[126:127], v173 offset:22016\n"
"v_cvt_pk_bf16_f32 v150, v76, v77\n"
"v_exp_f32 v80, v80\n"
"v_mfma_f32_16x16x32_bf16 v[96:99], a[80:83], v[44:47], v[96:99]\n"
"v_exp_f32 v81, v81\n"
"v_mfma_f32_16x16x32_bf16 v[104:107], a[84:87], v[44:47], v[104:107]\n"
"v_cvt_pk_bf16_f32 v151, v78, v79\n"
"v_mul_f32 v233, s58, v233\n"
"v_mfma_f32_16x16x32_bf16 v[112:115], a[88:91], v[44:47], v[112:115]\n"
"v_exp_f32 v82, v82\n"
"v_mfma_f32_16x16x32_bf16 v[120:123], a[92:95], v[44:47], v[120:123]\n"
"v_exp_f32 v83, v83\n"
"v_mfma_f32_16x16x32_bf16 v[100:103], a[80:83], v[60:63], v[100:103]\n"
"v_cvt_pk_bf16_f32 v152, v80, v81\n"
"v_mul_f32 v237, s58, v237\n"
"v_mfma_f32_16x16x32_bf16 v[108:111], a[84:87], v[60:63], v[108:111]\n"
"v_exp_f32 v88, v88\n"
"v_mfma_f32_16x16x32_bf16 v[116:119], a[88:91], v[60:63], v[116:119]\n"
"v_exp_f32 v89, v89\n"
"v_mfma_f32_16x16x32_bf16 v[124:127], a[92:95], v[60:63], v[124:127]\n"
"v_cvt_pk_bf16_f32 v153, v82, v83\n"
"v_cvt_pk_bf16_f32 v213, v233, v237\n"
"s_waitcnt vmcnt(22)\n"
"s_barrier\n"
"s_sleep 2\n"
".LAbar3:\n"
"v_mfma_f32_16x16x32_bf16 a[0:3], a[96:99], v[144:147], a[0:3]\n"
"ds_read_b128 a[64:67], v168 offset:32768\n"
"s_add_u32 m0, s60, 0\n"
"v_exp_f32 v90, v90\n"
"v_mfma_f32_16x16x32_bf16 a[8:11], a[100:103], v[144:147], a[8:11]\n"
"ds_read_b128 a[68:71], v168 offset:36864\n"
"buffer_load_dwordx4 v2, s[32:35], s68 offen offset:0 nt lds\n"
"v_mfma_f32_16x16x32_bf16 a[16:19], a[104:107], v[144:147], a[16:19]\n"
"ds_read_b128 a[72:75], v168 offset:40960\n"
"v_mul_f32 v241, s58, v241\n"
"v_exp_f32 v91, v91\n"
"v_mfma_f32_16x16x32_bf16 a[24:27], a[108:111], v[144:147], a[24:27]\n"
"ds_read_b128 a[76:79], v168 offset:45056\n"
"v_cvt_pk_bf16_f32 v154, v88, v89\n"
"v_mfma_f32_16x16x32_bf16 a[4:7], a[96:99], v[148:151], a[4:7]\n"
"ds_read_b64_tr_b16 a[96:97], v172 offset:24576\n"
"v_mul_f32 v245, s58, v245\n"
"v_exp_f32 v84, v84\n"
"v_mfma_f32_16x16x32_bf16 a[12:15], a[100:103], v[148:151], a[12:15]\n"
"ds_read_b64_tr_b16 a[98:99], v172 offset:28672\n"
"buffer_load_dwordx4 v3, s[32:35], s68 offen offset:1024 nt lds\n"
"v_mfma_f32_16x16x32_bf16 a[20:23], a[104:107], v[148:151], a[20:23]\n"
"ds_read_b64_tr_b16 a[100:101], v173 offset:24576\n"
"v_cvt_pk_bf16_f32 v214, v241, v245\n"
"v_exp_f32 v85, v85\n"
"v_mfma_f32_16x16x32_bf16 a[28:31], a[108:111], v[148:151], a[28:31]\n"
"ds_read_b64_tr_b16 a[102:103], v173 offset:28672\n"
"v_cvt_pk_bf16_f32 v155, v90, v91\n"
"v_mfma_f32_16x16x32_bf16 v[160:163], v[176:179], v[144:147], v[160:163]\n"
"ds_read_b64_tr_b16 a[104:105], v172 offset:25088\n"
"v_mul_f32 v249, s58, v249\n"
"v_exp_f32 v86, v86\n"
"v_mfma_f32_16x16x32_bf16 a[32:35], a[112:115], v[144:147], a[32:35]\n"
"ds_read_b64_tr_b16 a[106:107], v172 offset:29184\n"
"buffer_load_dwordx4 v4, s[32:35], s68 offen offset:2048 nt lds\n"
"v_mfma_f32_16x16x32_bf16 a[40:43], a[116:119], v[144:147], a[40:43]\n"
"s_waitcnt lgkmcnt(4)\n"
"ds_read_b64_tr_b16 a[108:109], v173 offset:25088\n"
"v_mul_f32 v253, s58, v253\n"
"v_exp_f32 v87, v87\n"
"v_mfma_f32_16x16x32_bf16 a[48:51], a[120:123], v[144:147], a[48:51]\n"
"ds_read_b64_tr_b16 a[110:111], v173 offset:29184\n"
"v_cvt_pk_bf16_f32 v156, v84, v85\n"
"v_mfma_f32_16x16x32_bf16 a[56:59], a[124:127], v[144:147], a[56:59]\n"
"v_exp_f32 v92, v92\n"
"v_mfma_f32_16x16x32_bf16 a[36:39], a[112:115], v[148:151], a[36:39]\n"
"ds_read_b64_tr_b16 a[112:113], v172 offset:25600\n"
"buffer_load_dwordx4 v5, s[32:35], s68 offen offset:3072 nt lds\n"
"v_exp_f32 v93, v93\n"
"v_mfma_f32_16x16x32_bf16 a[44:47], a[116:119], v[148:151], a[44:47]\n"
"ds_read_b64_tr_b16 a[114:115], v172 offset:29696\n"
"v_cvt_pk_bf16_f32 v157, v86, v87\n"
"v_mfma_f32_16x16x32_bf16 a[52:55], a[120:123], v[148:151], a[52:55]\n"
"ds_read_b64_tr_b16 a[116:117], v173 offset:25600\n"
"v_max3_f32 v188, v96, v97, v98\n"
"v_mfma_f32_16x16x32_bf16 a[60:63], a[124:127], v[148:151], a[60:63]\n"
"ds_read_b64_tr_b16 a[118:119], v173 offset:29696\n"
"v_max3_f32 v189, v100, v101, v102\n"
"v_exp_f32 v94, v94\n"
"v_mfma_f32_16x16x32_bf16 v[164:167], v[176:179], v[148:151], v[164:167]\n"
"ds_read_b64_tr_b16 a[120:121], v172 offset:26112\n"
"v_max3_f32 v188, v188, v99, v104\n"
"v_exp_f32 v95, v95\n"
"v_mfma_f32_16x16x32_bf16 a[0:3], a[96:99], v[152:155], a[0:3]\n"
"s_waitcnt lgkmcnt(4)\n"
"ds_read_b64_tr_b16 a[122:123], v172 offset:30208\n"
"v_cvt_pk_bf16_f32 v158, v92, v93\n"
"v_mfma_f32_16x16x32_bf16 a[8:11], a[100:103], v[152:155], a[8:11]\n"
"ds_read_b64_tr_b16 a[124:125], v173 offset:26112\n"
"v_cvt_pk_bf16_f32 v159, v94, v95\n"
"v_mfma_f32_16x16x32_bf16 a[16:19], a[104:107], v[152:155], a[16:19]\n"
"ds_read_b64_tr_b16 a[126:127], v173 offset:30208\n"
"v_add_u32 v172, 0xffff0000, v172\n"
"v_mfma_f32_16x16x32_bf16 a[24:27], a[108:111], v[152:155], a[24:27]\n"
"v_add_u32 v173, 0xffff0000, v173\n"
"v_max3_f32 v189, v189, v103, v108\n"
"v_mfma_f32_16x16x32_bf16 a[4:7], a[96:99], v[156:159], a[4:7]\n"
"ds_read_b128 a[80:83], v169 offset:32768\n"
"v_max3_f32 v188, v188, v105, v106\n"
"v_mfma_f32_16x16x32_bf16 a[12:15], a[100:103], v[156:159], a[12:15]\n"
"ds_read_b128 a[84:87], v169 offset:36864\n"
"v_max3_f32 v189, v189, v109, v110\n"
"v_mfma_f32_16x16x32_bf16 a[20:23], a[104:107], v[156:159], a[20:23]\n"
"ds_read_b128 a[88:91], v169 offset:40960\n"
"v_max3_f32 v188, v188, v107, v112\n"
"v_mfma_f32_16x16x32_bf16 a[28:31], a[108:111], v[156:159], a[28:31]\n"
"ds_read_b128 a[92:95], v169 offset:45056\n"
"v_max3_f32 v189, v189, v111, v116\n"
"v_mfma_f32_16x16x32_bf16 v[160:163], v[176:179], v[152:155], v[160:163]\n"
"s_waitcnt lgkmcnt(4)\n"
"ds_read_b64_tr_b16 a[96:97], v172 offset:16384\n"
"v_max3_f32 v188, v188, v113, v114\n"
"v_mfma_f32_16x16x32_bf16 a[32:35], a[112:115], v[152:155], a[32:35]\n"
"ds_read_b64_tr_b16 a[98:99], v172 offset:20480\n"
"v_max3_f32 v189, v189, v117, v118\n"
"v_mfma_f32_16x16x32_bf16 a[40:43], a[116:119], v[152:155], a[40:43]\n"
"ds_read_b64_tr_b16 a[100:101], v173 offset:16384\n"
"v_max3_f32 v188, v188, v115, v120\n"
"v_mfma_f32_16x16x32_bf16 a[48:51], a[120:123], v[152:155], a[48:51]\n"
"ds_read_b64_tr_b16 a[102:103], v173 offset:20480\n"
"v_max3_f32 v189, v189, v119, v124\n"
"v_mfma_f32_16x16x32_bf16 a[56:59], a[124:127], v[152:155], a[56:59]\n"
"ds_read_b64_tr_b16 a[104:105], v172 offset:16896\n"
"v_max3_f32 v188, v188, v121, v122\n"
"v_mfma_f32_16x16x32_bf16 a[36:39], a[112:115], v[156:159], a[36:39]\n"
"ds_read_b64_tr_b16 a[106:107], v172 offset:20992\n"
"v_max3_f32 v189, v189, v125, v126\n"
"v_mfma_f32_16x16x32_bf16 a[44:47], a[116:119], v[156:159], a[44:47]\n"
"ds_read_b64_tr_b16 a[108:109], v173 offset:16896\n"
"v_max3_f32 v190, v188, v123, v127\n"
"v_mfma_f32_16x16x32_bf16 a[52:55], a[120:123], v[156:159], a[52:55]\n"
"ds_read_b64_tr_b16 a[110:111], v173 offset:20992\n"
"v_max_f32 v190, v190, v189\n"
"v_mfma_f32_16x16x32_bf16 a[60:63], a[124:127], v[156:159], a[60:63]\n"
"v_cmp_lt_f32 vcc, s59, v190\n"
"v_cvt_pk_bf16_f32 v215, v249, v253\n"
"v_mfma_f32_16x16x32_bf16 v[164:167], v[176:179], v[156:159], v[164:167]\n"
"v_mul_f32 v226, s58, v226\n"
"v_mul_f32 v230, s58, v230\n"
"s_cbranch_vccz .LAskip3\n"
"s_swappc_b64 s[70:71], s[74:75]\n"
".LAskip3:\n"
"v_mfma_f32_16x16x32_bf16 v[64:67], a[64:67], v[32:35], v[128:131]\n"
"v_exp_f32 v96, v96\n"
"v_mfma_f32_16x16x32_bf16 v[72:75], a[68:71], v[32:35], v[128:131]\n"
"v_exp_f32 v97, v97\n"
"v_mfma_f32_16x16x32_bf16 v[80:83], a[72:75], v[32:35], v[128:131]\n"
"v_exp_f32 v98, v98\n"
"v_mfma_f32_16x16x32_bf16 v[88:91], a[76:79], v[32:35], v[128:131]\n"
"v_exp_f32 v99, v99\n"
"v_mfma_f32_16x16x32_bf16 v[68:71], a[64:67], v[48:51], v[132:135]\n"
"ds_read_b128 a[64:67], v170 offset:32768\n"
"v_cvt_pk_bf16_f32 v144, v96, v97\n"
"v_mfma_f32_16x16x32_bf16 v[76:79], a[68:71], v[48:51], v[132:135]\n"
"ds_read_b128 a[68:71], v170 offset:36864\n"
"v_cvt_pk_bf16_f32 v216, v226, v230\n"
"v_exp_f32 v104, v104\n"
"v_mfma_f32_16x16x32_bf16 v[84:87], a[72:75], v[48:51], v[132:135]\n"
"ds_read_b128 a[72:75], v170 offset:40960\n"
"v_mul_f32 v234, s58, v234\n"
"v_mfma_f32_16x16x32_bf16 v[92:95], a[76:79], v[48:51], v[132:135]\n"
"s_waitcnt lgkmcnt(3)\n"
"ds_read_b128 a[76:79], v170 offset:45056\n"
"v_mul_f32 v238, s58, v238\n"
"v_exp_f32 v105, v105\n"
"v_mfma_f32_16x16x32_bf16 v[64:67], a[80:83], v[36:39], v[64:67]\n"
"s_add_u32 m0, s60, 65536\n"
"v_cvt_pk_bf16_f32 v145, v98, v99\n"
"v_exp_f32 v106, v106\n"
"v_mfma_f32_16x16x32_bf16 v[72:75], a[84:87], v[36:39], v[72:75]\n"
"buffer_load_dwordx4 v2, s[4:7], s62 offen offset:0 nt lds\n"
"v_cvt_pk_bf16_f32 v217, v234, v238\n"
"v_mfma_f32_16x16x32_bf16 v[80:83], a[88:91], v[36:39], v[80:83]\n"
"v_exp_f32 v107, v107\n"
"v_mfma_f32_16x16x32_bf16 v[88:91], a[92:95], v[36:39], v[88:91]\n"
"v_cvt_pk_bf16_f32 v146, v104, v105\n"
"v_mul_f32 v242, s58, v242\n"
"v_exp_f32 v100, v100\n"
"v_mfma_f32_16x16x32_bf16 v[68:71], a[80:83], v[52:55], v[68:71]\n"
"ds_read_b128 a[80:83], v171 offset:32768\n"
"v_mul_f32 v246, s58, v246\n"
"v_mfma_f32_16x16x32_bf16 v[76:79], a[84:87], v[52:55], v[76:79]\n"
"ds_read_b128 a[84:87], v171 offset:36864\n"
"buffer_load_dwordx4 v3, s[4:7], s62 offen offset:1024 nt lds\n"
"v_exp_f32 v101, v101\n"
"v_mfma_f32_16x16x32_bf16 v[84:87], a[88:91], v[52:55], v[84:87]\n"
"ds_read_b128 a[88:91], v171 offset:40960\n"
"v_cvt_pk_bf16_f32 v147, v106, v107\n"
"v_exp_f32 v102, v102\n"
"v_mfma_f32_16x16x32_bf16 v[92:95], a[92:95], v[52:55], v[92:95]\n"
"s_waitcnt lgkmcnt(3)\n"
"ds_read_b128 a[92:95], v171 offset:45056\n"
"v_cvt_pk_bf16_f32 v218, v242, v246\n"
"v_mfma_f32_16x16x32_bf16 v[64:67], a[64:67], v[40:43], v[64:67]\n"
"ds_read_b64_tr_b16 a[112:113], v172 offset:17408\n"
"v_mul_f32 v250, s58, v250\n"
"v_exp_f32 v103, v103\n"
"v_mfma_f32_16x16x32_bf16 v[72:75], a[68:71], v[40:43], v[72:75]\n"
"ds_read_b64_tr_b16 a[114:115], v172 offset:21504\n"
"buffer_load_dwordx4 v4, s[4:7], s62 offen offset:2048 nt lds\n"
"v_cvt_pk_bf16_f32 v148, v100, v101\n"
"v_mfma_f32_16x16x32_bf16 v[80:83], a[72:75], v[40:43], v[80:83]\n"
"ds_read_b64_tr_b16 a[116:117], v173 offset:17408\n"
"v_mul_f32 v254, s58, v254\n"
"v_exp_f32 v108, v108\n"
"v_mfma_f32_16x16x32_bf16 v[88:91], a[76:79], v[40:43], v[88:91]\n"
"ds_read_b64_tr_b16 a[118:119], v173 offset:21504\n"
"v_cvt_pk_bf16_f32 v219, v250, v254\n"
"v_exp_f32 v109, v109\n"
"v_mfma_f32_16x16x32_bf16 v[68:71], a[64:67], v[56:59], v[68:71]\n"
"ds_read_b64_tr_b16 a[120:121], v172 offset:17920\n"
"v_cvt_pk_bf16_f32 v149, v102, v103\n"
"v_exp_f32 v110, v110\n"
"v_mfma_f32_16x16x32_bf16 v[76:79], a[68:71], v[56:59], v[76:79]\n"
"ds_read_b64_tr_b16 a[122:123], v172 offset:22016\n"
"buffer_load_dwordx4 v5, s[4:7], s62 offen offset:3072 nt lds\n"
"v_exp_f32 v111, v111\n"
"v_mfma_f32_16x16x32_bf16 v[84:87], a[72:75], v[56:59], v[84:87]\n"
"ds_read_b64_tr_b16 a[124:125], v173 offset:17920\n"
"v_cvt_pk_bf16_f32 v150, v108, v109\n"
"v_mfma_f32_16x16x32_bf16 v[92:95], a[76:79], v[56:59], v[92:95]\n"
"s_waitcnt lgkmcnt(3)\n"
"ds_read_b64_tr_b16 a[126:127], v173 offset:22016\n"
"v_mul_f32 v227, s58, v227\n"
"v_exp_f32 v112, v112\n"
"v_mfma_f32_16x16x32_bf16 v[64:67], a[80:83], v[44:47], v[64:67]\n"
"v_exp_f32 v113, v113\n"
"v_mfma_f32_16x16x32_bf16 v[72:75], a[84:87], v[44:47], v[72:75]\n"
"v_cvt_pk_bf16_f32 v151, v110, v111\n"
"v_mul_f32 v231, s58, v231\n"
"v_mfma_f32_16x16x32_bf16 v[80:83], a[88:91], v[44:47], v[80:83]\n"
"v_exp_f32 v114, v114\n"
"v_mfma_f32_16x16x32_bf16 v[88:91], a[92:95], v[44:47], v[88:91]\n"
"v_exp_f32 v115, v115\n"
"v_mfma_f32_16x16x32_bf16 v[68:71], a[80:83], v[60:63], v[68:71]\n"
"v_cvt_pk_bf16_f32 v152, v112, v113\n"
"v_cvt_pk_bf16_f32 v220, v227, v231\n"
"v_mfma_f32_16x16x32_bf16 v[76:79], a[84:87], v[60:63], v[76:79]\n"
"v_exp_f32 v120, v120\n"
"v_mfma_f32_16x16x32_bf16 v[84:87], a[88:91], v[60:63], v[84:87]\n"
"v_exp_f32 v121, v121\n"
"v_mfma_f32_16x16x32_bf16 v[92:95], a[92:95], v[60:63], v[92:95]\n"
"v_cvt_pk_bf16_f32 v153, v114, v115\n"
"v_mul_f32 v235, s58, v235\n"
"s_waitcnt vmcnt(20)\n"
"s_barrier\n"
"s_sleep 2\n"
".LAbar4:\n"
"v_mfma_f32_16x16x32_bf16 a[0:3], a[96:99], v[144:147], a[0:3]\n"
"ds_read_b128 a[64:67], v168 offset:49152\n"
"s_add_u32 m0, s60, 81920\n"
"v_exp_f32 v122, v122\n"
"v_mfma_f32_16x16x32_bf16 a[8:11], a[100:103], v[144:147], a[8:11]\n"
"ds_read_b128 a[68:71], v168 offset:53248\n"
"buffer_load_dwordx4 v2, s[32:35], s69 offen offset:0 nt lds\n"
"v_mfma_f32_16x16x32_bf16 a[16:19], a[104:107], v[144:147], a[16:19]\n"
"ds_read_b128 a[72:75], v168 offset:57344\n"
"v_mul_f32 v239, s58, v239\n"
"v_exp_f32 v123, v123\n"
"v_mfma_f32_16x16x32_bf16 a[24:27], a[108:111], v[144:147], a[24:27]\n"
"ds_read_b128 a[76:79], v168 offset:61440\n"
"v_cvt_pk_bf16_f32 v154, v120, v121\n"
"v_mfma_f32_16x16x32_bf16 a[4:7], a[96:99], v[148:151], a[4:7]\n"
"ds_read_b64_tr_b16 a[96:97], v172 offset:24576\n"
"v_cvt_pk_bf16_f32 v221, v235, v239\n"
"v_exp_f32 v116, v116\n"
"v_mfma_f32_16x16x32_bf16 a[12:15], a[100:103], v[148:151], a[12:15]\n"
"ds_read_b64_tr_b16 a[98:99], v172 offset:28672\n"
"buffer_load_dwordx4 v3, s[32:35], s69 offen offset:1024 nt lds\n"
"v_mfma_f32_16x16x32_bf16 a[20:23], a[104:107], v[148:151], a[20:23]\n"
"ds_read_b64_tr_b16 a[100:101], v173 offset:24576\n"
"v_mul_f32 v243, s58, v243\n"
"v_exp_f32 v117, v117\n"
"v_mfma_f32_16x16x32_bf16 a[28:31], a[108:111], v[148:151], a[28:31]\n"
"ds_read_b64_tr_b16 a[102:103], v173 offset:28672\n"
"v_cvt_pk_bf16_f32 v155, v122, v123\n"
"v_mfma_f32_16x16x32_bf16 v[160:163], v[176:179], v[144:147], v[160:163]\n"
"ds_read_b64_tr_b16 a[104:105], v172 offset:25088\n"
"v_mul_f32 v247, s58, v247\n"
"v_exp_f32 v118, v118\n"
"v_mfma_f32_16x16x32_bf16 a[32:35], a[112:115], v[144:147], a[32:35]\n"
"ds_read_b64_tr_b16 a[106:107], v172 offset:29184\n"
"buffer_load_dwordx4 v4, s[32:35], s69 offen offset:2048 nt lds\n"
"v_mfma_f32_16x16x32_bf16 a[40:43], a[116:119], v[144:147], a[40:43]\n"
"s_waitcnt lgkmcnt(4)\n"
"ds_read_b64_tr_b16 a[108:109], v173 offset:25088\n"
"v_cvt_pk_bf16_f32 v222, v243, v247\n"
"v_exp_f32 v119, v119\n"
"v_mfma_f32_16x16x32_bf16 a[48:51], a[120:123], v[144:147], a[48:51]\n"
"ds_read_b64_tr_b16 a[110:111], v173 offset:29184\n"
"v_cvt_pk_bf16_f32 v156, v116, v117\n"
"v_mfma_f32_16x16x32_bf16 a[56:59], a[124:127], v[144:147], a[56:59]\n"
"v_exp_f32 v124, v124\n"
"v_mfma_f32_16x16x32_bf16 a[36:39], a[112:115], v[148:151], a[36:39]\n"
"ds_read_b64_tr_b16 a[112:113], v172 offset:25600\n"
"buffer_load_dwordx4 v5, s[32:35], s69 offen offset:3072 nt lds\n"
"v_exp_f32 v125, v125\n"
"v_mfma_f32_16x16x32_bf16 a[44:47], a[116:119], v[148:151], a[44:47]\n"
"ds_read_b64_tr_b16 a[114:115], v172 offset:29696\n"
"v_cvt_pk_bf16_f32 v157, v118, v119\n"
"v_mfma_f32_16x16x32_bf16 a[52:55], a[120:123], v[148:151], a[52:55]\n"
"ds_read_b64_tr_b16 a[116:117], v173 offset:25600\n"
"v_max3_f32 v188, v64, v65, v66\n"
"v_mfma_f32_16x16x32_bf16 a[60:63], a[124:127], v[148:151], a[60:63]\n"
"ds_read_b64_tr_b16 a[118:119], v173 offset:29696\n"
"v_max3_f32 v189, v68, v69, v70\n"
"v_exp_f32 v126, v126\n"
"v_mfma_f32_16x16x32_bf16 v[164:167], v[176:179], v[148:151], v[164:167]\n"
"ds_read_b64_tr_b16 a[120:121], v172 offset:26112\n"
"v_max3_f32 v188, v188, v67, v72\n"
"v_exp_f32 v127, v127\n"
"v_mfma_f32_16x16x32_bf16 a[0:3], a[96:99], v[152:155], a[0:3]\n"
"s_waitcnt lgkmcnt(4)\n"
"ds_read_b64_tr_b16 a[122:123], v172 offset:30208\n"
"v_cvt_pk_bf16_f32 v158, v124, v125\n"
"v_mfma_f32_16x16x32_bf16 a[8:11], a[100:103], v[152:155], a[8:11]\n"
"ds_read_b64_tr_b16 a[124:125], v173 offset:26112\n"
"v_cvt_pk_bf16_f32 v159, v126, v127\n"
"v_mfma_f32_16x16x32_bf16 a[16:19], a[104:107], v[152:155], a[16:19]\n"
"ds_read_b64_tr_b16 a[126:127], v173 offset:30208\n"
"v_add_u32 v172, 0x20000, v172\n"
"v_mfma_f32_16x16x32_bf16 a[24:27], a[108:111], v[152:155], a[24:27]\n"
"v_add_u32 v173, 0x20000, v173\n"
"v_max3_f32 v189, v189, v71, v76\n"
"v_mfma_f32_16x16x32_bf16 a[4:7], a[96:99], v[156:159], a[4:7]\n"
"ds_read_b128 a[80:83], v169 offset:49152\n"
"v_max3_f32 v188, v188, v73, v74\n"
"v_mfma_f32_16x16x32_bf16 a[12:15], a[100:103], v[156:159], a[12:15]\n"
"ds_read_b128 a[84:87], v169 offset:53248\n"
"v_max3_f32 v189, v189, v77, v78\n"
"v_mfma_f32_16x16x32_bf16 a[20:23], a[104:107], v[156:159], a[20:23]\n"
"ds_read_b128 a[88:91], v169 offset:57344\n"
"v_max3_f32 v188, v188, v75, v80\n"
"v_mfma_f32_16x16x32_bf16 a[28:31], a[108:111], v[156:159], a[28:31]\n"
"ds_read_b128 a[92:95], v169 offset:61440\n"
"v_max3_f32 v189, v189, v79, v84\n"
"v_mfma_f32_16x16x32_bf16 v[160:163], v[176:179], v[152:155], v[160:163]\n"
"s_waitcnt lgkmcnt(4)\n"
"ds_read_b64_tr_b16 a[96:97], v172 offset:0\n"
"v_max3_f32 v188, v188, v81, v82\n"
"v_mfma_f32_16x16x32_bf16 a[32:35], a[112:115], v[152:155], a[32:35]\n"
"ds_read_b64_tr_b16 a[98:99], v172 offset:4096\n"
"v_max3_f32 v189, v189, v85, v86\n"
"v_mfma_f32_16x16x32_bf16 a[40:43], a[116:119], v[152:155], a[40:43]\n"
"ds_read_b64_tr_b16 a[100:101], v173 offset:0\n"
"v_max3_f32 v188, v188, v83, v88\n"
"v_mfma_f32_16x16x32_bf16 a[48:51], a[120:123], v[152:155], a[48:51]\n"
"ds_read_b64_tr_b16 a[102:103], v173 offset:4096\n"
"v_max3_f32 v189, v189, v87, v92\n"
"v_mfma_f32_16x16x32_bf16 a[56:59], a[124:127], v[152:155], a[56:59]\n"
"ds_read_b64_tr_b16 a[104:105], v172 offset:512\n"
"v_max3_f32 v188, v188, v89, v90\n"
"v_mfma_f32_16x16x32_bf16 a[36:39], a[112:115], v[156:159], a[36:39]\n"
"ds_read_b64_tr_b16 a[106:107], v172 offset:4608\n"
"v_max3_f32 v189, v189, v93, v94\n"
"v_mfma_f32_16x16x32_bf16 a[44:47], a[116:119], v[156:159], a[44:47]\n"
"ds_read_b64_tr_b16 a[108:109], v173 offset:512\n"
"v_max3_f32 v190, v188, v91, v95\n"
"v_mfma_f32_16x16x32_bf16 a[52:55], a[120:123], v[156:159], a[52:55]\n"
"ds_read_b64_tr_b16 a[110:111], v173 offset:4608\n"
"v_max_f32 v190, v190, v189\n"
"v_mfma_f32_16x16x32_bf16 a[60:63], a[124:127], v[156:159], a[60:63]\n"
"v_cmp_lt_f32 vcc, s59, v190\n"
"v_and_b32 v184, 7, v0\n"
"v_mfma_f32_16x16x32_bf16 v[164:167], v[176:179], v[156:159], v[164:167]\n"
"s_lshr_b32 s80, s53, 1\n"
"s_lshl_b32 s80, s80, 7\n"
"s_and_b32 s81, s53, 1\n"
"s_lshl_b32 s81, s81, 2\n"
"s_add_u32 s81, s81, s48\n"
"s_lshl_b32 s81, s81, 3\n"
"s_add_u32 s80, s80, s81\n"
"s_add_u32 s80, s80, 524352\n"
"v_lshl_add_u32 v185, v184, 9, s80\n"
"v_add_u32 v10, 1, v10\n"
"v_and_b32 v10, 0xffff, v10\n"
"v_add_u32 v11, 1, v11\n"
"v_and_b32 v11, 0xffff, v11\n"
"buffer_store_dword v10, v185, s[40:43], 0 offen offset:0 sc1\n"
"s_cbranch_vccz .LAskip4\n"
"s_swappc_b64 s[70:71], s[72:73]\n"
".LAskip4:\n"
"v_mfma_f32_16x16x32_bf16 v[96:99], a[64:67], v[32:35], v[128:131]\n"
"v_exp_f32 v64, v64\n"
"v_mfma_f32_16x16x32_bf16 v[104:107], a[68:71], v[32:35], v[128:131]\n"
"v_exp_f32 v65, v65\n"
"v_mfma_f32_16x16x32_bf16 v[112:115], a[72:75], v[32:35], v[128:131]\n"
"v_exp_f32 v66, v66\n"
"v_mfma_f32_16x16x32_bf16 v[120:123], a[76:79], v[32:35], v[128:131]\n"
"v_exp_f32 v67, v67\n"
"v_mfma_f32_16x16x32_bf16 v[100:103], a[64:67], v[48:51], v[132:135]\n"
"ds_read_b128 a[64:67], v170 offset:49152\n"
"v_cvt_pk_bf16_f32 v144, v64, v65\n"
"v_mfma_f32_16x16x32_bf16 v[108:111], a[68:71], v[48:51], v[132:135]\n"
"ds_read_b128 a[68:71], v170 offset:53248\n"
"v_mul_f32 v251, s58, v251\n"
"v_exp_f32 v72, v72\n"
"v_mfma_f32_16x16x32_bf16 v[116:119], a[72:75], v[48:51], v[132:135]\n"
"ds_read_b128 a[72:75], v170 offset:57344\n"
"v_mul_f32 v255, s58, v255\n"
"v_mfma_f32_16x16x32_bf16 v[124:127], a[76:79], v[48:51], v[132:135]\n"
"s_waitcnt lgkmcnt(3)\n"
"ds_read_b128 a[76:79], v170 offset:61440\n"
"v_cvt_pk_bf16_f32 v223, v251, v255\n"
"v_exp_f32 v73, v73\n"
"v_mfma_f32_16x16x32_bf16 v[96:99], a[80:83], v[36:39], v[96:99]\n"
"s_add_u32 m0, s60, 98304\n"
"v_cvt_pk_bf16_f32 v145, v66, v67\n"
"v_exp_f32 v74, v74\n"
"v_mfma_f32_16x16x32_bf16 v[104:107], a[84:87], v[36:39], v[104:107]\n"
"buffer_load_dwordx4 v2, s[4:7], s63 offen offset:0 nt lds\n"
"v_mfma_f32_16x16x32_bf16 v[112:115], a[88:91], v[36:39], v[112:115]\n"
"v_exp_f32 v75, v75\n"
"v_mfma_f32_16x16x32_bf16 v[120:123], a[92:95], v[36:39], v[120:123]\n"
"v_cvt_pk_bf16_f32 v146, v72, v73\n"
"v_exp_f32 v68, v68\n"
"v_mfma_f32_16x16x32_bf16 v[100:103], a[80:83], v[52:55], v[100:103]\n"
"ds_read_b128 a[80:83], v171 offset:49152\n"
"v_mfma_f32_16x16x32_bf16 v[108:111], a[84:87], v[52:55], v[108:111]\n"
"ds_read_b128 a[84:87], v171 offset:53248\n"
"buffer_load_dwordx4 v3, s[4:7], s63 offen offset:1024 nt lds\n"
"v_exp_f32 v69, v69\n"
"v_mfma_f32_16x16x32_bf16 v[116:119], a[88:91], v[52:55], v[116:119]\n"
"ds_read_b128 a[88:91], v171 offset:57344\n"
"v_cvt_pk_bf16_f32 v147, v74, v75\n"
"v_exp_f32 v70, v70\n"
"v_mfma_f32_16x16x32_bf16 v[124:127], a[92:95], v[52:55], v[124:127]\n"
"s_waitcnt lgkmcnt(3)\n"
"ds_read_b128 a[92:95], v171 offset:61440\n"
"v_mfma_f32_16x16x32_bf16 v[96:99], a[64:67], v[40:43], v[96:99]\n"
"ds_read_b64_tr_b16 a[112:113], v172 offset:1024\n"
"v_add_u32 v168, 0xffff0000, v168\n"
"v_exp_f32 v71, v71\n"
"v_mfma_f32_16x16x32_bf16 v[104:107], a[68:71], v[40:43], v[104:107]\n"
"ds_read_b64_tr_b16 a[114:115], v172 offset:5120\n"
"buffer_load_dwordx4 v4, s[4:7], s63 offen offset:2048 nt lds\n"
"v_cvt_pk_bf16_f32 v148, v68, v69\n"
"v_mfma_f32_16x16x32_bf16 v[112:115], a[72:75], v[40:43], v[112:115]\n"
"ds_read_b64_tr_b16 a[116:117], v173 offset:1024\n"
"v_add_u32 v169, 0xffff0000, v169\n"
"v_exp_f32 v76, v76\n"
"v_mfma_f32_16x16x32_bf16 v[120:123], a[76:79], v[40:43], v[120:123]\n"
"ds_read_b64_tr_b16 a[118:119], v173 offset:5120\n"
"v_add_u32 v170, 0xffff0000, v170\n"
"v_exp_f32 v77, v77\n"
"v_mfma_f32_16x16x32_bf16 v[100:103], a[64:67], v[56:59], v[100:103]\n"
"ds_read_b64_tr_b16 a[120:121], v172 offset:1536\n"
"v_add_u32 v171, 0xffff0000, v171\n"
"v_cvt_pk_bf16_f32 v149, v70, v71\n"
"v_mfma_f32_16x16x32_bf16 v[108:111], a[68:71], v[56:59], v[108:111]\n"
"ds_read_b64_tr_b16 a[122:123], v172 offset:5632\n"
"buffer_load_dwordx4 v5, s[4:7], s63 offen offset:3072 nt lds\n"
"v_exp_f32 v78, v78\n"
"v_mfma_f32_16x16x32_bf16 v[116:119], a[72:75], v[56:59], v[116:119]\n"
"ds_read_b64_tr_b16 a[124:125], v173 offset:1536\n"
"v_exp_f32 v79, v79\n"
"v_mfma_f32_16x16x32_bf16 v[124:127], a[76:79], v[56:59], v[124:127]\n"
"s_waitcnt lgkmcnt(3)\n"
"ds_read_b64_tr_b16 a[126:127], v173 offset:5632\n"
"v_cvt_pk_bf16_f32 v150, v76, v77\n"
"v_exp_f32 v80, v80\n"
"v_mfma_f32_16x16x32_bf16 v[96:99], a[80:83], v[44:47], v[96:99]\n"
"v_exp_f32 v81, v81\n"
"v_mfma_f32_16x16x32_bf16 v[104:107], a[84:87], v[44:47], v[104:107]\n"
"v_cvt_pk_bf16_f32 v151, v78, v79\n"
"v_mfma_f32_16x16x32_bf16 v[112:115], a[88:91], v[44:47], v[112:115]\n"
"v_exp_f32 v82, v82\n"
"v_mfma_f32_16x16x32_bf16 v[120:123], a[92:95], v[44:47], v[120:123]\n"
"v_exp_f32 v83, v83\n"
"v_mfma_f32_16x16x32_bf16 v[100:103], a[80:83], v[60:63], v[100:103]\n"
"v_cvt_pk_bf16_f32 v152, v80, v81\n"
"v_mfma_f32_16x16x32_bf16 v[108:111], a[84:87], v[60:63], v[108:111]\n"
"v_exp_f32 v88, v88\n"
"v_mfma_f32_16x16x32_bf16 v[116:119], a[88:91], v[60:63], v[116:119]\n"
"v_exp_f32 v89, v89\n"
"v_mfma_f32_16x16x32_bf16 v[124:127], a[92:95], v[60:63], v[124:127]\n"
"v_cvt_pk_bf16_f32 v153, v82, v83\n"
"s_waitcnt vmcnt(21)\n"
"s_barrier\n"
"s_sleep 2\n"
".LAbar5:\n"
"v_mfma_f32_16x16x32_bf16 a[0:3], a[96:99], v[144:147], a[0:3]\n"
"ds_read_b128 a[64:67], v168 offset:32768\n"
"s_add_u32 m0, s60, 16384\n"
"v_exp_f32 v90, v90\n"
"v_mfma_f32_16x16x32_bf16 a[8:11], a[100:103], v[144:147], a[8:11]\n"
"ds_read_b128 a[68:71], v168 offset:36864\n"
"buffer_load_dwordx4 v2, s[8:11], s62 offen offset:0 nt lds\n"
"v_mfma_f32_16x16x32_bf16 a[16:19], a[104:107], v[144:147], a[16:19]\n"
"ds_read_b128 a[72:75], v168 offset:40960\n"
"v_exp_f32 v91, v91\n"
"v_mfma_f32_16x16x32_bf16 a[24:27], a[108:111], v[144:147], a[24:27]\n"
"ds_read_b128 a[76:79], v168 offset:45056\n"
"v_cvt_pk_bf16_f32 v154, v88, v89\n"
"v_mfma_f32_16x16x32_bf16 a[4:7], a[96:99], v[148:151], a[4:7]\n"
"ds_read_b64_tr_b16 a[96:97], v172 offset:8192\n"
"v_exp_f32 v84, v84\n"
"v_mfma_f32_16x16x32_bf16 a[12:15], a[100:103], v[148:151], a[12:15]\n"
"ds_read_b64_tr_b16 a[98:99], v172 offset:12288\n"
"buffer_load_dwordx4 v3, s[8:11], s62 offen offset:1024 nt lds\n"
"v_mfma_f32_16x16x32_bf16 a[20:23], a[104:107], v[148:151], a[20:23]\n"
"ds_read_b64_tr_b16 a[100:101], v173 offset:8192\n"
"v_exp_f32 v85, v85\n"
"v_mfma_f32_16x16x32_bf16 a[28:31], a[108:111], v[148:151], a[28:31]\n"
"ds_read_b64_tr_b16 a[102:103], v173 offset:12288\n"
"v_cvt_pk_bf16_f32 v155, v90, v91\n"
"v_mfma_f32_16x16x32_bf16 v[160:163], v[176:179], v[144:147], v[160:163]\n"
"ds_read_b64_tr_b16 a[104:105], v172 offset:8704\n"
"v_exp_f32 v86, v86\n"
"v_mfma_f32_16x16x32_bf16 a[32:35], a[112:115], v[144:147], a[32:35]\n"
"ds_read_b64_tr_b16 a[106:107], v172 offset:12800\n"
"buffer_load_dwordx4 v4, s[8:11], s62 offen offset:2048 nt lds\n"
"v_mfma_f32_16x16x32_bf16 a[40:43], a[116:119], v[144:147], a[40:43]\n"
"s_waitcnt lgkmcnt(4)\n"
"ds_read_b64_tr_b16 a[108:109], v173 offset:8704\n"
"v_exp_f32 v87, v87\n"
"v_mfma_f32_16x16x32_bf16 a[48:51], a[120:123], v[144:147], a[48:51]\n"
"ds_read_b64_tr_b16 a[110:111], v173 offset:12800\n"
"v_cvt_pk_bf16_f32 v156, v84, v85\n"
"v_mfma_f32_16x16x32_bf16 a[56:59], a[124:127], v[144:147], a[56:59]\n"
"v_exp_f32 v92, v92\n"
"v_mfma_f32_16x16x32_bf16 a[36:39], a[112:115], v[148:151], a[36:39]\n"
"ds_read_b64_tr_b16 a[112:113], v172 offset:9216\n"
"buffer_load_dwordx4 v5, s[8:11], s62 offen offset:3072 nt lds\n"
"v_exp_f32 v93, v93\n"
"v_mfma_f32_16x16x32_bf16 a[44:47], a[116:119], v[148:151], a[44:47]\n"
"ds_read_b64_tr_b16 a[114:115], v172 offset:13312\n"
"v_cvt_pk_bf16_f32 v157, v86, v87\n"
"v_mfma_f32_16x16x32_bf16 a[52:55], a[120:123], v[148:151], a[52:55]\n"
"ds_read_b64_tr_b16 a[116:117], v173 offset:9216\n"
"v_max3_f32 v188, v96, v97, v98\n"
"v_mfma_f32_16x16x32_bf16 a[60:63], a[124:127], v[148:151], a[60:63]\n"
"ds_read_b64_tr_b16 a[118:119], v173 offset:13312\n"
"v_max3_f32 v189, v100, v101, v102\n"
"v_exp_f32 v94, v94\n"
"v_mfma_f32_16x16x32_bf16 v[164:167], v[176:179], v[148:151], v[164:167]\n"
"ds_read_b64_tr_b16 a[120:121], v172 offset:9728\n"
"v_max3_f32 v188, v188, v99, v104\n"
"v_exp_f32 v95, v95\n"
"v_mfma_f32_16x16x32_bf16 a[0:3], a[96:99], v[152:155], a[0:3]\n"
"s_waitcnt lgkmcnt(4)\n"
"ds_read_b64_tr_b16 a[122:123], v172 offset:13824\n"
"v_cvt_pk_bf16_f32 v158, v92, v93\n"
"v_mfma_f32_16x16x32_bf16 a[8:11], a[100:103], v[152:155], a[8:11]\n"
"ds_read_b64_tr_b16 a[124:125], v173 offset:9728\n"
"v_cvt_pk_bf16_f32 v159, v94, v95\n"
"v_mfma_f32_16x16x32_bf16 a[16:19], a[104:107], v[152:155], a[16:19]\n"
"ds_read_b64_tr_b16 a[126:127], v173 offset:13824\n"
"v_add_u32 v172, 0xfffe0000, v172\n"
"v_mfma_f32_16x16x32_bf16 a[24:27], a[108:111], v[152:155], a[24:27]\n"
"v_add_u32 v173, 0xfffe0000, v173\n"
"v_max3_f32 v189, v189, v103, v108\n"
"v_mfma_f32_16x16x32_bf16 a[4:7], a[96:99], v[156:159], a[4:7]\n"
"ds_read_b128 a[80:83], v169 offset:32768\n"
"v_max3_f32 v188, v188, v105, v106\n"
"v_mfma_f32_16x16x32_bf16 a[12:15], a[100:103], v[156:159], a[12:15]\n"
"ds_read_b128 a[84:87], v169 offset:36864\n"
"v_max3_f32 v189, v189, v109, v110\n"
"v_mfma_f32_16x16x32_bf16 a[20:23], a[104:107], v[156:159], a[20:23]\n"
"ds_read_b128 a[88:91], v169 offset:40960\n"
"v_max3_f32 v188, v188, v107, v112\n"
"v_mfma_f32_16x16x32_bf16 a[28:31], a[108:111], v[156:159], a[28:31]\n"
"ds_read_b128 a[92:95], v169 offset:45056\n"
"v_max3_f32 v189, v189, v111, v116\n"
"v_mfma_f32_16x16x32_bf16 v[160:163], v[176:179], v[152:155], v[160:163]\n"
"s_waitcnt lgkmcnt(4)\n"
"ds_read_b64_tr_b16 a[96:97], v172 offset:49152\n"
"v_max3_f32 v188, v188, v113, v114\n"
"v_mfma_f32_16x16x32_bf16 a[32:35], a[112:115], v[152:155], a[32:35]\n"
"ds_read_b64_tr_b16 a[98:99], v172 offset:53248\n"
"v_max3_f32 v189, v189, v117, v118\n"
"v_mfma_f32_16x16x32_bf16 a[40:43], a[116:119], v[152:155], a[40:43]\n"
"ds_read_b64_tr_b16 a[100:101], v173 offset:49152\n"
"v_max3_f32 v188, v188, v115, v120\n"
"v_mfma_f32_16x16x32_bf16 a[48:51], a[120:123], v[152:155], a[48:51]\n"
"ds_read_b64_tr_b16 a[102:103], v173 offset:53248\n"
"v_max3_f32 v189, v189, v119, v124\n"
"v_mfma_f32_16x16x32_bf16 a[56:59], a[124:127], v[152:155], a[56:59]\n"
"ds_read_b64_tr_b16 a[104:105], v172 offset:49664\n"
"v_max3_f32 v188, v188, v121, v122\n"
"v_mfma_f32_16x16x32_bf16 a[36:39], a[112:115], v[156:159], a[36:39]\n"
"ds_read_b64_tr_b16 a[106:107], v172 offset:53760\n"
"v_max3_f32 v189, v189, v125, v126\n"
"v_mfma_f32_16x16x32_bf16 a[44:47], a[116:119], v[156:159], a[44:47]\n"
"ds_read_b64_tr_b16 a[108:109], v173 offset:49664\n"
"v_max3_f32 v190, v188, v123, v127\n"
"v_mfma_f32_16x16x32_bf16 a[52:55], a[120:123], v[156:159], a[52:55]\n"
"ds_read_b64_tr_b16 a[110:111], v173 offset:53760\n"
"v_max_f32 v190, v190, v189\n"
"v_mfma_f32_16x16x32_bf16 a[60:63], a[124:127], v[156:159], a[60:63]\n"
"v_cmp_lt_f32 vcc, s59, v190\n"
"v_mfma_f32_16x16x32_bf16 v[164:167], v[176:179], v[156:159], v[164:167]\n"
"s_nop 0\n"
"s_cbranch_vccz .LAskip5\n"
"s_swappc_b64 s[70:71], s[74:75]\n"
".LAskip5:\n"
"v_mfma_f32_16x16x32_bf16 v[64:67], a[64:67], v[32:35], v[128:131]\n"
"v_exp_f32 v96, v96\n"
"v_mfma_f32_16x16x32_bf16 v[72:75], a[68:71], v[32:35], v[128:131]\n"
"v_exp_f32 v97, v97\n"
"v_mfma_f32_16x16x32_bf16 v[80:83], a[72:75], v[32:35], v[128:131]\n"
"v_exp_f32 v98, v98\n"
"v_mfma_f32_16x16x32_bf16 v[88:91], a[76:79], v[32:35], v[128:131]\n"
"v_exp_f32 v99, v99\n"
"v_mfma_f32_16x16x32_bf16 v[68:71], a[64:67], v[48:51], v[132:135]\n"
"ds_read_b128 a[64:67], v170 offset:32768\n"
"v_cvt_pk_bf16_f32 v144, v96, v97\n"
"v_mfma_f32_16x16x32_bf16 v[76:79], a[68:71], v[48:51], v[132:135]\n"
"ds_read_b128 a[68:71], v170 offset:36864\n"
"v_exp_f32 v104, v104\n"
"v_mfma_f32_16x16x32_bf16 v[84:87], a[72:75], v[48:51], v[132:135]\n"
"ds_read_b128 a[72:75], v170 offset:40960\n"
"v_mfma_f32_16x16x32_bf16 v[92:95], a[76:79], v[48:51], v[132:135]\n"
"s_waitcnt lgkmcnt(3)\n"
"ds_read_b128 a[76:79], v170 offset:45056\n"
"v_exp_f32 v105, v105\n"
"v_mfma_f32_16x16x32_bf16 v[64:67], a[80:83], v[36:39], v[64:67]\n"
"s_add_u32 m0, s60, 114688\n"
"v_cvt_pk_bf16_f32 v145, v98, v99\n"
"v_exp_f32 v106, v106\n"
"v_mfma_f32_16x16x32_bf16 v[72:75], a[84:87], v[36:39], v[72:75]\n"
"buffer_load_dwordx4 v2, s[4:7], s64 offen offset:0 nt lds\n"
"v_mfma_f32_16x16x32_bf16 v[80:83], a[88:91], v[36:39], v[80:83]\n"
"v_exp_f32 v107, v107\n"
"v_mfma_f32_16x16x32_bf16 v[88:91], a[92:95], v[36:39], v[88:91]\n"
"v_cvt_pk_bf16_f32 v146, v104, v105\n"
"v_exp_f32 v100, v100\n"
"v_mfma_f32_16x16x32_bf16 v[68:71], a[80:83], v[52:55], v[68:71]\n"
"ds_read_b128 a[80:83], v171 offset:32768\n"
"v_mfma_f32_16x16x32_bf16 v[76:79], a[84:87], v[52:55], v[76:79]\n"
"ds_read_b128 a[84:87], v171 offset:36864\n"
"buffer_load_dwordx4 v3, s[4:7], s64 offen offset:1024 nt lds\n"
"v_exp_f32 v101, v101\n"
"v_mfma_f32_16x16x32_bf16 v[84:87], a[88:91], v[52:55], v[84:87]\n"
"ds_read_b128 a[88:91], v171 offset:40960\n"
"v_cvt_pk_bf16_f32 v147, v106, v107\n"
"v_exp_f32 v102, v102\n"
"v_mfma_f32_16x16x32_bf16 v[92:95], a[92:95], v[52:55], v[92:95]\n"
"s_waitcnt lgkmcnt(3)\n"
"ds_read_b128 a[92:95], v171 offset:45056\n"
"v_mfma_f32_16x16x32_bf16 v[64:67], a[64:67], v[40:43], v[64:67]\n"
"ds_read_b64_tr_b16 a[112:113], v172 offset:50176\n"
"v_add_u32 v168, 0x20000, v168\n"
"v_exp_f32 v103, v103\n"
"v_mfma_f32_16x16x32_bf16 v[72:75], a[68:71], v[40:43], v[72:75]\n"
"ds_read_b64_tr_b16 a[114:115], v172 offset:54272\n"
"buffer_load_dwordx4 v4, s[4:7], s64 offen offset:2048 nt lds\n"
"v_cvt_pk_bf16_f32 v148, v100, v101\n"
"v_mfma_f32_16x16x32_bf16 v[80:83], a[72:75], v[40:43], v[80:83]\n"
"ds_read_b64_tr_b16 a[116:117], v173 offset:50176\n"
"v_add_u32 v169, 0x20000, v169\n"
"v_exp_f32 v108, v108\n"
"v_mfma_f32_16x16x32_bf16 v[88:91], a[76:79], v[40:43], v[88:91]\n"
"ds_read_b64_tr_b16 a[118:119], v173 offset:54272\n"
"v_add_u32 v170, 0x20000, v170\n"
"v_exp_f32 v109, v109\n"
"v_mfma_f32_16x16x32_bf16 v[68:71], a[64:67], v[56:59], v[68:71]\n"
"ds_read_b64_tr_b16 a[120:121], v172 offset:50688\n"
"v_add_u32 v171, 0x20000, v171\n"
"v_cvt_pk_bf16_f32 v149, v102, v103\n"
"v_mfma_f32_16x16x32_bf16 v[76:79], a[68:71], v[56:59], v[76:79]\n"
"ds_read_b64_tr_b16 a[122:123], v172 offset:54784\n"
"buffer_load_dwordx4 v5, s[4:7], s64 offen offset:3072 nt lds\n"
"v_exp_f32 v110, v110\n"
"v_mfma_f32_16x16x32_bf16 v[84:87], a[72:75], v[56:59], v[84:87]\n"
"ds_read_b64_tr_b16 a[124:125], v173 offset:50688\n"
"v_exp_f32 v111, v111\n"
"v_mfma_f32_16x16x32_bf16 v[92:95], a[76:79], v[56:59], v[92:95]\n"
"s_waitcnt lgkmcnt(3)\n"
"ds_read_b64_tr_b16 a[126:127], v173 offset:54784\n"
"v_cvt_pk_bf16_f32 v150, v108, v109\n"
"v_exp_f32 v112, v112\n"
"v_mfma_f32_16x16x32_bf16 v[64:67], a[80:83], v[44:47], v[64:67]\n"
"v_exp_f32 v113, v113\n"
"v_mfma_f32_16x16x32_bf16 v[72:75], a[84:87], v[44:47], v[72:75]\n"
"v_cvt_pk_bf16_f32 v151, v110, v111\n"
"v_mfma_f32_16x16x32_bf16 v[80:83], a[88:91], v[44:47], v[80:83]\n"
"v_exp_f32 v114, v114\n"
"v_mfma_f32_16x16x32_bf16 v[88:91], a[92:95], v[44:47], v[88:91]\n"
"v_exp_f32 v115, v115\n"
"v_mfma_f32_16x16x32_bf16 v[68:71], a[80:83], v[60:63], v[68:71]\n"
"v_cvt_pk_bf16_f32 v152, v112, v113\n"
"v_mfma_f32_16x16x32_bf16 v[76:79], a[84:87], v[60:63], v[76:79]\n"
"v_exp_f32 v120, v120\n"
"v_mfma_f32_16x16x32_bf16 v[84:87], a[88:91], v[60:63], v[84:87]\n"
"v_exp_f32 v121, v121\n"
"v_mfma_f32_16x16x32_bf16 v[92:95], a[92:95], v[60:63], v[92:95]\n"
"v_cvt_pk_bf16_f32 v153, v114, v115\n"
"s_waitcnt vmcnt(21)\n"
"s_barrier\n"
"s_sleep 2\n"
".LAbar6:\n"
"v_mfma_f32_16x16x32_bf16 a[0:3], a[96:99], v[144:147], a[0:3]\n"
"ds_read_b128 a[64:67], v168 offset:16384\n"
"s_add_u32 m0, s60, 32768\n"
"v_exp_f32 v122, v122\n"
"v_mfma_f32_16x16x32_bf16 a[8:11], a[100:103], v[144:147], a[8:11]\n"
"ds_read_b128 a[68:71], v168 offset:20480\n"
"buffer_load_dwordx4 v2, s[8:11], s63 offen offset:0 nt lds\n"
"v_mfma_f32_16x16x32_bf16 a[16:19], a[104:107], v[144:147], a[16:19]\n"
"ds_read_b128 a[72:75], v168 offset:24576\n"
"v_exp_f32 v123, v123\n"
"v_mfma_f32_16x16x32_bf16 a[24:27], a[108:111], v[144:147], a[24:27]\n"
"ds_read_b128 a[76:79], v168 offset:28672\n"
"v_cvt_pk_bf16_f32 v154, v120, v121\n"
"v_mfma_f32_16x16x32_bf16 a[4:7], a[96:99], v[148:151], a[4:7]\n"
"ds_read_b64_tr_b16 a[96:97], v172 offset:57344\n"
"v_exp_f32 v116, v116\n"
"v_mfma_f32_16x16x32_bf16 a[12:15], a[100:103], v[148:151], a[12:15]\n"
"ds_read_b64_tr_b16 a[98:99], v172 offset:61440\n"
"buffer_load_dwordx4 v3, s[8:11], s63 offen offset:1024 nt lds\n"
"v_mfma_f32_16x16x32_bf16 a[20:23], a[104:107], v[148:151], a[20:23]\n"
"ds_read_b64_tr_b16 a[100:101], v173 offset:57344\n"
"v_exp_f32 v117, v117\n"
"v_mfma_f32_16x16x32_bf16 a[28:31], a[108:111], v[148:151], a[28:31]\n"
"ds_read_b64_tr_b16 a[102:103], v173 offset:61440\n"
"v_cvt_pk_bf16_f32 v155, v122, v123\n"
"v_mfma_f32_16x16x32_bf16 v[160:163], v[176:179], v[144:147], v[160:163]\n"
"ds_read_b64_tr_b16 a[104:105], v172 offset:57856\n"
"v_exp_f32 v118, v118\n"
"v_mfma_f32_16x16x32_bf16 a[32:35], a[112:115], v[144:147], a[32:35]\n"
"ds_read_b64_tr_b16 a[106:107], v172 offset:61952\n"
"buffer_load_dwordx4 v4, s[8:11], s63 offen offset:2048 nt lds\n"
"v_mfma_f32_16x16x32_bf16 a[40:43], a[116:119], v[144:147], a[40:43]\n"
"s_waitcnt lgkmcnt(4)\n"
"ds_read_b64_tr_b16 a[108:109], v173 offset:57856\n"
"v_exp_f32 v119, v119\n"
"v_mfma_f32_16x16x32_bf16 a[48:51], a[120:123], v[144:147], a[48:51]\n"
"ds_read_b64_tr_b16 a[110:111], v173 offset:61952\n"
"v_cvt_pk_bf16_f32 v156, v116, v117\n"
"v_mfma_f32_16x16x32_bf16 a[56:59], a[124:127], v[144:147], a[56:59]\n"
"v_exp_f32 v124, v124\n"
"v_mfma_f32_16x16x32_bf16 a[36:39], a[112:115], v[148:151], a[36:39]\n"
"ds_read_b64_tr_b16 a[112:113], v172 offset:58368\n"
"buffer_load_dwordx4 v5, s[8:11], s63 offen offset:3072 nt lds\n"
"v_exp_f32 v125, v125\n"
"v_mfma_f32_16x16x32_bf16 a[44:47], a[116:119], v[148:151], a[44:47]\n"
"ds_read_b64_tr_b16 a[114:115], v172 offset:62464\n"
"v_cvt_pk_bf16_f32 v157, v118, v119\n"
"v_mfma_f32_16x16x32_bf16 a[52:55], a[120:123], v[148:151], a[52:55]\n"
"ds_read_b64_tr_b16 a[116:117], v173 offset:58368\n"
"v_max3_f32 v188, v64, v65, v66\n"
"v_mfma_f32_16x16x32_bf16 a[60:63], a[124:127], v[148:151], a[60:63]\n"
"ds_read_b64_tr_b16 a[118:119], v173 offset:62464\n"
"v_max3_f32 v189, v68, v69, v70\n"
"v_exp_f32 v126, v126\n"
"v_mfma_f32_16x16x32_bf16 v[164:167], v[176:179], v[148:151], v[164:167]\n"
"ds_read_b64_tr_b16 a[120:121], v172 offset:58880\n"
"v_max3_f32 v188, v188, v67, v72\n"
"v_exp_f32 v127, v127\n"
"v_mfma_f32_16x16x32_bf16 a[0:3], a[96:99], v[152:155], a[0:3]\n"
"s_waitcnt lgkmcnt(4)\n"
"ds_read_b64_tr_b16 a[122:123], v172 offset:62976\n"
"v_cvt_pk_bf16_f32 v158, v124, v125\n"
"v_mfma_f32_16x16x32_bf16 a[8:11], a[100:103], v[152:155], a[8:11]\n"
"ds_read_b64_tr_b16 a[124:125], v173 offset:58880\n"
"v_cvt_pk_bf16_f32 v159, v126, v127\n"
"v_mfma_f32_16x16x32_bf16 a[16:19], a[104:107], v[152:155], a[16:19]\n"
"ds_read_b64_tr_b16 a[126:127], v173 offset:62976\n"
"v_max3_f32 v189, v189, v71, v76\n"
"v_mfma_f32_16x16x32_bf16 a[24:27], a[108:111], v[152:155], a[24:27]\n"
"v_max3_f32 v188, v188, v73, v74\n"
"v_max3_f32 v189, v189, v77, v78\n"
"v_mfma_f32_16x16x32_bf16 a[4:7], a[96:99], v[156:159], a[4:7]\n"
"ds_read_b128 a[80:83], v169 offset:16384\n"
"v_max3_f32 v188, v188, v75, v80\n"
"v_mfma_f32_16x16x32_bf16 a[12:15], a[100:103], v[156:159], a[12:15]\n"
"ds_read_b128 a[84:87], v169 offset:20480\n"
"v_max3_f32 v189, v189, v79, v84\n"
"v_mfma_f32_16x16x32_bf16 a[20:23], a[104:107], v[156:159], a[20:23]\n"
"ds_read_b128 a[88:91], v169 offset:24576\n"
"v_max3_f32 v188, v188, v81, v82\n"
"v_mfma_f32_16x16x32_bf16 a[28:31], a[108:111], v[156:159], a[28:31]\n"
"ds_read_b128 a[92:95], v169 offset:28672\n"
"v_max3_f32 v189, v189, v85, v86\n"
"v_mfma_f32_16x16x32_bf16 v[160:163], v[176:179], v[152:155], v[160:163]\n"
"s_waitcnt lgkmcnt(4)\n"
"ds_read_b64_tr_b16 a[96:97], v172 offset:0\n"
"v_max3_f32 v188, v188, v83, v88\n"
"v_mfma_f32_16x16x32_bf16 a[32:35], a[112:115], v[152:155], a[32:35]\n"
"ds_read_b64_tr_b16 a[98:99], v172 offset:4096\n"
"v_max3_f32 v189, v189, v87, v92\n"
"v_mfma_f32_16x16x32_bf16 a[40:43], a[116:119], v[152:155], a[40:43]\n"
"ds_read_b64_tr_b16 a[100:101], v173 offset:0\n"
"v_max3_f32 v188, v188, v89, v90\n"
"v_mfma_f32_16x16x32_bf16 a[48:51], a[120:123], v[152:155], a[48:51]\n"
"ds_read_b64_tr_b16 a[102:103], v173 offset:4096\n"
"v_max3_f32 v189, v189, v93, v94\n"
"v_mfma_f32_16x16x32_bf16 a[56:59], a[124:127], v[152:155], a[56:59]\n"
"ds_read_b64_tr_b16 a[104:105], v172 offset:512\n"
"v_max3_f32 v190, v188, v91, v95\n"
"v_mfma_f32_16x16x32_bf16 a[36:39], a[112:115], v[156:159], a[36:39]\n"
"ds_read_b64_tr_b16 a[106:107], v172 offset:4608\n"
"v_max_f32 v190, v190, v189\n"
"v_mfma_f32_16x16x32_bf16 a[44:47], a[116:119], v[156:159], a[44:47]\n"
"ds_read_b64_tr_b16 a[108:109], v173 offset:512\n"
"v_cmp_lt_f32 vcc, s59, v190\n"
"v_mfma_f32_16x16x32_bf16 a[52:55], a[120:123], v[156:159], a[52:55]\n"
"ds_read_b64_tr_b16 a[110:111], v173 offset:4608\n"
"v_mfma_f32_16x16x32_bf16 a[60:63], a[124:127], v[156:159], a[60:63]\n"
"v_mfma_f32_16x16x32_bf16 v[164:167], v[176:179], v[156:159], v[164:167]\n"
"s_cbranch_vccz .LAskip6\n"
"s_swappc_b64 s[70:71], s[72:73]\n"
".LAskip6:\n"
"v_mfma_f32_16x16x32_bf16 v[96:99], a[64:67], v[32:35], v[128:131]\n"
"v_exp_f32 v64, v64\n"
"v_mfma_f32_16x16x32_bf16 v[104:107], a[68:71], v[32:35], v[128:131]\n"
"v_exp_f32 v65, v65\n"
"v_mfma_f32_16x16x32_bf16 v[112:115], a[72:75], v[32:35], v[128:131]\n"
"v_exp_f32 v66, v66\n"
"v_mfma_f32_16x16x32_bf16 v[120:123], a[76:79], v[32:35], v[128:131]\n"
"v_exp_f32 v67, v67\n"
"v_mfma_f32_16x16x32_bf16 v[100:103], a[64:67], v[48:51], v[132:135]\n"
"ds_read_b128 a[64:67], v170 offset:16384\n"
"v_cvt_pk_bf16_f32 v144, v64, v65\n"
"v_mfma_f32_16x16x32_bf16 v[108:111], a[68:71], v[48:51], v[132:135]\n"
"ds_read_b128 a[68:71], v170 offset:20480\n"
"v_exp_f32 v72, v72\n"
"v_mfma_f32_16x16x32_bf16 v[116:119], a[72:75], v[48:51], v[132:135]\n"
"ds_read_b128 a[72:75], v170 offset:24576\n"
"v_mfma_f32_16x16x32_bf16 v[124:127], a[76:79], v[48:51], v[132:135]\n"
"s_waitcnt lgkmcnt(3)\n"
"ds_read_b128 a[76:79], v170 offset:28672\n"
"v_exp_f32 v73, v73\n"
"v_mfma_f32_16x16x32_bf16 v[96:99], a[80:83], v[36:39], v[96:99]\n"
"s_add_u32 m0, s60, 131072\n"
"v_cvt_pk_bf16_f32 v145, v66, v67\n"
"v_exp_f32 v74, v74\n"
"v_mfma_f32_16x16x32_bf16 v[104:107], a[84:87], v[36:39], v[104:107]\n"
"buffer_load_dwordx4 v2, s[4:7], s65 offen offset:0 nt lds\n"
"v_mfma_f32_16x16x32_bf16 v[112:115], a[88:91], v[36:39], v[112:115]\n"
"v_exp_f32 v75, v75\n"
"v_mfma_f32_16x16x32_bf16 v[120:123], a[92:95], v[36:39], v[120:123]\n"
"v_cvt_pk_bf16_f32 v146, v72, v73\n"
"v_exp_f32 v68, v68\n"
"v_mfma_f32_16x16x32_bf16 v[100:103], a[80:83], v[52:55], v[100:103]\n"
"ds_read_b128 a[80:83], v171 offset:16384\n"
"v_mfma_f32_16x16x32_bf16 v[108:111], a[84:87], v[52:55], v[108:111]\n"
"ds_read_b128 a[84:87], v171 offset:20480\n"
"buffer_load_dwordx4 v3, s[4:7], s65 offen offset:1024 nt lds\n"
"v_exp_f32 v69, v69\n"
"v_mfma_f32_16x16x32_bf16 v[116:119], a[88:91], v[52:55], v[116:119]\n"
"ds_read_b128 a[88:91], v171 offset:24576\n"
"v_cvt_pk_bf16_f32 v147, v74, v75\n"
"v_exp_f32 v70, v70\n"
"v_mfma_f32_16x16x32_bf16 v[124:127], a[92:95], v[52:55], v[124:127]\n"
"s_waitcnt lgkmcnt(3)\n"
"ds_read_b128 a[92:95], v171 offset:28672\n"
"v_mfma_f32_16x16x32_bf16 v[96:99], a[64:67], v[40:43], v[96:99]\n"
"ds_read_b64_tr_b16 a[112:113], v172 offset:1024\n"
"v_add_u32 v168, 0xffff0000, v168\n"
"v_exp_f32 v71, v71\n"
"v_mfma_f32_16x16x32_bf16 v[104:107], a[68:71], v[40:43], v[104:107]\n"
"ds_read_b64_tr_b16 a[114:115], v172 offset:5120\n"
"buffer_load_dwordx4 v4, s[4:7], s65 offen offset:2048 nt lds\n"
"v_cvt_pk_bf16_f32 v148, v68, v69\n"
"v_mfma_f32_16x16x32_bf16 v[112:115], a[72:75], v[40:43], v[112:115]\n"
"ds_read_b64_tr_b16 a[116:117], v173 offset:1024\n"
"v_add_u32 v169, 0xffff0000, v169\n"
"v_exp_f32 v76, v76\n"
"v_mfma_f32_16x16x32_bf16 v[120:123], a[76:79], v[40:43], v[120:123]\n"
"ds_read_b64_tr_b16 a[118:119], v173 offset:5120\n"
"v_add_u32 v170, 0xffff0000, v170\n"
"v_exp_f32 v77, v77\n"
"v_mfma_f32_16x16x32_bf16 v[100:103], a[64:67], v[56:59], v[100:103]\n"
"ds_read_b64_tr_b16 a[120:121], v172 offset:1536\n"
"v_add_u32 v171, 0xffff0000, v171\n"
"v_cvt_pk_bf16_f32 v149, v70, v71\n"
"v_mfma_f32_16x16x32_bf16 v[108:111], a[68:71], v[56:59], v[108:111]\n"
"ds_read_b64_tr_b16 a[122:123], v172 offset:5632\n"
"buffer_load_dwordx4 v5, s[4:7], s65 offen offset:3072 nt lds\n"
"v_exp_f32 v78, v78\n"
"v_mfma_f32_16x16x32_bf16 v[116:119], a[72:75], v[56:59], v[116:119]\n"
"ds_read_b64_tr_b16 a[124:125], v173 offset:1536\n"
"v_exp_f32 v79, v79\n"
"v_mfma_f32_16x16x32_bf16 v[124:127], a[76:79], v[56:59], v[124:127]\n"
"s_waitcnt lgkmcnt(3)\n"
"ds_read_b64_tr_b16 a[126:127], v173 offset:5632\n"
"v_cvt_pk_bf16_f32 v150, v76, v77\n"
"v_exp_f32 v80, v80\n"
"v_mfma_f32_16x16x32_bf16 v[96:99], a[80:83], v[44:47], v[96:99]\n"
"v_exp_f32 v81, v81\n"
"v_mfma_f32_16x16x32_bf16 v[104:107], a[84:87], v[44:47], v[104:107]\n"
"v_cvt_pk_bf16_f32 v151, v78, v79\n"
"v_mfma_f32_16x16x32_bf16 v[112:115], a[88:91], v[44:47], v[112:115]\n"
"v_exp_f32 v82, v82\n"
"v_mfma_f32_16x16x32_bf16 v[120:123], a[92:95], v[44:47], v[120:123]\n"
"v_exp_f32 v83, v83\n"
"v_mfma_f32_16x16x32_bf16 v[100:103], a[80:83], v[60:63], v[100:103]\n"
"v_cvt_pk_bf16_f32 v152, v80, v81\n"
"v_mfma_f32_16x16x32_bf16 v[108:111], a[84:87], v[60:63], v[108:111]\n"
"v_exp_f32 v88, v88\n"
"v_mfma_f32_16x16x32_bf16 v[116:119], a[88:91], v[60:63], v[116:119]\n"
"v_exp_f32 v89, v89\n"
"v_mfma_f32_16x16x32_bf16 v[124:127], a[92:95], v[60:63], v[124:127]\n"
"v_cvt_pk_bf16_f32 v153, v82, v83\n"
"s_waitcnt vmcnt(21)\n"
"s_barrier\n"
"s_sleep 2\n"
".LAbar7:\n"
"v_mfma_f32_16x16x32_bf16 a[0:3], a[96:99], v[144:147], a[0:3]\n"
"ds_read_b128 a[64:67], v168 offset:0\n"
"s_add_u32 m0, s60, 49152\n"
"v_exp_f32 v90, v90\n"
"v_mfma_f32_16x16x32_bf16 a[8:11], a[100:103], v[144:147], a[8:11]\n"
"ds_read_b128 a[68:71], v168 offset:4096\n"
"buffer_load_dwordx4 v2, s[8:11], s64 offen offset:0 nt lds\n"
"v_mfma_f32_16x16x32_bf16 a[16:19], a[104:107], v[144:147], a[16:19]\n"
"ds_read_b128 a[72:75], v168 offset:8192\n"
"v_exp_f32 v91, v91\n"
"v_mfma_f32_16x16x32_bf16 a[24:27], a[108:111], v[144:147], a[24:27]\n"
"ds_read_b128 a[76:79], v168 offset:12288\n"
"v_cvt_pk_bf16_f32 v154, v88, v89\n"
"v_mfma_f32_16x16x32_bf16 a[4:7], a[96:99], v[148:151], a[4:7]\n"
"ds_read_b64_tr_b16 a[96:97], v172 offset:8192\n"
"v_exp_f32 v84, v84\n"
"v_mfma_f32_16x16x32_bf16 a[12:15], a[100:103], v[148:151], a[12:15]\n"
"ds_read_b64_tr_b16 a[98:99], v172 offset:12288\n"
"buffer_load_dwordx4 v3, s[8:11], s64 offen offset:1024 nt lds\n"
"v_mfma_f32_16x16x32_bf16 a[20:23], a[104:107], v[148:151], a[20:23]\n"
"ds_read_b64_tr_b16 a[100:101], v173 offset:8192\n"
"v_exp_f32 v85, v85\n"
"v_mfma_f32_16x16x32_bf16 a[28:31], a[108:111], v[148:151], a[28:31]\n"
"ds_read_b64_tr_b16 a[102:103], v173 offset:12288\n"
"v_cvt_pk_bf16_f32 v155, v90, v91\n"
"v_mfma_f32_16x16x32_bf16 v[160:163], v[176:179], v[144:147], v[160:163]\n"
"ds_read_b64_tr_b16 a[104:105], v172 offset:8704\n"
"v_exp_f32 v86, v86\n"
"v_mfma_f32_16x16x32_bf16 a[32:35], a[112:115], v[144:147], a[32:35]\n"
"ds_read_b64_tr_b16 a[106:107], v172 offset:12800\n"
"buffer_load_dwordx4 v4, s[8:11], s64 offen offset:2048 nt lds\n"
"v_mfma_f32_16x16x32_bf16 a[40:43], a[116:119], v[144:147], a[40:43]\n"
"s_waitcnt lgkmcnt(4)\n"
"ds_read_b64_tr_b16 a[108:109], v173 offset:8704\n"
"v_exp_f32 v87, v87\n"
"v_mfma_f32_16x16x32_bf16 a[48:51], a[120:123], v[144:147], a[48:51]\n"
"ds_read_b64_tr_b16 a[110:111], v173 offset:12800\n"
"v_cvt_pk_bf16_f32 v156, v84, v85\n"
"v_mfma_f32_16x16x32_bf16 a[56:59], a[124:127], v[144:147], a[56:59]\n"
"v_exp_f32 v92, v92\n"
"v_mfma_f32_16x16x32_bf16 a[36:39], a[112:115], v[148:151], a[36:39]\n"
"ds_read_b64_tr_b16 a[112:113], v172 offset:9216\n"
"buffer_load_dwordx4 v5, s[8:11], s64 offen offset:3072 nt lds\n"
"v_exp_f32 v93, v93\n"
"v_mfma_f32_16x16x32_bf16 a[44:47], a[116:119], v[148:151], a[44:47]\n"
"ds_read_b64_tr_b16 a[114:115], v172 offset:13312\n"
"v_cvt_pk_bf16_f32 v157, v86, v87\n"
"v_mfma_f32_16x16x32_bf16 a[52:55], a[120:123], v[148:151], a[52:55]\n"
"ds_read_b64_tr_b16 a[116:117], v173 offset:9216\n"
"v_max3_f32 v188, v96, v97, v98\n"
"v_mfma_f32_16x16x32_bf16 a[60:63], a[124:127], v[148:151], a[60:63]\n"
"ds_read_b64_tr_b16 a[118:119], v173 offset:13312\n"
"v_max3_f32 v189, v100, v101, v102\n"
"v_exp_f32 v94, v94\n"
"v_mfma_f32_16x16x32_bf16 v[164:167], v[176:179], v[148:151], v[164:167]\n"
"ds_read_b64_tr_b16 a[120:121], v172 offset:9728\n"
"v_max3_f32 v188, v188, v99, v104\n"
"v_exp_f32 v95, v95\n"
"v_mfma_f32_16x16x32_bf16 a[0:3], a[96:99], v[152:155], a[0:3]\n"
"s_waitcnt lgkmcnt(4)\n"
"ds_read_b64_tr_b16 a[122:123], v172 offset:13824\n"
"v_cvt_pk_bf16_f32 v158, v92, v93\n"
"v_mfma_f32_16x16x32_bf16 a[8:11], a[100:103], v[152:155], a[8:11]\n"
"ds_read_b64_tr_b16 a[124:125], v173 offset:9728\n"
"v_cvt_pk_bf16_f32 v159, v94, v95\n"
"v_mfma_f32_16x16x32_bf16 a[16:19], a[104:107], v[152:155], a[16:19]\n"
"ds_read_b64_tr_b16 a[126:127], v173 offset:13824\n"
"v_add_u32 v172, 0x10000, v172\n"
"v_mfma_f32_16x16x32_bf16 a[24:27], a[108:111], v[152:155], a[24:27]\n"
"v_add_u32 v173, 0x10000, v173\n"
"v_max3_f32 v189, v189, v103, v108\n"
"v_mfma_f32_16x16x32_bf16 a[4:7], a[96:99], v[156:159], a[4:7]\n"
"ds_read_b128 a[80:83], v169 offset:0\n"
"v_max3_f32 v188, v188, v105, v106\n"
"v_mfma_f32_16x16x32_bf16 a[12:15], a[100:103], v[156:159], a[12:15]\n"
"ds_read_b128 a[84:87], v169 offset:4096\n"
"v_max3_f32 v189, v189, v109, v110\n"
"v_mfma_f32_16x16x32_bf16 a[20:23], a[104:107], v[156:159], a[20:23]\n"
"ds_read_b128 a[88:91], v169 offset:8192\n"
"v_max3_f32 v188, v188, v107, v112\n"
"v_mfma_f32_16x16x32_bf16 a[28:31], a[108:111], v[156:159], a[28:31]\n"
"ds_read_b128 a[92:95], v169 offset:12288\n"
"v_max3_f32 v189, v189, v111, v116\n"
"v_mfma_f32_16x16x32_bf16 v[160:163], v[176:179], v[152:155], v[160:163]\n"
"s_waitcnt lgkmcnt(4)\n"
"ds_read_b64_tr_b16 a[96:97], v172 offset:16384\n"
"v_max3_f32 v188, v188, v113, v114\n"
"v_mfma_f32_16x16x32_bf16 a[32:35], a[112:115], v[152:155], a[32:35]\n"
"ds_read_b64_tr_b16 a[98:99], v172 offset:20480\n"
"v_max3_f32 v189, v189, v117, v118\n"
"v_mfma_f32_16x16x32_bf16 a[40:43], a[116:119], v[152:155], a[40:43]\n"
"ds_read_b64_tr_b16 a[100:101], v173 offset:16384\n"
"v_max3_f32 v188, v188, v115, v120\n"
"v_mfma_f32_16x16x32_bf16 a[48:51], a[120:123], v[152:155], a[48:51]\n"
"ds_read_b64_tr_b16 a[102:103], v173 offset:20480\n"
"v_max3_f32 v189, v189, v119, v124\n"
"v_mfma_f32_16x16x32_bf16 a[56:59], a[124:127], v[152:155], a[56:59]\n"
"ds_read_b64_tr_b16 a[104:105], v172 offset:16896\n"
"v_max3_f32 v188, v188, v121, v122\n"
"v_mfma_f32_16x16x32_bf16 a[36:39], a[112:115], v[156:159], a[36:39]\n"
"ds_read_b64_tr_b16 a[106:107], v172 offset:20992\n"
"v_max3_f32 v189, v189, v125, v126\n"
"v_mfma_f32_16x16x32_bf16 a[44:47], a[116:119], v[156:159], a[44:47]\n"
"ds_read_b64_tr_b16 a[108:109], v173 offset:16896\n"
"v_max3_f32 v190, v188, v123, v127\n"
"v_mfma_f32_16x16x32_bf16 a[52:55], a[120:123], v[156:159], a[52:55]\n"
"ds_read_b64_tr_b16 a[110:111], v173 offset:20992\n"
"v_max_f32 v190, v190, v189\n"
"v_mfma_f32_16x16x32_bf16 a[60:63], a[124:127], v[156:159], a[60:63]\n"
"v_cmp_lt_f32 vcc, s59, v190\n"
"v_mfma_f32_16x16x32_bf16 v[164:167], v[176:179], v[156:159], v[164:167]\n"
"s_nop 0\n"
"s_cbranch_vccz .LAskip7\n"
"s_swappc_b64 s[70:71], s[74:75]\n"
".LAskip7:\n"
"v_mfma_f32_16x16x32_bf16 v[64:67], a[64:67], v[192:195], 0\n"
"v_exp_f32 v96, v96\n"
"v_mfma_f32_16x16x32_bf16 v[72:75], a[68:71], v[192:195], 0\n"
"v_exp_f32 v97, v97\n"
"v_mfma_f32_16x16x32_bf16 v[80:83], a[72:75], v[192:195], 0\n"
"v_exp_f32 v98, v98\n"
"v_mfma_f32_16x16x32_bf16 v[88:91], a[76:79], v[192:195], 0\n"
"v_exp_f32 v99, v99\n"
"v_mfma_f32_16x16x32_bf16 v[68:71], a[64:67], v[208:211], 0\n"
"ds_read_b128 a[64:67], v170 offset:0\n"
"v_cvt_pk_bf16_f32 v144, v96, v97\n"
"v_mfma_f32_16x16x32_bf16 v[76:79], a[68:71], v[208:211], 0\n"
"ds_read_b128 a[68:71], v170 offset:4096\n"
"v_exp_f32 v104, v104\n"
"v_mfma_f32_16x16x32_bf16 v[84:87], a[72:75], v[208:211], 0\n"
"ds_read_b128 a[72:75], v170 offset:8192\n"
"v_mfma_f32_16x16x32_bf16 v[92:95], a[76:79], v[208:211], 0\n"
"s_waitcnt lgkmcnt(3)\n"
"ds_read_b128 a[76:79], v170 offset:12288\n"
"v_exp_f32 v105, v105\n"
"v_mfma_f32_16x16x32_bf16 v[64:67], a[80:83], v[196:199], v[64:67]\n"
"s_add_u32 m0, s60, 147456\n"
"v_cvt_pk_bf16_f32 v145, v98, v99\n"
"v_exp_f32 v106, v106\n"
"v_mfma_f32_16x16x32_bf16 v[72:75], a[84:87], v[196:199], v[72:75]\n"
"buffer_load_dwordx4 v2, s[4:7], s66 offen offset:0 nt lds\n"
"v_mfma_f32_16x16x32_bf16 v[80:83], a[88:91], v[196:199], v[80:83]\n"
"v_exp_f32 v107, v107\n"
"v_mfma_f32_16x16x32_bf16 v[88:91], a[92:95], v[196:199], v[88:91]\n"
"v_cvt_pk_bf16_f32 v146, v104, v105\n"
"v_exp_f32 v100, v100\n"
"v_mfma_f32_16x16x32_bf16 v[68:71], a[80:83], v[212:215], v[68:71]\n"
"ds_read_b128 a[80:83], v171 offset:0\n"
"v_mfma_f32_16x16x32_bf16 v[76:79], a[84:87], v[212:215], v[76:79]\n"
"ds_read_b128 a[84:87], v171 offset:4096\n"
"buffer_load_dwordx4 v3, s[4:7], s66 offen offset:1024 nt lds\n"
"v_exp_f32 v101, v101\n"
"v_mfma_f32_16x16x32_bf16 v[84:87], a[88:91], v[212:215], v[84:87]\n"
"ds_read_b128 a[88:91], v171 offset:8192\n"
"v_cvt_pk_bf16_f32 v147, v106, v107\n"
"v_exp_f32 v102, v102\n"
"v_mfma_f32_16x16x32_bf16 v[92:95], a[92:95], v[212:215], v[92:95]\n"
"s_waitcnt lgkmcnt(3)\n"
"ds_read_b128 a[92:95], v171 offset:12288\n"
"v_mfma_f32_16x16x32_bf16 v[64:67], a[64:67], v[200:203], v[64:67]\n"
"ds_read_b64_tr_b16 a[112:113], v172 offset:17408\n"
"v_exp_f32 v103, v103\n"
"v_mfma_f32_16x16x32_bf16 v[72:75], a[68:71], v[200:203], v[72:75]\n"
"ds_read_b64_tr_b16 a[114:115], v172 offset:21504\n"
"buffer_load_dwordx4 v4, s[4:7], s66 offen offset:2048 nt lds\n"
"v_cvt_pk_bf16_f32 v148, v100, v101\n"
"v_mfma_f32_16x16x32_bf16 v[80:83], a[72:75], v[200:203], v[80:83]\n"
"ds_read_b64_tr_b16 a[116:117], v173 offset:17408\n"
"v_exp_f32 v108, v108\n"
"v_mfma_f32_16x16x32_bf16 v[88:91], a[76:79], v[200:203], v[88:91]\n"
"ds_read_b64_tr_b16 a[118:119], v173 offset:21504\n"
"v_exp_f32 v109, v109\n"
"v_mfma_f32_16x16x32_bf16 v[68:71], a[64:67], v[216:219], v[68:71]\n"
"ds_read_b64_tr_b16 a[120:121], v172 offset:17920\n"
"v_cvt_pk_bf16_f32 v149, v102, v103\n"
"v_exp_f32 v110, v110\n"
"v_mfma_f32_16x16x32_bf16 v[76:79], a[68:71], v[216:219], v[76:79]\n"
"ds_read_b64_tr_b16 a[122:123], v172 offset:22016\n"
"buffer_load_dwordx4 v5, s[4:7], s66 offen offset:3072 nt lds\n"
"v_exp_f32 v111, v111\n"
"v_mfma_f32_16x16x32_bf16 v[84:87], a[72:75], v[216:219], v[84:87]\n"
"ds_read_b64_tr_b16 a[124:125], v173 offset:17920\n"
"v_cvt_pk_bf16_f32 v150, v108, v109\n"
"v_mfma_f32_16x16x32_bf16 v[92:95], a[76:79], v[216:219], v[92:95]\n"
"s_waitcnt lgkmcnt(4)\n"
"ds_read_b64_tr_b16 a[126:127], v173 offset:22016\n"
"v_exp_f32 v112, v112\n"
"v_mfma_f32_16x16x32_bf16 v[64:67], a[80:83], v[204:207], v[64:67]\n"
"v_exp_f32 v113, v113\n"
"v_mfma_f32_16x16x32_bf16 v[72:75], a[84:87], v[204:207], v[72:75]\n"
"v_cvt_pk_bf16_f32 v151, v110, v111\n"
"v_mfma_f32_16x16x32_bf16 v[80:83], a[88:91], v[204:207], v[80:83]\n"
"v_exp_f32 v114, v114\n"
"v_mfma_f32_16x16x32_bf16 v[88:91], a[92:95], v[204:207], v[88:91]\n"
"v_exp_f32 v115, v115\n"
"v_mfma_f32_16x16x32_bf16 v[68:71], a[80:83], v[220:223], v[68:71]\n"
"v_cvt_pk_bf16_f32 v152, v112, v113\n"
"v_mfma_f32_16x16x32_bf16 v[76:79], a[84:87], v[220:223], v[76:79]\n"
"v_exp_f32 v120, v120\n"
"v_mfma_f32_16x16x32_bf16 v[84:87], a[88:91], v[220:223], v[84:87]\n"
"v_exp_f32 v121, v121\n"
"v_mfma_f32_16x16x32_bf16 v[92:95], a[92:95], v[220:223], v[92:95]\n"
"v_cvt_pk_bf16_f32 v153, v114, v115\n"
"s_waitcnt vmcnt(20)\n"
"s_barrier\n"
"s_mov_b32 s61, 1\n"
"s_sleep 2\n"
".LAbar8:\n"
"v_mfma_f32_16x16x32_bf16 a[0:3], a[96:99], v[144:147], a[0:3]\n"
"ds_read_b128 a[64:67], v168 offset:32768\n"
"s_add_u32 m0, s60, 0\n"
"v_exp_f32 v122, v122\n"
"v_mfma_f32_16x16x32_bf16 a[8:11], a[100:103], v[144:147], a[8:11]\n"
"ds_read_b128 a[68:71], v168 offset:36864\n"
"buffer_load_dwordx4 v2, s[8:11], s65 offen offset:0 nt lds\n"
"v_mfma_f32_16x16x32_bf16 a[16:19], a[104:107], v[144:147], a[16:19]\n"
"ds_read_b128 a[72:75], v168 offset:40960\n"
"v_exp_f32 v123, v123\n"
"v_mfma_f32_16x16x32_bf16 a[24:27], a[108:111], v[144:147], a[24:27]\n"
"ds_read_b128 a[76:79], v168 offset:45056\n"
"v_cvt_pk_bf16_f32 v154, v120, v121\n"
"v_mfma_f32_16x16x32_bf16 a[4:7], a[96:99], v[148:151], a[4:7]\n"
"ds_read_b64_tr_b16 a[96:97], v172 offset:24576\n"
"v_exp_f32 v116, v116\n"
"v_mfma_f32_16x16x32_bf16 a[12:15], a[100:103], v[148:151], a[12:15]\n"
"ds_read_b64_tr_b16 a[98:99], v172 offset:28672\n"
"buffer_load_dwordx4 v3, s[8:11], s65 offen offset:1024 nt lds\n"
"v_mfma_f32_16x16x32_bf16 a[20:23], a[104:107], v[148:151], a[20:23]\n"
"ds_read_b64_tr_b16 a[100:101], v173 offset:24576\n"
"v_exp_f32 v117, v117\n"
"v_mfma_f32_16x16x32_bf16 a[28:31], a[108:111], v[148:151], a[28:31]\n"
"ds_read_b64_tr_b16 a[102:103], v173 offset:28672\n"
"v_cvt_pk_bf16_f32 v155, v122, v123\n"
"v_mfma_f32_16x16x32_bf16 v[160:163], v[176:179], v[144:147], v[160:163]\n"
"ds_read_b64_tr_b16 a[104:105], v172 offset:25088\n"
"v_exp_f32 v118, v118\n"
"v_mfma_f32_16x16x32_bf16 a[32:35], a[112:115], v[144:147], a[32:35]\n"
"s_waitcnt lgkmcnt(4)\n"
"ds_read_b64_tr_b16 a[106:107], v172 offset:29184\n"
"buffer_load_dwordx4 v4, s[8:11], s65 offen offset:2048 nt lds\n"
"v_mfma_f32_16x16x32_bf16 a[40:43], a[116:119], v[144:147], a[40:43]\n"
"ds_read_b64_tr_b16 a[108:109], v173 offset:25088\n"
"v_exp_f32 v119, v119\n"
"v_mfma_f32_16x16x32_bf16 a[48:51], a[120:123], v[144:147], a[48:51]\n"
"ds_read_b64_tr_b16 a[110:111], v173 offset:29184\n"
"v_cvt_pk_bf16_f32 v156, v116, v117\n"
"v_mfma_f32_16x16x32_bf16 a[56:59], a[124:127], v[144:147], a[56:59]\n"
"v_exp_f32 v124, v124\n"
"v_mfma_f32_16x16x32_bf16 a[36:39], a[112:115], v[148:151], a[36:39]\n"
"ds_read_b64_tr_b16 a[112:113], v172 offset:25600\n"
"buffer_load_dwordx4 v5, s[8:11], s65 offen offset:3072 nt lds\n"
"v_exp_f32 v125, v125\n"
"v_mfma_f32_16x16x32_bf16 a[44:47], a[116:119], v[148:151], a[44:47]\n"
"ds_read_b64_tr_b16 a[114:115], v172 offset:29696\n"
"v_cvt_pk_bf16_f32 v157, v118, v119\n"
"v_mfma_f32_16x16x32_bf16 a[52:55], a[120:123], v[148:151], a[52:55]\n"
"ds_read_b64_tr_b16 a[116:117], v173 offset:25600\n"
"v_max3_f32 v188, v64, v65, v66\n"
"v_mfma_f32_16x16x32_bf16 a[60:63], a[124:127], v[148:151], a[60:63]\n"
"ds_read_b64_tr_b16 a[118:119], v173 offset:29696\n"
"v_max3_f32 v189, v68, v69, v70\n"
"v_exp_f32 v126, v126\n"
"v_mfma_f32_16x16x32_bf16 v[164:167], v[176:179], v[148:151], v[164:167]\n"
"s_waitcnt lgkmcnt(4)\n"
"ds_read_b64_tr_b16 a[120:121], v172 offset:26112\n"
"v_max3_f32 v188, v188, v67, v72\n"
"v_exp_f32 v127, v127\n"
"v_mfma_f32_16x16x32_bf16 a[0:3], a[96:99], v[152:155], a[0:3]\n"
"ds_read_b64_tr_b16 a[122:123], v172 offset:30208\n"
"v_cvt_pk_bf16_f32 v158, v124, v125\n"
"v_mfma_f32_16x16x32_bf16 a[8:11], a[100:103], v[152:155], a[8:11]\n"
"ds_read_b64_tr_b16 a[124:125], v173 offset:26112\n"
"v_cvt_pk_bf16_f32 v159, v126, v127\n"
"v_mfma_f32_16x16x32_bf16 a[16:19], a[104:107], v[152:155], a[16:19]\n"
"ds_read_b64_tr_b16 a[126:127], v173 offset:30208\n"
"v_add_u32 v172, 0xffff0000, v172\n"
"v_mfma_f32_16x16x32_bf16 a[24:27], a[108:111], v[152:155], a[24:27]\n"
"v_add_u32 v173, 0xffff0000, v173\n"
"v_max3_f32 v189, v189, v71, v76\n"
"v_mfma_f32_16x16x32_bf16 a[4:7], a[96:99], v[156:159], a[4:7]\n"
"ds_read_b128 a[80:83], v169 offset:32768\n"
"v_max3_f32 v188, v188, v73, v74\n"
"v_mfma_f32_16x16x32_bf16 a[12:15], a[100:103], v[156:159], a[12:15]\n"
"ds_read_b128 a[84:87], v169 offset:36864\n"
"v_max3_f32 v189, v189, v77, v78\n"
"v_mfma_f32_16x16x32_bf16 a[20:23], a[104:107], v[156:159], a[20:23]\n"
"ds_read_b128 a[88:91], v169 offset:40960\n"
"v_max3_f32 v188, v188, v75, v80\n"
"v_mfma_f32_16x16x32_bf16 a[28:31], a[108:111], v[156:159], a[28:31]\n"
"ds_read_b128 a[92:95], v169 offset:45056\n"
"v_max3_f32 v189, v189, v79, v84\n"
"v_mfma_f32_16x16x32_bf16 v[160:163], v[176:179], v[152:155], v[160:163]\n"
"s_waitcnt lgkmcnt(4)\n"
"ds_read_b64_tr_b16 a[96:97], v172 offset:16384\n"
"v_max3_f32 v188, v188, v81, v82\n"
"v_mfma_f32_16x16x32_bf16 a[32:35], a[112:115], v[152:155], a[32:35]\n"
"ds_read_b64_tr_b16 a[98:99], v172 offset:20480\n"
"v_max3_f32 v189, v189, v85, v86\n"
"v_mfma_f32_16x16x32_bf16 a[40:43], a[116:119], v[152:155], a[40:43]\n"
"ds_read_b64_tr_b16 a[100:101], v173 offset:16384\n"
"v_max3_f32 v188, v188, v83, v88\n"
"v_mfma_f32_16x16x32_bf16 a[48:51], a[120:123], v[152:155], a[48:51]\n"
"ds_read_b64_tr_b16 a[102:103], v173 offset:20480\n"
"v_max3_f32 v189, v189, v87, v92\n"
"v_mfma_f32_16x16x32_bf16 a[56:59], a[124:127], v[152:155], a[56:59]\n"
"ds_read_b64_tr_b16 a[104:105], v172 offset:16896\n"
"v_max3_f32 v188, v188, v89, v90\n"
"v_mfma_f32_16x16x32_bf16 a[36:39], a[112:115], v[156:159], a[36:39]\n"
"ds_read_b64_tr_b16 a[106:107], v172 offset:20992\n"
"v_max3_f32 v189, v189, v93, v94\n"
"v_mfma_f32_16x16x32_bf16 a[44:47], a[116:119], v[156:159], a[44:47]\n"
"ds_read_b64_tr_b16 a[108:109], v173 offset:16896\n"
"v_max_f32 v188, v188, v91\n"
"v_mfma_f32_16x16x32_bf16 a[52:55], a[120:123], v[156:159], a[52:55]\n"
"ds_read_b64_tr_b16 a[110:111], v173 offset:20992\n"
"v_max_f32 v189, v189, v95\n"
"v_mfma_f32_16x16x32_bf16 a[60:63], a[124:127], v[156:159], a[60:63]\n"
"s_nop 0\n"
"v_permlane32_swap_b32 v188, v189\n"
"v_mfma_f32_16x16x32_bf16 v[164:167], v[176:179], v[156:159], v[164:167]\n"
"v_max_f32 v188, v188, v189\n"
"v_mov_b32 v189, v188\n"
"s_nop 0\n"
"s_nop 0\n"
"v_permlane16_swap_b32 v188, v189\n"
"v_max_f32 v188, v188, v189\n"
"v_mov_b32 v190, v188\n"
"s_nop 0\n"
"s_nop 0\n"
"v_permlane32_swap_b32 v188, v190\n"
"v_sub_f32 v128, 0, v188\n"
"v_sub_f32 v129, 0, v188\n"
"v_sub_f32 v130, 0, v188\n"
"v_sub_f32 v131, 0, v188\n"
"v_sub_f32 v64, v64, v188\n"
"v_sub_f32 v65, v65, v188\n"
"v_sub_f32 v66, v66, v188\n"
"v_sub_f32 v67, v67, v188\n"
"v_sub_f32 v72, v72, v188\n"
"v_sub_f32 v73, v73, v188\n"
"v_sub_f32 v74, v74, v188\n"
"v_sub_f32 v75, v75, v188\n"
"v_sub_f32 v80, v80, v188\n"
"v_sub_f32 v81, v81, v188\n"
"v_sub_f32 v82, v82, v188\n"
"v_sub_f32 v83, v83, v188\n"
"v_sub_f32 v88, v88, v188\n"
"v_sub_f32 v89, v89, v188\n"
"v_sub_f32 v90, v90, v188\n"
"v_sub_f32 v91, v91, v188\n"
"v_sub_f32 v132, 0, v190\n"
"v_sub_f32 v133, 0, v190\n"
"v_sub_f32 v134, 0, v190\n"
"v_sub_f32 v135, 0, v190\n"
"v_sub_f32 v68, v68, v190\n"
"v_sub_f32 v69, v69, v190\n"
"v_sub_f32 v70, v70, v190\n"
"v_sub_f32 v71, v71, v190\n"
"v_sub_f32 v76, v76, v190\n"
"v_sub_f32 v77, v77, v190\n"
"v_sub_f32 v78, v78, v190\n"
"v_sub_f32 v79, v79, v190\n"
"v_sub_f32 v84, v84, v190\n"
"v_sub_f32 v85, v85, v190\n"
"v_sub_f32 v86, v86, v190\n"
"v_sub_f32 v87, v87, v190\n"
"v_sub_f32 v92, v92, v190\n"
"v_sub_f32 v93, v93, v190\n"
"v_sub_f32 v94, v94, v190\n"
"v_sub_f32 v95, v95, v190\n"
"v_mfma_f32_16x16x32_bf16 v[96:99], a[64:67], v[192:195], v[128:131]\n"
"v_exp_f32 v64, v64\n"
"v_mfma_f32_16x16x32_bf16 v[104:107], a[68:71], v[192:195], v[128:131]\n"
"v_exp_f32 v65, v65\n"
"v_mfma_f32_16x16x32_bf16 v[112:115], a[72:75], v[192:195], v[128:131]\n"
"v_exp_f32 v66, v66\n"
"v_mfma_f32_16x16x32_bf16 v[120:123], a[76:79], v[192:195], v[128:131]\n"
"v_exp_f32 v67, v67\n"
"v_mfma_f32_16x16x32_bf16 v[100:103], a[64:67], v[208:211], v[132:135]\n"
"ds_read_b128 a[64:67], v170 offset:32768\n"
"v_cvt_pk_bf16_f32 v144, v64, v65\n"
"v_mfma_f32_16x16x32_bf16 v[108:111], a[68:71], v[208:211], v[132:135]\n"
"ds_read_b128 a[68:71], v170 offset:36864\n"
"v_exp_f32 v72, v72\n"
"v_mfma_f32_16x16x32_bf16 v[116:119], a[72:75], v[208:211], v[132:135]\n"
"ds_read_b128 a[72:75], v170 offset:40960\n"
"v_mfma_f32_16x16x32_bf16 v[124:127], a[76:79], v[208:211], v[132:135]\n"
"s_waitcnt lgkmcnt(3)\n"
"ds_read_b128 a[76:79], v170 offset:45056\n"
"v_exp_f32 v73, v73\n"
"v_mfma_f32_16x16x32_bf16 v[96:99], a[80:83], v[196:199], v[96:99]\n"
"s_add_u32 m0, s60, 65536\n"
"v_cvt_pk_bf16_f32 v145, v66, v67\n"
"v_exp_f32 v74, v74\n"
"v_mfma_f32_16x16x32_bf16 v[104:107], a[84:87], v[196:199], v[104:107]\n"
"buffer_load_dwordx4 v2, s[4:7], s67 offen offset:0 nt lds\n"
"v_mfma_f32_16x16x32_bf16 v[112:115], a[88:91], v[196:199], v[112:115]\n"
"v_exp_f32 v75, v75\n"
"v_mfma_f32_16x16x32_bf16 v[120:123], a[92:95], v[196:199], v[120:123]\n"
"v_cvt_pk_bf16_f32 v146, v72, v73\n"
"v_exp_f32 v68, v68\n"
"v_mfma_f32_16x16x32_bf16 v[100:103], a[80:83], v[212:215], v[100:103]\n"
"ds_read_b128 a[80:83], v171 offset:32768\n"
"v_mfma_f32_16x16x32_bf16 v[108:111], a[84:87], v[212:215], v[108:111]\n"
"ds_read_b128 a[84:87], v171 offset:36864\n"
"buffer_load_dwordx4 v3, s[4:7], s67 offen offset:1024 nt lds\n"
"v_exp_f32 v69, v69\n"
"v_mfma_f32_16x16x32_bf16 v[116:119], a[88:91], v[212:215], v[116:119]\n"
"ds_read_b128 a[88:91], v171 offset:40960\n"
"v_cvt_pk_bf16_f32 v147, v74, v75\n"
"v_exp_f32 v70, v70\n"
"v_mfma_f32_16x16x32_bf16 v[124:127], a[92:95], v[212:215], v[124:127]\n"
"s_waitcnt lgkmcnt(3)\n"
"ds_read_b128 a[92:95], v171 offset:45056\n"
"v_mfma_f32_16x16x32_bf16 v[96:99], a[64:67], v[200:203], v[96:99]\n"
"ds_read_b64_tr_b16 a[112:113], v172 offset:17408\n"
"v_exp_f32 v71, v71\n"
"v_mfma_f32_16x16x32_bf16 v[104:107], a[68:71], v[200:203], v[104:107]\n"
"ds_read_b64_tr_b16 a[114:115], v172 offset:21504\n"
"buffer_load_dwordx4 v4, s[4:7], s67 offen offset:2048 nt lds\n"
"v_cvt_pk_bf16_f32 v148, v68, v69\n"
"v_mfma_f32_16x16x32_bf16 v[112:115], a[72:75], v[200:203], v[112:115]\n"
"ds_read_b64_tr_b16 a[116:117], v173 offset:17408\n"
"v_exp_f32 v76, v76\n"
"v_mfma_f32_16x16x32_bf16 v[120:123], a[76:79], v[200:203], v[120:123]\n"
"ds_read_b64_tr_b16 a[118:119], v173 offset:21504\n"
"v_exp_f32 v77, v77\n"
"v_mfma_f32_16x16x32_bf16 v[100:103], a[64:67], v[216:219], v[100:103]\n"
"ds_read_b64_tr_b16 a[120:121], v172 offset:17920\n"
"v_cvt_pk_bf16_f32 v149, v70, v71\n"
"v_exp_f32 v78, v78\n"
"v_mfma_f32_16x16x32_bf16 v[108:111], a[68:71], v[216:219], v[108:111]\n"
"ds_read_b64_tr_b16 a[122:123], v172 offset:22016\n"
"buffer_load_dwordx4 v5, s[4:7], s67 offen offset:3072 nt lds\n"
"v_exp_f32 v79, v79\n"
"v_mfma_f32_16x16x32_bf16 v[116:119], a[72:75], v[216:219], v[116:119]\n"
"ds_read_b64_tr_b16 a[124:125], v173 offset:17920\n"
"v_cvt_pk_bf16_f32 v150, v76, v77\n"
"v_mfma_f32_16x16x32_bf16 v[124:127], a[76:79], v[216:219], v[124:127]\n"
"s_waitcnt lgkmcnt(4)\n"
"ds_read_b64_tr_b16 a[126:127], v173 offset:22016\n"
"v_exp_f32 v80, v80\n"
"v_mfma_f32_16x16x32_bf16 v[96:99], a[80:83], v[204:207], v[96:99]\n"
"v_exp_f32 v81, v81\n"
"v_mfma_f32_16x16x32_bf16 v[104:107], a[84:87], v[204:207], v[104:107]\n"
"v_cvt_pk_bf16_f32 v151, v78, v79\n"
"v_mfma_f32_16x16x32_bf16 v[112:115], a[88:91], v[204:207], v[112:115]\n"
"v_exp_f32 v82, v82\n"
"v_mfma_f32_16x16x32_bf16 v[120:123], a[92:95], v[204:207], v[120:123]\n"
"v_exp_f32 v83, v83\n"
"v_mfma_f32_16x16x32_bf16 v[100:103], a[80:83], v[220:223], v[100:103]\n"
"v_cvt_pk_bf16_f32 v152, v80, v81\n"
"v_mfma_f32_16x16x32_bf16 v[108:111], a[84:87], v[220:223], v[108:111]\n"
"v_exp_f32 v88, v88\n"
"v_mfma_f32_16x16x32_bf16 v[116:119], a[88:91], v[220:223], v[116:119]\n"
"v_exp_f32 v89, v89\n"
"v_mfma_f32_16x16x32_bf16 v[124:127], a[92:95], v[220:223], v[124:127]\n"
"v_cvt_pk_bf16_f32 v153, v82, v83\n"
"s_waitcnt vmcnt(20)\n"
"s_barrier\n"
"s_sleep 2\n"
".LAbar9:\n"
"v_mfma_f32_16x16x32_bf16 a[160:163], a[96:99], v[144:147], 0\n"
"ds_read_b128 a[64:67], v168 offset:49152\n"
"s_add_u32 m0, s60, 81920\n"
"v_exp_f32 v90, v90\n"
"v_mfma_f32_16x16x32_bf16 a[168:171], a[100:103], v[144:147], 0\n"
"ds_read_b128 a[68:71], v168 offset:53248\n"
"buffer_load_dwordx4 v2, s[8:11], s66 offen offset:0 nt lds\n"
"v_mfma_f32_16x16x32_bf16 a[176:179], a[104:107], v[144:147], 0\n"
"ds_read_b128 a[72:75], v168 offset:57344\n"
"v_exp_f32 v91, v91\n"
"v_mfma_f32_16x16x32_bf16 a[184:187], a[108:111], v[144:147], 0\n"
"ds_read_b128 a[76:79], v168 offset:61440\n"
"v_cvt_pk_bf16_f32 v154, v88, v89\n"
"v_mfma_f32_16x16x32_bf16 a[164:167], a[96:99], v[148:151], 0\n"
"ds_read_b64_tr_b16 a[96:97], v172 offset:24576\n"
"v_exp_f32 v84, v84\n"
"v_mfma_f32_16x16x32_bf16 a[172:175], a[100:103], v[148:151], 0\n"
"ds_read_b64_tr_b16 a[98:99], v172 offset:28672\n"
"buffer_load_dwordx4 v3, s[8:11], s66 offen offset:1024 nt lds\n"
"v_mfma_f32_16x16x32_bf16 a[180:183], a[104:107], v[148:151], 0\n"
"ds_read_b64_tr_b16 a[100:101], v173 offset:24576\n"
"v_exp_f32 v85, v85\n"
"v_mfma_f32_16x16x32_bf16 a[188:191], a[108:111], v[148:151], 0\n"
"ds_read_b64_tr_b16 a[102:103], v173 offset:28672\n"
"v_cvt_pk_bf16_f32 v155, v90, v91\n"
"v_mfma_f32_16x16x32_bf16 v[136:139], v[176:179], v[144:147], 0\n"
"ds_read_b64_tr_b16 a[104:105], v172 offset:25088\n"
"v_exp_f32 v86, v86\n"
"v_mfma_f32_16x16x32_bf16 a[192:195], a[112:115], v[144:147], 0\n"
"s_waitcnt lgkmcnt(4)\n"
"ds_read_b64_tr_b16 a[106:107], v172 offset:29184\n"
"buffer_load_dwordx4 v4, s[8:11], s66 offen offset:2048 nt lds\n"
"v_mfma_f32_16x16x32_bf16 a[200:203], a[116:119], v[144:147], 0\n"
"ds_read_b64_tr_b16 a[108:109], v173 offset:25088\n"
"v_exp_f32 v87, v87\n"
"v_mfma_f32_16x16x32_bf16 a[208:211], a[120:123], v[144:147], 0\n"
"ds_read_b64_tr_b16 a[110:111], v173 offset:29184\n"
"v_cvt_pk_bf16_f32 v156, v84, v85\n"
"v_mfma_f32_16x16x32_bf16 a[216:219], a[124:127], v[144:147], 0\n"
"v_exp_f32 v92, v92\n"
"v_mfma_f32_16x16x32_bf16 a[196:199], a[112:115], v[148:151], 0\n"
"ds_read_b64_tr_b16 a[112:113], v172 offset:25600\n"
"buffer_load_dwordx4 v5, s[8:11], s66 offen offset:3072 nt lds\n"
"v_exp_f32 v93, v93\n"
"v_mfma_f32_16x16x32_bf16 a[204:207], a[116:119], v[148:151], 0\n"
"ds_read_b64_tr_b16 a[114:115], v172 offset:29696\n"
"v_cvt_pk_bf16_f32 v157, v86, v87\n"
"v_mfma_f32_16x16x32_bf16 a[212:215], a[120:123], v[148:151], 0\n"
"ds_read_b64_tr_b16 a[116:117], v173 offset:25600\n"
"v_max3_f32 v188, v96, v97, v98\n"
"v_mfma_f32_16x16x32_bf16 a[220:223], a[124:127], v[148:151], 0\n"
"ds_read_b64_tr_b16 a[118:119], v173 offset:29696\n"
"v_max3_f32 v189, v100, v101, v102\n"
"v_exp_f32 v94, v94\n"
"v_mfma_f32_16x16x32_bf16 v[140:143], v[176:179], v[148:151], 0\n"
"s_waitcnt lgkmcnt(4)\n"
"ds_read_b64_tr_b16 a[120:121], v172 offset:26112\n"
"v_max3_f32 v188, v188, v99, v104\n"
"v_exp_f32 v95, v95\n"
"v_mfma_f32_16x16x32_bf16 a[160:163], a[96:99], v[152:155], a[160:163]\n"
"ds_read_b64_tr_b16 a[122:123], v172 offset:30208\n"
"v_cvt_pk_bf16_f32 v158, v92, v93\n"
"v_mfma_f32_16x16x32_bf16 a[168:171], a[100:103], v[152:155], a[168:171]\n"
"ds_read_b64_tr_b16 a[124:125], v173 offset:26112\n"
"v_cvt_pk_bf16_f32 v159, v94, v95\n"
"v_mfma_f32_16x16x32_bf16 a[176:179], a[104:107], v[152:155], a[176:179]\n"
"ds_read_b64_tr_b16 a[126:127], v173 offset:30208\n"
"v_max3_f32 v189, v189, v103, v108\n"
"v_mfma_f32_16x16x32_bf16 a[184:187], a[108:111], v[152:155], a[184:187]\n"
"v_max3_f32 v188, v188, v105, v106\n"
"v_max3_f32 v189, v189, v109, v110\n"
"v_mfma_f32_16x16x32_bf16 a[164:167], a[96:99], v[156:159], a[164:167]\n"
"ds_read_b128 a[80:83], v169 offset:49152\n"
"v_max3_f32 v188, v188, v107, v112\n"
"v_mfma_f32_16x16x32_bf16 a[172:175], a[100:103], v[156:159], a[172:175]\n"
"ds_read_b128 a[84:87], v169 offset:53248\n"
"v_max3_f32 v189, v189, v111, v116\n"
"v_mfma_f32_16x16x32_bf16 a[180:183], a[104:107], v[156:159], a[180:183]\n"
"ds_read_b128 a[88:91], v169 offset:57344\n"
"v_max3_f32 v188, v188, v113, v114\n"
"v_mfma_f32_16x16x32_bf16 a[188:191], a[108:111], v[156:159], a[188:191]\n"
"ds_read_b128 a[92:95], v169 offset:61440\n"
"v_max3_f32 v189, v189, v117, v118\n"
"v_mfma_f32_16x16x32_bf16 v[136:139], v[176:179], v[152:155], v[136:139]\n"
"s_waitcnt lgkmcnt(4)\n"
"ds_read_b64_tr_b16 a[96:97], v172 offset:32768\n"
"v_max3_f32 v188, v188, v115, v120\n"
"v_mfma_f32_16x16x32_bf16 a[192:195], a[112:115], v[152:155], a[192:195]\n"
"ds_read_b64_tr_b16 a[98:99], v172 offset:36864\n"
"v_max3_f32 v189, v189, v119, v124\n"
"v_mfma_f32_16x16x32_bf16 a[200:203], a[116:119], v[152:155], a[200:203]\n"
"ds_read_b64_tr_b16 a[100:101], v173 offset:32768\n"
"v_max3_f32 v188, v188, v121, v122\n"
"v_mfma_f32_16x16x32_bf16 a[208:211], a[120:123], v[152:155], a[208:211]\n"
"ds_read_b64_tr_b16 a[102:103], v173 offset:36864\n"
"v_max3_f32 v189, v189, v125, v126\n"
"v_mfma_f32_16x16x32_bf16 a[216:219], a[124:127], v[152:155], a[216:219]\n"
"ds_read_b64_tr_b16 a[104:105], v172 offset:33280\n"
"v_max3_f32 v190, v188, v123, v127\n"
"v_mfma_f32_16x16x32_bf16 a[196:199], a[112:115], v[156:159], a[196:199]\n"
"ds_read_b64_tr_b16 a[106:107], v172 offset:37376\n"
"v_max_f32 v190, v190, v189\n"
"v_mfma_f32_16x16x32_bf16 a[204:207], a[116:119], v[156:159], a[204:207]\n"
"ds_read_b64_tr_b16 a[108:109], v173 offset:33280\n"
"v_cmp_lt_f32 vcc, s59, v190\n"
"v_mfma_f32_16x16x32_bf16 a[212:215], a[120:123], v[156:159], a[212:215]\n"
"ds_read_b64_tr_b16 a[110:111], v173 offset:37376\n"
"v_mfma_f32_16x16x32_bf16 a[220:223], a[124:127], v[156:159], a[220:223]\n"
"v_rcp_f32 v6, v160\n"
"v_mfma_f32_16x16x32_bf16 v[140:143], v[176:179], v[156:159], v[140:143]\n"
"v_rcp_f32 v7, v164\n"
"s_cbranch_vccz .LAskip9\n"
"s_swappc_b64 s[70:71], s[74:75]\n"
".LAskip9:\n"
"v_mfma_f32_16x16x32_bf16 v[64:67], a[64:67], v[192:195], v[128:131]\n"
"v_exp_f32 v96, v96\n"
"v_mfma_f32_16x16x32_bf16 v[72:75], a[68:71], v[192:195], v[128:131]\n"
"v_exp_f32 v97, v97\n"
"v_mfma_f32_16x16x32_bf16 v[80:83], a[72:75], v[192:195], v[128:131]\n"
"v_exp_f32 v98, v98\n"
"v_mfma_f32_16x16x32_bf16 v[88:91], a[76:79], v[192:195], v[128:131]\n"
"v_exp_f32 v99, v99\n"
"v_mfma_f32_16x16x32_bf16 v[68:71], a[64:67], v[208:211], v[132:135]\n"
"ds_read_b128 a[64:67], v170 offset:49152\n"
"v_cvt_pk_bf16_f32 v144, v96, v97\n"
"v_mfma_f32_16x16x32_bf16 v[76:79], a[68:71], v[208:211], v[132:135]\n"
"ds_read_b128 a[68:71], v170 offset:53248\n"
"v_accvgpr_read_b32 v16, a0\n"
"v_exp_f32 v104, v104\n"
"v_mfma_f32_16x16x32_bf16 v[84:87], a[72:75], v[208:211], v[132:135]\n"
"ds_read_b128 a[72:75], v170 offset:57344\n"
"v_accvgpr_read_b32 v17, a1\n"
"v_mfma_f32_16x16x32_bf16 v[92:95], a[76:79], v[208:211], v[132:135]\n"
"s_waitcnt lgkmcnt(3)\n"
"ds_read_b128 a[76:79], v170 offset:61440\n"
"v_accvgpr_read_b32 v18, a2\n"
"v_exp_f32 v105, v105\n"
"v_mfma_f32_16x16x32_bf16 v[64:67], a[80:83], v[196:199], v[64:67]\n"
"s_add_u32 m0, s60, 98304\n"
"v_cvt_pk_bf16_f32 v145, v98, v99\n"
"v_exp_f32 v106, v106\n"
"v_mfma_f32_16x16x32_bf16 v[72:75], a[84:87], v[196:199], v[72:75]\n"
"buffer_load_dwordx4 v2, s[4:7], s68 offen offset:0 nt lds\n"
"v_accvgpr_read_b32 v19, a3\n"
"v_mfma_f32_16x16x32_bf16 v[80:83], a[88:91], v[196:199], v[80:83]\n"
"v_exp_f32 v107, v107\n"
"v_mfma_f32_16x16x32_bf16 v[88:91], a[92:95], v[196:199], v[88:91]\n"
"v_cvt_pk_bf16_f32 v146, v104, v105\n"
"v_accvgpr_read_b32 v20, a8\n"
"v_exp_f32 v100, v100\n"
"v_mfma_f32_16x16x32_bf16 v[68:71], a[80:83], v[212:215], v[68:71]\n"
"ds_read_b128 a[80:83], v171 offset:49152\n"
"v_accvgpr_read_b32 v21, a9\n"
"v_mfma_f32_16x16x32_bf16 v[76:79], a[84:87], v[212:215], v[76:79]\n"
"ds_read_b128 a[84:87], v171 offset:53248\n"
"buffer_load_dwordx4 v3, s[4:7], s68 offen offset:1024 nt lds\n"
"v_exp_f32 v101, v101\n"
"v_mfma_f32_16x16x32_bf16 v[84:87], a[88:91], v[212:215], v[84:87]\n"
"ds_read_b128 a[88:91], v171 offset:57344\n"
"v_cvt_pk_bf16_f32 v147, v106, v107\n"
"v_exp_f32 v102, v102\n"
"v_mfma_f32_16x16x32_bf16 v[92:95], a[92:95], v[212:215], v[92:95]\n"
"s_waitcnt lgkmcnt(3)\n"
"ds_read_b128 a[92:95], v171 offset:61440\n"
"v_accvgpr_read_b32 v22, a10\n"
"v_mfma_f32_16x16x32_bf16 v[64:67], a[64:67], v[200:203], v[64:67]\n"
"ds_read_b64_tr_b16 a[112:113], v172 offset:33792\n"
"v_add_u32 v168, 0x10000, v168\n"
"v_exp_f32 v103, v103\n"
"v_mfma_f32_16x16x32_bf16 v[72:75], a[68:71], v[200:203], v[72:75]\n"
"ds_read_b64_tr_b16 a[114:115], v172 offset:37888\n"
"buffer_load_dwordx4 v4, s[4:7], s68 offen offset:2048 nt lds\n"
"v_cvt_pk_bf16_f32 v148, v100, v101\n"
"v_mfma_f32_16x16x32_bf16 v[80:83], a[72:75], v[200:203], v[80:83]\n"
"ds_read_b64_tr_b16 a[116:117], v173 offset:33792\n"
"v_add_u32 v169, 0x10000, v169\n"
"v_exp_f32 v108, v108\n"
"v_mfma_f32_16x16x32_bf16 v[88:91], a[76:79], v[200:203], v[88:91]\n"
"ds_read_b64_tr_b16 a[118:119], v173 offset:37888\n"
"v_add_u32 v170, 0x10000, v170\n"
"v_exp_f32 v109, v109\n"
"v_mfma_f32_16x16x32_bf16 v[68:71], a[64:67], v[216:219], v[68:71]\n"
"ds_read_b64_tr_b16 a[120:121], v172 offset:34304\n"
"v_add_u32 v171, 0x10000, v171\n"
"v_cvt_pk_bf16_f32 v149, v102, v103\n"
"v_mfma_f32_16x16x32_bf16 v[76:79], a[68:71], v[216:219], v[76:79]\n"
"ds_read_b64_tr_b16 a[122:123], v172 offset:38400\n"
"buffer_load_dwordx4 v5, s[4:7], s68 offen offset:3072 nt lds\n"
"v_exp_f32 v110, v110\n"
"v_mfma_f32_16x16x32_bf16 v[84:87], a[72:75], v[216:219], v[84:87]\n"
"ds_read_b64_tr_b16 a[124:125], v173 offset:34304\n"
"v_accvgpr_read_b32 v23, a11\n"
"v_exp_f32 v111, v111\n"
"v_mfma_f32_16x16x32_bf16 v[92:95], a[76:79], v[216:219], v[92:95]\n"
"s_waitcnt lgkmcnt(3)\n"
"ds_read_b64_tr_b16 a[126:127], v173 offset:38400\n"
"v_cvt_pk_bf16_f32 v150, v108, v109\n"
"v_exp_f32 v112, v112\n"
"v_mfma_f32_16x16x32_bf16 v[64:67], a[80:83], v[204:207], v[64:67]\n"
"v_exp_f32 v113, v113\n"
"v_mfma_f32_16x16x32_bf16 v[72:75], a[84:87], v[204:207], v[72:75]\n"
"v_cvt_pk_bf16_f32 v151, v110, v111\n"
"v_mul_f32 v16, v16, v6\n"
"v_mfma_f32_16x16x32_bf16 v[80:83], a[88:91], v[204:207], v[80:83]\n"
"v_exp_f32 v114, v114\n"
"v_mfma_f32_16x16x32_bf16 v[88:91], a[92:95], v[204:207], v[88:91]\n"
"v_exp_f32 v115, v115\n"
"v_mfma_f32_16x16x32_bf16 v[68:71], a[80:83], v[220:223], v[68:71]\n"
"v_cvt_pk_bf16_f32 v152, v112, v113\n"
"v_mul_f32 v17, v17, v6\n"
"v_mfma_f32_16x16x32_bf16 v[76:79], a[84:87], v[220:223], v[76:79]\n"
"v_exp_f32 v120, v120\n"
"v_mfma_f32_16x16x32_bf16 v[84:87], a[88:91], v[220:223], v[84:87]\n"
"v_exp_f32 v121, v121\n"
"v_mfma_f32_16x16x32_bf16 v[92:95], a[92:95], v[220:223], v[92:95]\n"
"v_cvt_pk_bf16_f32 v153, v114, v115\n"
"v_mul_f32 v18, v18, v6\n"
"s_waitcnt vmcnt(20)\n"
"s_barrier\n"
"s_sleep 2\n"
".LAbar10:\n"
"v_mfma_f32_16x16x32_bf16 a[160:163], a[96:99], v[144:147], a[160:163]\n"
"ds_read_b128 a[64:67], v168 offset:0\n"
"s_add_u32 m0, s60, 114688\n"
"v_exp_f32 v122, v122\n"
"v_mfma_f32_16x16x32_bf16 a[168:171], a[100:103], v[144:147], a[168:171]\n"
"ds_read_b128 a[68:71], v168 offset:4096\n"
"buffer_load_dwordx4 v2, s[8:11], s67 offen offset:0 nt lds\n"
"v_mfma_f32_16x16x32_bf16 a[176:179], a[104:107], v[144:147], a[176:179]\n"
"ds_read_b128 a[72:75], v168 offset:8192\n"
"v_mul_f32 v19, v19, v6\n"
"v_exp_f32 v123, v123\n"
"v_mfma_f32_16x16x32_bf16 a[184:187], a[108:111], v[144:147], a[184:187]\n"
"ds_read_b128 a[76:79], v168 offset:12288\n"
"v_cvt_pk_bf16_f32 v154, v120, v121\n"
"v_mfma_f32_16x16x32_bf16 a[164:167], a[96:99], v[148:151], a[164:167]\n"
"ds_read_b64_tr_b16 a[96:97], v172 offset:40960\n"
"v_mul_f32 v20, v20, v6\n"
"v_exp_f32 v116, v116\n"
"v_mfma_f32_16x16x32_bf16 a[172:175], a[100:103], v[148:151], a[172:175]\n"
"ds_read_b64_tr_b16 a[98:99], v172 offset:45056\n"
"buffer_load_dwordx4 v3, s[8:11], s67 offen offset:1024 nt lds\n"
"v_mfma_f32_16x16x32_bf16 a[180:183], a[104:107], v[148:151], a[180:183]\n"
"ds_read_b64_tr_b16 a[100:101], v173 offset:40960\n"
"v_mul_f32 v21, v21, v6\n"
"v_exp_f32 v117, v117\n"
"v_mfma_f32_16x16x32_bf16 a[188:191], a[108:111], v[148:151], a[188:191]\n"
"ds_read_b64_tr_b16 a[102:103], v173 offset:45056\n"
"v_cvt_pk_bf16_f32 v155, v122, v123\n"
"v_mfma_f32_16x16x32_bf16 v[136:139], v[176:179], v[144:147], v[136:139]\n"
"ds_read_b64_tr_b16 a[104:105], v172 offset:41472\n"
"v_mul_f32 v22, v22, v6\n"
"v_exp_f32 v118, v118\n"
"v_mfma_f32_16x16x32_bf16 a[192:195], a[112:115], v[144:147], a[192:195]\n"
"ds_read_b64_tr_b16 a[106:107], v172 offset:45568\n"
"buffer_load_dwordx4 v4, s[8:11], s67 offen offset:2048 nt lds\n"
"v_mfma_f32_16x16x32_bf16 a[200:203], a[116:119], v[144:147], a[200:203]\n"
"s_waitcnt lgkmcnt(4)\n"
"ds_read_b64_tr_b16 a[108:109], v173 offset:41472\n"
"v_mul_f32 v23, v23, v6\n"
"v_exp_f32 v119, v119\n"
"v_mfma_f32_16x16x32_bf16 a[208:211], a[120:123], v[144:147], a[208:211]\n"
"ds_read_b64_tr_b16 a[110:111], v173 offset:45568\n"
"v_cvt_pk_bf16_f32 v156, v116, v117\n"
"v_mfma_f32_16x16x32_bf16 a[216:219], a[124:127], v[144:147], a[216:219]\n"
"v_exp_f32 v124, v124\n"
"v_mfma_f32_16x16x32_bf16 a[196:199], a[112:115], v[148:151], a[196:199]\n"
"ds_read_b64_tr_b16 a[112:113], v172 offset:41984\n"
"buffer_load_dwordx4 v5, s[8:11], s67 offen offset:3072 nt lds\n"
"v_exp_f32 v125, v125\n"
"v_mfma_f32_16x16x32_bf16 a[204:207], a[116:119], v[148:151], a[204:207]\n"
"ds_read_b64_tr_b16 a[114:115], v172 offset:46080\n"
"v_cvt_pk_bf16_f32 v157, v118, v119\n"
"v_mfma_f32_16x16x32_bf16 a[212:215], a[120:123], v[148:151], a[212:215]\n"
"ds_read_b64_tr_b16 a[116:117], v173 offset:41984\n"
"v_max3_f32 v188, v64, v65, v66\n"
"v_mfma_f32_16x16x32_bf16 a[220:223], a[124:127], v[148:151], a[220:223]\n"
"ds_read_b64_tr_b16 a[118:119], v173 offset:46080\n"
"v_max3_f32 v189, v68, v69, v70\n"
"v_exp_f32 v126, v126\n"
"v_mfma_f32_16x16x32_bf16 v[140:143], v[176:179], v[148:151], v[140:143]\n"
"ds_read_b64_tr_b16 a[120:121], v172 offset:42496\n"
"v_max3_f32 v188, v188, v67, v72\n"
"v_exp_f32 v127, v127\n"
"v_mfma_f32_16x16x32_bf16 a[160:163], a[96:99], v[152:155], a[160:163]\n"
"s_waitcnt lgkmcnt(4)\n"
"ds_read_b64_tr_b16 a[122:123], v172 offset:46592\n"
"v_cvt_pk_bf16_f32 v158, v124, v125\n"
"v_mfma_f32_16x16x32_bf16 a[168:171], a[100:103], v[152:155], a[168:171]\n"
"ds_read_b64_tr_b16 a[124:125], v173 offset:42496\n"
"v_cvt_pk_bf16_f32 v159, v126, v127\n"
"v_mfma_f32_16x16x32_bf16 a[176:179], a[104:107], v[152:155], a[176:179]\n"
"ds_read_b64_tr_b16 a[126:127], v173 offset:46592\n"
"v_max3_f32 v189, v189, v71, v76\n"
"v_mfma_f32_16x16x32_bf16 a[184:187], a[108:111], v[152:155], a[184:187]\n"
"v_max3_f32 v188, v188, v73, v74\n"
"v_max3_f32 v189, v189, v77, v78\n"
"v_mfma_f32_16x16x32_bf16 a[164:167], a[96:99], v[156:159], a[164:167]\n"
"ds_read_b128 a[80:83], v169 offset:0\n"
"v_max3_f32 v188, v188, v75, v80\n"
"v_mfma_f32_16x16x32_bf16 a[172:175], a[100:103], v[156:159], a[172:175]\n"
"ds_read_b128 a[84:87], v169 offset:4096\n"
"v_max3_f32 v189, v189, v79, v84\n"
"v_mfma_f32_16x16x32_bf16 a[180:183], a[104:107], v[156:159], a[180:183]\n"
"ds_read_b128 a[88:91], v169 offset:8192\n"
"v_max3_f32 v188, v188, v81, v82\n"
"v_mfma_f32_16x16x32_bf16 a[188:191], a[108:111], v[156:159], a[188:191]\n"
"ds_read_b128 a[92:95], v169 offset:12288\n"
"v_max3_f32 v189, v189, v85, v86\n"
"v_mfma_f32_16x16x32_bf16 v[136:139], v[176:179], v[152:155], v[136:139]\n"
"s_waitcnt lgkmcnt(4)\n"
"ds_read_b64_tr_b16 a[96:97], v172 offset:49152\n"
"v_max3_f32 v188, v188, v83, v88\n"
"v_mfma_f32_16x16x32_bf16 a[192:195], a[112:115], v[152:155], a[192:195]\n"
"ds_read_b64_tr_b16 a[98:99], v172 offset:53248\n"
"v_max3_f32 v189, v189, v87, v92\n"
"v_mfma_f32_16x16x32_bf16 a[200:203], a[116:119], v[152:155], a[200:203]\n"
"ds_read_b64_tr_b16 a[100:101], v173 offset:49152\n"
"v_max3_f32 v188, v188, v89, v90\n"
"v_mfma_f32_16x16x32_bf16 a[208:211], a[120:123], v[152:155], a[208:211]\n"
"ds_read_b64_tr_b16 a[102:103], v173 offset:53248\n"
"v_max3_f32 v189, v189, v93, v94\n"
"v_mfma_f32_16x16x32_bf16 a[216:219], a[124:127], v[152:155], a[216:219]\n"
"ds_read_b64_tr_b16 a[104:105], v172 offset:49664\n"
"v_max3_f32 v190, v188, v91, v95\n"
"v_mfma_f32_16x16x32_bf16 a[196:199], a[112:115], v[156:159], a[196:199]\n"
"ds_read_b64_tr_b16 a[106:107], v172 offset:53760\n"
"v_max_f32 v190, v190, v189\n"
"v_mfma_f32_16x16x32_bf16 a[204:207], a[116:119], v[156:159], a[204:207]\n"
"ds_read_b64_tr_b16 a[108:109], v173 offset:49664\n"
"v_cmp_lt_f32 vcc, s59, v190\n"
"v_mfma_f32_16x16x32_bf16 a[212:215], a[120:123], v[156:159], a[212:215]\n"
"ds_read_b64_tr_b16 a[110:111], v173 offset:53760\n"
"v_cvt_pk_bf16_f32 v32, v16, v17\n"
"v_mfma_f32_16x16x32_bf16 a[220:223], a[124:127], v[156:159], a[220:223]\n"
"v_cvt_pk_bf16_f32 v33, v18, v19\n"
"v_cvt_pk_bf16_f32 v34, v20, v21\n"
"v_mfma_f32_16x16x32_bf16 v[140:143], v[176:179], v[156:159], v[140:143]\n"
"v_cvt_pk_bf16_f32 v35, v22, v23\n"
"v_accvgpr_read_b32 v24, a4\n"
"s_cbranch_vccz .LAskip10\n"
"s_swappc_b64 s[70:71], s[72:73]\n"
".LAskip10:\n"
"v_mfma_f32_16x16x32_bf16 v[96:99], a[64:67], v[192:195], v[128:131]\n"
"v_exp_f32 v64, v64\n"
"v_mfma_f32_16x16x32_bf16 v[104:107], a[68:71], v[192:195], v[128:131]\n"
"v_exp_f32 v65, v65\n"
"v_mfma_f32_16x16x32_bf16 v[112:115], a[72:75], v[192:195], v[128:131]\n"
"v_exp_f32 v66, v66\n"
"v_mfma_f32_16x16x32_bf16 v[120:123], a[76:79], v[192:195], v[128:131]\n"
"v_exp_f32 v67, v67\n"
"v_mfma_f32_16x16x32_bf16 v[100:103], a[64:67], v[208:211], v[132:135]\n"
"ds_read_b128 a[64:67], v170 offset:0\n"
"v_cvt_pk_bf16_f32 v144, v64, v65\n"
"v_mfma_f32_16x16x32_bf16 v[108:111], a[68:71], v[208:211], v[132:135]\n"
"ds_read_b128 a[68:71], v170 offset:4096\n"
"v_accvgpr_read_b32 v25, a5\n"
"v_exp_f32 v72, v72\n"
"v_mfma_f32_16x16x32_bf16 v[116:119], a[72:75], v[208:211], v[132:135]\n"
"ds_read_b128 a[72:75], v170 offset:8192\n"
"v_accvgpr_read_b32 v26, a6\n"
"v_mfma_f32_16x16x32_bf16 v[124:127], a[76:79], v[208:211], v[132:135]\n"
"s_waitcnt lgkmcnt(3)\n"
"ds_read_b128 a[76:79], v170 offset:12288\n"
"v_accvgpr_read_b32 v27, a7\n"
"v_exp_f32 v73, v73\n"
"v_mfma_f32_16x16x32_bf16 v[96:99], a[80:83], v[196:199], v[96:99]\n"
"v_cvt_pk_bf16_f32 v145, v66, v67\n"
"v_accvgpr_read_b32 v28, a12\n"
"v_exp_f32 v74, v74\n"
"v_mfma_f32_16x16x32_bf16 v[104:107], a[84:87], v[196:199], v[104:107]\n"
"v_exp_f32 v75, v75\n"
"v_mfma_f32_16x16x32_bf16 v[112:115], a[88:91], v[196:199], v[112:115]\n"
"v_cvt_pk_bf16_f32 v146, v72, v73\n"
"v_accvgpr_read_b32 v29, a13\n"
"v_mfma_f32_16x16x32_bf16 v[120:123], a[92:95], v[196:199], v[120:123]\n"
"v_exp_f32 v68, v68\n"
"v_mfma_f32_16x16x32_bf16 v[100:103], a[80:83], v[212:215], v[100:103]\n"
"ds_read_b128 a[80:83], v171 offset:0\n"
"v_accvgpr_read_b32 v30, a14\n"
"v_mfma_f32_16x16x32_bf16 v[108:111], a[84:87], v[212:215], v[108:111]\n"
"ds_read_b128 a[84:87], v171 offset:4096\n"
"v_accvgpr_read_b32 v31, a15\n"
"v_exp_f32 v69, v69\n"
"v_mfma_f32_16x16x32_bf16 v[116:119], a[88:91], v[212:215], v[116:119]\n"
"ds_read_b128 a[88:91], v171 offset:8192\n"
"v_cvt_pk_bf16_f32 v147, v74, v75\n"
"v_exp_f32 v70, v70\n"
"v_mfma_f32_16x16x32_bf16 v[124:127], a[92:95], v[212:215], v[124:127]\n"
"s_waitcnt lgkmcnt(3)\n"
"ds_read_b128 a[92:95], v171 offset:12288\n"
"v_mul_f32 v24, v24, v7\n"
"v_mfma_f32_16x16x32_bf16 v[96:99], a[64:67], v[200:203], v[96:99]\n"
"ds_read_b64_tr_b16 a[112:113], v172 offset:50176\n"
"v_mul_f32 v25, v25, v7\n"
"v_exp_f32 v71, v71\n"
"v_mfma_f32_16x16x32_bf16 v[104:107], a[68:71], v[200:203], v[104:107]\n"
"ds_read_b64_tr_b16 a[114:115], v172 offset:54272\n"
"v_cvt_pk_bf16_f32 v148, v68, v69\n"
"v_exp_f32 v76, v76\n"
"v_mfma_f32_16x16x32_bf16 v[112:115], a[72:75], v[200:203], v[112:115]\n"
"ds_read_b64_tr_b16 a[116:117], v173 offset:50176\n"
"v_mul_f32 v26, v26, v7\n"
"v_exp_f32 v77, v77\n"
"v_mfma_f32_16x16x32_bf16 v[120:123], a[76:79], v[200:203], v[120:123]\n"
"ds_read_b64_tr_b16 a[118:119], v173 offset:54272\n"
"v_cvt_pk_bf16_f32 v149, v70, v71\n"
"v_mfma_f32_16x16x32_bf16 v[100:103], a[64:67], v[216:219], v[100:103]\n"
"ds_read_b64_tr_b16 a[120:121], v172 offset:50688\n"
"v_mul_f32 v27, v27, v7\n"
"v_exp_f32 v78, v78\n"
"v_mfma_f32_16x16x32_bf16 v[108:111], a[68:71], v[216:219], v[108:111]\n"
"ds_read_b64_tr_b16 a[122:123], v172 offset:54784\n"
"v_mul_f32 v28, v28, v7\n"
"v_exp_f32 v79, v79\n"
"v_mfma_f32_16x16x32_bf16 v[116:119], a[72:75], v[216:219], v[116:119]\n"
"ds_read_b64_tr_b16 a[124:125], v173 offset:50688\n"
"v_cvt_pk_bf16_f32 v150, v76, v77\n"
"v_mfma_f32_16x16x32_bf16 v[124:127], a[76:79], v[216:219], v[124:127]\n"
"s_waitcnt lgkmcnt(4)\n"
"ds_read_b64_tr_b16 a[126:127], v173 offset:54784\n"
"v_mul_f32 v29, v29, v7\n"
"v_exp_f32 v80, v80\n"
"v_mfma_f32_16x16x32_bf16 v[96:99], a[80:83], v[204:207], v[96:99]\n"
"v_exp_f32 v81, v81\n"
"v_mfma_f32_16x16x32_bf16 v[104:107], a[84:87], v[204:207], v[104:107]\n"
"v_cvt_pk_bf16_f32 v151, v78, v79\n"
"v_mul_f32 v30, v30, v7\n"
"v_mfma_f32_16x16x32_bf16 v[112:115], a[88:91], v[204:207], v[112:115]\n"
"v_exp_f32 v82, v82\n"
"v_mfma_f32_16x16x32_bf16 v[120:123], a[92:95], v[204:207], v[120:123]\n"
"v_exp_f32 v83, v83\n"
"v_mfma_f32_16x16x32_bf16 v[100:103], a[80:83], v[220:223], v[100:103]\n"
"v_cvt_pk_bf16_f32 v152, v80, v81\n"
"v_mul_f32 v31, v31, v7\n"
"v_mfma_f32_16x16x32_bf16 v[108:111], a[84:87], v[220:223], v[108:111]\n"
"v_exp_f32 v88, v88\n"
"v_mfma_f32_16x16x32_bf16 v[116:119], a[88:91], v[220:223], v[116:119]\n"
"v_exp_f32 v89, v89\n"
"v_mfma_f32_16x16x32_bf16 v[124:127], a[92:95], v[220:223], v[124:127]\n"
"v_cvt_pk_bf16_f32 v153, v82, v83\n"
"v_cvt_pk_bf16_f32 v36, v24, v25\n"
"s_waitcnt vmcnt(16)\n"
"s_barrier\n"
"s_sleep 2\n"
".LAbar11:\n"
"v_mfma_f32_16x16x32_bf16 a[160:163], a[96:99], v[144:147], a[160:163]\n"
"ds_read_b128 a[64:67], v168 offset:16384\n"
"s_add_u32 m0, s60, 32768\n"
"v_exp_f32 v90, v90\n"
"v_mfma_f32_16x16x32_bf16 a[168:171], a[100:103], v[144:147], a[168:171]\n"
"ds_read_b128 a[68:71], v168 offset:20480\n"
"buffer_load_dwordx4 v2, s[4:7], s69 offen offset:0 nt lds\n"
"v_mfma_f32_16x16x32_bf16 a[176:179], a[104:107], v[144:147], a[176:179]\n"
"ds_read_b128 a[72:75], v168 offset:24576\n"
"v_cvt_pk_bf16_f32 v37, v26, v27\n"
"v_exp_f32 v91, v91\n"
"v_mfma_f32_16x16x32_bf16 a[184:187], a[108:111], v[144:147], a[184:187]\n"
"ds_read_b128 a[76:79], v168 offset:28672\n"
"v_cvt_pk_bf16_f32 v154, v88, v89\n"
"v_mfma_f32_16x16x32_bf16 a[164:167], a[96:99], v[148:151], a[164:167]\n"
"ds_read_b64_tr_b16 a[96:97], v172 offset:57344\n"
"v_cvt_pk_bf16_f32 v38, v28, v29\n"
"v_exp_f32 v84, v84\n"
"v_mfma_f32_16x16x32_bf16 a[172:175], a[100:103], v[148:151], a[172:175]\n"
"ds_read_b64_tr_b16 a[98:99], v172 offset:61440\n"
"buffer_load_dwordx4 v3, s[4:7], s69 offen offset:1024 nt lds\n"
"v_mfma_f32_16x16x32_bf16 a[180:183], a[104:107], v[148:151], a[180:183]\n"
"ds_read_b64_tr_b16 a[100:101], v173 offset:57344\n"
"v_cvt_pk_bf16_f32 v39, v30, v31\n"
"v_exp_f32 v85, v85\n"
"v_mfma_f32_16x16x32_bf16 a[188:191], a[108:111], v[148:151], a[188:191]\n"
"ds_read_b64_tr_b16 a[102:103], v173 offset:61440\n"
"v_cvt_pk_bf16_f32 v155, v90, v91\n"
"v_mfma_f32_16x16x32_bf16 v[136:139], v[176:179], v[144:147], v[136:139]\n"
"ds_read_b64_tr_b16 a[104:105], v172 offset:57856\n"
"v_accvgpr_read_b32 v16, a16\n"
"v_exp_f32 v86, v86\n"
"v_mfma_f32_16x16x32_bf16 a[192:195], a[112:115], v[144:147], a[192:195]\n"
"s_waitcnt lgkmcnt(4)\n"
"ds_read_b64_tr_b16 a[106:107], v172 offset:61952\n"
"buffer_load_dwordx4 v4, s[4:7], s69 offen offset:2048 nt lds\n"
"v_mfma_f32_16x16x32_bf16 a[200:203], a[116:119], v[144:147], a[200:203]\n"
"ds_read_b64_tr_b16 a[108:109], v173 offset:57856\n"
"v_accvgpr_read_b32 v17, a17\n"
"v_exp_f32 v87, v87\n"
"v_mfma_f32_16x16x32_bf16 a[208:211], a[120:123], v[144:147], a[208:211]\n"
"ds_read_b64_tr_b16 a[110:111], v173 offset:61952\n"
"v_cvt_pk_bf16_f32 v156, v84, v85\n"
"v_mfma_f32_16x16x32_bf16 a[216:219], a[124:127], v[144:147], a[216:219]\n"
"v_exp_f32 v92, v92\n"
"v_mfma_f32_16x16x32_bf16 a[196:199], a[112:115], v[148:151], a[196:199]\n"
"ds_read_b64_tr_b16 a[112:113], v172 offset:58368\n"
"buffer_load_dwordx4 v5, s[4:7], s69 offen offset:3072 nt lds\n"
"v_exp_f32 v93, v93\n"
"v_mfma_f32_16x16x32_bf16 a[204:207], a[116:119], v[148:151], a[204:207]\n"
"ds_read_b64_tr_b16 a[114:115], v172 offset:62464\n"
"v_cvt_pk_bf16_f32 v157, v86, v87\n"
"v_mfma_f32_16x16x32_bf16 a[212:215], a[120:123], v[148:151], a[212:215]\n"
"ds_read_b64_tr_b16 a[116:117], v173 offset:58368\n"
"v_max3_f32 v188, v96, v97, v98\n"
"v_mfma_f32_16x16x32_bf16 a[220:223], a[124:127], v[148:151], a[220:223]\n"
"ds_read_b64_tr_b16 a[118:119], v173 offset:62464\n"
"v_max3_f32 v189, v100, v101, v102\n"
"v_exp_f32 v94, v94\n"
"v_mfma_f32_16x16x32_bf16 v[140:143], v[176:179], v[148:151], v[140:143]\n"
"s_waitcnt lgkmcnt(4)\n"
"ds_read_b64_tr_b16 a[120:121], v172 offset:58880\n"
"v_max3_f32 v188, v188, v99, v104\n"
"v_exp_f32 v95, v95\n"
"v_mfma_f32_16x16x32_bf16 a[160:163], a[96:99], v[152:155], a[160:163]\n"
"ds_read_b64_tr_b16 a[122:123], v172 offset:62976\n"
"v_cvt_pk_bf16_f32 v158, v92, v93\n"
"v_mfma_f32_16x16x32_bf16 a[168:171], a[100:103], v[152:155], a[168:171]\n"
"ds_read_b64_tr_b16 a[124:125], v173 offset:58880\n"
"v_cvt_pk_bf16_f32 v159, v94, v95\n"
"v_mfma_f32_16x16x32_bf16 a[176:179], a[104:107], v[152:155], a[176:179]\n"
"ds_read_b64_tr_b16 a[126:127], v173 offset:62976\n"
"v_max3_f32 v189, v189, v103, v108\n"
"v_mfma_f32_16x16x32_bf16 a[184:187], a[108:111], v[152:155], a[184:187]\n"
"v_max3_f32 v188, v188, v105, v106\n"
"v_max3_f32 v189, v189, v109, v110\n"
"v_mfma_f32_16x16x32_bf16 a[164:167], a[96:99], v[156:159], a[164:167]\n"
"ds_read_b128 a[80:83], v169 offset:16384\n"
"v_max3_f32 v188, v188, v107, v112\n"
"v_mfma_f32_16x16x32_bf16 a[172:175], a[100:103], v[156:159], a[172:175]\n"
"ds_read_b128 a[84:87], v169 offset:20480\n"
"v_max3_f32 v189, v189, v111, v116\n"
"v_mfma_f32_16x16x32_bf16 a[180:183], a[104:107], v[156:159], a[180:183]\n"
"ds_read_b128 a[88:91], v169 offset:24576\n"
"v_max3_f32 v188, v188, v113, v114\n"
"v_mfma_f32_16x16x32_bf16 a[188:191], a[108:111], v[156:159], a[188:191]\n"
"ds_read_b128 a[92:95], v169 offset:28672\n"
"v_max3_f32 v189, v189, v117, v118\n"
"v_mfma_f32_16x16x32_bf16 v[136:139], v[176:179], v[152:155], v[136:139]\n"
"s_waitcnt lgkmcnt(4)\n"
"ds_read_b64_tr_b16 a[96:97], v172 offset:0\n"
"v_max3_f32 v188, v188, v115, v120\n"
"v_mfma_f32_16x16x32_bf16 a[192:195], a[112:115], v[152:155], a[192:195]\n"
"ds_read_b64_tr_b16 a[98:99], v172 offset:4096\n"
"v_max3_f32 v189, v189, v119, v124\n"
"v_mfma_f32_16x16x32_bf16 a[200:203], a[116:119], v[152:155], a[200:203]\n"
"ds_read_b64_tr_b16 a[100:101], v173 offset:0\n"
"v_max3_f32 v188, v188, v121, v122\n"
"v_mfma_f32_16x16x32_bf16 a[208:211], a[120:123], v[152:155], a[208:211]\n"
"ds_read_b64_tr_b16 a[102:103], v173 offset:4096\n"
"v_max3_f32 v189, v189, v125, v126\n"
"v_mfma_f32_16x16x32_bf16 a[216:219], a[124:127], v[152:155], a[216:219]\n"
"ds_read_b64_tr_b16 a[104:105], v172 offset:512\n"
"v_max3_f32 v190, v188, v123, v127\n"
"v_mfma_f32_16x16x32_bf16 a[196:199], a[112:115], v[156:159], a[196:199]\n"
"ds_read_b64_tr_b16 a[106:107], v172 offset:4608\n"
"v_max_f32 v190, v190, v189\n"
"v_mfma_f32_16x16x32_bf16 a[204:207], a[116:119], v[156:159], a[204:207]\n"
"ds_read_b64_tr_b16 a[108:109], v173 offset:512\n"
"v_cmp_lt_f32 vcc, s59, v190\n"
"v_mfma_f32_16x16x32_bf16 a[212:215], a[120:123], v[156:159], a[212:215]\n"
"ds_read_b64_tr_b16 a[110:111], v173 offset:4608\n"
"v_accvgpr_read_b32 v18, a18\n"
"v_mfma_f32_16x16x32_bf16 a[220:223], a[124:127], v[156:159], a[220:223]\n"
"v_accvgpr_read_b32 v19, a19\n"
"v_accvgpr_read_b32 v20, a24\n"
"v_mfma_f32_16x16x32_bf16 v[140:143], v[176:179], v[156:159], v[140:143]\n"
"v_accvgpr_read_b32 v21, a25\n"
"v_accvgpr_read_b32 v22, a26\n"
"s_cbranch_vccz .LAskip11\n"
"s_swappc_b64 s[70:71], s[74:75]\n"
".LAskip11:\n"
"v_mfma_f32_16x16x32_bf16 v[64:67], a[64:67], v[192:195], v[128:131]\n"
"v_exp_f32 v96, v96\n"
"v_mfma_f32_16x16x32_bf16 v[72:75], a[68:71], v[192:195], v[128:131]\n"
"v_exp_f32 v97, v97\n"
"v_mfma_f32_16x16x32_bf16 v[80:83], a[72:75], v[192:195], v[128:131]\n"
"v_exp_f32 v98, v98\n"
"v_mfma_f32_16x16x32_bf16 v[88:91], a[76:79], v[192:195], v[128:131]\n"
"v_exp_f32 v99, v99\n"
"v_mfma_f32_16x16x32_bf16 v[68:71], a[64:67], v[208:211], v[132:135]\n"
"ds_read_b128 a[64:67], v170 offset:16384\n"
"v_cvt_pk_bf16_f32 v144, v96, v97\n"
"v_mfma_f32_16x16x32_bf16 v[76:79], a[68:71], v[208:211], v[132:135]\n"
"ds_read_b128 a[68:71], v170 offset:20480\n"
"v_accvgpr_read_b32 v23, a27\n"
"v_exp_f32 v104, v104\n"
"v_mfma_f32_16x16x32_bf16 v[84:87], a[72:75], v[208:211], v[132:135]\n"
"ds_read_b128 a[72:75], v170 offset:24576\n"
"v_mul_f32 v16, v16, v6\n"
"v_mfma_f32_16x16x32_bf16 v[92:95], a[76:79], v[208:211], v[132:135]\n"
"s_waitcnt lgkmcnt(3)\n"
"ds_read_b128 a[76:79], v170 offset:28672\n"
"v_mul_f32 v17, v17, v6\n"
"v_exp_f32 v105, v105\n"
"v_mfma_f32_16x16x32_bf16 v[64:67], a[80:83], v[196:199], v[64:67]\n"
"s_add_u32 m0, s60, 131072\n"
"v_cvt_pk_bf16_f32 v145, v98, v99\n"
"v_exp_f32 v106, v106\n"
"v_mfma_f32_16x16x32_bf16 v[72:75], a[84:87], v[196:199], v[72:75]\n"
"buffer_load_dwordx4 v2, s[8:11], s68 offen offset:0 nt lds\n"
"v_mul_f32 v18, v18, v6\n"
"v_mfma_f32_16x16x32_bf16 v[80:83], a[88:91], v[196:199], v[80:83]\n"
"v_exp_f32 v107, v107\n"
"v_mfma_f32_16x16x32_bf16 v[88:91], a[92:95], v[196:199], v[88:91]\n"
"v_cvt_pk_bf16_f32 v146, v104, v105\n"
"v_mul_f32 v19, v19, v6\n"
"v_exp_f32 v100, v100\n"
"v_mfma_f32_16x16x32_bf16 v[68:71], a[80:83], v[212:215], v[68:71]\n"
"ds_read_b128 a[80:83], v171 offset:16384\n"
"v_mul_f32 v20, v20, v6\n"
"v_mfma_f32_16x16x32_bf16 v[76:79], a[84:87], v[212:215], v[76:79]\n"
"ds_read_b128 a[84:87], v171 offset:20480\n"
"buffer_load_dwordx4 v3, s[8:11], s68 offen offset:1024 nt lds\n"
"v_exp_f32 v101, v101\n"
"v_mfma_f32_16x16x32_bf16 v[84:87], a[88:91], v[212:215], v[84:87]\n"
"ds_read_b128 a[88:91], v171 offset:24576\n"
"v_cvt_pk_bf16_f32 v147, v106, v107\n"
"v_exp_f32 v102, v102\n"
"v_mfma_f32_16x16x32_bf16 v[92:95], a[92:95], v[212:215], v[92:95]\n"
"s_waitcnt lgkmcnt(3)\n"
"ds_read_b128 a[92:95], v171 offset:28672\n"
"v_mul_f32 v21, v21, v6\n"
"v_mfma_f32_16x16x32_bf16 v[64:67], a[64:67], v[200:203], v[64:67]\n"
"ds_read_b64_tr_b16 a[112:113], v172 offset:1024\n"
"v_add_u32 v168, 0xffff0000, v168\n"
"v_exp_f32 v103, v103\n"
"v_mfma_f32_16x16x32_bf16 v[72:75], a[68:71], v[200:203], v[72:75]\n"
"ds_read_b64_tr_b16 a[114:115], v172 offset:5120\n"
"buffer_load_dwordx4 v4, s[8:11], s68 offen offset:2048 nt lds\n"
"v_cvt_pk_bf16_f32 v148, v100, v101\n"
"v_mfma_f32_16x16x32_bf16 v[80:83], a[72:75], v[200:203], v[80:83]\n"
"ds_read_b64_tr_b16 a[116:117], v173 offset:1024\n"
"v_add_u32 v169, 0xffff0000, v169\n"
"v_exp_f32 v108, v108\n"
"v_mfma_f32_16x16x32_bf16 v[88:91], a[76:79], v[200:203], v[88:91]\n"
"ds_read_b64_tr_b16 a[118:119], v173 offset:5120\n"
"v_add_u32 v170, 0xffff0000, v170\n"
"v_exp_f32 v109, v109\n"
"v_mfma_f32_16x16x32_bf16 v[68:71], a[64:67], v[216:219], v[68:71]\n"
"ds_read_b64_tr_b16 a[120:121], v172 offset:1536\n"
"v_add_u32 v171, 0xffff0000, v171\n"
"v_cvt_pk_bf16_f32 v149, v102, v103\n"
"v_mfma_f32_16x16x32_bf16 v[76:79], a[68:71], v[216:219], v[76:79]\n"
"ds_read_b64_tr_b16 a[122:123], v172 offset:5632\n"
"buffer_load_dwordx4 v5, s[8:11], s68 offen offset:3072 nt lds\n"
"v_exp_f32 v110, v110\n"
"v_mfma_f32_16x16x32_bf16 v[84:87], a[72:75], v[216:219], v[84:87]\n"
"ds_read_b64_tr_b16 a[124:125], v173 offset:1536\n"
"v_mul_f32 v22, v22, v6\n"
"v_exp_f32 v111, v111\n"
"v_mfma_f32_16x16x32_bf16 v[92:95], a[76:79], v[216:219], v[92:95]\n"
"s_waitcnt lgkmcnt(3)\n"
"ds_read_b64_tr_b16 a[126:127], v173 offset:5632\n"
"v_cvt_pk_bf16_f32 v150, v108, v109\n"
"v_exp_f32 v112, v112\n"
"v_mfma_f32_16x16x32_bf16 v[64:67], a[80:83], v[204:207], v[64:67]\n"
"v_exp_f32 v113, v113\n"
"v_mfma_f32_16x16x32_bf16 v[72:75], a[84:87], v[204:207], v[72:75]\n"
"v_cvt_pk_bf16_f32 v151, v110, v111\n"
"v_mul_f32 v23, v23, v6\n"
"v_mfma_f32_16x16x32_bf16 v[80:83], a[88:91], v[204:207], v[80:83]\n"
"v_exp_f32 v114, v114\n"
"v_mfma_f32_16x16x32_bf16 v[88:91], a[92:95], v[204:207], v[88:91]\n"
"v_exp_f32 v115, v115\n"
"v_mfma_f32_16x16x32_bf16 v[68:71], a[80:83], v[220:223], v[68:71]\n"
"v_cvt_pk_bf16_f32 v152, v112, v113\n"
"v_cvt_pk_bf16_f32 v40, v16, v17\n"
"v_mfma_f32_16x16x32_bf16 v[76:79], a[84:87], v[220:223], v[76:79]\n"
"v_exp_f32 v120, v120\n"
"v_mfma_f32_16x16x32_bf16 v[84:87], a[88:91], v[220:223], v[84:87]\n"
"v_exp_f32 v121, v121\n"
"v_mfma_f32_16x16x32_bf16 v[92:95], a[92:95], v[220:223], v[92:95]\n"
"v_cvt_pk_bf16_f32 v153, v114, v115\n"
"v_cvt_pk_bf16_f32 v41, v18, v19\n"
"s_waitcnt vmcnt(16)\n"
"s_barrier\n"
"s_sleep 2\n"
".LAbar12:\n"
"v_mfma_f32_16x16x32_bf16 a[160:163], a[96:99], v[144:147], a[160:163]\n"
"ds_read_b128 a[64:67], v168 offset:0\n"
"s_add_u32 m0, s60, 147456\n"
"v_exp_f32 v122, v122\n"
"v_mfma_f32_16x16x32_bf16 a[168:171], a[100:103], v[144:147], a[168:171]\n"
"ds_read_b128 a[68:71], v168 offset:4096\n"
"buffer_load_dwordx4 v2, s[8:11], s69 offen offset:0 nt lds\n"
"v_mfma_f32_16x16x32_bf16 a[176:179], a[104:107], v[144:147], a[176:179]\n"
"ds_read_b128 a[72:75], v168 offset:8192\n"
"v_cvt_pk_bf16_f32 v42, v20, v21\n"
"v_exp_f32 v123, v123\n"
"v_mfma_f32_16x16x32_bf16 a[184:187], a[108:111], v[144:147], a[184:187]\n"
"ds_read_b128 a[76:79], v168 offset:12288\n"
"v_cvt_pk_bf16_f32 v154, v120, v121\n"
"v_mfma_f32_16x16x32_bf16 a[164:167], a[96:99], v[148:151], a[164:167]\n"
"ds_read_b64_tr_b16 a[96:97], v172 offset:8192\n"
"v_cvt_pk_bf16_f32 v43, v22, v23\n"
"v_exp_f32 v116, v116\n"
"v_mfma_f32_16x16x32_bf16 a[172:175], a[100:103], v[148:151], a[172:175]\n"
"ds_read_b64_tr_b16 a[98:99], v172 offset:12288\n"
"buffer_load_dwordx4 v3, s[8:11], s69 offen offset:1024 nt lds\n"
"v_mfma_f32_16x16x32_bf16 a[180:183], a[104:107], v[148:151], a[180:183]\n"
"ds_read_b64_tr_b16 a[100:101], v173 offset:8192\n"
"v_accvgpr_read_b32 v24, a20\n"
"v_exp_f32 v117, v117\n"
"v_mfma_f32_16x16x32_bf16 a[188:191], a[108:111], v[148:151], a[188:191]\n"
"ds_read_b64_tr_b16 a[102:103], v173 offset:12288\n"
"v_cvt_pk_bf16_f32 v155, v122, v123\n"
"v_mfma_f32_16x16x32_bf16 v[136:139], v[176:179], v[144:147], v[136:139]\n"
"ds_read_b64_tr_b16 a[104:105], v172 offset:8704\n"
"v_accvgpr_read_b32 v25, a21\n"
"v_exp_f32 v118, v118\n"
"v_mfma_f32_16x16x32_bf16 a[192:195], a[112:115], v[144:147], a[192:195]\n"
"ds_read_b64_tr_b16 a[106:107], v172 offset:12800\n"
"buffer_load_dwordx4 v4, s[8:11], s69 offen offset:2048 nt lds\n"
"v_mfma_f32_16x16x32_bf16 a[200:203], a[116:119], v[144:147], a[200:203]\n"
"s_waitcnt lgkmcnt(4)\n"
"ds_read_b64_tr_b16 a[108:109], v173 offset:8704\n"
"v_accvgpr_read_b32 v26, a22\n"
"v_exp_f32 v119, v119\n"
"v_mfma_f32_16x16x32_bf16 a[208:211], a[120:123], v[144:147], a[208:211]\n"
"ds_read_b64_tr_b16 a[110:111], v173 offset:12800\n"
"v_cvt_pk_bf16_f32 v156, v116, v117\n"
"v_mfma_f32_16x16x32_bf16 a[216:219], a[124:127], v[144:147], a[216:219]\n"
"v_exp_f32 v124, v124\n"
"v_mfma_f32_16x16x32_bf16 a[196:199], a[112:115], v[148:151], a[196:199]\n"
"ds_read_b64_tr_b16 a[112:113], v172 offset:9216\n"
"buffer_load_dwordx4 v5, s[8:11], s69 offen offset:3072 nt lds\n"
"v_exp_f32 v125, v125\n"
"v_mfma_f32_16x16x32_bf16 a[204:207], a[116:119], v[148:151], a[204:207]\n"
"ds_read_b64_tr_b16 a[114:115], v172 offset:13312\n"
"v_cvt_pk_bf16_f32 v157, v118, v119\n"
"v_mfma_f32_16x16x32_bf16 a[212:215], a[120:123], v[148:151], a[212:215]\n"
"ds_read_b64_tr_b16 a[116:117], v173 offset:9216\n"
"v_max3_f32 v188, v64, v65, v66\n"
"v_mfma_f32_16x16x32_bf16 a[220:223], a[124:127], v[148:151], a[220:223]\n"
"ds_read_b64_tr_b16 a[118:119], v173 offset:13312\n"
"v_max3_f32 v189, v68, v69, v70\n"
"v_exp_f32 v126, v126\n"
"v_mfma_f32_16x16x32_bf16 v[140:143], v[176:179], v[148:151], v[140:143]\n"
"ds_read_b64_tr_b16 a[120:121], v172 offset:9728\n"
"v_max3_f32 v188, v188, v67, v72\n"
"v_exp_f32 v127, v127\n"
"v_mfma_f32_16x16x32_bf16 a[160:163], a[96:99], v[152:155], a[160:163]\n"
"s_waitcnt lgkmcnt(4)\n"
"ds_read_b64_tr_b16 a[122:123], v172 offset:13824\n"
"v_cvt_pk_bf16_f32 v158, v124, v125\n"
"v_mfma_f32_16x16x32_bf16 a[168:171], a[100:103], v[152:155], a[168:171]\n"
"ds_read_b64_tr_b16 a[124:125], v173 offset:9728\n"
"v_cvt_pk_bf16_f32 v159, v126, v127\n"
"v_mfma_f32_16x16x32_bf16 a[176:179], a[104:107], v[152:155], a[176:179]\n"
"ds_read_b64_tr_b16 a[126:127], v173 offset:13824\n"
"v_add_u32 v172, 0x10000, v172\n"
"v_mfma_f32_16x16x32_bf16 a[184:187], a[108:111], v[152:155], a[184:187]\n"
"v_add_u32 v173, 0x10000, v173\n"
"v_max3_f32 v189, v189, v71, v76\n"
"v_mfma_f32_16x16x32_bf16 a[164:167], a[96:99], v[156:159], a[164:167]\n"
"ds_read_b128 a[80:83], v169 offset:0\n"
"v_max3_f32 v188, v188, v73, v74\n"
"v_mfma_f32_16x16x32_bf16 a[172:175], a[100:103], v[156:159], a[172:175]\n"
"ds_read_b128 a[84:87], v169 offset:4096\n"
"v_max3_f32 v189, v189, v77, v78\n"
"v_mfma_f32_16x16x32_bf16 a[180:183], a[104:107], v[156:159], a[180:183]\n"
"ds_read_b128 a[88:91], v169 offset:8192\n"
"v_max3_f32 v188, v188, v75, v80\n"
"v_mfma_f32_16x16x32_bf16 a[188:191], a[108:111], v[156:159], a[188:191]\n"
"ds_read_b128 a[92:95], v169 offset:12288\n"
"v_max3_f32 v189, v189, v79, v84\n"
"v_mfma_f32_16x16x32_bf16 v[136:139], v[176:179], v[152:155], v[136:139]\n"
"s_waitcnt lgkmcnt(4)\n"
"ds_read_b64_tr_b16 a[96:97], v172 offset:16384\n"
"v_max3_f32 v188, v188, v81, v82\n"
"v_mfma_f32_16x16x32_bf16 a[192:195], a[112:115], v[152:155], a[192:195]\n"
"ds_read_b64_tr_b16 a[98:99], v172 offset:20480\n"
"v_max3_f32 v189, v189, v85, v86\n"
"v_mfma_f32_16x16x32_bf16 a[200:203], a[116:119], v[152:155], a[200:203]\n"
"ds_read_b64_tr_b16 a[100:101], v173 offset:16384\n"
"v_max3_f32 v188, v188, v83, v88\n"
"v_mfma_f32_16x16x32_bf16 a[208:211], a[120:123], v[152:155], a[208:211]\n"
"ds_read_b64_tr_b16 a[102:103], v173 offset:20480\n"
"v_max3_f32 v189, v189, v87, v92\n"
"v_mfma_f32_16x16x32_bf16 a[216:219], a[124:127], v[152:155], a[216:219]\n"
"ds_read_b64_tr_b16 a[104:105], v172 offset:16896\n"
"v_max3_f32 v188, v188, v89, v90\n"
"v_mfma_f32_16x16x32_bf16 a[196:199], a[112:115], v[156:159], a[196:199]\n"
"ds_read_b64_tr_b16 a[106:107], v172 offset:20992\n"
"v_max3_f32 v189, v189, v93, v94\n"
"v_mfma_f32_16x16x32_bf16 a[204:207], a[116:119], v[156:159], a[204:207]\n"
"ds_read_b64_tr_b16 a[108:109], v173 offset:16896\n"
"v_max3_f32 v190, v188, v91, v95\n"
"v_mfma_f32_16x16x32_bf16 a[212:215], a[120:123], v[156:159], a[212:215]\n"
"ds_read_b64_tr_b16 a[110:111], v173 offset:20992\n"
"v_max_f32 v190, v190, v189\n"
"v_mfma_f32_16x16x32_bf16 a[220:223], a[124:127], v[156:159], a[220:223]\n"
"v_cmp_lt_f32 vcc, s59, v190\n"
"v_accvgpr_read_b32 v27, a23\n"
"v_mfma_f32_16x16x32_bf16 v[140:143], v[176:179], v[156:159], v[140:143]\n"
"v_accvgpr_read_b32 v28, a28\n"
"v_accvgpr_read_b32 v29, a29\n"
"s_cbranch_vccz .LAskip12\n"
"s_swappc_b64 s[70:71], s[72:73]\n"
".LAskip12:\n"
"v_mfma_f32_16x16x32_bf16 v[96:99], a[64:67], v[192:195], v[128:131]\n"
"v_exp_f32 v64, v64\n"
"v_mfma_f32_16x16x32_bf16 v[104:107], a[68:71], v[192:195], v[128:131]\n"
"v_exp_f32 v65, v65\n"
"v_mfma_f32_16x16x32_bf16 v[112:115], a[72:75], v[192:195], v[128:131]\n"
"v_exp_f32 v66, v66\n"
"v_mfma_f32_16x16x32_bf16 v[120:123], a[76:79], v[192:195], v[128:131]\n"
"v_exp_f32 v67, v67\n"
"v_mfma_f32_16x16x32_bf16 v[100:103], a[64:67], v[208:211], v[132:135]\n"
"ds_read_b128 a[64:67], v170 offset:0\n"
"v_cvt_pk_bf16_f32 v144, v64, v65\n"
"v_mfma_f32_16x16x32_bf16 v[108:111], a[68:71], v[208:211], v[132:135]\n"
"ds_read_b128 a[68:71], v170 offset:4096\n"
"v_accvgpr_read_b32 v30, a30\n"
"v_exp_f32 v72, v72\n"
"v_mfma_f32_16x16x32_bf16 v[116:119], a[72:75], v[208:211], v[132:135]\n"
"ds_read_b128 a[72:75], v170 offset:8192\n"
"v_accvgpr_read_b32 v31, a31\n"
"v_mfma_f32_16x16x32_bf16 v[124:127], a[76:79], v[208:211], v[132:135]\n"
"s_waitcnt lgkmcnt(3)\n"
"ds_read_b128 a[76:79], v170 offset:12288\n"
"v_mul_f32 v24, v24, v7\n"
"v_exp_f32 v73, v73\n"
"v_mfma_f32_16x16x32_bf16 v[96:99], a[80:83], v[196:199], v[96:99]\n"
"v_cvt_pk_bf16_f32 v145, v66, v67\n"
"v_mul_f32 v25, v25, v7\n"
"v_exp_f32 v74, v74\n"
"v_mfma_f32_16x16x32_bf16 v[104:107], a[84:87], v[196:199], v[104:107]\n"
"v_exp_f32 v75, v75\n"
"v_mfma_f32_16x16x32_bf16 v[112:115], a[88:91], v[196:199], v[112:115]\n"
"v_cvt_pk_bf16_f32 v146, v72, v73\n"
"v_mul_f32 v26, v26, v7\n"
"v_mfma_f32_16x16x32_bf16 v[120:123], a[92:95], v[196:199], v[120:123]\n"
"v_exp_f32 v68, v68\n"
"v_mfma_f32_16x16x32_bf16 v[100:103], a[80:83], v[212:215], v[100:103]\n"
"ds_read_b128 a[80:83], v171 offset:0\n"
"v_mul_f32 v27, v27, v7\n"
"v_mfma_f32_16x16x32_bf16 v[108:111], a[84:87], v[212:215], v[108:111]\n"
"ds_read_b128 a[84:87], v171 offset:4096\n"
"v_mul_f32 v28, v28, v7\n"
"v_exp_f32 v69, v69\n"
"v_mfma_f32_16x16x32_bf16 v[116:119], a[88:91], v[212:215], v[116:119]\n"
"ds_read_b128 a[88:91], v171 offset:8192\n"
"v_cvt_pk_bf16_f32 v147, v74, v75\n"
"v_exp_f32 v70, v70\n"
"v_mfma_f32_16x16x32_bf16 v[124:127], a[92:95], v[212:215], v[124:127]\n"
"s_waitcnt lgkmcnt(3)\n"
"ds_read_b128 a[92:95], v171 offset:12288\n"
"v_mul_f32 v29, v29, v7\n"
"v_mfma_f32_16x16x32_bf16 v[96:99], a[64:67], v[200:203], v[96:99]\n"
"ds_read_b64_tr_b16 a[112:113], v172 offset:17408\n"
"v_mul_f32 v30, v30, v7\n"
"v_exp_f32 v71, v71\n"
"v_mfma_f32_16x16x32_bf16 v[104:107], a[68:71], v[200:203], v[104:107]\n"
"ds_read_b64_tr_b16 a[114:115], v172 offset:21504\n"
"v_cvt_pk_bf16_f32 v148, v68, v69\n"
"v_exp_f32 v76, v76\n"
"v_mfma_f32_16x16x32_bf16 v[112:115], a[72:75], v[200:203], v[112:115]\n"
"ds_read_b64_tr_b16 a[116:117], v173 offset:17408\n"
"v_mul_f32 v31, v31, v7\n"
"v_exp_f32 v77, v77\n"
"v_mfma_f32_16x16x32_bf16 v[120:123], a[76:79], v[200:203], v[120:123]\n"
"ds_read_b64_tr_b16 a[118:119], v173 offset:21504\n"
"v_cvt_pk_bf16_f32 v149, v70, v71\n"
"v_mfma_f32_16x16x32_bf16 v[100:103], a[64:67], v[216:219], v[100:103]\n"
"ds_read_b64_tr_b16 a[120:121], v172 offset:17920\n"
"v_cvt_pk_bf16_f32 v44, v24, v25\n"
"v_exp_f32 v78, v78\n"
"v_mfma_f32_16x16x32_bf16 v[108:111], a[68:71], v[216:219], v[108:111]\n"
"ds_read_b64_tr_b16 a[122:123], v172 offset:22016\n"
"v_cvt_pk_bf16_f32 v45, v26, v27\n"
"v_exp_f32 v79, v79\n"
"v_mfma_f32_16x16x32_bf16 v[116:119], a[72:75], v[216:219], v[116:119]\n"
"ds_read_b64_tr_b16 a[124:125], v173 offset:17920\n"
"v_cvt_pk_bf16_f32 v150, v76, v77\n"
"v_mfma_f32_16x16x32_bf16 v[124:127], a[76:79], v[216:219], v[124:127]\n"
"s_waitcnt lgkmcnt(4)\n"
"ds_read_b64_tr_b16 a[126:127], v173 offset:22016\n"
"v_cvt_pk_bf16_f32 v46, v28, v29\n"
"v_exp_f32 v80, v80\n"
"v_mfma_f32_16x16x32_bf16 v[96:99], a[80:83], v[204:207], v[96:99]\n"
"v_exp_f32 v81, v81\n"
"v_mfma_f32_16x16x32_bf16 v[104:107], a[84:87], v[204:207], v[104:107]\n"
"v_cvt_pk_bf16_f32 v151, v78, v79\n"
"v_cvt_pk_bf16_f32 v47, v30, v31\n"
"v_mfma_f32_16x16x32_bf16 v[112:115], a[88:91], v[204:207], v[112:115]\n"
"v_exp_f32 v82, v82\n"
"v_mfma_f32_16x16x32_bf16 v[120:123], a[92:95], v[204:207], v[120:123]\n"
"v_exp_f32 v83, v83\n"
"v_mfma_f32_16x16x32_bf16 v[100:103], a[80:83], v[220:223], v[100:103]\n"
"v_cvt_pk_bf16_f32 v152, v80, v81\n"
"v_accvgpr_read_b32 v16, a32\n"
"v_mfma_f32_16x16x32_bf16 v[108:111], a[84:87], v[220:223], v[108:111]\n"
"v_exp_f32 v88, v88\n"
"v_mfma_f32_16x16x32_bf16 v[116:119], a[88:91], v[220:223], v[116:119]\n"
"v_exp_f32 v89, v89\n"
"v_mfma_f32_16x16x32_bf16 v[124:127], a[92:95], v[220:223], v[124:127]\n"
"v_cvt_pk_bf16_f32 v153, v82, v83\n"
"v_accvgpr_read_b32 v17, a33\n"
"s_waitcnt vmcnt(12)\n"
"s_barrier\n"
"s_sleep 2\n"
".LAbar13:\n"
"v_mfma_f32_16x16x32_bf16 a[160:163], a[96:99], v[144:147], a[160:163]\n"
"ds_read_b128 a[64:67], v168 offset:32768\n"
"v_accvgpr_read_b32 v18, a34\n"
"v_exp_f32 v90, v90\n"
"v_mfma_f32_16x16x32_bf16 a[168:171], a[100:103], v[144:147], a[168:171]\n"
"ds_read_b128 a[68:71], v168 offset:36864\n"
"v_accvgpr_read_b32 v19, a35\n"
"v_mfma_f32_16x16x32_bf16 a[176:179], a[104:107], v[144:147], a[176:179]\n"
"ds_read_b128 a[72:75], v168 offset:40960\n"
"v_accvgpr_read_b32 v20, a40\n"
"v_exp_f32 v91, v91\n"
"v_mfma_f32_16x16x32_bf16 a[184:187], a[108:111], v[144:147], a[184:187]\n"
"ds_read_b128 a[76:79], v168 offset:45056\n"
"v_cvt_pk_bf16_f32 v154, v88, v89\n"
"v_mfma_f32_16x16x32_bf16 a[164:167], a[96:99], v[148:151], a[164:167]\n"
"ds_read_b64_tr_b16 a[96:97], v172 offset:24576\n"
"v_accvgpr_read_b32 v21, a41\n"
"v_exp_f32 v84, v84\n"
"v_mfma_f32_16x16x32_bf16 a[172:175], a[100:103], v[148:151], a[172:175]\n"
"ds_read_b64_tr_b16 a[98:99], v172 offset:28672\n"
"v_accvgpr_read_b32 v22, a42\n"
"v_mfma_f32_16x16x32_bf16 a[180:183], a[104:107], v[148:151], a[180:183]\n"
"ds_read_b64_tr_b16 a[100:101], v173 offset:24576\n"
"v_accvgpr_read_b32 v23, a43\n"
"v_exp_f32 v85, v85\n"
"v_mfma_f32_16x16x32_bf16 a[188:191], a[108:111], v[148:151], a[188:191]\n"
"ds_read_b64_tr_b16 a[102:103], v173 offset:28672\n"
"v_cvt_pk_bf16_f32 v155, v90, v91\n"
"v_mfma_f32_16x16x32_bf16 v[136:139], v[176:179], v[144:147], v[136:139]\n"
"ds_read_b64_tr_b16 a[104:105], v172 offset:25088\n"
"v_mul_f32 v16, v16, v6\n"
"v_exp_f32 v86, v86\n"
"v_mfma_f32_16x16x32_bf16 a[192:195], a[112:115], v[144:147], a[192:195]\n"
"s_waitcnt lgkmcnt(4)\n"
"ds_read_b64_tr_b16 a[106:107], v172 offset:29184\n"
"v_mul_f32 v17, v17, v6\n"
"v_mfma_f32_16x16x32_bf16 a[200:203], a[116:119], v[144:147], a[200:203]\n"
"ds_read_b64_tr_b16 a[108:109], v173 offset:25088\n"
"v_mul_f32 v18, v18, v6\n"
"v_exp_f32 v87, v87\n"
"v_mfma_f32_16x16x32_bf16 a[208:211], a[120:123], v[144:147], a[208:211]\n"
"ds_read_b64_tr_b16 a[110:111], v173 offset:29184\n"
"v_cvt_pk_bf16_f32 v156, v84, v85\n"
"v_mfma_f32_16x16x32_bf16 a[216:219], a[124:127], v[144:147], a[216:219]\n"
"v_exp_f32 v92, v92\n"
"v_mfma_f32_16x16x32_bf16 a[196:199], a[112:115], v[148:151], a[196:199]\n"
"ds_read_b64_tr_b16 a[112:113], v172 offset:25600\n"
"v_mul_f32 v19, v19, v6\n"
"v_exp_f32 v93, v93\n"
"v_mfma_f32_16x16x32_bf16 a[204:207], a[116:119], v[148:151], a[204:207]\n"
"ds_read_b64_tr_b16 a[114:115], v172 offset:29696\n"
"v_cvt_pk_bf16_f32 v157, v86, v87\n"
"v_mfma_f32_16x16x32_bf16 a[212:215], a[120:123], v[148:151], a[212:215]\n"
"ds_read_b64_tr_b16 a[116:117], v173 offset:25600\n"
"v_max3_f32 v188, v96, v97, v98\n"
"v_mfma_f32_16x16x32_bf16 a[220:223], a[124:127], v[148:151], a[220:223]\n"
"ds_read_b64_tr_b16 a[118:119], v173 offset:29696\n"
"v_max3_f32 v189, v100, v101, v102\n"
"v_exp_f32 v94, v94\n"
"v_mfma_f32_16x16x32_bf16 v[140:143], v[176:179], v[148:151], v[140:143]\n"
"s_waitcnt lgkmcnt(4)\n"
"ds_read_b64_tr_b16 a[120:121], v172 offset:26112\n"
"v_max3_f32 v188, v188, v99, v104\n"
"v_exp_f32 v95, v95\n"
"v_mfma_f32_16x16x32_bf16 a[160:163], a[96:99], v[152:155], a[160:163]\n"
"ds_read_b64_tr_b16 a[122:123], v172 offset:30208\n"
"v_cvt_pk_bf16_f32 v158, v92, v93\n"
"v_mfma_f32_16x16x32_bf16 a[168:171], a[100:103], v[152:155], a[168:171]\n"
"ds_read_b64_tr_b16 a[124:125], v173 offset:26112\n"
"v_cvt_pk_bf16_f32 v159, v94, v95\n"
"v_mfma_f32_16x16x32_bf16 a[176:179], a[104:107], v[152:155], a[176:179]\n"
"ds_read_b64_tr_b16 a[126:127], v173 offset:30208\n"
"v_max3_f32 v189, v189, v103, v108\n"
"v_mfma_f32_16x16x32_bf16 a[184:187], a[108:111], v[152:155], a[184:187]\n"
"v_max3_f32 v188, v188, v105, v106\n"
"v_max3_f32 v189, v189, v109, v110\n"
"v_mfma_f32_16x16x32_bf16 a[164:167], a[96:99], v[156:159], a[164:167]\n"
"ds_read_b128 a[80:83], v169 offset:32768\n"
"v_max3_f32 v188, v188, v107, v112\n"
"v_mfma_f32_16x16x32_bf16 a[172:175], a[100:103], v[156:159], a[172:175]\n"
"ds_read_b128 a[84:87], v169 offset:36864\n"
"v_max3_f32 v189, v189, v111, v116\n"
"v_mfma_f32_16x16x32_bf16 a[180:183], a[104:107], v[156:159], a[180:183]\n"
"ds_read_b128 a[88:91], v169 offset:40960\n"
"v_max3_f32 v188, v188, v113, v114\n"
"v_mfma_f32_16x16x32_bf16 a[188:191], a[108:111], v[156:159], a[188:191]\n"
"ds_read_b128 a[92:95], v169 offset:45056\n"
"v_max3_f32 v189, v189, v117, v118\n"
"v_mfma_f32_16x16x32_bf16 v[136:139], v[176:179], v[152:155], v[136:139]\n"
"s_waitcnt lgkmcnt(4)\n"
"ds_read_b64_tr_b16 a[96:97], v172 offset:49152\n"
"v_max3_f32 v188, v188, v115, v120\n"
"v_mfma_f32_16x16x32_bf16 a[192:195], a[112:115], v[152:155], a[192:195]\n"
"ds_read_b64_tr_b16 a[98:99], v172 offset:53248\n"
"v_max3_f32 v189, v189, v119, v124\n"
"v_mfma_f32_16x16x32_bf16 a[200:203], a[116:119], v[152:155], a[200:203]\n"
"ds_read_b64_tr_b16 a[100:101], v173 offset:49152\n"
"v_max3_f32 v188, v188, v121, v122\n"
"v_mfma_f32_16x16x32_bf16 a[208:211], a[120:123], v[152:155], a[208:211]\n"
"ds_read_b64_tr_b16 a[102:103], v173 offset:53248\n"
"v_max3_f32 v189, v189, v125, v126\n"
"v_mfma_f32_16x16x32_bf16 a[216:219], a[124:127], v[152:155], a[216:219]\n"
"ds_read_b64_tr_b16 a[104:105], v172 offset:49664\n"
"v_max3_f32 v190, v188, v123, v127\n"
"v_mfma_f32_16x16x32_bf16 a[196:199], a[112:115], v[156:159], a[196:199]\n"
"ds_read_b64_tr_b16 a[106:107], v172 offset:53760\n"
"v_max_f32 v190, v190, v189\n"
"v_mfma_f32_16x16x32_bf16 a[204:207], a[116:119], v[156:159], a[204:207]\n"
"ds_read_b64_tr_b16 a[108:109], v173 offset:49664\n"
"v_cmp_lt_f32 vcc, s59, v190\n"
"v_mfma_f32_16x16x32_bf16 a[212:215], a[120:123], v[156:159], a[212:215]\n"
"ds_read_b64_tr_b16 a[110:111], v173 offset:53760\n"
"v_mul_f32 v20, v20, v6\n"
"v_mfma_f32_16x16x32_bf16 a[220:223], a[124:127], v[156:159], a[220:223]\n"
"v_mul_f32 v21, v21, v6\n"
"v_mul_f32 v22, v22, v6\n"
"v_mfma_f32_16x16x32_bf16 v[140:143], v[176:179], v[156:159], v[140:143]\n"
"v_mul_f32 v23, v23, v6\n"
"v_cvt_pk_bf16_f32 v48, v16, v17\n"
"s_cbranch_vccz .LAskip13\n"
"s_swappc_b64 s[70:71], s[74:75]\n"
".LAskip13:\n"
"v_mfma_f32_16x16x32_bf16 v[64:67], a[64:67], v[192:195], v[128:131]\n"
"v_exp_f32 v96, v96\n"
"v_mfma_f32_16x16x32_bf16 v[72:75], a[68:71], v[192:195], v[128:131]\n"
"v_exp_f32 v97, v97\n"
"v_mfma_f32_16x16x32_bf16 v[80:83], a[72:75], v[192:195], v[128:131]\n"
"v_exp_f32 v98, v98\n"
"v_mfma_f32_16x16x32_bf16 v[88:91], a[76:79], v[192:195], v[128:131]\n"
"v_exp_f32 v99, v99\n"
"v_mfma_f32_16x16x32_bf16 v[68:71], a[64:67], v[208:211], v[132:135]\n"
"ds_read_b128 a[64:67], v170 offset:32768\n"
"v_cvt_pk_bf16_f32 v144, v96, v97\n"
"v_mfma_f32_16x16x32_bf16 v[76:79], a[68:71], v[208:211], v[132:135]\n"
"ds_read_b128 a[68:71], v170 offset:36864\n"
"v_cvt_pk_bf16_f32 v49, v18, v19\n"
"v_exp_f32 v104, v104\n"
"v_mfma_f32_16x16x32_bf16 v[84:87], a[72:75], v[208:211], v[132:135]\n"
"ds_read_b128 a[72:75], v170 offset:40960\n"
"v_cvt_pk_bf16_f32 v50, v20, v21\n"
"v_mfma_f32_16x16x32_bf16 v[92:95], a[76:79], v[208:211], v[132:135]\n"
"s_waitcnt lgkmcnt(3)\n"
"ds_read_b128 a[76:79], v170 offset:45056\n"
"v_cvt_pk_bf16_f32 v51, v22, v23\n"
"v_exp_f32 v105, v105\n"
"v_mfma_f32_16x16x32_bf16 v[64:67], a[80:83], v[196:199], v[64:67]\n"
"s_add_u32 s84, s51, 0\n"
"s_and_b32 s84, s84, 15\n"
"v_cvt_pk_bf16_f32 v145, v98, v99\n"
"v_mfma_f32_16x16x32_bf16 v[72:75], a[84:87], v[196:199], v[72:75]\n"
"s_lshl_b32 s84, s84, 15\n"
"v_accvgpr_read_b32 v24, a36\n"
"v_exp_f32 v106, v106\n"
"v_mfma_f32_16x16x32_bf16 v[80:83], a[88:91], v[196:199], v[80:83]\n"
"buffer_load_dwordx4 v[224:227], v13, s[36:39], s84 offen offset:0\n"
"v_accvgpr_read_b32 v25, a37\n"
"v_exp_f32 v107, v107\n"
"v_mfma_f32_16x16x32_bf16 v[88:91], a[92:95], v[196:199], v[88:91]\n"
"v_cvt_pk_bf16_f32 v146, v104, v105\n"
"v_accvgpr_read_b32 v26, a38\n"
"v_exp_f32 v100, v100\n"
"v_mfma_f32_16x16x32_bf16 v[68:71], a[80:83], v[212:215], v[68:71]\n"
"ds_read_b128 a[80:83], v171 offset:32768\n"
"v_accvgpr_read_b32 v27, a39\n"
"v_mfma_f32_16x16x32_bf16 v[76:79], a[84:87], v[212:215], v[76:79]\n"
"ds_read_b128 a[84:87], v171 offset:36864\n"
"v_accvgpr_read_b32 v28, a44\n"
"v_exp_f32 v101, v101\n"
"v_mfma_f32_16x16x32_bf16 v[84:87], a[88:91], v[212:215], v[84:87]\n"
"ds_read_b128 a[88:91], v171 offset:40960\n"
"buffer_load_dwordx4 v[228:231], v13, s[36:39], s84 offen offset:1024\n"
"v_cvt_pk_bf16_f32 v147, v106, v107\n"
"v_mfma_f32_16x16x32_bf16 v[92:95], a[92:95], v[212:215], v[92:95]\n"
"s_waitcnt lgkmcnt(3)\n"
"ds_read_b128 a[92:95], v171 offset:45056\n"
"v_accvgpr_read_b32 v29, a45\n"
"v_exp_f32 v102, v102\n"
"v_mfma_f32_16x16x32_bf16 v[64:67], a[64:67], v[200:203], v[64:67]\n"
"ds_read_b64_tr_b16 a[112:113], v172 offset:50176\n"
"v_add_u32 v168, 0xffff0000, v168\n"
"v_exp_f32 v103, v103\n"
"v_mfma_f32_16x16x32_bf16 v[72:75], a[68:71], v[200:203], v[72:75]\n"
"ds_read_b64_tr_b16 a[114:115], v172 offset:54272\n"
"v_add_u32 v169, 0xffff0000, v169\n"
"v_cvt_pk_bf16_f32 v148, v100, v101\n"
"v_mfma_f32_16x16x32_bf16 v[80:83], a[72:75], v[200:203], v[80:83]\n"
"ds_read_b64_tr_b16 a[116:117], v173 offset:50176\n"
"buffer_load_dwordx4 v[232:235], v13, s[36:39], s84 offen offset:2048\n"
"v_exp_f32 v108, v108\n"
"v_mfma_f32_16x16x32_bf16 v[88:91], a[76:79], v[200:203], v[88:91]\n"
"ds_read_b64_tr_b16 a[118:119], v173 offset:54272\n"
"v_add_u32 v170, 0xffff0000, v170\n"
"v_exp_f32 v109, v109\n"
"v_mfma_f32_16x16x32_bf16 v[68:71], a[64:67], v[216:219], v[68:71]\n"
"ds_read_b64_tr_b16 a[120:121], v172 offset:50688\n"
"v_add_u32 v171, 0xffff0000, v171\n"
"v_cvt_pk_bf16_f32 v149, v102, v103\n"
"v_mfma_f32_16x16x32_bf16 v[76:79], a[68:71], v[216:219], v[76:79]\n"
"ds_read_b64_tr_b16 a[122:123], v172 offset:54784\n"
"v_accvgpr_read_b32 v30, a46\n"
"v_exp_f32 v110, v110\n"
"v_mfma_f32_16x16x32_bf16 v[84:87], a[72:75], v[216:219], v[84:87]\n"
"ds_read_b64_tr_b16 a[124:125], v173 offset:50688\n"
"buffer_load_dwordx4 v[236:239], v13, s[36:39], s84 offen offset:3072\n"
"v_exp_f32 v111, v111\n"
"v_mfma_f32_16x16x32_bf16 v[92:95], a[76:79], v[216:219], v[92:95]\n"
"s_waitcnt lgkmcnt(3)\n"
"ds_read_b64_tr_b16 a[126:127], v173 offset:54784\n"
"v_cvt_pk_bf16_f32 v150, v108, v109\n"
"v_exp_f32 v112, v112\n"
"v_mfma_f32_16x16x32_bf16 v[64:67], a[80:83], v[204:207], v[64:67]\n"
"v_exp_f32 v113, v113\n"
"v_mfma_f32_16x16x32_bf16 v[72:75], a[84:87], v[204:207], v[72:75]\n"
"v_cvt_pk_bf16_f32 v151, v110, v111\n"
"v_accvgpr_read_b32 v31, a47\n"
"v_mfma_f32_16x16x32_bf16 v[80:83], a[88:91], v[204:207], v[80:83]\n"
"buffer_load_dwordx4 v[240:243], v14, s[36:39], s84 offen offset:0\n"
"v_mul_f32 v24, v24, v7\n"
"v_exp_f32 v114, v114\n"
"v_mfma_f32_16x16x32_bf16 v[88:91], a[92:95], v[204:207], v[88:91]\n"
"v_exp_f32 v115, v115\n"
"v_mfma_f32_16x16x32_bf16 v[68:71], a[80:83], v[220:223], v[68:71]\n"
"v_cvt_pk_bf16_f32 v152, v112, v113\n"
"v_mul_f32 v25, v25, v7\n"
"v_mfma_f32_16x16x32_bf16 v[76:79], a[84:87], v[220:223], v[76:79]\n"
"v_exp_f32 v120, v120\n"
"v_mfma_f32_16x16x32_bf16 v[84:87], a[88:91], v[220:223], v[84:87]\n"
"buffer_load_dwordx4 v[244:247], v14, s[36:39], s84 offen offset:1024\n"
"v_mul_f32 v26, v26, v7\n"
"v_exp_f32 v121, v121\n"
"v_mfma_f32_16x16x32_bf16 v[92:95], a[92:95], v[220:223], v[92:95]\n"
"v_cvt_pk_bf16_f32 v153, v114, v115\n"
"v_mul_f32 v27, v27, v7\n"
"buffer_load_dwordx4 v[248:251], v14, s[36:39], s84 offen offset:2048\n"
"buffer_load_dwordx4 v[252:255], v14, s[36:39], s84 offen offset:3072\n"
"v_mul_f32 v28, v28, v7\n"
"v_mul_f32 v29, v29, v7\n"
"v_mul_f32 v30, v30, v7\n"
"v_mul_f32 v31, v31, v7\n"
"v_cvt_pk_bf16_f32 v52, v24, v25\n"
"v_cvt_pk_bf16_f32 v53, v26, v27\n"
"v_cvt_pk_bf16_f32 v54, v28, v29\n"
"v_cvt_pk_bf16_f32 v55, v30, v31\n"
"v_accvgpr_read_b32 v16, a48\n"
"v_accvgpr_read_b32 v17, a49\n"
"v_accvgpr_read_b32 v18, a50\n"
"v_accvgpr_read_b32 v19, a51\n"
"v_accvgpr_read_b32 v20, a56\n"
"v_accvgpr_read_b32 v21, a57\n"
"v_accvgpr_read_b32 v22, a58\n"
"v_accvgpr_read_b32 v23, a59\n"
"v_mul_f32 v16, v16, v6\n"
"v_mul_f32 v17, v17, v6\n"
"v_mul_f32 v18, v18, v6\n"
"v_mul_f32 v19, v19, v6\n"
"v_mul_f32 v20, v20, v6\n"
"v_mul_f32 v21, v21, v6\n"
"v_mul_f32 v22, v22, v6\n"
"v_mul_f32 v23, v23, v6\n"
"v_cvt_pk_bf16_f32 v56, v16, v17\n"
"v_cvt_pk_bf16_f32 v57, v18, v19\n"
"v_cvt_pk_bf16_f32 v58, v20, v21\n"
"v_cvt_pk_bf16_f32 v59, v22, v23\n"
"v_accvgpr_read_b32 v24, a52\n"
"v_accvgpr_read_b32 v25, a53\n"
"v_accvgpr_read_b32 v26, a54\n"
"v_accvgpr_read_b32 v27, a55\n"
"v_accvgpr_read_b32 v28, a60\n"
"v_accvgpr_read_b32 v29, a61\n"
"v_accvgpr_read_b32 v30, a62\n"
"v_accvgpr_read_b32 v31, a63\n"
"v_mul_f32 v24, v24, v7\n"
"v_mul_f32 v25, v25, v7\n"
"v_mul_f32 v26, v26, v7\n"
"v_mul_f32 v27, v27, v7\n"
"v_mul_f32 v28, v28, v7\n"
"v_mul_f32 v29, v29, v7\n"
"v_mul_f32 v30, v30, v7\n"
"v_mul_f32 v31, v31, v7\n"
"v_cvt_pk_bf16_f32 v60, v24, v25\n"
"v_cvt_pk_bf16_f32 v61, v26, v27\n"
"v_cvt_pk_bf16_f32 v62, v28, v29\n"
"v_cvt_pk_bf16_f32 v63, v30, v31\n"
"s_waitcnt vmcnt(16)\n"
"s_barrier\n"
"s_sleep 2\n"
".LAbar14:\n"
"v_mfma_f32_16x16x32_bf16 a[160:163], a[96:99], v[144:147], a[160:163]\n"
"ds_read_b128 a[64:67], v168 offset:32768\n"
"v_exp_f32 v122, v122\n"
"v_mfma_f32_16x16x32_bf16 a[168:171], a[100:103], v[144:147], a[168:171]\n"
"ds_read_b128 a[68:71], v168 offset:36864\n"
"v_mfma_f32_16x16x32_bf16 a[176:179], a[104:107], v[144:147], a[176:179]\n"
"ds_read_b128 a[72:75], v168 offset:40960\n"
"v_exp_f32 v123, v123\n"
"v_mfma_f32_16x16x32_bf16 a[184:187], a[108:111], v[144:147], a[184:187]\n"
"ds_read_b128 a[76:79], v168 offset:45056\n"
"v_cvt_pk_bf16_f32 v154, v120, v121\n"
"v_mfma_f32_16x16x32_bf16 a[164:167], a[96:99], v[148:151], a[164:167]\n"
"ds_read_b64_tr_b16 a[96:97], v172 offset:57344\n"
"v_exp_f32 v116, v116\n"
"v_mfma_f32_16x16x32_bf16 a[172:175], a[100:103], v[148:151], a[172:175]\n"
"ds_read_b64_tr_b16 a[98:99], v172 offset:61440\n"
"v_mfma_f32_16x16x32_bf16 a[180:183], a[104:107], v[148:151], a[180:183]\n"
"ds_read_b64_tr_b16 a[100:101], v173 offset:57344\n"
"v_exp_f32 v117, v117\n"
"v_mfma_f32_16x16x32_bf16 a[188:191], a[108:111], v[148:151], a[188:191]\n"
"ds_read_b64_tr_b16 a[102:103], v173 offset:61440\n"
"v_cvt_pk_bf16_f32 v155, v122, v123\n"
"v_mfma_f32_16x16x32_bf16 v[136:139], v[176:179], v[144:147], v[136:139]\n"
"ds_read_b64_tr_b16 a[104:105], v172 offset:57856\n"
"v_exp_f32 v118, v118\n"
"v_mfma_f32_16x16x32_bf16 a[192:195], a[112:115], v[144:147], a[192:195]\n"
"ds_read_b64_tr_b16 a[106:107], v172 offset:61952\n"
"v_mfma_f32_16x16x32_bf16 a[200:203], a[116:119], v[144:147], a[200:203]\n"
"s_waitcnt lgkmcnt(5)\n"
"ds_read_b64_tr_b16 a[108:109], v173 offset:57856\n"
"v_exp_f32 v119, v119\n"
"v_mfma_f32_16x16x32_bf16 a[208:211], a[120:123], v[144:147], a[208:211]\n"
"ds_read_b64_tr_b16 a[110:111], v173 offset:61952\n"
"v_cvt_pk_bf16_f32 v156, v116, v117\n"
"v_mfma_f32_16x16x32_bf16 a[216:219], a[124:127], v[144:147], a[216:219]\n"
"ds_write_b128 v175, v[32:35] offset:0\n"
"v_exp_f32 v124, v124\n"
"v_mfma_f32_16x16x32_bf16 a[196:199], a[112:115], v[148:151], a[196:199]\n"
"ds_read_b64_tr_b16 a[112:113], v172 offset:58368\n"
"v_exp_f32 v125, v125\n"
"v_mfma_f32_16x16x32_bf16 a[204:207], a[116:119], v[148:151], a[204:207]\n"
"ds_read_b64_tr_b16 a[114:115], v172 offset:62464\n"
"v_cvt_pk_bf16_f32 v157, v118, v119\n"
"v_mfma_f32_16x16x32_bf16 a[212:215], a[120:123], v[148:151], a[212:215]\n"
"ds_read_b64_tr_b16 a[116:117], v173 offset:58368\n"
"v_max3_f32 v188, v64, v65, v66\n"
"v_mfma_f32_16x16x32_bf16 a[220:223], a[124:127], v[148:151], a[220:223]\n"
"ds_read_b64_tr_b16 a[118:119], v173 offset:62464\n"
"v_max3_f32 v189, v68, v69, v70\n"
"v_exp_f32 v126, v126\n"
"v_mfma_f32_16x16x32_bf16 v[140:143], v[176:179], v[148:151], v[140:143]\n"
"s_waitcnt lgkmcnt(4)\n"
"ds_read_b64_tr_b16 a[120:121], v172 offset:58880\n"
"v_max3_f32 v188, v188, v67, v72\n"
"v_exp_f32 v127, v127\n"
"v_mfma_f32_16x16x32_bf16 a[160:163], a[96:99], v[152:155], a[160:163]\n"
"ds_read_b64_tr_b16 a[122:123], v172 offset:62976\n"
"v_cvt_pk_bf16_f32 v158, v124, v125\n"
"v_mfma_f32_16x16x32_bf16 a[168:171], a[100:103], v[152:155], a[168:171]\n"
"ds_read_b64_tr_b16 a[124:125], v173 offset:58880\n"
"v_cvt_pk_bf16_f32 v159, v126, v127\n"
"v_mfma_f32_16x16x32_bf16 a[176:179], a[104:107], v[152:155], a[176:179]\n"
"ds_read_b64_tr_b16 a[126:127], v173 offset:62976\n"
"v_max3_f32 v189, v189, v71, v76\n"
"v_mfma_f32_16x16x32_bf16 a[184:187], a[108:111], v[152:155], a[184:187]\n"
"ds_write_b128 v175, v[36:39] offset:1024\n"
"v_mfma_f32_16x16x32_bf16 a[164:167], a[96:99], v[156:159], a[164:167]\n"
"ds_read_b128 a[80:83], v169 offset:32768\n"
"v_max3_f32 v188, v188, v73, v74\n"
"v_mfma_f32_16x16x32_bf16 a[172:175], a[100:103], v[156:159], a[172:175]\n"
"ds_read_b128 a[84:87], v169 offset:36864\n"
"v_max3_f32 v189, v189, v77, v78\n"
"v_mfma_f32_16x16x32_bf16 a[180:183], a[104:107], v[156:159], a[180:183]\n"
"ds_read_b128 a[88:91], v169 offset:40960\n"
"v_max3_f32 v188, v188, v75, v80\n"
"v_mfma_f32_16x16x32_bf16 a[188:191], a[108:111], v[156:159], a[188:191]\n"
"ds_read_b128 a[92:95], v169 offset:45056\n"
"v_max3_f32 v189, v189, v79, v84\n"
"v_mfma_f32_16x16x32_bf16 v[136:139], v[176:179], v[152:155], v[136:139]\n"
"s_waitcnt lgkmcnt(5)\n"
"ds_write_b128 v175, v[40:43] offset:2048\n"
"v_mfma_f32_16x16x32_bf16 a[192:195], a[112:115], v[152:155], a[192:195]\n"
"ds_write_b128 v175, v[44:47] offset:3072\n"
"v_mfma_f32_16x16x32_bf16 a[200:203], a[116:119], v[152:155], a[200:203]\n"
"ds_write_b128 v175, v[48:51] offset:4096\n"
"v_mfma_f32_16x16x32_bf16 a[208:211], a[120:123], v[152:155], a[208:211]\n"
"ds_write_b128 v175, v[52:55] offset:5120\n"
"v_mfma_f32_16x16x32_bf16 a[216:219], a[124:127], v[152:155], a[216:219]\n"
"ds_write_b128 v175, v[56:59] offset:6144\n"
"v_mfma_f32_16x16x32_bf16 a[196:199], a[112:115], v[156:159], a[196:199]\n"
"ds_write_b128 v175, v[60:63] offset:7168\n"
"v_mfma_f32_16x16x32_bf16 a[204:207], a[116:119], v[156:159], a[204:207]\n"
"v_max3_f32 v188, v188, v81, v82\n"
"v_max3_f32 v189, v189, v85, v86\n"
"v_mfma_f32_16x16x32_bf16 a[212:215], a[120:123], v[156:159], a[212:215]\n"
"v_max3_f32 v188, v188, v83, v88\n"
"v_max3_f32 v189, v189, v87, v92\n"
"v_mfma_f32_16x16x32_bf16 a[220:223], a[124:127], v[156:159], a[220:223]\n"
"v_max3_f32 v188, v188, v89, v90\n"
"v_max3_f32 v189, v189, v93, v94\n"
"v_mfma_f32_16x16x32_bf16 v[140:143], v[176:179], v[156:159], v[140:143]\n"
"v_max3_f32 v190, v188, v91, v95\n"
"v_max_f32 v190, v190, v189\n"
"v_cmp_lt_f32 vcc, s59, v190\n"
"s_nop 1\n"
"s_cbranch_vccz .LAskip14\n"
"s_swappc_b64 s[70:71], s[72:73]\n"
".LAskip14:\n"
"v_mfma_f32_16x16x32_bf16 v[96:99], a[64:67], v[192:195], v[128:131]\n"
"v_exp_f32 v64, v64\n"
"v_mfma_f32_16x16x32_bf16 v[104:107], a[68:71], v[192:195], v[128:131]\n"
"v_exp_f32 v65, v65\n"
"v_mfma_f32_16x16x32_bf16 v[112:115], a[72:75], v[192:195], v[128:131]\n"
"v_exp_f32 v66, v66\n"
"v_exp_f32 v67, v67\n"
"v_mfma_f32_16x16x32_bf16 v[120:123], a[76:79], v[192:195], v[128:131]\n"
"v_cvt_pk_bf16_f32 v144, v64, v65\n"
"v_mfma_f32_16x16x32_bf16 v[100:103], a[64:67], v[208:211], v[132:135]\n"
"ds_read_b128 a[64:67], v170 offset:32768\n"
"v_exp_f32 v72, v72\n"
"v_mfma_f32_16x16x32_bf16 v[108:111], a[68:71], v[208:211], v[132:135]\n"
"ds_read_b128 a[68:71], v170 offset:36864\n"
"v_exp_f32 v73, v73\n"
"v_mfma_f32_16x16x32_bf16 v[116:119], a[72:75], v[208:211], v[132:135]\n"
"ds_read_b128 a[72:75], v170 offset:40960\n"
"v_cvt_pk_bf16_f32 v145, v66, v67\n"
"v_exp_f32 v74, v74\n"
"v_mfma_f32_16x16x32_bf16 v[124:127], a[76:79], v[208:211], v[132:135]\n"
"s_waitcnt lgkmcnt(3)\n"
"ds_read_b128 a[76:79], v170 offset:45056\n"
"v_exp_f32 v75, v75\n"
"v_mfma_f32_16x16x32_bf16 v[96:99], a[80:83], v[196:199], v[96:99]\n"
"s_add_u32 s85, s51, 1\n"
"s_and_b32 s85, s85, 15\n"
"v_cvt_pk_bf16_f32 v146, v72, v73\n"
"v_mfma_f32_16x16x32_bf16 v[104:107], a[84:87], v[196:199], v[104:107]\n"
"s_lshl_b32 s85, s85, 15\n"
"v_exp_f32 v68, v68\n"
"v_mfma_f32_16x16x32_bf16 v[112:115], a[88:91], v[196:199], v[112:115]\n"
"buffer_load_dwordx4 v[32:35], v13, s[36:39], s85 offen offset:0\n"
"v_exp_f32 v69, v69\n"
"v_mfma_f32_16x16x32_bf16 v[120:123], a[92:95], v[196:199], v[120:123]\n"
"v_cvt_pk_bf16_f32 v147, v74, v75\n"
"v_exp_f32 v70, v70\n"
"v_mfma_f32_16x16x32_bf16 v[100:103], a[80:83], v[212:215], v[100:103]\n"
"ds_read_b128 a[80:83], v171 offset:32768\n"
"v_exp_f32 v71, v71\n"
"v_mfma_f32_16x16x32_bf16 v[108:111], a[84:87], v[212:215], v[108:111]\n"
"ds_read_b128 a[84:87], v171 offset:36864\n"
"v_cvt_pk_bf16_f32 v148, v68, v69\n"
"v_exp_f32 v76, v76\n"
"v_mfma_f32_16x16x32_bf16 v[116:119], a[88:91], v[212:215], v[116:119]\n"
"ds_read_b128 a[88:91], v171 offset:40960\n"
"buffer_load_dwordx4 v[36:39], v13, s[36:39], s85 offen offset:1024\n"
"v_exp_f32 v77, v77\n"
"v_mfma_f32_16x16x32_bf16 v[124:127], a[92:95], v[212:215], v[124:127]\n"
"s_waitcnt lgkmcnt(3)\n"
"ds_read_b128 a[92:95], v171 offset:45056\n"
"v_cvt_pk_bf16_f32 v149, v70, v71\n"
"v_exp_f32 v78, v78\n"
"v_mfma_f32_16x16x32_bf16 v[96:99], a[64:67], v[200:203], v[96:99]\n"
"v_exp_f32 v79, v79\n"
"v_cvt_pk_bf16_f32 v150, v76, v77\n"
"v_mfma_f32_16x16x32_bf16 v[104:107], a[68:71], v[200:203], v[104:107]\n"
"v_exp_f32 v80, v80\n"
"v_exp_f32 v81, v81\n"
"v_mfma_f32_16x16x32_bf16 v[112:115], a[72:75], v[200:203], v[112:115]\n"
"buffer_load_dwordx4 v[40:43], v13, s[36:39], s85 offen offset:2048\n"
"v_cvt_pk_bf16_f32 v151, v78, v79\n"
"v_exp_f32 v82, v82\n"
"v_mfma_f32_16x16x32_bf16 v[120:123], a[76:79], v[200:203], v[120:123]\n"
"v_exp_f32 v83, v83\n"
"v_cvt_pk_bf16_f32 v152, v80, v81\n"
"v_mfma_f32_16x16x32_bf16 v[100:103], a[64:67], v[216:219], v[100:103]\n"
"v_exp_f32 v88, v88\n"
"v_exp_f32 v89, v89\n"
"v_mfma_f32_16x16x32_bf16 v[108:111], a[68:71], v[216:219], v[108:111]\n"
"v_cvt_pk_bf16_f32 v153, v82, v83\n"
"v_mfma_f32_16x16x32_bf16 v[116:119], a[72:75], v[216:219], v[116:119]\n"
"buffer_load_dwordx4 v[44:47], v13, s[36:39], s85 offen offset:3072\n"
"v_exp_f32 v90, v90\n"
"v_mfma_f32_16x16x32_bf16 v[124:127], a[76:79], v[216:219], v[124:127]\n"
"s_waitcnt lgkmcnt(0)\n"
"v_exp_f32 v91, v91\n"
"v_cvt_pk_bf16_f32 v154, v88, v89\n"
"v_mfma_f32_16x16x32_bf16 v[96:99], a[80:83], v[204:207], v[96:99]\n"
"v_exp_f32 v84, v84\n"
"v_exp_f32 v85, v85\n"
"v_mfma_f32_16x16x32_bf16 v[104:107], a[84:87], v[204:207], v[104:107]\n"
"v_cvt_pk_bf16_f32 v155, v90, v91\n"
"v_mfma_f32_16x16x32_bf16 v[112:115], a[88:91], v[204:207], v[112:115]\n"
"buffer_load_dwordx4 v[48:51], v14, s[36:39], s85 offen offset:0\n"
"v_exp_f32 v86, v86\n"
"v_mfma_f32_16x16x32_bf16 v[120:123], a[92:95], v[204:207], v[120:123]\n"
"v_exp_f32 v87, v87\n"
"v_cvt_pk_bf16_f32 v156, v84, v85\n"
"v_mfma_f32_16x16x32_bf16 v[100:103], a[80:83], v[220:223], v[100:103]\n"
"v_exp_f32 v92, v92\n"
"v_exp_f32 v93, v93\n"
"v_mfma_f32_16x16x32_bf16 v[108:111], a[84:87], v[220:223], v[108:111]\n"
"v_cvt_pk_bf16_f32 v157, v86, v87\n"
"v_mfma_f32_16x16x32_bf16 v[116:119], a[88:91], v[220:223], v[116:119]\n"
"buffer_load_dwordx4 v[52:55], v14, s[36:39], s85 offen offset:1024\n"
"v_exp_f32 v94, v94\n"
"v_mfma_f32_16x16x32_bf16 v[124:127], a[92:95], v[220:223], v[124:127]\n"
"v_exp_f32 v95, v95\n"
"v_cvt_pk_bf16_f32 v158, v92, v93\n"
"buffer_load_dwordx4 v[56:59], v14, s[36:39], s85 offen offset:2048\n"
"buffer_load_dwordx4 v[60:63], v14, s[36:39], s85 offen offset:3072\n"
"v_cvt_pk_bf16_f32 v159, v94, v95\n"
"s_waitcnt vmcnt(20)\n"
"s_barrier\n"
".LAbar15:\n"
"v_add_u32 v172, 0x10000, v172\n"
"v_add_u32 v173, 0x10000, v173\n"
"ds_read_b64_tr_b16 a[96:97], v172 offset:0\n"
"ds_read_b64_tr_b16 a[98:99], v172 offset:4096\n"
"ds_read_b64_tr_b16 a[100:101], v173 offset:0\n"
"ds_read_b64_tr_b16 a[102:103], v173 offset:4096\n"
"ds_read_b64_tr_b16 a[104:105], v172 offset:512\n"
"ds_read_b64_tr_b16 a[106:107], v172 offset:4608\n"
"ds_read_b64_tr_b16 a[108:109], v173 offset:512\n"
"ds_read_b64_tr_b16 a[110:111], v173 offset:4608\n"
"v_mfma_f32_16x16x32_bf16 v[136:139], v[176:179], v[144:147], v[136:139]\n"
"ds_read_b64_tr_b16 a[112:113], v172 offset:1024\n"
"ds_read_b64_tr_b16 a[114:115], v172 offset:5120\n"
"v_mfma_f32_16x16x32_bf16 v[140:143], v[176:179], v[148:151], v[140:143]\n"
"ds_read_b64_tr_b16 a[116:117], v173 offset:1024\n"
"ds_read_b64_tr_b16 a[118:119], v173 offset:5120\n"
"v_mfma_f32_16x16x32_bf16 v[136:139], v[176:179], v[152:155], v[136:139]\n"
"ds_read_b64_tr_b16 a[120:121], v172 offset:1536\n"
"ds_read_b64_tr_b16 a[122:123], v172 offset:5632\n"
"v_mfma_f32_16x16x32_bf16 v[140:143], v[176:179], v[156:159], v[140:143]\n"
"s_waitcnt lgkmcnt(11)\n"
"ds_read_b64_tr_b16 a[124:125], v173 offset:1536\n"
"ds_read_b64_tr_b16 a[126:127], v173 offset:5632\n"
"v_max3_f32 v188, v96, v97, v98\n"
"v_max3_f32 v189, v100, v101, v102\n"
"v_max3_f32 v188, v188, v99, v104\n"
"v_max3_f32 v189, v189, v103, v108\n"
"v_mfma_f32_16x16x32_bf16 a[160:163], a[96:99], v[144:147], a[160:163]\n"
"s_waitcnt lgkmcnt(8)\n"
"s_add_u32 s84, s51, 2\n"
"s_and_b32 s84, s84, 15\n"
"v_mfma_f32_16x16x32_bf16 a[168:171], a[100:103], v[144:147], a[168:171]\n"
"s_lshl_b32 s84, s84, 15\n"
"v_mov_b32 v2, v10\n"
"v_mfma_f32_16x16x32_bf16 a[176:179], a[104:107], v[144:147], a[176:179]\n"
"buffer_load_dwordx4 v[192:195], v13, s[36:39], s84 offen offset:0\n"
"v_mov_b32 v3, v11\n"
"v_mfma_f32_16x16x32_bf16 a[184:187], a[108:111], v[144:147], a[184:187]\n"
"v_mfma_f32_16x16x32_bf16 a[164:167], a[96:99], v[148:151], a[164:167]\n"
"ds_read_b64_tr_b16 a[96:97], v172 offset:8192\n"
"v_mfma_f32_16x16x32_bf16 a[172:175], a[100:103], v[148:151], a[172:175]\n"
"ds_read_b64_tr_b16 a[98:99], v172 offset:12288\n"
"v_mfma_f32_16x16x32_bf16 a[180:183], a[104:107], v[148:151], a[180:183]\n"
"ds_read_b64_tr_b16 a[100:101], v173 offset:8192\n"
"buffer_load_dwordx4 v[196:199], v13, s[36:39], s84 offen offset:1024\n"
"v_mfma_f32_16x16x32_bf16 a[188:191], a[108:111], v[148:151], a[188:191]\n"
"s_waitcnt lgkmcnt(3)\n"
"ds_read_b64_tr_b16 a[102:103], v173 offset:12288\n"
"v_mfma_f32_16x16x32_bf16 a[192:195], a[112:115], v[144:147], a[192:195]\n"
"ds_read_b64_tr_b16 a[104:105], v172 offset:8704\n"
"v_mfma_f32_16x16x32_bf16 a[200:203], a[116:119], v[144:147], a[200:203]\n"
"ds_read_b64_tr_b16 a[106:107], v172 offset:12800\n"
"v_mfma_f32_16x16x32_bf16 a[208:211], a[120:123], v[144:147], a[208:211]\n"
"ds_read_b64_tr_b16 a[108:109], v173 offset:8704\n"
"buffer_load_dwordx4 v[200:203], v13, s[36:39], s84 offen offset:2048\n"
"v_mfma_f32_16x16x32_bf16 a[216:219], a[124:127], v[144:147], a[216:219]\n"
"ds_read_b64_tr_b16 a[110:111], v173 offset:12800\n"
"v_mfma_f32_16x16x32_bf16 a[196:199], a[112:115], v[148:151], a[196:199]\n"
"ds_read_b64_tr_b16 a[112:113], v172 offset:9216\n"
"v_mfma_f32_16x16x32_bf16 a[204:207], a[116:119], v[148:151], a[204:207]\n"
"ds_read_b64_tr_b16 a[114:115], v172 offset:13312\n"
"v_mfma_f32_16x16x32_bf16 a[212:215], a[120:123], v[148:151], a[212:215]\n"
"ds_read_b64_tr_b16 a[116:117], v173 offset:9216\n"
"buffer_load_dwordx4 v[204:207], v13, s[36:39], s84 offen offset:3072\n"
"v_mfma_f32_16x16x32_bf16 a[220:223], a[124:127], v[148:151], a[220:223]\n"
"s_waitcnt lgkmcnt(5)\n"
"ds_read_b64_tr_b16 a[118:119], v173 offset:13312\n"
"v_max3_f32 v188, v188, v105, v106\n"
"v_mfma_f32_16x16x32_bf16 a[160:163], a[96:99], v[152:155], a[160:163]\n"
"ds_read_b64_tr_b16 a[120:121], v172 offset:9728\n"
"v_max3_f32 v189, v189, v109, v110\n"
"v_mfma_f32_16x16x32_bf16 a[168:171], a[100:103], v[152:155], a[168:171]\n"
"ds_read_b64_tr_b16 a[122:123], v172 offset:13824\n"
"v_max3_f32 v188, v188, v107, v112\n"
"v_mfma_f32_16x16x32_bf16 a[176:179], a[104:107], v[152:155], a[176:179]\n"
"s_waitcnt lgkmcnt(5)\n"
"ds_read_b64_tr_b16 a[124:125], v173 offset:9728\n"
"buffer_load_dwordx4 v[208:211], v14, s[36:39], s84 offen offset:0\n"
"v_mfma_f32_16x16x32_bf16 a[184:187], a[108:111], v[152:155], a[184:187]\n"
"ds_read_b64_tr_b16 a[126:127], v173 offset:13824\n"
"v_max3_f32 v189, v189, v111, v116\n"
"v_mfma_f32_16x16x32_bf16 a[164:167], a[96:99], v[156:159], a[164:167]\n"
"v_max3_f32 v188, v188, v113, v114\n"
"v_max3_f32 v189, v189, v117, v118\n"
"v_mfma_f32_16x16x32_bf16 a[172:175], a[100:103], v[156:159], a[172:175]\n"
"v_max3_f32 v188, v188, v115, v120\n"
"v_max3_f32 v189, v189, v119, v124\n"
"v_mfma_f32_16x16x32_bf16 a[180:183], a[104:107], v[156:159], a[180:183]\n"
"buffer_load_dwordx4 v[212:215], v14, s[36:39], s84 offen offset:1024\n"
"v_max3_f32 v188, v188, v121, v122\n"
"v_mfma_f32_16x16x32_bf16 a[188:191], a[108:111], v[156:159], a[188:191]\n"
"s_waitcnt lgkmcnt(2)\n"
"v_max3_f32 v189, v189, v125, v126\n"
"v_max3_f32 v190, v188, v123, v127\n"
"v_mfma_f32_16x16x32_bf16 a[192:195], a[112:115], v[152:155], a[192:195]\n"
"v_max_f32 v190, v190, v189\n"
"v_cmp_lt_f32 vcc, s59, v190\n"
"v_mfma_f32_16x16x32_bf16 a[200:203], a[116:119], v[152:155], a[200:203]\n"
"v_mfma_f32_16x16x32_bf16 a[208:211], a[120:123], v[152:155], a[208:211]\n"
"s_waitcnt lgkmcnt(0)\n"
"buffer_load_dwordx4 v[216:219], v14, s[36:39], s84 offen offset:2048\n"
"v_mfma_f32_16x16x32_bf16 a[216:219], a[124:127], v[152:155], a[216:219]\n"
"v_mfma_f32_16x16x32_bf16 a[196:199], a[112:115], v[156:159], a[196:199]\n"
"v_mfma_f32_16x16x32_bf16 a[204:207], a[116:119], v[156:159], a[204:207]\n"
"v_mfma_f32_16x16x32_bf16 a[212:215], a[120:123], v[156:159], a[212:215]\n"
"buffer_load_dwordx4 v[220:223], v14, s[36:39], s84 offen offset:3072\n"
"v_mfma_f32_16x16x32_bf16 a[220:223], a[124:127], v[156:159], a[220:223]\n"
"s_cbranch_vccz .LAskip15\n"
"s_swappc_b64 s[70:71], s[74:75]\n"
".LAskip15:\n"
"v_exp_f32 v96, v96\n"
"v_exp_f32 v97, v97\n"
"v_exp_f32 v98, v98\n"
"v_exp_f32 v99, v99\n"
"v_cvt_pk_bf16_f32 v144, v96, v97\n"
"v_exp_f32 v104, v104\n"
"v_exp_f32 v105, v105\n"
"v_cvt_pk_bf16_f32 v145, v98, v99\n"
"v_exp_f32 v106, v106\n"
"v_exp_f32 v107, v107\n"
"v_cvt_pk_bf16_f32 v146, v104, v105\n"
"v_exp_f32 v100, v100\n"
"v_exp_f32 v101, v101\n"
"v_cvt_pk_bf16_f32 v147, v106, v107\n"
"v_exp_f32 v102, v102\n"
"v_exp_f32 v103, v103\n"
"v_cvt_pk_bf16_f32 v148, v100, v101\n"
"v_exp_f32 v108, v108\n"
"v_exp_f32 v109, v109\n"
"v_cvt_pk_bf16_f32 v149, v102, v103\n"
"v_exp_f32 v110, v110\n"
"v_exp_f32 v111, v111\n"
"v_cvt_pk_bf16_f32 v150, v108, v109\n"
"v_exp_f32 v112, v112\n"
"v_exp_f32 v113, v113\n"
"v_cvt_pk_bf16_f32 v151, v110, v111\n"
"v_exp_f32 v114, v114\n"
"v_exp_f32 v115, v115\n"
"v_cvt_pk_bf16_f32 v152, v112, v113\n"
"v_exp_f32 v120, v120\n"
"v_exp_f32 v121, v121\n"
"v_cvt_pk_bf16_f32 v153, v114, v115\n"
"v_exp_f32 v122, v122\n"
"v_exp_f32 v123, v123\n"
"v_cvt_pk_bf16_f32 v154, v120, v121\n"
"v_exp_f32 v116, v116\n"
"v_exp_f32 v117, v117\n"
"v_cvt_pk_bf16_f32 v155, v122, v123\n"
"v_exp_f32 v118, v118\n"
"v_exp_f32 v119, v119\n"
"v_cvt_pk_bf16_f32 v156, v116, v117\n"
"v_exp_f32 v124, v124\n"
"v_exp_f32 v125, v125\n"
"v_cvt_pk_bf16_f32 v157, v118, v119\n"
"v_exp_f32 v126, v126\n"
"v_exp_f32 v127, v127\n"
"v_cvt_pk_bf16_f32 v158, v124, v125\n"
"v_cvt_pk_bf16_f32 v159, v126, v127\n"
"s_waitcnt vmcnt(24)\n"
"s_barrier\n"
".LAbar16:\n"
"ds_read_b64_tr_b16 a[96:97], v172 offset:16384\n"
"ds_read_b64_tr_b16 a[98:99], v172 offset:20480\n"
"ds_read_b64_tr_b16 a[100:101], v173 offset:16384\n"
"ds_read_b64_tr_b16 a[102:103], v173 offset:20480\n"
"ds_read_b64_tr_b16 a[104:105], v172 offset:24576\n"
"ds_read_b64_tr_b16 a[106:107], v172 offset:28672\n"
"ds_read_b64_tr_b16 a[108:109], v173 offset:24576\n"
"ds_read_b64_tr_b16 a[110:111], v173 offset:28672\n"
"v_mfma_f32_16x16x32_bf16 v[136:139], v[176:179], v[144:147], v[136:139]\n"
"ds_read_b64_tr_b16 a[112:113], v172 offset:16896\n"
"ds_read_b64_tr_b16 a[114:115], v172 offset:20992\n"
"v_mfma_f32_16x16x32_bf16 v[140:143], v[176:179], v[148:151], v[140:143]\n"
"ds_read_b64_tr_b16 a[116:117], v173 offset:16896\n"
"ds_read_b64_tr_b16 a[118:119], v173 offset:20992\n"
"v_mfma_f32_16x16x32_bf16 v[136:139], v[176:179], v[152:155], v[136:139]\n"
"ds_read_b64_tr_b16 a[120:121], v172 offset:25088\n"
"ds_read_b64_tr_b16 a[122:123], v172 offset:29184\n"
"v_mfma_f32_16x16x32_bf16 v[140:143], v[176:179], v[156:159], v[140:143]\n"
"s_waitcnt lgkmcnt(12)\n"
"ds_read_b64_tr_b16 a[124:125], v173 offset:25088\n"
"ds_read_b64_tr_b16 a[126:127], v173 offset:29184\n"
"v_mfma_f32_16x16x32_bf16 a[160:163], a[96:99], v[144:147], a[160:163]\n"
"s_waitcnt lgkmcnt(12)\n"
"v_rcp_f32 v6, v136\n"
"v_mfma_f32_16x16x32_bf16 a[168:171], a[100:103], v[144:147], a[168:171]\n"
"s_nop 0\n"
"v_rcp_f32 v7, v140\n"
"v_mfma_f32_16x16x32_bf16 a[164:167], a[96:99], v[148:151], a[164:167]\n"
"ds_read_b64_tr_b16 a[96:97], v172 offset:17408\n"
"v_mfma_f32_16x16x32_bf16 a[172:175], a[100:103], v[148:151], a[172:175]\n"
"s_waitcnt lgkmcnt(5)\n"
"ds_read_b64_tr_b16 a[98:99], v172 offset:21504\n"
"v_mfma_f32_16x16x32_bf16 a[160:163], a[104:107], v[152:155], a[160:163]\n"
"ds_read_b64_tr_b16 a[100:101], v173 offset:17408\n"
"v_mfma_f32_16x16x32_bf16 a[168:171], a[108:111], v[152:155], a[168:171]\n"
"ds_read_b64_tr_b16 a[102:103], v173 offset:21504\n"
"v_mfma_f32_16x16x32_bf16 a[164:167], a[104:107], v[156:159], a[164:167]\n"
"ds_read_b64_tr_b16 a[104:105], v172 offset:25600\n"
"v_mfma_f32_16x16x32_bf16 a[172:175], a[108:111], v[156:159], a[172:175]\n"
"ds_read_b64_tr_b16 a[106:107], v172 offset:29696\n"
"v_mfma_f32_16x16x32_bf16 a[176:179], a[112:115], v[144:147], a[176:179]\n"
"ds_read_b64_tr_b16 a[108:109], v173 offset:25600\n"
"v_mfma_f32_16x16x32_bf16 a[184:187], a[116:119], v[144:147], a[184:187]\n"
"ds_read_b64_tr_b16 a[110:111], v173 offset:29696\n"
"v_mfma_f32_16x16x32_bf16 a[180:183], a[112:115], v[148:151], a[180:183]\n"
"ds_read_b64_tr_b16 a[112:113], v172 offset:17920\n"
"v_accvgpr_read_b32 v64, a160\n"
"v_mfma_f32_16x16x32_bf16 a[188:191], a[116:119], v[148:151], a[188:191]\n"
"s_waitcnt lgkmcnt(5)\n"
"ds_read_b64_tr_b16 a[114:115], v172 offset:22016\n"
"v_accvgpr_read_b32 v65, a161\n"
"v_mfma_f32_16x16x32_bf16 a[176:179], a[120:123], v[152:155], a[176:179]\n"
"ds_read_b64_tr_b16 a[116:117], v173 offset:17920\n"
"v_accvgpr_read_b32 v66, a162\n"
"v_mfma_f32_16x16x32_bf16 a[184:187], a[124:127], v[152:155], a[184:187]\n"
"ds_read_b64_tr_b16 a[118:119], v173 offset:22016\n"
"v_accvgpr_read_b32 v67, a163\n"
"v_mfma_f32_16x16x32_bf16 a[180:183], a[120:123], v[156:159], a[180:183]\n"
"ds_read_b64_tr_b16 a[120:121], v172 offset:26112\n"
"v_accvgpr_read_b32 v68, a168\n"
"v_mfma_f32_16x16x32_bf16 a[188:191], a[124:127], v[156:159], a[188:191]\n"
"ds_read_b64_tr_b16 a[122:123], v172 offset:30208\n"
"v_accvgpr_read_b32 v69, a169\n"
"v_mfma_f32_16x16x32_bf16 a[192:195], a[96:99], v[144:147], a[192:195]\n"
"ds_read_b64_tr_b16 a[124:125], v173 offset:26112\n"
"v_accvgpr_read_b32 v70, a170\n"
"v_mfma_f32_16x16x32_bf16 a[200:203], a[100:103], v[144:147], a[200:203]\n"
"ds_read_b64_tr_b16 a[126:127], v173 offset:30208\n"
"v_accvgpr_read_b32 v71, a171\n"
"v_mfma_f32_16x16x32_bf16 a[196:199], a[96:99], v[148:151], a[196:199]\n"
"v_mul_f32 v64, v64, v6\n"
"v_mul_f32 v65, v65, v6\n"
"v_mfma_f32_16x16x32_bf16 a[204:207], a[100:103], v[148:151], a[204:207]\n"
"s_waitcnt lgkmcnt(4)\n"
"v_mul_f32 v66, v66, v6\n"
"v_mul_f32 v67, v67, v6\n"
"v_mfma_f32_16x16x32_bf16 a[192:195], a[104:107], v[152:155], a[192:195]\n"
"v_mul_f32 v68, v68, v6\n"
"v_mul_f32 v69, v69, v6\n"
"v_mfma_f32_16x16x32_bf16 a[200:203], a[108:111], v[152:155], a[200:203]\n"
"v_mul_f32 v70, v70, v6\n"
"v_mul_f32 v71, v71, v6\n"
"v_mfma_f32_16x16x32_bf16 a[196:199], a[104:107], v[156:159], a[196:199]\n"
"v_cvt_pk_bf16_f32 v180, v64, v65\n"
"v_cvt_pk_bf16_f32 v181, v66, v67\n"
"v_mfma_f32_16x16x32_bf16 a[204:207], a[108:111], v[156:159], a[204:207]\n"
"v_cvt_pk_bf16_f32 v182, v68, v69\n"
"v_cvt_pk_bf16_f32 v183, v70, v71\n"
"v_mfma_f32_16x16x32_bf16 a[208:211], a[112:115], v[144:147], a[208:211]\n"
"ds_write_b128 v175, v[180:183] offset:32768\n"
"v_mfma_f32_16x16x32_bf16 a[216:219], a[116:119], v[144:147], a[216:219]\n"
"v_accvgpr_read_b32 v72, a164\n"
"v_accvgpr_read_b32 v73, a165\n"
"v_mfma_f32_16x16x32_bf16 a[212:215], a[112:115], v[148:151], a[212:215]\n"
"v_accvgpr_read_b32 v74, a166\n"
"v_accvgpr_read_b32 v75, a167\n"
"v_mfma_f32_16x16x32_bf16 a[220:223], a[116:119], v[148:151], a[220:223]\n"
"s_waitcnt lgkmcnt(1)\n"
"v_accvgpr_read_b32 v76, a172\n"
"v_accvgpr_read_b32 v77, a173\n"
"v_mfma_f32_16x16x32_bf16 a[208:211], a[120:123], v[152:155], a[208:211]\n"
"v_accvgpr_read_b32 v78, a174\n"
"v_accvgpr_read_b32 v79, a175\n"
"v_mfma_f32_16x16x32_bf16 a[216:219], a[124:127], v[152:155], a[216:219]\n"
"v_mul_f32 v72, v72, v7\n"
"v_mul_f32 v73, v73, v7\n"
"v_mfma_f32_16x16x32_bf16 a[212:215], a[120:123], v[156:159], a[212:215]\n"
"v_mul_f32 v74, v74, v7\n"
"v_mul_f32 v75, v75, v7\n"
"v_mfma_f32_16x16x32_bf16 a[220:223], a[124:127], v[156:159], a[220:223]\n"
"v_mul_f32 v76, v76, v7\n"
"v_mul_f32 v77, v77, v7\n"
"v_mul_f32 v78, v78, v7\n"
"v_mul_f32 v79, v79, v7\n"
"v_cvt_pk_bf16_f32 v184, v72, v73\n"
"v_cvt_pk_bf16_f32 v185, v74, v75\n"
"v_cvt_pk_bf16_f32 v186, v76, v77\n"
"v_cvt_pk_bf16_f32 v187, v78, v79\n"
"ds_write_b128 v175, v[184:187] offset:33792\n"
"v_accvgpr_read_b32 v64, a176\n"
"v_accvgpr_read_b32 v65, a177\n"
"v_accvgpr_read_b32 v66, a178\n"
"v_accvgpr_read_b32 v67, a179\n"
"v_accvgpr_read_b32 v68, a184\n"
"v_accvgpr_read_b32 v69, a185\n"
"v_accvgpr_read_b32 v70, a186\n"
"v_accvgpr_read_b32 v71, a187\n"
"v_mul_f32 v64, v64, v6\n"
"v_mul_f32 v65, v65, v6\n"
"v_mul_f32 v66, v66, v6\n"
"v_mul_f32 v67, v67, v6\n"
"v_mul_f32 v68, v68, v6\n"
"v_mul_f32 v69, v69, v6\n"
"v_mul_f32 v70, v70, v6\n"
"v_mul_f32 v71, v71, v6\n"
"s_waitcnt lgkmcnt(1)\n"
"v_cvt_pk_bf16_f32 v180, v64, v65\n"
"v_cvt_pk_bf16_f32 v181, v66, v67\n"
"v_cvt_pk_bf16_f32 v182, v68, v69\n"
"v_cvt_pk_bf16_f32 v183, v70, v71\n"
"ds_write_b128 v175, v[180:183] offset:34816\n"
"v_accvgpr_read_b32 v72, a180\n"
"v_accvgpr_read_b32 v73, a181\n"
"v_accvgpr_read_b32 v74, a182\n"
"v_accvgpr_read_b32 v75, a183\n"
"v_accvgpr_read_b32 v76, a188\n"
"v_accvgpr_read_b32 v77, a189\n"
"v_accvgpr_read_b32 v78, a190\n"
"v_accvgpr_read_b32 v79, a191\n"
"v_mul_f32 v72, v72, v7\n"
"v_mul_f32 v73, v73, v7\n"
"v_mul_f32 v74, v74, v7\n"
"v_mul_f32 v75, v75, v7\n"
"v_mul_f32 v76, v76, v7\n"
"v_mul_f32 v77, v77, v7\n"
"v_mul_f32 v78, v78, v7\n"
"v_mul_f32 v79, v79, v7\n"
"s_waitcnt lgkmcnt(1)\n"
"v_cvt_pk_bf16_f32 v184, v72, v73\n"
"v_cvt_pk_bf16_f32 v185, v74, v75\n"
"v_cvt_pk_bf16_f32 v186, v76, v77\n"
"v_cvt_pk_bf16_f32 v187, v78, v79\n"
"ds_write_b128 v175, v[184:187] offset:35840\n"
"v_accvgpr_read_b32 v64, a192\n"
"v_accvgpr_read_b32 v65, a193\n"
"v_accvgpr_read_b32 v66, a194\n"
"v_accvgpr_read_b32 v67, a195\n"
"v_accvgpr_read_b32 v68, a200\n"
"v_accvgpr_read_b32 v69, a201\n"
"v_accvgpr_read_b32 v70, a202\n"
"v_accvgpr_read_b32 v71, a203\n"
"v_mul_f32 v64, v64, v6\n"
"v_mul_f32 v65, v65, v6\n"
"v_mul_f32 v66, v66, v6\n"
"v_mul_f32 v67, v67, v6\n"
"v_mul_f32 v68, v68, v6\n"
"v_mul_f32 v69, v69, v6\n"
"v_mul_f32 v70, v70, v6\n"
"v_mul_f32 v71, v71, v6\n"
"s_waitcnt lgkmcnt(1)\n"
"v_cvt_pk_bf16_f32 v180, v64, v65\n"
"v_cvt_pk_bf16_f32 v181, v66, v67\n"
"v_cvt_pk_bf16_f32 v182, v68, v69\n"
"v_cvt_pk_bf16_f32 v183, v70, v71\n"
"ds_write_b128 v175, v[180:183] offset:36864\n"
"v_accvgpr_read_b32 v72, a196\n"
"v_accvgpr_read_b32 v73, a197\n"
"v_accvgpr_read_b32 v74, a198\n"
"v_accvgpr_read_b32 v75, a199\n"
"v_accvgpr_read_b32 v76, a204\n"
"v_accvgpr_read_b32 v77, a205\n"
"v_accvgpr_read_b32 v78, a206\n"
"v_accvgpr_read_b32 v79, a207\n"
"v_mul_f32 v72, v72, v7\n"
"v_mul_f32 v73, v73, v7\n"
"v_mul_f32 v74, v74, v7\n"
"v_mul_f32 v75, v75, v7\n"
"v_mul_f32 v76, v76, v7\n"
"v_mul_f32 v77, v77, v7\n"
"v_mul_f32 v78, v78, v7\n"
"v_mul_f32 v79, v79, v7\n"
"s_waitcnt lgkmcnt(1)\n"
"v_cvt_pk_bf16_f32 v184, v72, v73\n"
"v_cvt_pk_bf16_f32 v185, v74, v75\n"
"v_cvt_pk_bf16_f32 v186, v76, v77\n"
"v_cvt_pk_bf16_f32 v187, v78, v79\n"
"ds_write_b128 v175, v[184:187] offset:37888\n"
"v_accvgpr_read_b32 v64, a208\n"
"v_accvgpr_read_b32 v65, a209\n"
"v_accvgpr_read_b32 v66, a210\n"
"v_accvgpr_read_b32 v67, a211\n"
"v_accvgpr_read_b32 v68, a216\n"
"v_accvgpr_read_b32 v69, a217\n"
"v_accvgpr_read_b32 v70, a218\n"
"v_accvgpr_read_b32 v71, a219\n"
"v_mul_f32 v64, v64, v6\n"
"v_mul_f32 v65, v65, v6\n"
"v_mul_f32 v66, v66, v6\n"
"v_mul_f32 v67, v67, v6\n"
"v_mul_f32 v68, v68, v6\n"
"v_mul_f32 v69, v69, v6\n"
"v_mul_f32 v70, v70, v6\n"
"v_mul_f32 v71, v71, v6\n"
"s_waitcnt lgkmcnt(1)\n"
"v_cvt_pk_bf16_f32 v180, v64, v65\n"
"v_cvt_pk_bf16_f32 v181, v66, v67\n"
"v_cvt_pk_bf16_f32 v182, v68, v69\n"
"v_cvt_pk_bf16_f32 v183, v70, v71\n"
"ds_write_b128 v175, v[180:183] offset:38912\n"
"v_accvgpr_read_b32 v72, a212\n"
"v_accvgpr_read_b32 v73, a213\n"
"v_accvgpr_read_b32 v74, a214\n"
"v_accvgpr_read_b32 v75, a215\n"
"v_accvgpr_read_b32 v76, a220\n"
"v_accvgpr_read_b32 v77, a221\n"
"v_accvgpr_read_b32 v78, a222\n"
"v_accvgpr_read_b32 v79, a223\n"
"v_mul_f32 v72, v72, v7\n"
"v_mul_f32 v73, v73, v7\n"
"v_mul_f32 v74, v74, v7\n"
"v_mul_f32 v75, v75, v7\n"
"v_mul_f32 v76, v76, v7\n"
"v_mul_f32 v77, v77, v7\n"
"v_mul_f32 v78, v78, v7\n"
"v_mul_f32 v79, v79, v7\n"
"s_waitcnt lgkmcnt(1)\n"
"v_cvt_pk_bf16_f32 v184, v72, v73\n"
"v_cvt_pk_bf16_f32 v185, v74, v75\n"
"v_cvt_pk_bf16_f32 v186, v76, v77\n"
"v_cvt_pk_bf16_f32 v187, v78, v79\n"
"ds_write_b128 v175, v[184:187] offset:39936\n"
"s_waitcnt vmcnt(16)\n"
"s_nop 7\n"
"s_branch .Lo\n"
".LtypeB:\n"
"s_getpc_b64 s[100:101]\n"
".LBpfb:\n"
"s_sub_u32 s100, s100, .LBpfb-.Lbeg\n"
"s_subb_u32 s101, s101, 0\n"
"s_lshl_b32 s82, s52, 2\n"
"s_add_u32 s82, s82, s48\n"
"s_lshl_b32 s82, s82, 10\n"
"v_lshlrev_b32 v26, 7, v0\n"
"v_add_u32 v26, s82, v26\n"
"v_min_u32 v26, .Lend-.Lbeg-4, v26\n"
"s_mov_b64 exec, 255\n"
"global_load_dword v25, v26, s[100:101]\n"
"s_mov_b64 exec, -1\n"
"v_bfe_u32 v18, v0, 2, 3\n"
"v_lshrrev_b32 v19, 5, v0\n"
"v_and_b32 v20, 3, v0\n"
"v_lshrrev_b32 v21, 2, v18\n"
"v_lshlrev_b32 v21, 1, v21\n"
"v_xor_b32 v20, v20, v21\n"
"v_lshlrev_b32 v18, 8, v18\n"
"v_lshl_add_u32 v18, v19, 6, v18\n"
"v_lshl_add_u32 v22, v20, 4, v18\n"
"v_xor_b32 v20, 1, v20\n"
"v_lshl_add_u32 v23, v20, 4, v18\n"
"v_add_u32 v2, 1024, v22\n"
"v_add_u32 v3, 128, v22\n"
"v_add_u32 v4, 1024, v23\n"
"v_add_u32 v5, 128, v23\n"
"s_add_u32 s80, s50, 0\n"
"s_and_b32 s80, s80, 7\n"
"s_lshl_b32 s80, s80, 14\n"
"s_add_u32 s62, s80, s60\n"
"s_add_u32 s80, s50, 1\n"
"s_and_b32 s80, s80, 7\n"
"s_lshl_b32 s80, s80, 14\n"
"s_add_u32 s63, s80, s60\n"
"s_add_u32 s80, s50, 2\n"
"s_and_b32 s80, s80, 7\n"
"s_lshl_b32 s80, s80, 14\n"
"s_add_u32 s64, s80, s60\n"
"s_add_u32 s80, s50, 3\n"
"s_and_b32 s80, s80, 7\n"
"s_lshl_b32 s80, s80, 14\n"
"s_add_u32 s65, s80, s60\n"
"s_add_u32 s80, s50, 4\n"
"s_and_b32 s80, s80, 7\n"
"s_lshl_b32 s80, s80, 14\n"
"s_add_u32 s66, s80, s60\n"
"s_add_u32 s80, s50, 5\n"
"s_and_b32 s80, s80, 7\n"
"s_lshl_b32 s80, s80, 14\n"
"s_add_u32 s67, s80, s60\n"
"s_add_u32 s80, s50, 6\n"
"s_and_b32 s80, s80, 7\n"
"s_lshl_b32 s80, s80, 14\n"
"s_add_u32 s68, s80, s60\n"
"s_add_u32 s80, s50, 7\n"
"s_and_b32 s80, s80, 7\n"
"s_lshl_b32 s80, s80, 14\n"
"s_add_u32 s69, s80, s60\n"
"v_lshrrev_b32 v16, 3, v0\n"
"v_and_b32 v17, 6, v16\n"
"v_and_b32 v18, 7, v0\n"
"v_xor_b32 v17, v17, v18\n"
"v_lshlrev_b32 v17, 4, v17\n"
"v_lshl_add_u32 v1, v16, 10, v17\n"
"v_lshrrev_b32 v16, 4, v0\n"
"v_and_b32 v17, 15, v0\n"
"v_lshlrev_b32 v18, 4, v17\n"
"v_lshl_add_u32 v8, v16, 16, v18\n"
"v_add_u32 v9, 8192, v8\n"
"v_add_u32 v10, 16384, v8\n"
"v_add_u32 v11, 24576, v8\n"
"v_add_u32 v12, 32768, v8\n"
"v_add_u32 v13, 40960, v8\n"
"v_add_u32 v14, 49152, v8\n"
"v_add_u32 v15, 57344, v8\n"
"v_and_b32 v18, 6, v17\n"
"v_xor_b32 v18, v18, v16\n"
"v_lshlrev_b32 v18, 4, v18\n"
"v_and_b32 v19, 7, v17\n"
"v_lshl_add_u32 v18, v19, 7, v18\n"
"v_lshrrev_b32 v19, 3, v17\n"
"v_lshl_add_u32 v18, v19, 10, v18\n"
"v_add_u32 v6, 131072, v18\n"
"v_xor_b32 v7, 64, v6\n"
"s_mov_b32 s56, 0x05040100\n"
"s_mov_b32 s57, 0x07060302\n"
"s_mov_b32 s58, 0x3e0293ee\n"
"s_mov_b32 s59, 0x41000000\n"
"s_mov_b32 s78, 0xffff0000\n"
"s_mov_b32 s79, 0xffff0000\n"
"s_mov_b32 s61, 0\n"
"s_getpc_b64 s[72:73]\n"
".LBpc0:\n"
"s_add_u32 s72, s72, .Lrare0-.LBpc0\n"
"s_addc_u32 s73, s73, 0\n"
"s_getpc_b64 s[74:75]\n"
".LBpc1:\n"
"s_add_u32 s74, s74, .Lrare1-.LBpc1\n"
"s_addc_u32 s75, s75, 0\n"
"s_waitcnt lgkmcnt(0)\n"
"s_sub_u32 s6, s6, 1024\n"
"s_subb_u32 s7, s7, 0\n"
"s_sub_u32 s8, s8, 1024\n"
"s_subb_u32 s9, s9, 0\n"
"s_lshl_b32 s83, s51, 4\n"
"s_add_u32 s83, s83, s53\n"
"s_lshl_b32 s83, s83, 17\n"
"s_add_u32 s28, s6, s83\n"
"s_addc_u32 s29, s7, 0\n"
"s_and_b32 s29, s29, 65535\n"
"s_mov_b32 s30, 1180672\n"
"s_mov_b32 s31, 131072\n"
"s_add_u32 s32, s8, s83\n"
"s_addc_u32 s33, s9, 0\n"
"s_and_b32 s33, s33, 65535\n"
"s_mov_b32 s34, 1180672\n"
"s_mov_b32 s35, 131072\n"
"v_lshlrev_b32 v30, 6, v0\n"
"v_add_u32 v30, 1024, v30\n"
"s_add_u32 m0, s60, 0\n"
"s_nop 0\n"
"buffer_load_dwordx4 v2, s[28:31], s62 offen offset:0 nt lds\n"
"buffer_load_dwordx4 v3, s[28:31], s62 offen offset:1024 nt lds\n"
"buffer_load_dwordx4 v4, s[28:31], s62 offen offset:2048 nt lds\n"
"buffer_load_dwordx4 v5, s[28:31], s62 offen offset:3072 nt lds\n"
"s_add_u32 m0, s60, 16384\n"
"s_nop 0\n"
"buffer_load_dwordx4 v2, s[28:31], s63 offen offset:0 nt lds\n"
"buffer_load_dwordx4 v3, s[28:31], s63 offen offset:1024 nt lds\n"
"buffer_load_dwordx4 v4, s[28:31], s63 offen offset:2048 nt lds\n"
"buffer_load_dwordx4 v5, s[28:31], s63 offen offset:3072 nt lds\n"
"s_lshl_b32 s83, s51, 16\n"
"s_add_u32 s20, s4, s83\n"
"s_addc_u32 s21, s5, 0\n"
"s_and_b32 s21, s21, 65535\n"
"s_mov_b32 s22, 65536\n"
"s_mov_b32 s23, 131072\n"
"s_lshl_b32 s83, s53, 10\n"
"s_lshl_b32 s76, s48, 8\n"
"s_add_u32 s83, s83, s76\n"
"s_sub_u32 s77, 4194304, s83\n"
"s_add_u32 s24, s10, s83\n"
"s_addc_u32 s25, s11, 0\n"
"s_and_b32 s25, s25, 65535\n"
"s_mov_b32 s26, s77\n"
"s_mov_b32 s27, 131072\n"
"s_lshl_b32 s83, s53, 19\n"
"s_lshl_b32 s76, s48, 8\n"
"s_add_u32 s83, s83, s76\n"
"s_sub_u32 s77, 524288, s76\n"
"s_add_u32 s36, s12, s83\n"
"s_addc_u32 s37, s13, 0\n"
"s_and_b32 s37, s37, 65535\n"
"s_mov_b32 s38, s77\n"
"s_mov_b32 s39, 131072\n"
"s_lshl_b32 s83, s54, 4\n"
"s_add_u32 s83, s83, s51\n"
"s_mul_i32 s83, s83, 561152\n"
"s_add_u32 s40, s16, s83\n"
"s_addc_u32 s41, s17, 0\n"
"s_and_b32 s41, s41, 65535\n"
"s_mov_b32 s42, 561152\n"
"s_mov_b32 s43, 131072\n"
"s_lshl_b32 s83, s51, 17\n"
"s_lshl_b32 s76, s54, 10\n"
"s_add_u32 s83, s83, s76\n"
"s_sub_u32 s77, 131072, s76\n"
"s_add_u32 s44, s18, s83\n"
"s_addc_u32 s45, s19, 0\n"
"s_and_b32 s45, s45, 65535\n"
"s_mov_b32 s46, s77\n"
"s_mov_b32 s47, 131072\n"
"s_add_u32 s4, s28, 1048576\n"
"s_addc_u32 s5, s29, 0\n"
"s_mov_b32 s6, 132096\n"
"s_mov_b32 s7, 131072\n"
"s_add_u32 s8, s32, 1048576\n"
"s_addc_u32 s9, s33, 0\n"
"s_mov_b32 s10, 132096\n"
"s_mov_b32 s11, 131072\n"
"s_add_u32 m0, s60, 32768\n"
"s_nop 0\n"
"buffer_load_dwordx4 v2, s[32:35], s62 offen offset:0 nt lds\n"
"buffer_load_dwordx4 v3, s[32:35], s62 offen offset:1024 nt lds\n"
"buffer_load_dwordx4 v4, s[32:35], s62 offen offset:2048 nt lds\n"
"buffer_load_dwordx4 v5, s[32:35], s62 offen offset:3072 nt lds\n"
"s_add_u32 m0, s60, 65536\n"
"s_nop 0\n"
"buffer_load_dwordx4 v2, s[28:31], s64 offen offset:0 nt lds\n"
"buffer_load_dwordx4 v3, s[28:31], s64 offen offset:1024 nt lds\n"
"buffer_load_dwordx4 v4, s[28:31], s64 offen offset:2048 nt lds\n"
"buffer_load_dwordx4 v5, s[28:31], s64 offen offset:3072 nt lds\n"
"s_add_u32 m0, s60, 49152\n"
"s_nop 0\n"
"buffer_load_dwordx4 v2, s[32:35], s63 offen offset:0 nt lds\n"
"buffer_load_dwordx4 v3, s[32:35], s63 offen offset:1024 nt lds\n"
"buffer_load_dwordx4 v4, s[32:35], s63 offen offset:2048 nt lds\n"
"buffer_load_dwordx4 v5, s[32:35], s63 offen offset:3072 nt lds\n"
"s_add_u32 m0, s60, 81920\n"
"s_nop 0\n"
"buffer_load_dwordx4 v2, s[28:31], s65 offen offset:0 nt lds\n"
"buffer_load_dwordx4 v3, s[28:31], s65 offen offset:1024 nt lds\n"
"buffer_load_dwordx4 v4, s[28:31], s65 offen offset:2048 nt lds\n"
"buffer_load_dwordx4 v5, s[28:31], s65 offen offset:3072 nt lds\n"
"s_add_u32 m0, s60, 98304\n"
"s_nop 0\n"
"buffer_load_dwordx4 v2, s[32:35], s64 offen offset:0 nt lds\n"
"buffer_load_dwordx4 v3, s[32:35], s64 offen offset:1024 nt lds\n"
"buffer_load_dwordx4 v4, s[32:35], s64 offen offset:2048 nt lds\n"
"buffer_load_dwordx4 v5, s[32:35], s64 offen offset:3072 nt lds\n"
"s_add_u32 m0, s60, 114688\n"
"s_nop 0\n"
"buffer_load_dwordx4 v2, s[28:31], s66 offen offset:0 nt lds\n"
"buffer_load_dwordx4 v3, s[28:31], s66 offen offset:1024 nt lds\n"
"buffer_load_dwordx4 v4, s[28:31], s66 offen offset:2048 nt lds\n"
"buffer_load_dwordx4 v5, s[28:31], s66 offen offset:3072 nt lds\n"
"s_waitcnt vmcnt(28)\n"
"buffer_load_dwordx4 a[144:147], v2, s[32:35], s65 offen offset:0 nt\n"
"buffer_load_dwordx4 a[148:151], v3, s[32:35], s65 offen offset:1024 nt\n"
"buffer_load_dwordx4 a[152:155], v4, s[32:35], s65 offen offset:2048 nt\n"
"buffer_load_dwordx4 a[156:159], v5, s[32:35], s65 offen offset:3072 nt\n"
"s_waitcnt vmcnt(28)\n"
"buffer_load_dwordx4 a[128:131], v2, s[28:31], s67 offen offset:0 nt\n"
"buffer_load_dwordx4 a[132:135], v3, s[28:31], s67 offen offset:1024 nt\n"
"buffer_load_dwordx4 a[136:139], v4, s[28:31], s67 offen offset:2048 nt\n"
"buffer_load_dwordx4 a[140:143], v5, s[28:31], s67 offen offset:3072 nt\n"
"s_waitcnt vmcnt(28)\n"
"buffer_load_dwordx4 a[160:163], v2, s[32:35], s66 offen offset:0 nt\n"
"buffer_load_dwordx4 a[164:167], v3, s[32:35], s66 offen offset:1024 nt\n"
"buffer_load_dwordx4 a[168:171], v4, s[32:35], s66 offen offset:2048 nt\n"
"buffer_load_dwordx4 a[172:175], v5, s[32:35], s66 offen offset:3072 nt\n"
"s_waitcnt vmcnt(28)\n"
"buffer_load_dwordx4 a[176:179], v2, s[28:31], s68 offen offset:0 nt\n"
"buffer_load_dwordx4 a[180:183], v3, s[28:31], s68 offen offset:1024 nt\n"
"buffer_load_dwordx4 a[184:187], v4, s[28:31], s68 offen offset:2048 nt\n"
"buffer_load_dwordx4 a[188:191], v5, s[28:31], s68 offen offset:3072 nt\n"
"s_waitcnt vmcnt(28)\n"
"buffer_load_dwordx4 a[192:195], v2, s[32:35], s67 offen offset:0 nt\n"
"buffer_load_dwordx4 a[196:199], v3, s[32:35], s67 offen offset:1024 nt\n"
"buffer_load_dwordx4 a[200:203], v4, s[32:35], s67 offen offset:2048 nt\n"
"buffer_load_dwordx4 a[204:207], v5, s[32:35], s67 offen offset:3072 nt\n"
"s_add_u32 s80, s49, 0\n"
"s_and_b32 s80, s80, 7\n"
"s_lshl_b32 s80, s80, 7\n"
"s_lshl_b32 s81, s60, 2\n"
"s_add_u32 s80, s80, s81\n"
"s_add_u32 s81, s80, 8192\n"
"s_lshr_b32 s82, s60, 1\n"
"s_add_u32 m0, s82, 131072\n"
"s_nop 0\n"
"buffer_load_dwordx4 v1, s[20:23], s80 offen offset:0 lds\n"
"s_add_u32 m0, s82, 132096\n"
"s_nop 0\n"
"buffer_load_dwordx4 v1, s[20:23], s81 offen offset:0 lds\n"
"s_add_u32 s80, s49, 1\n"
"s_and_b32 s80, s80, 7\n"
"s_lshl_b32 s80, s80, 7\n"
"s_lshl_b32 s81, s60, 2\n"
"s_add_u32 s80, s80, s81\n"
"s_add_u32 s81, s80, 8192\n"
"s_lshr_b32 s82, s60, 1\n"
"s_add_u32 m0, s82, 139264\n"
"s_nop 0\n"
"buffer_load_dwordx4 v1, s[20:23], s80 offen offset:0 lds\n"
"s_add_u32 m0, s82, 140288\n"
"s_nop 0\n"
"buffer_load_dwordx4 v1, s[20:23], s81 offen offset:0 lds\n"
"s_add_u32 s80, s49, 2\n"
"s_and_b32 s80, s80, 7\n"
"s_lshl_b32 s80, s80, 7\n"
"s_lshl_b32 s81, s60, 2\n"
"s_add_u32 s80, s80, s81\n"
"s_add_u32 s81, s80, 8192\n"
"s_lshr_b32 s82, s60, 1\n"
"s_add_u32 m0, s82, 147456\n"
"s_nop 0\n"
"buffer_load_dwordx4 v1, s[20:23], s80 offen offset:0 lds\n"
"s_add_u32 m0, s82, 148480\n"
"s_nop 0\n"
"buffer_load_dwordx4 v1, s[20:23], s81 offen offset:0 lds\n"
"s_add_u32 s80, s49, 3\n"
"s_and_b32 s80, s80, 7\n"
"s_lshl_b32 s80, s80, 7\n"
"s_lshl_b32 s81, s60, 2\n"
"s_add_u32 s80, s80, s81\n"
"s_add_u32 s81, s80, 8192\n"
"s_lshr_b32 s82, s60, 1\n"
"s_add_u32 m0, s82, 155648\n"
"s_nop 0\n"
"buffer_load_dwordx4 v1, s[20:23], s80 offen offset:0 lds\n"
"s_add_u32 m0, s82, 156672\n"
"s_nop 0\n"
"buffer_load_dwordx4 v1, s[20:23], s81 offen offset:0 lds\n"
"s_waitcnt vmcnt(36)\n"
"buffer_load_dwordx4 a[208:211], v2, s[28:31], s69 offen offset:0 nt\n"
"buffer_load_dwordx4 a[212:215], v3, s[28:31], s69 offen offset:1024 nt\n"
"buffer_load_dwordx4 a[216:219], v4, s[28:31], s69 offen offset:2048 nt\n"
"buffer_load_dwordx4 a[220:223], v5, s[28:31], s69 offen offset:3072 nt\n"
"s_waitcnt vmcnt(36)\n"
"buffer_load_dwordx4 a[224:227], v2, s[32:35], s68 offen offset:0 nt\n"
"buffer_load_dwordx4 a[228:231], v3, s[32:35], s68 offen offset:1024 nt\n"
"buffer_load_dwordx4 a[232:235], v4, s[32:35], s68 offen offset:2048 nt\n"
"buffer_load_dwordx4 a[236:239], v5, s[32:35], s68 offen offset:3072 nt\n"
"s_waitcnt vmcnt(36)\n"
"buffer_load_dwordx4 a[240:243], v2, s[4:7], s62 offen offset:0 nt\n"
"buffer_load_dwordx4 a[244:247], v3, s[4:7], s62 offen offset:1024 nt\n"
"buffer_load_dwordx4 a[248:251], v4, s[4:7], s62 offen offset:2048 nt\n"
"buffer_load_dwordx4 a[252:255], v5, s[4:7], s62 offen offset:3072 nt\n"
"s_waitcnt vmcnt(28)\n"
"s_add_u32 s84, s49, 0\n"
"s_and_b32 s84, s84, 7\n"
"s_lshl_b32 s84, s84, 19\n"
"buffer_load_dwordx4 v[32:35], v8, s[24:27], s84 offen offset:0\n"
"buffer_load_dwordx4 v[36:39], v9, s[24:27], s84 offen offset:0\n"
"buffer_load_dwordx4 v[40:43], v10, s[24:27], s84 offen offset:0\n"
"buffer_load_dwordx4 v[44:47], v11, s[24:27], s84 offen offset:0\n"
"buffer_load_dwordx4 v[48:51], v12, s[24:27], s84 offen offset:0\n"
"buffer_load_dwordx4 v[52:55], v13, s[24:27], s84 offen offset:0\n"
"buffer_load_dwordx4 v[56:59], v14, s[24:27], s84 offen offset:0\n"
"buffer_load_dwordx4 v[60:63], v15, s[24:27], s84 offen offset:0\n"
"s_add_u32 s85, s49, 0\n"
"s_and_b32 s85, s85, 7\n"
"s_lshl_b32 s85, s85, 19\n"
"s_add_u32 s85, s85, 262144\n"
"buffer_load_dwordx4 v[64:67], v8, s[24:27], s85 offen offset:0\n"
"buffer_load_dwordx4 v[68:71], v9, s[24:27], s85 offen offset:0\n"
"buffer_load_dwordx4 v[72:75], v10, s[24:27], s85 offen offset:0\n"
"buffer_load_dwordx4 v[76:79], v11, s[24:27], s85 offen offset:0\n"
"buffer_load_dwordx4 v[80:83], v12, s[24:27], s85 offen offset:0\n"
"buffer_load_dwordx4 v[84:87], v13, s[24:27], s85 offen offset:0\n"
"buffer_load_dwordx4 v[88:91], v14, s[24:27], s85 offen offset:0\n"
"buffer_load_dwordx4 v[92:95], v15, s[24:27], s85 offen offset:0\n"
"s_waitcnt vmcnt(32)\n"
"s_barrier\n"
"ds_read_b128 a[0:3], v6 offset:0\n"
"ds_read_b128 a[4:7], v6 offset:2048\n"
"ds_read_b128 a[8:11], v6 offset:4096\n"
"ds_read_b128 a[12:15], v6 offset:6144\n"
"s_waitcnt vmcnt(8)\n"
"v_perm_b32 v16, v36, v32, s56\n"
"v_perm_b32 v17, v44, v40, s56\n"
"v_perm_b32 v18, v52, v48, s56\n"
"v_perm_b32 v19, v60, v56, s56\n"
"v_perm_b32 v20, v36, v32, s57\n"
"v_perm_b32 v21, v44, v40, s57\n"
"v_perm_b32 v22, v52, v48, s57\n"
"v_perm_b32 v23, v60, v56, s57\n"
"s_waitcnt lgkmcnt(3)\n"
"v_mfma_f32_16x16x32_bf16 v[128:131], v[16:19], a[0:3], 0\n"
"v_perm_b32 v24, v37, v33, s56\n"
"v_perm_b32 v25, v45, v41, s56\n"
"v_mfma_f32_16x16x32_bf16 v[132:135], v[20:23], a[0:3], 0\n"
"s_waitcnt lgkmcnt(2)\n"
"v_perm_b32 v26, v53, v49, s56\n"
"v_perm_b32 v27, v61, v57, s56\n"
"v_mfma_f32_16x16x32_bf16 v[160:163], v[16:19], a[4:7], 0\n"
"v_perm_b32 v28, v37, v33, s57\n"
"v_perm_b32 v29, v45, v41, s57\n"
"v_mfma_f32_16x16x32_bf16 v[164:167], v[20:23], a[4:7], 0\n"
"s_waitcnt lgkmcnt(0)\n"
"v_perm_b32 v30, v53, v49, s57\n"
"v_perm_b32 v31, v61, v57, s57\n"
"v_mfma_f32_16x16x32_bf16 v[192:195], v[16:19], a[8:11], 0\n"
"s_add_u32 s84, s49, 1\n"
"s_and_b32 s84, s84, 7\n"
"v_mfma_f32_16x16x32_bf16 v[196:199], v[20:23], a[8:11], 0\n"
"s_lshl_b32 s84, s84, 19\n"
"v_mfma_f32_16x16x32_bf16 v[224:227], v[16:19], a[12:15], 0\n"
"ds_read_b128 a[16:19], v7 offset:0\n"
"buffer_load_dwordx4 v[96:99], v8, s[24:27], s84 offen offset:0\n"
"v_mfma_f32_16x16x32_bf16 v[228:231], v[20:23], a[12:15], 0\n"
"ds_read_b128 a[20:23], v7 offset:2048\n"
"v_perm_b32 v16, v38, v34, s56\n"
"v_mfma_f32_16x16x32_bf16 v[136:139], v[24:27], a[0:3], 0\n"
"ds_read_b128 a[24:27], v7 offset:4096\n"
"v_perm_b32 v17, v46, v42, s56\n"
"v_mfma_f32_16x16x32_bf16 v[140:143], v[28:31], a[0:3], 0\n"
"ds_read_b128 a[28:31], v7 offset:6144\n"
"v_perm_b32 v18, v54, v50, s56\n"
"v_mfma_f32_16x16x32_bf16 v[168:171], v[24:27], a[4:7], 0\n"
"buffer_load_dwordx4 v[100:103], v9, s[24:27], s84 offen offset:0\n"
"v_perm_b32 v19, v62, v58, s56\n"
"v_mfma_f32_16x16x32_bf16 v[172:175], v[28:31], a[4:7], 0\n"
"v_perm_b32 v20, v38, v34, s57\n"
"v_perm_b32 v21, v46, v42, s57\n"
"v_mfma_f32_16x16x32_bf16 v[200:203], v[24:27], a[8:11], 0\n"
"v_perm_b32 v22, v54, v50, s57\n"
"v_perm_b32 v23, v62, v58, s57\n"
"v_mfma_f32_16x16x32_bf16 v[204:207], v[28:31], a[8:11], 0\n"
"v_mfma_f32_16x16x32_bf16 v[232:235], v[24:27], a[12:15], 0\n"
"buffer_load_dwordx4 v[104:107], v10, s[24:27], s84 offen offset:0\n"
"v_perm_b32 v24, v39, v35, s56\n"
"v_mfma_f32_16x16x32_bf16 v[236:239], v[28:31], a[12:15], 0\n"
"v_perm_b32 v25, v47, v43, s56\n"
"v_perm_b32 v26, v55, v51, s56\n"
"v_mfma_f32_16x16x32_bf16 v[144:147], v[16:19], a[0:3], 0\n"
"v_perm_b32 v27, v63, v59, s56\n"
"v_perm_b32 v28, v39, v35, s57\n"
"v_mfma_f32_16x16x32_bf16 v[148:151], v[20:23], a[0:3], 0\n"
"v_perm_b32 v29, v47, v43, s57\n"
"v_perm_b32 v30, v55, v51, s57\n"
"v_mfma_f32_16x16x32_bf16 v[176:179], v[16:19], a[4:7], 0\n"
"buffer_load_dwordx4 v[108:111], v11, s[24:27], s84 offen offset:0\n"
"v_perm_b32 v31, v63, v59, s57\n"
"v_mfma_f32_16x16x32_bf16 v[180:183], v[20:23], a[4:7], 0\n"
"buffer_load_dwordx4 v[112:115], v12, s[24:27], s84 offen offset:0\n"
"buffer_load_dwordx4 v[116:119], v13, s[24:27], s84 offen offset:0\n"
"buffer_load_dwordx4 v[120:123], v14, s[24:27], s84 offen offset:0\n"
"buffer_load_dwordx4 v[124:127], v15, s[24:27], s84 offen offset:0\n"
"v_mfma_f32_16x16x32_bf16 v[208:211], v[16:19], a[8:11], 0\n"
"v_mfma_f32_16x16x32_bf16 v[212:215], v[20:23], a[8:11], 0\n"
"v_mfma_f32_16x16x32_bf16 v[240:243], v[16:19], a[12:15], 0\n"
"s_waitcnt vmcnt(8)\n"
"v_perm_b32 v16, v68, v64, s56\n"
"v_perm_b32 v17, v76, v72, s56\n"
"v_mfma_f32_16x16x32_bf16 v[244:247], v[20:23], a[12:15], 0\n"
"v_perm_b32 v18, v84, v80, s56\n"
"v_perm_b32 v19, v92, v88, s56\n"
"v_mfma_f32_16x16x32_bf16 v[152:155], v[24:27], a[0:3], 0\n"
"v_perm_b32 v20, v68, v64, s57\n"
"v_perm_b32 v21, v76, v72, s57\n"
"v_mfma_f32_16x16x32_bf16 v[156:159], v[28:31], a[0:3], 0\n"
"v_perm_b32 v22, v84, v80, s57\n"
"v_perm_b32 v23, v92, v88, s57\n"
"v_mfma_f32_16x16x32_bf16 v[184:187], v[24:27], a[4:7], 0\n"
"s_waitcnt lgkmcnt(0)\n"
"v_mfma_f32_16x16x32_bf16 v[188:191], v[28:31], a[4:7], 0\n"
"s_add_u32 s85, s49, 1\n"
"s_and_b32 s85, s85, 7\n"
"v_mfma_f32_16x16x32_bf16 v[216:219], v[24:27], a[8:11], 0\n"
"s_lshl_b32 s85, s85, 19\n"
"s_add_u32 s85, s85, 262144\n"
"v_mfma_f32_16x16x32_bf16 v[220:223], v[28:31], a[8:11], 0\n"
"buffer_load_dwordx4 v[32:35], v8, s[24:27], s85 offen offset:0\n"
"v_mfma_f32_16x16x32_bf16 v[248:251], v[24:27], a[12:15], 0\n"
"v_perm_b32 v24, v69, v65, s56\n"
"v_perm_b32 v25, v77, v73, s56\n"
"v_mfma_f32_16x16x32_bf16 v[252:255], v[28:31], a[12:15], 0\n"
"v_perm_b32 v26, v85, v81, s56\n"
"v_perm_b32 v27, v93, v89, s56\n"
"v_mfma_f32_16x16x32_bf16 v[128:131], v[16:19], a[16:19], v[128:131]\n"
"v_perm_b32 v28, v69, v65, s57\n"
"v_perm_b32 v29, v77, v73, s57\n"
"v_mfma_f32_16x16x32_bf16 v[132:135], v[20:23], a[16:19], v[132:135]\n"
"buffer_load_dwordx4 v[36:39], v9, s[24:27], s85 offen offset:0\n"
"v_perm_b32 v30, v85, v81, s57\n"
"v_mfma_f32_16x16x32_bf16 v[160:163], v[16:19], a[20:23], v[160:163]\n"
"v_perm_b32 v31, v93, v89, s57\n"
"v_mfma_f32_16x16x32_bf16 v[164:167], v[20:23], a[20:23], v[164:167]\n"
"v_mfma_f32_16x16x32_bf16 v[192:195], v[16:19], a[24:27], v[192:195]\n"
"v_mfma_f32_16x16x32_bf16 v[196:199], v[20:23], a[24:27], v[196:199]\n"
"buffer_load_dwordx4 v[40:43], v10, s[24:27], s85 offen offset:0\n"
"v_mfma_f32_16x16x32_bf16 v[224:227], v[16:19], a[28:31], v[224:227]\n"
"ds_read_b128 a[0:3], v6 offset:8192\n"
"v_perm_b32 v16, v70, v66, s56\n"
"v_mfma_f32_16x16x32_bf16 v[228:231], v[20:23], a[28:31], v[228:231]\n"
"ds_read_b128 a[4:7], v6 offset:10240\n"
"v_perm_b32 v17, v78, v74, s56\n"
"v_mfma_f32_16x16x32_bf16 v[136:139], v[24:27], a[16:19], v[136:139]\n"
"ds_read_b128 a[8:11], v6 offset:12288\n"
"v_perm_b32 v18, v86, v82, s56\n"
"v_mfma_f32_16x16x32_bf16 v[140:143], v[28:31], a[16:19], v[140:143]\n"
"ds_read_b128 a[12:15], v6 offset:14336\n"
"buffer_load_dwordx4 v[44:47], v11, s[24:27], s85 offen offset:0\n"
"v_mfma_f32_16x16x32_bf16 v[168:171], v[24:27], a[20:23], v[168:171]\n"
"v_perm_b32 v19, v94, v90, s56\n"
"v_perm_b32 v20, v70, v66, s57\n"
"v_mfma_f32_16x16x32_bf16 v[172:175], v[28:31], a[20:23], v[172:175]\n"
"v_perm_b32 v21, v78, v74, s57\n"
"v_perm_b32 v22, v86, v82, s57\n"
"v_mfma_f32_16x16x32_bf16 v[200:203], v[24:27], a[24:27], v[200:203]\n"
"v_perm_b32 v23, v94, v90, s57\n"
"v_mfma_f32_16x16x32_bf16 v[204:207], v[28:31], a[24:27], v[204:207]\n"
"buffer_load_dwordx4 v[48:51], v12, s[24:27], s85 offen offset:0\n"
"v_mfma_f32_16x16x32_bf16 v[232:235], v[24:27], a[28:31], v[232:235]\n"
"v_perm_b32 v24, v71, v67, s56\n"
"v_perm_b32 v25, v79, v75, s56\n"
"v_mfma_f32_16x16x32_bf16 v[236:239], v[28:31], a[28:31], v[236:239]\n"
"v_perm_b32 v26, v87, v83, s56\n"
"v_perm_b32 v27, v95, v91, s56\n"
"v_mfma_f32_16x16x32_bf16 v[144:147], v[16:19], a[16:19], v[144:147]\n"
"v_perm_b32 v28, v71, v67, s57\n"
"v_perm_b32 v29, v79, v75, s57\n"
"v_mfma_f32_16x16x32_bf16 v[148:151], v[20:23], a[16:19], v[148:151]\n"
"buffer_load_dwordx4 v[52:55], v13, s[24:27], s85 offen offset:0\n"
"v_perm_b32 v30, v87, v83, s57\n"
"v_mfma_f32_16x16x32_bf16 v[176:179], v[16:19], a[20:23], v[176:179]\n"
"v_perm_b32 v31, v95, v91, s57\n"
"v_mfma_f32_16x16x32_bf16 v[180:183], v[20:23], a[20:23], v[180:183]\n"
"v_mfma_f32_16x16x32_bf16 v[208:211], v[16:19], a[24:27], v[208:211]\n"
"v_mfma_f32_16x16x32_bf16 v[212:215], v[20:23], a[24:27], v[212:215]\n"
"buffer_load_dwordx4 v[56:59], v14, s[24:27], s85 offen offset:0\n"
"v_mfma_f32_16x16x32_bf16 v[240:243], v[16:19], a[28:31], v[240:243]\n"
"s_waitcnt vmcnt(7)\n"
"v_perm_b32 v16, v100, v96, s56\n"
"v_perm_b32 v17, v108, v104, s56\n"
"v_mfma_f32_16x16x32_bf16 v[244:247], v[20:23], a[28:31], v[244:247]\n"
"v_perm_b32 v18, v116, v112, s56\n"
"v_perm_b32 v19, v124, v120, s56\n"
"v_mfma_f32_16x16x32_bf16 v[152:155], v[24:27], a[16:19], v[152:155]\n"
"v_perm_b32 v20, v100, v96, s57\n"
"v_perm_b32 v21, v108, v104, s57\n"
"buffer_load_dwordx4 v[60:63], v15, s[24:27], s85 offen offset:0\n"
"v_mfma_f32_16x16x32_bf16 v[156:159], v[28:31], a[16:19], v[156:159]\n"
"v_perm_b32 v22, v116, v112, s57\n"
"v_perm_b32 v23, v124, v120, s57\n"
"v_mfma_f32_16x16x32_bf16 v[184:187], v[24:27], a[20:23], v[184:187]\n"
"s_waitcnt lgkmcnt(0)\n"
"v_mfma_f32_16x16x32_bf16 v[188:191], v[28:31], a[20:23], v[188:191]\n"
"s_add_u32 s84, s49, 2\n"
"s_and_b32 s84, s84, 7\n"
"v_mfma_f32_16x16x32_bf16 v[216:219], v[24:27], a[24:27], v[216:219]\n"
"s_lshl_b32 s84, s84, 19\n"
"v_mfma_f32_16x16x32_bf16 v[220:223], v[28:31], a[24:27], v[220:223]\n"
"buffer_load_dwordx4 v[64:67], v8, s[24:27], s84 offen offset:0\n"
"v_mfma_f32_16x16x32_bf16 v[248:251], v[24:27], a[28:31], v[248:251]\n"
"v_perm_b32 v24, v101, v97, s56\n"
"v_perm_b32 v25, v109, v105, s56\n"
"v_mfma_f32_16x16x32_bf16 v[252:255], v[28:31], a[28:31], v[252:255]\n"
"v_perm_b32 v26, v117, v113, s56\n"
"v_perm_b32 v27, v125, v121, s56\n"
"v_mfma_f32_16x16x32_bf16 v[128:131], v[16:19], a[0:3], v[128:131]\n"
"v_perm_b32 v28, v101, v97, s57\n"
"v_perm_b32 v29, v109, v105, s57\n"
"v_mfma_f32_16x16x32_bf16 v[132:135], v[20:23], a[0:3], v[132:135]\n"
"buffer_load_dwordx4 v[68:71], v9, s[24:27], s84 offen offset:0\n"
"v_perm_b32 v30, v117, v113, s57\n"
"v_mfma_f32_16x16x32_bf16 v[160:163], v[16:19], a[4:7], v[160:163]\n"
"v_perm_b32 v31, v125, v121, s57\n"
"v_mfma_f32_16x16x32_bf16 v[164:167], v[20:23], a[4:7], v[164:167]\n"
"v_mfma_f32_16x16x32_bf16 v[192:195], v[16:19], a[8:11], v[192:195]\n"
"v_mfma_f32_16x16x32_bf16 v[196:199], v[20:23], a[8:11], v[196:199]\n"
"buffer_load_dwordx4 v[72:75], v10, s[24:27], s84 offen offset:0\n"
"v_mfma_f32_16x16x32_bf16 v[224:227], v[16:19], a[12:15], v[224:227]\n"
"ds_read_b128 a[16:19], v7 offset:8192\n"
"v_perm_b32 v16, v102, v98, s56\n"
"v_mfma_f32_16x16x32_bf16 v[228:231], v[20:23], a[12:15], v[228:231]\n"
"ds_read_b128 a[20:23], v7 offset:10240\n"
"v_perm_b32 v17, v110, v106, s56\n"
"v_mfma_f32_16x16x32_bf16 v[136:139], v[24:27], a[0:3], v[136:139]\n"
"ds_read_b128 a[24:27], v7 offset:12288\n"
"v_perm_b32 v18, v118, v114, s56\n"
"v_mfma_f32_16x16x32_bf16 v[140:143], v[28:31], a[0:3], v[140:143]\n"
"ds_read_b128 a[28:31], v7 offset:14336\n"
"buffer_load_dwordx4 v[76:79], v11, s[24:27], s84 offen offset:0\n"
"v_mfma_f32_16x16x32_bf16 v[168:171], v[24:27], a[4:7], v[168:171]\n"
"v_perm_b32 v19, v126, v122, s56\n"
"v_perm_b32 v20, v102, v98, s57\n"
"v_mfma_f32_16x16x32_bf16 v[172:175], v[28:31], a[4:7], v[172:175]\n"
"v_perm_b32 v21, v110, v106, s57\n"
"v_perm_b32 v22, v118, v114, s57\n"
"v_mfma_f32_16x16x32_bf16 v[200:203], v[24:27], a[8:11], v[200:203]\n"
"v_perm_b32 v23, v126, v122, s57\n"
"v_mfma_f32_16x16x32_bf16 v[204:207], v[28:31], a[8:11], v[204:207]\n"
"buffer_load_dwordx4 v[80:83], v12, s[24:27], s84 offen offset:0\n"
"v_mfma_f32_16x16x32_bf16 v[232:235], v[24:27], a[12:15], v[232:235]\n"
"v_perm_b32 v24, v103, v99, s56\n"
"v_perm_b32 v25, v111, v107, s56\n"
"v_mfma_f32_16x16x32_bf16 v[236:239], v[28:31], a[12:15], v[236:239]\n"
"v_perm_b32 v26, v119, v115, s56\n"
"v_perm_b32 v27, v127, v123, s56\n"
"v_mfma_f32_16x16x32_bf16 v[144:147], v[16:19], a[0:3], v[144:147]\n"
"v_perm_b32 v28, v103, v99, s57\n"
"v_perm_b32 v29, v111, v107, s57\n"
"v_mfma_f32_16x16x32_bf16 v[148:151], v[20:23], a[0:3], v[148:151]\n"
"buffer_load_dwordx4 v[84:87], v13, s[24:27], s84 offen offset:0\n"
"v_perm_b32 v30, v119, v115, s57\n"
"v_mfma_f32_16x16x32_bf16 v[176:179], v[16:19], a[4:7], v[176:179]\n"
"v_perm_b32 v31, v127, v123, s57\n"
"v_mfma_f32_16x16x32_bf16 v[180:183], v[20:23], a[4:7], v[180:183]\n"
"v_mfma_f32_16x16x32_bf16 v[208:211], v[16:19], a[8:11], v[208:211]\n"
"v_mfma_f32_16x16x32_bf16 v[212:215], v[20:23], a[8:11], v[212:215]\n"
"buffer_load_dwordx4 v[88:91], v14, s[24:27], s84 offen offset:0\n"
"v_mfma_f32_16x16x32_bf16 v[240:243], v[16:19], a[12:15], v[240:243]\n"
"s_waitcnt vmcnt(7)\n"
"v_perm_b32 v16, v36, v32, s56\n"
"v_perm_b32 v17, v44, v40, s56\n"
"v_mfma_f32_16x16x32_bf16 v[244:247], v[20:23], a[12:15], v[244:247]\n"
"v_perm_b32 v18, v52, v48, s56\n"
"v_perm_b32 v19, v60, v56, s56\n"
"v_mfma_f32_16x16x32_bf16 v[152:155], v[24:27], a[0:3], v[152:155]\n"
"v_perm_b32 v20, v36, v32, s57\n"
"v_perm_b32 v21, v44, v40, s57\n"
"buffer_load_dwordx4 v[92:95], v15, s[24:27], s84 offen offset:0\n"
"v_mfma_f32_16x16x32_bf16 v[156:159], v[28:31], a[0:3], v[156:159]\n"
"v_perm_b32 v22, v52, v48, s57\n"
"v_perm_b32 v23, v60, v56, s57\n"
"v_mfma_f32_16x16x32_bf16 v[184:187], v[24:27], a[4:7], v[184:187]\n"
"s_waitcnt lgkmcnt(0)\n"
"v_mfma_f32_16x16x32_bf16 v[188:191], v[28:31], a[4:7], v[188:191]\n"
"s_add_u32 s85, s49, 2\n"
"s_and_b32 s85, s85, 7\n"
"v_mfma_f32_16x16x32_bf16 v[216:219], v[24:27], a[8:11], v[216:219]\n"
"s_lshl_b32 s85, s85, 19\n"
"s_add_u32 s85, s85, 262144\n"
"v_mfma_f32_16x16x32_bf16 v[220:223], v[28:31], a[8:11], v[220:223]\n"
"buffer_load_dwordx4 v[96:99], v8, s[24:27], s85 offen offset:0\n"
"v_mfma_f32_16x16x32_bf16 v[248:251], v[24:27], a[12:15], v[248:251]\n"
"v_perm_b32 v24, v37, v33, s56\n"
"v_perm_b32 v25, v45, v41, s56\n"
"v_mfma_f32_16x16x32_bf16 v[252:255], v[28:31], a[12:15], v[252:255]\n"
"v_perm_b32 v26, v53, v49, s56\n"
"v_perm_b32 v27, v61, v57, s56\n"
"v_mfma_f32_16x16x32_bf16 v[128:131], v[16:19], a[16:19], v[128:131]\n"
"v_perm_b32 v28, v37, v33, s57\n"
"v_perm_b32 v29, v45, v41, s57\n"
"v_mfma_f32_16x16x32_bf16 v[132:135], v[20:23], a[16:19], v[132:135]\n"
"buffer_load_dwordx4 v[100:103], v9, s[24:27], s85 offen offset:0\n"
"v_perm_b32 v30, v53, v49, s57\n"
"v_mfma_f32_16x16x32_bf16 v[160:163], v[16:19], a[20:23], v[160:163]\n"
"v_perm_b32 v31, v61, v57, s57\n"
"v_mfma_f32_16x16x32_bf16 v[164:167], v[20:23], a[20:23], v[164:167]\n"
"v_mfma_f32_16x16x32_bf16 v[192:195], v[16:19], a[24:27], v[192:195]\n"
"buffer_load_dwordx4 v[104:107], v10, s[24:27], s85 offen offset:0\n"
"buffer_load_dwordx4 v[108:111], v11, s[24:27], s85 offen offset:0\n"
"buffer_load_dwordx4 v[112:115], v12, s[24:27], s85 offen offset:0\n"
"buffer_load_dwordx4 v[116:119], v13, s[24:27], s85 offen offset:0\n"
"buffer_load_dwordx4 v[120:123], v14, s[24:27], s85 offen offset:0\n"
"buffer_load_dwordx4 v[124:127], v15, s[24:27], s85 offen offset:0\n"
"s_barrier\n"
"v_mfma_f32_16x16x32_bf16 v[196:199], v[20:23], a[24:27], v[196:199]\n"
"v_mfma_f32_16x16x32_bf16 v[224:227], v[16:19], a[28:31], v[224:227]\n"
"ds_read_b128 a[0:3], v6 offset:16384\n"
"v_perm_b32 v16, v38, v34, s56\n"
"v_mfma_f32_16x16x32_bf16 v[228:231], v[20:23], a[28:31], v[228:231]\n"
"ds_read_b128 a[4:7], v6 offset:18432\n"
"v_perm_b32 v17, v46, v42, s56\n"
"v_mfma_f32_16x16x32_bf16 v[136:139], v[24:27], a[16:19], v[136:139]\n"
"ds_read_b128 a[8:11], v6 offset:20480\n"
"v_perm_b32 v18, v54, v50, s56\n"
"v_mfma_f32_16x16x32_bf16 v[140:143], v[28:31], a[16:19], v[140:143]\n"
"ds_read_b128 a[12:15], v6 offset:22528\n"
"v_perm_b32 v19, v62, v58, s56\n"
"v_mfma_f32_16x16x32_bf16 v[168:171], v[24:27], a[20:23], v[168:171]\n"
"v_perm_b32 v20, v38, v34, s57\n"
"v_perm_b32 v21, v46, v42, s57\n"
"v_mfma_f32_16x16x32_bf16 v[172:175], v[28:31], a[20:23], v[172:175]\n"
"v_perm_b32 v22, v54, v50, s57\n"
"v_perm_b32 v23, v62, v58, s57\n"
"v_mfma_f32_16x16x32_bf16 v[200:203], v[24:27], a[24:27], v[200:203]\n"
"s_add_u32 s80, s49, 4\n"
"s_and_b32 s80, s80, 7\n"
"v_mfma_f32_16x16x32_bf16 v[204:207], v[28:31], a[24:27], v[204:207]\n"
"s_lshl_b32 s80, s80, 7\n"
"s_lshl_b32 s81, s60, 2\n"
"v_mfma_f32_16x16x32_bf16 v[232:235], v[24:27], a[28:31], v[232:235]\n"
"v_perm_b32 v24, v39, v35, s56\n"
"v_perm_b32 v25, v47, v43, s56\n"
"v_mfma_f32_16x16x32_bf16 v[236:239], v[28:31], a[28:31], v[236:239]\n"
"v_perm_b32 v26, v55, v51, s56\n"
"v_perm_b32 v27, v63, v59, s56\n"
"v_mfma_f32_16x16x32_bf16 v[144:147], v[16:19], a[16:19], v[144:147]\n"
"v_perm_b32 v28, v39, v35, s57\n"
"v_perm_b32 v29, v47, v43, s57\n"
"v_mfma_f32_16x16x32_bf16 v[148:151], v[20:23], a[16:19], v[148:151]\n"
"v_perm_b32 v30, v55, v51, s57\n"
"v_perm_b32 v31, v63, v59, s57\n"
"v_mfma_f32_16x16x32_bf16 v[176:179], v[16:19], a[20:23], v[176:179]\n"
"s_add_u32 s80, s80, s81\n"
"s_add_u32 s81, s80, 8192\n"
"v_mfma_f32_16x16x32_bf16 v[180:183], v[20:23], a[20:23], v[180:183]\n"
"s_lshr_b32 s82, s60, 1\n"
"s_add_u32 m0, s82, 131072\n"
"v_mfma_f32_16x16x32_bf16 v[208:211], v[16:19], a[24:27], v[208:211]\n"
"buffer_load_dwordx4 v1, s[20:23], s80 offen offset:0 lds\n"
"s_add_u32 m0, s82, 132096\n"
"v_mfma_f32_16x16x32_bf16 v[212:215], v[20:23], a[24:27], v[212:215]\n"
"v_mfma_f32_16x16x32_bf16 v[240:243], v[16:19], a[28:31], v[240:243]\n"
"s_waitcnt vmcnt(9)\n"
"v_perm_b32 v16, v68, v64, s56\n"
"v_perm_b32 v17, v76, v72, s56\n"
"v_mfma_f32_16x16x32_bf16 v[244:247], v[20:23], a[28:31], v[244:247]\n"
"v_perm_b32 v18, v84, v80, s56\n"
"v_perm_b32 v19, v92, v88, s56\n"
"v_mfma_f32_16x16x32_bf16 v[152:155], v[24:27], a[16:19], v[152:155]\n"
"buffer_load_dwordx4 v1, s[20:23], s81 offen offset:0 lds\n"
"v_perm_b32 v20, v68, v64, s57\n"
"v_mfma_f32_16x16x32_bf16 v[156:159], v[28:31], a[16:19], v[156:159]\n"
"v_perm_b32 v21, v76, v72, s57\n"
"v_perm_b32 v22, v84, v80, s57\n"
"v_mfma_f32_16x16x32_bf16 v[184:187], v[24:27], a[20:23], v[184:187]\n"
"s_waitcnt lgkmcnt(0)\n"
"v_perm_b32 v23, v92, v88, s57\n"
"s_add_u32 s80, s49, 5\n"
"v_mfma_f32_16x16x32_bf16 v[188:191], v[28:31], a[20:23], v[188:191]\n"
"s_and_b32 s80, s80, 7\n"
"s_lshl_b32 s80, s80, 7\n"
"v_mfma_f32_16x16x32_bf16 v[216:219], v[24:27], a[24:27], v[216:219]\n"
"s_lshl_b32 s81, s60, 2\n"
"s_add_u32 s80, s80, s81\n"
"v_mfma_f32_16x16x32_bf16 v[220:223], v[28:31], a[24:27], v[220:223]\n"
"s_add_u32 s81, s80, 8192\n"
"s_lshr_b32 s82, s60, 1\n"
"v_mfma_f32_16x16x32_bf16 v[248:251], v[24:27], a[28:31], v[248:251]\n"
"v_perm_b32 v24, v69, v65, s56\n"
"v_perm_b32 v25, v77, v73, s56\n"
"v_mfma_f32_16x16x32_bf16 v[252:255], v[28:31], a[28:31], v[252:255]\n"
"v_perm_b32 v26, v85, v81, s56\n"
"v_perm_b32 v27, v93, v89, s56\n"
"v_mfma_f32_16x16x32_bf16 v[128:131], v[16:19], a[0:3], v[128:131]\n"
"v_perm_b32 v28, v69, v65, s57\n"
"v_perm_b32 v29, v77, v73, s57\n"
"v_mfma_f32_16x16x32_bf16 v[132:135], v[20:23], a[0:3], v[132:135]\n"
"v_perm_b32 v30, v85, v81, s57\n"
"v_perm_b32 v31, v93, v89, s57\n"
"v_mfma_f32_16x16x32_bf16 v[160:163], v[16:19], a[4:7], v[160:163]\n"
"s_add_u32 m0, s82, 139264\n"
"v_mfma_f32_16x16x32_bf16 v[164:167], v[20:23], a[4:7], v[164:167]\n"
"buffer_load_dwordx4 v1, s[20:23], s80 offen offset:0 lds\n"
"s_add_u32 m0, s82, 140288\n"
"v_mfma_f32_16x16x32_bf16 v[192:195], v[16:19], a[8:11], v[192:195]\n"
"v_mfma_f32_16x16x32_bf16 v[196:199], v[20:23], a[8:11], v[196:199]\n"
"v_mfma_f32_16x16x32_bf16 v[224:227], v[16:19], a[12:15], v[224:227]\n"
"ds_read_b128 a[16:19], v7 offset:16384\n"
"v_perm_b32 v16, v70, v66, s56\n"
"v_mfma_f32_16x16x32_bf16 v[228:231], v[20:23], a[12:15], v[228:231]\n"
"ds_read_b128 a[20:23], v7 offset:18432\n"
"buffer_load_dwordx4 v1, s[20:23], s81 offen offset:0 lds\n"
"v_mfma_f32_16x16x32_bf16 v[136:139], v[24:27], a[0:3], v[136:139]\n"
"ds_read_b128 a[24:27], v7 offset:20480\n"
"v_perm_b32 v17, v78, v74, s56\n"
"v_mfma_f32_16x16x32_bf16 v[140:143], v[28:31], a[0:3], v[140:143]\n"
"ds_read_b128 a[28:31], v7 offset:22528\n"
"v_perm_b32 v18, v86, v82, s56\n"
"v_mfma_f32_16x16x32_bf16 v[168:171], v[24:27], a[4:7], v[168:171]\n"
"v_perm_b32 v19, v94, v90, s56\n"
"v_perm_b32 v20, v70, v66, s57\n"
"v_mfma_f32_16x16x32_bf16 v[172:175], v[28:31], a[4:7], v[172:175]\n"
"v_perm_b32 v21, v78, v74, s57\n"
"v_perm_b32 v22, v86, v82, s57\n"
"v_mfma_f32_16x16x32_bf16 v[200:203], v[24:27], a[8:11], v[200:203]\n"
"v_perm_b32 v23, v94, v90, s57\n"
"s_add_u32 s84, s49, 3\n"
"v_mfma_f32_16x16x32_bf16 v[204:207], v[28:31], a[8:11], v[204:207]\n"
"s_and_b32 s84, s84, 7\n"
"s_lshl_b32 s84, s84, 19\n"
"v_mfma_f32_16x16x32_bf16 v[232:235], v[24:27], a[12:15], v[232:235]\n"
"buffer_load_dwordx4 v[32:35], v8, s[24:27], s84 offen offset:0\n"
"v_perm_b32 v24, v71, v67, s56\n"
"v_mfma_f32_16x16x32_bf16 v[236:239], v[28:31], a[12:15], v[236:239]\n"
"v_perm_b32 v25, v79, v75, s56\n"
"v_perm_b32 v26, v87, v83, s56\n"
"v_mfma_f32_16x16x32_bf16 v[144:147], v[16:19], a[0:3], v[144:147]\n"
"v_perm_b32 v27, v95, v91, s56\n"
"v_perm_b32 v28, v71, v67, s57\n"
"v_mfma_f32_16x16x32_bf16 v[148:151], v[20:23], a[0:3], v[148:151]\n"
"v_perm_b32 v29, v79, v75, s57\n"
"v_perm_b32 v30, v87, v83, s57\n"
"v_mfma_f32_16x16x32_bf16 v[176:179], v[16:19], a[4:7], v[176:179]\n"
"buffer_load_dwordx4 v[36:39], v9, s[24:27], s84 offen offset:0\n"
"v_perm_b32 v31, v95, v91, s57\n"
"v_mfma_f32_16x16x32_bf16 v[180:183], v[20:23], a[4:7], v[180:183]\n"
"v_mfma_f32_16x16x32_bf16 v[208:211], v[16:19], a[8:11], v[208:211]\n"
"v_mfma_f32_16x16x32_bf16 v[212:215], v[20:23], a[8:11], v[212:215]\n"
"v_mfma_f32_16x16x32_bf16 v[240:243], v[16:19], a[12:15], v[240:243]\n"
"s_waitcnt vmcnt(6)\n"
"buffer_load_dwordx4 v[40:43], v10, s[24:27], s84 offen offset:0\n"
"v_perm_b32 v16, v100, v96, s56\n"
"v_mfma_f32_16x16x32_bf16 v[244:247], v[20:23], a[12:15], v[244:247]\n"
"v_perm_b32 v17, v108, v104, s56\n"
"v_perm_b32 v18, v116, v112, s56\n"
"v_mfma_f32_16x16x32_bf16 v[152:155], v[24:27], a[0:3], v[152:155]\n"
"v_perm_b32 v19, v124, v120, s56\n"
"v_perm_b32 v20, v100, v96, s57\n"
"buffer_load_dwordx4 v[44:47], v11, s[24:27], s84 offen offset:0\n"
"buffer_load_dwordx4 v[48:51], v12, s[24:27], s84 offen offset:0\n"
"buffer_load_dwordx4 v[52:55], v13, s[24:27], s84 offen offset:0\n"
"buffer_load_dwordx4 v[56:59], v14, s[24:27], s84 offen offset:0\n"
"buffer_load_dwordx4 v[60:63], v15, s[24:27], s84 offen offset:0\n"
"v_mfma_f32_16x16x32_bf16 v[156:159], v[28:31], a[0:3], v[156:159]\n"
"v_perm_b32 v21, v108, v104, s57\n"
"v_perm_b32 v22, v116, v112, s57\n"
"v_mfma_f32_16x16x32_bf16 v[184:187], v[24:27], a[4:7], v[184:187]\n"
"s_waitcnt lgkmcnt(0)\n"
"v_perm_b32 v23, v124, v120, s57\n"
"v_mfma_f32_16x16x32_bf16 v[188:191], v[28:31], a[4:7], v[188:191]\n"
"s_add_u32 s85, s49, 3\n"
"s_and_b32 s85, s85, 7\n"
"v_mfma_f32_16x16x32_bf16 v[216:219], v[24:27], a[8:11], v[216:219]\n"
"s_lshl_b32 s85, s85, 19\n"
"s_add_u32 s85, s85, 262144\n"
"v_mfma_f32_16x16x32_bf16 v[220:223], v[28:31], a[8:11], v[220:223]\n"
"buffer_load_dwordx4 v[64:67], v8, s[24:27], s85 offen offset:0\n"
"v_mfma_f32_16x16x32_bf16 v[248:251], v[24:27], a[12:15], v[248:251]\n"
"v_perm_b32 v24, v101, v97, s56\n"
"v_perm_b32 v25, v109, v105, s56\n"
"v_mfma_f32_16x16x32_bf16 v[252:255], v[28:31], a[12:15], v[252:255]\n"
"v_perm_b32 v26, v117, v113, s56\n"
"v_perm_b32 v27, v125, v121, s56\n"
"v_mfma_f32_16x16x32_bf16 v[128:131], v[16:19], a[16:19], v[128:131]\n"
"v_perm_b32 v28, v101, v97, s57\n"
"v_perm_b32 v29, v109, v105, s57\n"
"v_mfma_f32_16x16x32_bf16 v[132:135], v[20:23], a[16:19], v[132:135]\n"
"buffer_load_dwordx4 v[68:71], v9, s[24:27], s85 offen offset:0\n"
"v_perm_b32 v30, v117, v113, s57\n"
"v_mfma_f32_16x16x32_bf16 v[160:163], v[16:19], a[20:23], v[160:163]\n"
"v_perm_b32 v31, v125, v121, s57\n"
"v_mfma_f32_16x16x32_bf16 v[164:167], v[20:23], a[20:23], v[164:167]\n"
"v_mfma_f32_16x16x32_bf16 v[192:195], v[16:19], a[24:27], v[192:195]\n"
"v_mfma_f32_16x16x32_bf16 v[196:199], v[20:23], a[24:27], v[196:199]\n"
"buffer_load_dwordx4 v[72:75], v10, s[24:27], s85 offen offset:0\n"
"v_mfma_f32_16x16x32_bf16 v[224:227], v[16:19], a[28:31], v[224:227]\n"
"ds_read_b128 a[0:3], v6 offset:24576\n"
"v_perm_b32 v16, v102, v98, s56\n"
"v_mfma_f32_16x16x32_bf16 v[228:231], v[20:23], a[28:31], v[228:231]\n"
"ds_read_b128 a[4:7], v6 offset:26624\n"
"v_perm_b32 v17, v110, v106, s56\n"
"v_mfma_f32_16x16x32_bf16 v[136:139], v[24:27], a[16:19], v[136:139]\n"
"ds_read_b128 a[8:11], v6 offset:28672\n"
"v_perm_b32 v18, v118, v114, s56\n"
"v_mfma_f32_16x16x32_bf16 v[140:143], v[28:31], a[16:19], v[140:143]\n"
"ds_read_b128 a[12:15], v6 offset:30720\n"
"buffer_load_dwordx4 v[76:79], v11, s[24:27], s85 offen offset:0\n"
"v_mfma_f32_16x16x32_bf16 v[168:171], v[24:27], a[20:23], v[168:171]\n"
"v_perm_b32 v19, v126, v122, s56\n"
"v_perm_b32 v20, v102, v98, s57\n"
"v_mfma_f32_16x16x32_bf16 v[172:175], v[28:31], a[20:23], v[172:175]\n"
"v_perm_b32 v21, v110, v106, s57\n"
"v_perm_b32 v22, v118, v114, s57\n"
"v_mfma_f32_16x16x32_bf16 v[200:203], v[24:27], a[24:27], v[200:203]\n"
"v_perm_b32 v23, v126, v122, s57\n"
"v_mfma_f32_16x16x32_bf16 v[204:207], v[28:31], a[24:27], v[204:207]\n"
"buffer_load_dwordx4 v[80:83], v12, s[24:27], s85 offen offset:0\n"
"v_mfma_f32_16x16x32_bf16 v[232:235], v[24:27], a[28:31], v[232:235]\n"
"v_perm_b32 v24, v103, v99, s56\n"
"v_perm_b32 v25, v111, v107, s56\n"
"v_mfma_f32_16x16x32_bf16 v[236:239], v[28:31], a[28:31], v[236:239]\n"
"v_perm_b32 v26, v119, v115, s56\n"
"v_perm_b32 v27, v127, v123, s56\n"
"v_mfma_f32_16x16x32_bf16 v[144:147], v[16:19], a[16:19], v[144:147]\n"
"v_perm_b32 v28, v103, v99, s57\n"
"v_perm_b32 v29, v111, v107, s57\n"
"v_mfma_f32_16x16x32_bf16 v[148:151], v[20:23], a[16:19], v[148:151]\n"
"buffer_load_dwordx4 v[84:87], v13, s[24:27], s85 offen offset:0\n"
"v_perm_b32 v30, v119, v115, s57\n"
"v_mfma_f32_16x16x32_bf16 v[176:179], v[16:19], a[20:23], v[176:179]\n"
"v_perm_b32 v31, v127, v123, s57\n"
"v_mfma_f32_16x16x32_bf16 v[180:183], v[20:23], a[20:23], v[180:183]\n"
"v_mfma_f32_16x16x32_bf16 v[208:211], v[16:19], a[24:27], v[208:211]\n"
"v_mfma_f32_16x16x32_bf16 v[212:215], v[20:23], a[24:27], v[212:215]\n"
"buffer_load_dwordx4 v[88:91], v14, s[24:27], s85 offen offset:0\n"
"v_mfma_f32_16x16x32_bf16 v[240:243], v[16:19], a[28:31], v[240:243]\n"
"s_waitcnt vmcnt(7)\n"
"v_perm_b32 v16, v36, v32, s56\n"
"v_perm_b32 v17, v44, v40, s56\n"
"v_mfma_f32_16x16x32_bf16 v[244:247], v[20:23], a[28:31], v[244:247]\n"
"v_perm_b32 v18, v52, v48, s56\n"
"v_perm_b32 v19, v60, v56, s56\n"
"v_mfma_f32_16x16x32_bf16 v[152:155], v[24:27], a[16:19], v[152:155]\n"
"v_perm_b32 v20, v36, v32, s57\n"
"v_perm_b32 v21, v44, v40, s57\n"
"buffer_load_dwordx4 v[92:95], v15, s[24:27], s85 offen offset:0\n"
"v_mfma_f32_16x16x32_bf16 v[156:159], v[28:31], a[16:19], v[156:159]\n"
"v_perm_b32 v22, v52, v48, s57\n"
"v_perm_b32 v23, v60, v56, s57\n"
"v_mfma_f32_16x16x32_bf16 v[184:187], v[24:27], a[20:23], v[184:187]\n"
"s_waitcnt lgkmcnt(0)\n"
"v_mfma_f32_16x16x32_bf16 v[188:191], v[28:31], a[20:23], v[188:191]\n"
"s_add_u32 s84, s49, 4\n"
"s_and_b32 s84, s84, 7\n"
"v_mfma_f32_16x16x32_bf16 v[216:219], v[24:27], a[24:27], v[216:219]\n"
"s_lshl_b32 s84, s84, 19\n"
"v_mfma_f32_16x16x32_bf16 v[220:223], v[28:31], a[24:27], v[220:223]\n"
"buffer_load_dwordx4 v[96:99], v8, s[24:27], s84 offen offset:0\n"
"v_mfma_f32_16x16x32_bf16 v[248:251], v[24:27], a[28:31], v[248:251]\n"
"v_perm_b32 v24, v37, v33, s56\n"
"v_perm_b32 v25, v45, v41, s56\n"
"v_mfma_f32_16x16x32_bf16 v[252:255], v[28:31], a[28:31], v[252:255]\n"
"v_perm_b32 v26, v53, v49, s56\n"
"v_perm_b32 v27, v61, v57, s56\n"
"v_mfma_f32_16x16x32_bf16 v[128:131], v[16:19], a[0:3], v[128:131]\n"
"v_perm_b32 v28, v37, v33, s57\n"
"v_perm_b32 v29, v45, v41, s57\n"
"v_mfma_f32_16x16x32_bf16 v[132:135], v[20:23], a[0:3], v[132:135]\n"
"buffer_load_dwordx4 v[100:103], v9, s[24:27], s84 offen offset:0\n"
"v_perm_b32 v30, v53, v49, s57\n"
"v_mfma_f32_16x16x32_bf16 v[160:163], v[16:19], a[4:7], v[160:163]\n"
"v_perm_b32 v31, v61, v57, s57\n"
"v_mfma_f32_16x16x32_bf16 v[164:167], v[20:23], a[4:7], v[164:167]\n"
"v_mfma_f32_16x16x32_bf16 v[192:195], v[16:19], a[8:11], v[192:195]\n"
"v_mfma_f32_16x16x32_bf16 v[196:199], v[20:23], a[8:11], v[196:199]\n"
"buffer_load_dwordx4 v[104:107], v10, s[24:27], s84 offen offset:0\n"
"v_mfma_f32_16x16x32_bf16 v[224:227], v[16:19], a[12:15], v[224:227]\n"
"ds_read_b128 a[16:19], v7 offset:24576\n"
"v_perm_b32 v16, v38, v34, s56\n"
"v_mfma_f32_16x16x32_bf16 v[228:231], v[20:23], a[12:15], v[228:231]\n"
"ds_read_b128 a[20:23], v7 offset:26624\n"
"v_perm_b32 v17, v46, v42, s56\n"
"v_mfma_f32_16x16x32_bf16 v[136:139], v[24:27], a[0:3], v[136:139]\n"
"ds_read_b128 a[24:27], v7 offset:28672\n"
"v_perm_b32 v18, v54, v50, s56\n"
"v_mfma_f32_16x16x32_bf16 v[140:143], v[28:31], a[0:3], v[140:143]\n"
"ds_read_b128 a[28:31], v7 offset:30720\n"
"buffer_load_dwordx4 v[108:111], v11, s[24:27], s84 offen offset:0\n"
"v_mfma_f32_16x16x32_bf16 v[168:171], v[24:27], a[4:7], v[168:171]\n"
"v_perm_b32 v19, v62, v58, s56\n"
"v_perm_b32 v20, v38, v34, s57\n"
"v_mfma_f32_16x16x32_bf16 v[172:175], v[28:31], a[4:7], v[172:175]\n"
"v_perm_b32 v21, v46, v42, s57\n"
"v_perm_b32 v22, v54, v50, s57\n"
"v_mfma_f32_16x16x32_bf16 v[200:203], v[24:27], a[8:11], v[200:203]\n"
"v_perm_b32 v23, v62, v58, s57\n"
"v_mfma_f32_16x16x32_bf16 v[204:207], v[28:31], a[8:11], v[204:207]\n"
"buffer_load_dwordx4 v[112:115], v12, s[24:27], s84 offen offset:0\n"
"v_mfma_f32_16x16x32_bf16 v[232:235], v[24:27], a[12:15], v[232:235]\n"
"v_perm_b32 v24, v39, v35, s56\n"
"v_perm_b32 v25, v47, v43, s56\n"
"v_mfma_f32_16x16x32_bf16 v[236:239], v[28:31], a[12:15], v[236:239]\n"
"v_perm_b32 v26, v55, v51, s56\n"
"v_perm_b32 v27, v63, v59, s56\n"
"v_mfma_f32_16x16x32_bf16 v[144:147], v[16:19], a[0:3], v[144:147]\n"
"v_perm_b32 v28, v39, v35, s57\n"
"v_perm_b32 v29, v47, v43, s57\n"
"v_mfma_f32_16x16x32_bf16 v[148:151], v[20:23], a[0:3], v[148:151]\n"
"buffer_load_dwordx4 v[116:119], v13, s[24:27], s84 offen offset:0\n"
"v_perm_b32 v30, v55, v51, s57\n"
"v_mfma_f32_16x16x32_bf16 v[176:179], v[16:19], a[4:7], v[176:179]\n"
"v_perm_b32 v31, v63, v59, s57\n"
"v_mfma_f32_16x16x32_bf16 v[180:183], v[20:23], a[4:7], v[180:183]\n"
"v_mfma_f32_16x16x32_bf16 v[208:211], v[16:19], a[8:11], v[208:211]\n"
"v_mfma_f32_16x16x32_bf16 v[212:215], v[20:23], a[8:11], v[212:215]\n"
"buffer_load_dwordx4 v[120:123], v14, s[24:27], s84 offen offset:0\n"
"v_mfma_f32_16x16x32_bf16 v[240:243], v[16:19], a[12:15], v[240:243]\n"
"s_waitcnt vmcnt(7)\n"
"v_perm_b32 v16, v68, v64, s56\n"
"v_perm_b32 v17, v76, v72, s56\n"
"v_mfma_f32_16x16x32_bf16 v[244:247], v[20:23], a[12:15], v[244:247]\n"
"v_perm_b32 v18, v84, v80, s56\n"
"v_perm_b32 v19, v92, v88, s56\n"
"v_mfma_f32_16x16x32_bf16 v[152:155], v[24:27], a[0:3], v[152:155]\n"
"v_perm_b32 v20, v68, v64, s57\n"
"v_perm_b32 v21, v76, v72, s57\n"
"buffer_load_dwordx4 v[124:127], v15, s[24:27], s84 offen offset:0\n"
"v_mfma_f32_16x16x32_bf16 v[156:159], v[28:31], a[0:3], v[156:159]\n"
"v_perm_b32 v22, v84, v80, s57\n"
"v_perm_b32 v23, v92, v88, s57\n"
"v_mfma_f32_16x16x32_bf16 v[184:187], v[24:27], a[4:7], v[184:187]\n"
"s_waitcnt lgkmcnt(0)\n"
"v_mfma_f32_16x16x32_bf16 v[188:191], v[28:31], a[4:7], v[188:191]\n"
"s_add_u32 s85, s49, 4\n"
"s_and_b32 s85, s85, 7\n"
"v_mfma_f32_16x16x32_bf16 v[216:219], v[24:27], a[8:11], v[216:219]\n"
"s_lshl_b32 s85, s85, 19\n"
"s_add_u32 s85, s85, 262144\n"
"v_mfma_f32_16x16x32_bf16 v[220:223], v[28:31], a[8:11], v[220:223]\n"
"buffer_load_dwordx4 v[32:35], v8, s[24:27], s85 offen offset:0\n"
"v_mfma_f32_16x16x32_bf16 v[248:251], v[24:27], a[12:15], v[248:251]\n"
"v_perm_b32 v24, v69, v65, s56\n"
"v_perm_b32 v25, v77, v73, s56\n"
"v_mfma_f32_16x16x32_bf16 v[252:255], v[28:31], a[12:15], v[252:255]\n"
"v_perm_b32 v26, v85, v81, s56\n"
"v_perm_b32 v27, v93, v89, s56\n"
"v_mfma_f32_16x16x32_bf16 v[128:131], v[16:19], a[16:19], v[128:131]\n"
"v_perm_b32 v28, v69, v65, s57\n"
"v_perm_b32 v29, v77, v73, s57\n"
"v_mfma_f32_16x16x32_bf16 v[132:135], v[20:23], a[16:19], v[132:135]\n"
"buffer_load_dwordx4 v[36:39], v9, s[24:27], s85 offen offset:0\n"
"v_perm_b32 v30, v85, v81, s57\n"
"v_mfma_f32_16x16x32_bf16 v[160:163], v[16:19], a[20:23], v[160:163]\n"
"v_perm_b32 v31, v93, v89, s57\n"
"v_mfma_f32_16x16x32_bf16 v[164:167], v[20:23], a[20:23], v[164:167]\n"
"v_mfma_f32_16x16x32_bf16 v[192:195], v[16:19], a[24:27], v[192:195]\n"
"buffer_load_dwordx4 v[40:43], v10, s[24:27], s85 offen offset:0\n"
"buffer_load_dwordx4 v[44:47], v11, s[24:27], s85 offen offset:0\n"
"buffer_load_dwordx4 v[48:51], v12, s[24:27], s85 offen offset:0\n"
"buffer_load_dwordx4 v[52:55], v13, s[24:27], s85 offen offset:0\n"
"buffer_load_dwordx4 v[56:59], v14, s[24:27], s85 offen offset:0\n"
"buffer_load_dwordx4 v[60:63], v15, s[24:27], s85 offen offset:0\n"
"s_barrier\n"
"v_mfma_f32_16x16x32_bf16 v[196:199], v[20:23], a[24:27], v[196:199]\n"
"v_mfma_f32_16x16x32_bf16 v[224:227], v[16:19], a[28:31], v[224:227]\n"
"ds_read_b128 a[0:3], v6 offset:0\n"
"v_perm_b32 v16, v70, v66, s56\n"
"v_mfma_f32_16x16x32_bf16 v[228:231], v[20:23], a[28:31], v[228:231]\n"
"ds_read_b128 a[4:7], v6 offset:2048\n"
"v_perm_b32 v17, v78, v74, s56\n"
"v_mfma_f32_16x16x32_bf16 v[136:139], v[24:27], a[16:19], v[136:139]\n"
"ds_read_b128 a[8:11], v6 offset:4096\n"
"v_perm_b32 v18, v86, v82, s56\n"
"v_mfma_f32_16x16x32_bf16 v[140:143], v[28:31], a[16:19], v[140:143]\n"
"ds_read_b128 a[12:15], v6 offset:6144\n"
"v_perm_b32 v19, v94, v90, s56\n"
"v_mfma_f32_16x16x32_bf16 v[168:171], v[24:27], a[20:23], v[168:171]\n"
"v_perm_b32 v20, v70, v66, s57\n"
"v_perm_b32 v21, v78, v74, s57\n"
"v_mfma_f32_16x16x32_bf16 v[172:175], v[28:31], a[20:23], v[172:175]\n"
"v_perm_b32 v22, v86, v82, s57\n"
"v_perm_b32 v23, v94, v90, s57\n"
"v_mfma_f32_16x16x32_bf16 v[200:203], v[24:27], a[24:27], v[200:203]\n"
"s_add_u32 s80, s49, 6\n"
"s_and_b32 s80, s80, 7\n"
"v_mfma_f32_16x16x32_bf16 v[204:207], v[28:31], a[24:27], v[204:207]\n"
"s_lshl_b32 s80, s80, 7\n"
"s_lshl_b32 s81, s60, 2\n"
"v_mfma_f32_16x16x32_bf16 v[232:235], v[24:27], a[28:31], v[232:235]\n"
"v_perm_b32 v24, v71, v67, s56\n"
"v_perm_b32 v25, v79, v75, s56\n"
"v_mfma_f32_16x16x32_bf16 v[236:239], v[28:31], a[28:31], v[236:239]\n"
"v_perm_b32 v26, v87, v83, s56\n"
"v_perm_b32 v27, v95, v91, s56\n"
"v_mfma_f32_16x16x32_bf16 v[144:147], v[16:19], a[16:19], v[144:147]\n"
"v_perm_b32 v28, v71, v67, s57\n"
"v_perm_b32 v29, v79, v75, s57\n"
"v_mfma_f32_16x16x32_bf16 v[148:151], v[20:23], a[16:19], v[148:151]\n"
"v_perm_b32 v30, v87, v83, s57\n"
"v_perm_b32 v31, v95, v91, s57\n"
"v_mfma_f32_16x16x32_bf16 v[176:179], v[16:19], a[20:23], v[176:179]\n"
"s_add_u32 s80, s80, s81\n"
"s_add_u32 s81, s80, 8192\n"
"v_mfma_f32_16x16x32_bf16 v[180:183], v[20:23], a[20:23], v[180:183]\n"
"s_lshr_b32 s82, s60, 1\n"
"s_add_u32 m0, s82, 147456\n"
"v_mfma_f32_16x16x32_bf16 v[208:211], v[16:19], a[24:27], v[208:211]\n"
"buffer_load_dwordx4 v1, s[20:23], s80 offen offset:0 lds\n"
"s_add_u32 m0, s82, 148480\n"
"v_mfma_f32_16x16x32_bf16 v[212:215], v[20:23], a[24:27], v[212:215]\n"
"v_mfma_f32_16x16x32_bf16 v[240:243], v[16:19], a[28:31], v[240:243]\n"
"s_waitcnt vmcnt(9)\n"
"v_perm_b32 v16, v100, v96, s56\n"
"v_perm_b32 v17, v108, v104, s56\n"
"v_mfma_f32_16x16x32_bf16 v[244:247], v[20:23], a[28:31], v[244:247]\n"
"v_perm_b32 v18, v116, v112, s56\n"
"v_perm_b32 v19, v124, v120, s56\n"
"v_mfma_f32_16x16x32_bf16 v[152:155], v[24:27], a[16:19], v[152:155]\n"
"buffer_load_dwordx4 v1, s[20:23], s81 offen offset:0 lds\n"
"v_perm_b32 v20, v100, v96, s57\n"
"v_mfma_f32_16x16x32_bf16 v[156:159], v[28:31], a[16:19], v[156:159]\n"
"v_perm_b32 v21, v108, v104, s57\n"
"v_perm_b32 v22, v116, v112, s57\n"
"v_mfma_f32_16x16x32_bf16 v[184:187], v[24:27], a[20:23], v[184:187]\n"
"s_waitcnt lgkmcnt(0)\n"
"v_perm_b32 v23, v124, v120, s57\n"
"s_add_u32 s80, s49, 7\n"
"v_mfma_f32_16x16x32_bf16 v[188:191], v[28:31], a[20:23], v[188:191]\n"
"s_and_b32 s80, s80, 7\n"
"s_lshl_b32 s80, s80, 7\n"
"v_mfma_f32_16x16x32_bf16 v[216:219], v[24:27], a[24:27], v[216:219]\n"
"s_lshl_b32 s81, s60, 2\n"
"s_add_u32 s80, s80, s81\n"
"v_mfma_f32_16x16x32_bf16 v[220:223], v[28:31], a[24:27], v[220:223]\n"
"s_add_u32 s81, s80, 8192\n"
"s_lshr_b32 s82, s60, 1\n"
"v_mfma_f32_16x16x32_bf16 v[248:251], v[24:27], a[28:31], v[248:251]\n"
"v_perm_b32 v24, v101, v97, s56\n"
"v_perm_b32 v25, v109, v105, s56\n"
"v_mfma_f32_16x16x32_bf16 v[252:255], v[28:31], a[28:31], v[252:255]\n"
"v_perm_b32 v26, v117, v113, s56\n"
"v_perm_b32 v27, v125, v121, s56\n"
"v_mfma_f32_16x16x32_bf16 v[128:131], v[16:19], a[0:3], v[128:131]\n"
"v_perm_b32 v28, v101, v97, s57\n"
"v_perm_b32 v29, v109, v105, s57\n"
"v_mfma_f32_16x16x32_bf16 v[132:135], v[20:23], a[0:3], v[132:135]\n"
"v_perm_b32 v30, v117, v113, s57\n"
"v_perm_b32 v31, v125, v121, s57\n"
"v_mfma_f32_16x16x32_bf16 v[160:163], v[16:19], a[4:7], v[160:163]\n"
"s_add_u32 m0, s82, 155648\n"
"v_mfma_f32_16x16x32_bf16 v[164:167], v[20:23], a[4:7], v[164:167]\n"
"buffer_load_dwordx4 v1, s[20:23], s80 offen offset:0 lds\n"
"s_add_u32 m0, s82, 156672\n"
"v_mfma_f32_16x16x32_bf16 v[192:195], v[16:19], a[8:11], v[192:195]\n"
"v_mfma_f32_16x16x32_bf16 v[196:199], v[20:23], a[8:11], v[196:199]\n"
"v_mfma_f32_16x16x32_bf16 v[224:227], v[16:19], a[12:15], v[224:227]\n"
"ds_read_b128 a[16:19], v7 offset:0\n"
"v_perm_b32 v16, v102, v98, s56\n"
"v_mfma_f32_16x16x32_bf16 v[228:231], v[20:23], a[12:15], v[228:231]\n"
"ds_read_b128 a[20:23], v7 offset:2048\n"
"buffer_load_dwordx4 v1, s[20:23], s81 offen offset:0 lds\n"
"v_mfma_f32_16x16x32_bf16 v[136:139], v[24:27], a[0:3], v[136:139]\n"
"ds_read_b128 a[24:27], v7 offset:4096\n"
"v_perm_b32 v17, v110, v106, s56\n"
"v_mfma_f32_16x16x32_bf16 v[140:143], v[28:31], a[0:3], v[140:143]\n"
"ds_read_b128 a[28:31], v7 offset:6144\n"
"v_perm_b32 v18, v118, v114, s56\n"
"v_mfma_f32_16x16x32_bf16 v[168:171], v[24:27], a[4:7], v[168:171]\n"
"v_perm_b32 v19, v126, v122, s56\n"
"v_perm_b32 v20, v102, v98, s57\n"
"v_mfma_f32_16x16x32_bf16 v[172:175], v[28:31], a[4:7], v[172:175]\n"
"v_perm_b32 v21, v110, v106, s57\n"
"v_perm_b32 v22, v118, v114, s57\n"
"v_mfma_f32_16x16x32_bf16 v[200:203], v[24:27], a[8:11], v[200:203]\n"
"v_perm_b32 v23, v126, v122, s57\n"
"s_add_u32 s84, s49, 5\n"
"v_mfma_f32_16x16x32_bf16 v[204:207], v[28:31], a[8:11], v[204:207]\n"
"s_and_b32 s84, s84, 7\n"
"s_lshl_b32 s84, s84, 19\n"
"v_mfma_f32_16x16x32_bf16 v[232:235], v[24:27], a[12:15], v[232:235]\n"
"buffer_load_dwordx4 v[64:67], v8, s[24:27], s84 offen offset:0\n"
"v_perm_b32 v24, v103, v99, s56\n"
"v_mfma_f32_16x16x32_bf16 v[236:239], v[28:31], a[12:15], v[236:239]\n"
"v_perm_b32 v25, v111, v107, s56\n"
"v_perm_b32 v26, v119, v115, s56\n"
"v_mfma_f32_16x16x32_bf16 v[144:147], v[16:19], a[0:3], v[144:147]\n"
"v_perm_b32 v27, v127, v123, s56\n"
"v_perm_b32 v28, v103, v99, s57\n"
"v_mfma_f32_16x16x32_bf16 v[148:151], v[20:23], a[0:3], v[148:151]\n"
"v_perm_b32 v29, v111, v107, s57\n"
"v_perm_b32 v30, v119, v115, s57\n"
"v_mfma_f32_16x16x32_bf16 v[176:179], v[16:19], a[4:7], v[176:179]\n"
"buffer_load_dwordx4 v[68:71], v9, s[24:27], s84 offen offset:0\n"
"v_perm_b32 v31, v127, v123, s57\n"
"v_mfma_f32_16x16x32_bf16 v[180:183], v[20:23], a[4:7], v[180:183]\n"
"v_mfma_f32_16x16x32_bf16 v[208:211], v[16:19], a[8:11], v[208:211]\n"
"v_mfma_f32_16x16x32_bf16 v[212:215], v[20:23], a[8:11], v[212:215]\n"
"v_mfma_f32_16x16x32_bf16 v[240:243], v[16:19], a[12:15], v[240:243]\n"
"s_waitcnt vmcnt(6)\n"
"buffer_load_dwordx4 v[72:75], v10, s[24:27], s84 offen offset:0\n"
"v_perm_b32 v16, v36, v32, s56\n"
"v_mfma_f32_16x16x32_bf16 v[244:247], v[20:23], a[12:15], v[244:247]\n"
"v_perm_b32 v17, v44, v40, s56\n"
"v_perm_b32 v18, v52, v48, s56\n"
"v_mfma_f32_16x16x32_bf16 v[152:155], v[24:27], a[0:3], v[152:155]\n"
"v_perm_b32 v19, v60, v56, s56\n"
"v_perm_b32 v20, v36, v32, s57\n"
"buffer_load_dwordx4 v[76:79], v11, s[24:27], s84 offen offset:0\n"
"buffer_load_dwordx4 v[80:83], v12, s[24:27], s84 offen offset:0\n"
"buffer_load_dwordx4 v[84:87], v13, s[24:27], s84 offen offset:0\n"
"buffer_load_dwordx4 v[88:91], v14, s[24:27], s84 offen offset:0\n"
"buffer_load_dwordx4 v[92:95], v15, s[24:27], s84 offen offset:0\n"
"v_mfma_f32_16x16x32_bf16 v[156:159], v[28:31], a[0:3], v[156:159]\n"
"v_perm_b32 v21, v44, v40, s57\n"
"v_perm_b32 v22, v52, v48, s57\n"
"v_mfma_f32_16x16x32_bf16 v[184:187], v[24:27], a[4:7], v[184:187]\n"
"s_waitcnt lgkmcnt(0)\n"
"v_perm_b32 v23, v60, v56, s57\n"
"v_mfma_f32_16x16x32_bf16 v[188:191], v[28:31], a[4:7], v[188:191]\n"
"s_add_u32 s85, s49, 5\n"
"s_and_b32 s85, s85, 7\n"
"v_mfma_f32_16x16x32_bf16 v[216:219], v[24:27], a[8:11], v[216:219]\n"
"s_lshl_b32 s85, s85, 19\n"
"s_add_u32 s85, s85, 262144\n"
"v_mfma_f32_16x16x32_bf16 v[220:223], v[28:31], a[8:11], v[220:223]\n"
"buffer_load_dwordx4 v[96:99], v8, s[24:27], s85 offen offset:0\n"
"v_mfma_f32_16x16x32_bf16 v[248:251], v[24:27], a[12:15], v[248:251]\n"
"v_perm_b32 v24, v37, v33, s56\n"
"v_perm_b32 v25, v45, v41, s56\n"
"v_mfma_f32_16x16x32_bf16 v[252:255], v[28:31], a[12:15], v[252:255]\n"
"v_perm_b32 v26, v53, v49, s56\n"
"v_perm_b32 v27, v61, v57, s56\n"
"v_mfma_f32_16x16x32_bf16 v[128:131], v[16:19], a[16:19], v[128:131]\n"
"v_perm_b32 v28, v37, v33, s57\n"
"v_perm_b32 v29, v45, v41, s57\n"
"v_mfma_f32_16x16x32_bf16 v[132:135], v[20:23], a[16:19], v[132:135]\n"
"buffer_load_dwordx4 v[100:103], v9, s[24:27], s85 offen offset:0\n"
"v_perm_b32 v30, v53, v49, s57\n"
"v_mfma_f32_16x16x32_bf16 v[160:163], v[16:19], a[20:23], v[160:163]\n"
"v_perm_b32 v31, v61, v57, s57\n"
"v_mfma_f32_16x16x32_bf16 v[164:167], v[20:23], a[20:23], v[164:167]\n"
"v_mfma_f32_16x16x32_bf16 v[192:195], v[16:19], a[24:27], v[192:195]\n"
"v_mfma_f32_16x16x32_bf16 v[196:199], v[20:23], a[24:27], v[196:199]\n"
"buffer_load_dwordx4 v[104:107], v10, s[24:27], s85 offen offset:0\n"
"v_mfma_f32_16x16x32_bf16 v[224:227], v[16:19], a[28:31], v[224:227]\n"
"ds_read_b128 a[0:3], v6 offset:8192\n"
"v_perm_b32 v16, v38, v34, s56\n"
"v_mfma_f32_16x16x32_bf16 v[228:231], v[20:23], a[28:31], v[228:231]\n"
"ds_read_b128 a[4:7], v6 offset:10240\n"
"v_perm_b32 v17, v46, v42, s56\n"
"v_mfma_f32_16x16x32_bf16 v[136:139], v[24:27], a[16:19], v[136:139]\n"
"ds_read_b128 a[8:11], v6 offset:12288\n"
"v_perm_b32 v18, v54, v50, s56\n"
"v_mfma_f32_16x16x32_bf16 v[140:143], v[28:31], a[16:19], v[140:143]\n"
"ds_read_b128 a[12:15], v6 offset:14336\n"
"buffer_load_dwordx4 v[108:111], v11, s[24:27], s85 offen offset:0\n"
"v_mfma_f32_16x16x32_bf16 v[168:171], v[24:27], a[20:23], v[168:171]\n"
"v_perm_b32 v19, v62, v58, s56\n"
"v_perm_b32 v20, v38, v34, s57\n"
"v_mfma_f32_16x16x32_bf16 v[172:175], v[28:31], a[20:23], v[172:175]\n"
"v_perm_b32 v21, v46, v42, s57\n"
"v_perm_b32 v22, v54, v50, s57\n"
"v_mfma_f32_16x16x32_bf16 v[200:203], v[24:27], a[24:27], v[200:203]\n"
"v_perm_b32 v23, v62, v58, s57\n"
"v_mfma_f32_16x16x32_bf16 v[204:207], v[28:31], a[24:27], v[204:207]\n"
"buffer_load_dwordx4 v[112:115], v12, s[24:27], s85 offen offset:0\n"
"v_mfma_f32_16x16x32_bf16 v[232:235], v[24:27], a[28:31], v[232:235]\n"
"v_perm_b32 v24, v39, v35, s56\n"
"v_perm_b32 v25, v47, v43, s56\n"
"v_mfma_f32_16x16x32_bf16 v[236:239], v[28:31], a[28:31], v[236:239]\n"
"v_perm_b32 v26, v55, v51, s56\n"
"v_perm_b32 v27, v63, v59, s56\n"
"v_mfma_f32_16x16x32_bf16 v[144:147], v[16:19], a[16:19], v[144:147]\n"
"v_perm_b32 v28, v39, v35, s57\n"
"v_perm_b32 v29, v47, v43, s57\n"
"v_mfma_f32_16x16x32_bf16 v[148:151], v[20:23], a[16:19], v[148:151]\n"
"buffer_load_dwordx4 v[116:119], v13, s[24:27], s85 offen offset:0\n"
"v_perm_b32 v30, v55, v51, s57\n"
"v_mfma_f32_16x16x32_bf16 v[176:179], v[16:19], a[20:23], v[176:179]\n"
"v_perm_b32 v31, v63, v59, s57\n"
"v_mfma_f32_16x16x32_bf16 v[180:183], v[20:23], a[20:23], v[180:183]\n"
"v_mfma_f32_16x16x32_bf16 v[208:211], v[16:19], a[24:27], v[208:211]\n"
"v_mfma_f32_16x16x32_bf16 v[212:215], v[20:23], a[24:27], v[212:215]\n"
"buffer_load_dwordx4 v[120:123], v14, s[24:27], s85 offen offset:0\n"
"v_mfma_f32_16x16x32_bf16 v[240:243], v[16:19], a[28:31], v[240:243]\n"
"s_waitcnt vmcnt(7)\n"
"v_perm_b32 v16, v68, v64, s56\n"
"v_perm_b32 v17, v76, v72, s56\n"
"v_mfma_f32_16x16x32_bf16 v[244:247], v[20:23], a[28:31], v[244:247]\n"
"v_perm_b32 v18, v84, v80, s56\n"
"v_perm_b32 v19, v92, v88, s56\n"
"v_mfma_f32_16x16x32_bf16 v[152:155], v[24:27], a[16:19], v[152:155]\n"
"v_perm_b32 v20, v68, v64, s57\n"
"v_perm_b32 v21, v76, v72, s57\n"
"buffer_load_dwordx4 v[124:127], v15, s[24:27], s85 offen offset:0\n"
"v_mfma_f32_16x16x32_bf16 v[156:159], v[28:31], a[16:19], v[156:159]\n"
"v_perm_b32 v22, v84, v80, s57\n"
"v_perm_b32 v23, v92, v88, s57\n"
"v_mfma_f32_16x16x32_bf16 v[184:187], v[24:27], a[20:23], v[184:187]\n"
"s_waitcnt lgkmcnt(0)\n"
"v_mfma_f32_16x16x32_bf16 v[188:191], v[28:31], a[20:23], v[188:191]\n"
"s_add_u32 s84, s49, 6\n"
"s_and_b32 s84, s84, 7\n"
"v_mfma_f32_16x16x32_bf16 v[216:219], v[24:27], a[24:27], v[216:219]\n"
"s_lshl_b32 s84, s84, 19\n"
"v_mfma_f32_16x16x32_bf16 v[220:223], v[28:31], a[24:27], v[220:223]\n"
"buffer_load_dwordx4 v[32:35], v8, s[24:27], s84 offen offset:0\n"
"v_mfma_f32_16x16x32_bf16 v[248:251], v[24:27], a[28:31], v[248:251]\n"
"v_perm_b32 v24, v69, v65, s56\n"
"v_perm_b32 v25, v77, v73, s56\n"
"v_mfma_f32_16x16x32_bf16 v[252:255], v[28:31], a[28:31], v[252:255]\n"
"v_perm_b32 v26, v85, v81, s56\n"
"v_perm_b32 v27, v93, v89, s56\n"
"v_mfma_f32_16x16x32_bf16 v[128:131], v[16:19], a[0:3], v[128:131]\n"
"v_perm_b32 v28, v69, v65, s57\n"
"v_perm_b32 v29, v77, v73, s57\n"
"v_mfma_f32_16x16x32_bf16 v[132:135], v[20:23], a[0:3], v[132:135]\n"
"buffer_load_dwordx4 v[36:39], v9, s[24:27], s84 offen offset:0\n"
"v_perm_b32 v30, v85, v81, s57\n"
"v_mfma_f32_16x16x32_bf16 v[160:163], v[16:19], a[4:7], v[160:163]\n"
"v_perm_b32 v31, v93, v89, s57\n"
"v_mfma_f32_16x16x32_bf16 v[164:167], v[20:23], a[4:7], v[164:167]\n"
"v_mfma_f32_16x16x32_bf16 v[192:195], v[16:19], a[8:11], v[192:195]\n"
"v_mfma_f32_16x16x32_bf16 v[196:199], v[20:23], a[8:11], v[196:199]\n"
"buffer_load_dwordx4 v[40:43], v10, s[24:27], s84 offen offset:0\n"
"v_mfma_f32_16x16x32_bf16 v[224:227], v[16:19], a[12:15], v[224:227]\n"
"ds_read_b128 a[16:19], v7 offset:8192\n"
"v_perm_b32 v16, v70, v66, s56\n"
"v_mfma_f32_16x16x32_bf16 v[228:231], v[20:23], a[12:15], v[228:231]\n"
"ds_read_b128 a[20:23], v7 offset:10240\n"
"v_perm_b32 v17, v78, v74, s56\n"
"v_mfma_f32_16x16x32_bf16 v[136:139], v[24:27], a[0:3], v[136:139]\n"
"ds_read_b128 a[24:27], v7 offset:12288\n"
"v_perm_b32 v18, v86, v82, s56\n"
"v_mfma_f32_16x16x32_bf16 v[140:143], v[28:31], a[0:3], v[140:143]\n"
"ds_read_b128 a[28:31], v7 offset:14336\n"
"buffer_load_dwordx4 v[44:47], v11, s[24:27], s84 offen offset:0\n"
"v_mfma_f32_16x16x32_bf16 v[168:171], v[24:27], a[4:7], v[168:171]\n"
"v_perm_b32 v19, v94, v90, s56\n"
"v_perm_b32 v20, v70, v66, s57\n"
"v_mfma_f32_16x16x32_bf16 v[172:175], v[28:31], a[4:7], v[172:175]\n"
"v_perm_b32 v21, v78, v74, s57\n"
"v_perm_b32 v22, v86, v82, s57\n"
"v_mfma_f32_16x16x32_bf16 v[200:203], v[24:27], a[8:11], v[200:203]\n"
"v_perm_b32 v23, v94, v90, s57\n"
"v_mfma_f32_16x16x32_bf16 v[204:207], v[28:31], a[8:11], v[204:207]\n"
"buffer_load_dwordx4 v[48:51], v12, s[24:27], s84 offen offset:0\n"
"v_mfma_f32_16x16x32_bf16 v[232:235], v[24:27], a[12:15], v[232:235]\n"
"v_perm_b32 v24, v71, v67, s56\n"
"v_perm_b32 v25, v79, v75, s56\n"
"v_mfma_f32_16x16x32_bf16 v[236:239], v[28:31], a[12:15], v[236:239]\n"
"v_perm_b32 v26, v87, v83, s56\n"
"v_perm_b32 v27, v95, v91, s56\n"
"v_mfma_f32_16x16x32_bf16 v[144:147], v[16:19], a[0:3], v[144:147]\n"
"v_perm_b32 v28, v71, v67, s57\n"
"v_perm_b32 v29, v79, v75, s57\n"
"v_mfma_f32_16x16x32_bf16 v[148:151], v[20:23], a[0:3], v[148:151]\n"
"buffer_load_dwordx4 v[52:55], v13, s[24:27], s84 offen offset:0\n"
"v_perm_b32 v30, v87, v83, s57\n"
"v_mfma_f32_16x16x32_bf16 v[176:179], v[16:19], a[4:7], v[176:179]\n"
"v_perm_b32 v31, v95, v91, s57\n"
"v_mfma_f32_16x16x32_bf16 v[180:183], v[20:23], a[4:7], v[180:183]\n"
"v_mfma_f32_16x16x32_bf16 v[208:211], v[16:19], a[8:11], v[208:211]\n"
"v_mfma_f32_16x16x32_bf16 v[212:215], v[20:23], a[8:11], v[212:215]\n"
"buffer_load_dwordx4 v[56:59], v14, s[24:27], s84 offen offset:0\n"
"v_mfma_f32_16x16x32_bf16 v[240:243], v[16:19], a[12:15], v[240:243]\n"
"s_waitcnt vmcnt(7)\n"
"v_perm_b32 v16, v100, v96, s56\n"
"v_perm_b32 v17, v108, v104, s56\n"
"v_mfma_f32_16x16x32_bf16 v[244:247], v[20:23], a[12:15], v[244:247]\n"
"v_perm_b32 v18, v116, v112, s56\n"
"v_perm_b32 v19, v124, v120, s56\n"
"v_mfma_f32_16x16x32_bf16 v[152:155], v[24:27], a[0:3], v[152:155]\n"
"v_perm_b32 v20, v100, v96, s57\n"
"v_perm_b32 v21, v108, v104, s57\n"
"buffer_load_dwordx4 v[60:63], v15, s[24:27], s84 offen offset:0\n"
"v_mfma_f32_16x16x32_bf16 v[156:159], v[28:31], a[0:3], v[156:159]\n"
"v_perm_b32 v22, v116, v112, s57\n"
"v_perm_b32 v23, v124, v120, s57\n"
"v_mfma_f32_16x16x32_bf16 v[184:187], v[24:27], a[4:7], v[184:187]\n"
"s_waitcnt lgkmcnt(0)\n"
"v_mfma_f32_16x16x32_bf16 v[188:191], v[28:31], a[4:7], v[188:191]\n"
"s_add_u32 s85, s49, 6\n"
"s_and_b32 s85, s85, 7\n"
"v_mfma_f32_16x16x32_bf16 v[216:219], v[24:27], a[8:11], v[216:219]\n"
"s_lshl_b32 s85, s85, 19\n"
"s_add_u32 s85, s85, 262144\n"
"v_mfma_f32_16x16x32_bf16 v[220:223], v[28:31], a[8:11], v[220:223]\n"
"buffer_load_dwordx4 v[64:67], v8, s[24:27], s85 offen offset:0\n"
"v_mfma_f32_16x16x32_bf16 v[248:251], v[24:27], a[12:15], v[248:251]\n"
"v_perm_b32 v24, v101, v97, s56\n"
"v_perm_b32 v25, v109, v105, s56\n"
"v_mfma_f32_16x16x32_bf16 v[252:255], v[28:31], a[12:15], v[252:255]\n"
"v_perm_b32 v26, v117, v113, s56\n"
"v_perm_b32 v27, v125, v121, s56\n"
"v_mfma_f32_16x16x32_bf16 v[128:131], v[16:19], a[16:19], v[128:131]\n"
"v_perm_b32 v28, v101, v97, s57\n"
"v_perm_b32 v29, v109, v105, s57\n"
"v_mfma_f32_16x16x32_bf16 v[132:135], v[20:23], a[16:19], v[132:135]\n"
"buffer_load_dwordx4 v[68:71], v9, s[24:27], s85 offen offset:0\n"
"v_perm_b32 v30, v117, v113, s57\n"
"v_mfma_f32_16x16x32_bf16 v[160:163], v[16:19], a[20:23], v[160:163]\n"
"v_perm_b32 v31, v125, v121, s57\n"
"v_mfma_f32_16x16x32_bf16 v[164:167], v[20:23], a[20:23], v[164:167]\n"
"v_mfma_f32_16x16x32_bf16 v[192:195], v[16:19], a[24:27], v[192:195]\n"
"buffer_load_dwordx4 v[72:75], v10, s[24:27], s85 offen offset:0\n"
"buffer_load_dwordx4 v[76:79], v11, s[24:27], s85 offen offset:0\n"
"buffer_load_dwordx4 v[80:83], v12, s[24:27], s85 offen offset:0\n"
"buffer_load_dwordx4 v[84:87], v13, s[24:27], s85 offen offset:0\n"
"buffer_load_dwordx4 v[88:91], v14, s[24:27], s85 offen offset:0\n"
"buffer_load_dwordx4 v[92:95], v15, s[24:27], s85 offen offset:0\n"
"s_barrier\n"
"v_mfma_f32_16x16x32_bf16 v[196:199], v[20:23], a[24:27], v[196:199]\n"
"v_mfma_f32_16x16x32_bf16 v[224:227], v[16:19], a[28:31], v[224:227]\n"
"ds_read_b128 a[0:3], v6 offset:16384\n"
"v_perm_b32 v16, v102, v98, s56\n"
"v_mfma_f32_16x16x32_bf16 v[228:231], v[20:23], a[28:31], v[228:231]\n"
"ds_read_b128 a[4:7], v6 offset:18432\n"
"v_perm_b32 v17, v110, v106, s56\n"
"v_mfma_f32_16x16x32_bf16 v[136:139], v[24:27], a[16:19], v[136:139]\n"
"ds_read_b128 a[8:11], v6 offset:20480\n"
"v_perm_b32 v18, v118, v114, s56\n"
"v_mfma_f32_16x16x32_bf16 v[140:143], v[28:31], a[16:19], v[140:143]\n"
"ds_read_b128 a[12:15], v6 offset:22528\n"
"v_perm_b32 v19, v126, v122, s56\n"
"v_mfma_f32_16x16x32_bf16 v[168:171], v[24:27], a[20:23], v[168:171]\n"
"v_perm_b32 v20, v102, v98, s57\n"
"v_perm_b32 v21, v110, v106, s57\n"
"v_mfma_f32_16x16x32_bf16 v[172:175], v[28:31], a[20:23], v[172:175]\n"
"v_perm_b32 v22, v118, v114, s57\n"
"v_perm_b32 v23, v126, v122, s57\n"
"v_mfma_f32_16x16x32_bf16 v[200:203], v[24:27], a[24:27], v[200:203]\n"
"v_mfma_f32_16x16x32_bf16 v[204:207], v[28:31], a[24:27], v[204:207]\n"
"v_mfma_f32_16x16x32_bf16 v[232:235], v[24:27], a[28:31], v[232:235]\n"
"v_perm_b32 v24, v103, v99, s56\n"
"v_perm_b32 v25, v111, v107, s56\n"
"v_mfma_f32_16x16x32_bf16 v[236:239], v[28:31], a[28:31], v[236:239]\n"
"v_perm_b32 v26, v119, v115, s56\n"
"v_perm_b32 v27, v127, v123, s56\n"
"v_mfma_f32_16x16x32_bf16 v[144:147], v[16:19], a[16:19], v[144:147]\n"
"v_perm_b32 v28, v103, v99, s57\n"
"v_perm_b32 v29, v111, v107, s57\n"
"v_mfma_f32_16x16x32_bf16 v[148:151], v[20:23], a[16:19], v[148:151]\n"
"v_perm_b32 v30, v119, v115, s57\n"
"v_perm_b32 v31, v127, v123, s57\n"
"v_mfma_f32_16x16x32_bf16 v[176:179], v[16:19], a[20:23], v[176:179]\n"
"v_mfma_f32_16x16x32_bf16 v[180:183], v[20:23], a[20:23], v[180:183]\n"
"v_mfma_f32_16x16x32_bf16 v[208:211], v[16:19], a[24:27], v[208:211]\n"
"v_mfma_f32_16x16x32_bf16 v[212:215], v[20:23], a[24:27], v[212:215]\n"
"v_mfma_f32_16x16x32_bf16 v[240:243], v[16:19], a[28:31], v[240:243]\n"
"s_waitcnt vmcnt(8)\n"
"v_perm_b32 v16, v36, v32, s56\n"
"v_perm_b32 v17, v44, v40, s56\n"
"v_mfma_f32_16x16x32_bf16 v[244:247], v[20:23], a[28:31], v[244:247]\n"
"v_perm_b32 v18, v52, v48, s56\n"
"v_perm_b32 v19, v60, v56, s56\n"
"v_mfma_f32_16x16x32_bf16 v[152:155], v[24:27], a[16:19], v[152:155]\n"
"v_perm_b32 v20, v36, v32, s57\n"
"v_perm_b32 v21, v44, v40, s57\n"
"v_mfma_f32_16x16x32_bf16 v[156:159], v[28:31], a[16:19], v[156:159]\n"
"v_perm_b32 v22, v52, v48, s57\n"
"v_perm_b32 v23, v60, v56, s57\n"
"v_mfma_f32_16x16x32_bf16 v[184:187], v[24:27], a[20:23], v[184:187]\n"
"s_waitcnt lgkmcnt(0)\n"
"v_mfma_f32_16x16x32_bf16 v[188:191], v[28:31], a[20:23], v[188:191]\n"
"s_add_u32 s84, s49, 7\n"
"s_and_b32 s84, s84, 7\n"
"v_mfma_f32_16x16x32_bf16 v[216:219], v[24:27], a[24:27], v[216:219]\n"
"s_lshl_b32 s84, s84, 19\n"
"v_mfma_f32_16x16x32_bf16 v[220:223], v[28:31], a[24:27], v[220:223]\n"
"buffer_load_dwordx4 v[96:99], v8, s[24:27], s84 offen offset:0\n"
"v_mfma_f32_16x16x32_bf16 v[248:251], v[24:27], a[28:31], v[248:251]\n"
"v_perm_b32 v24, v37, v33, s56\n"
"v_perm_b32 v25, v45, v41, s56\n"
"v_mfma_f32_16x16x32_bf16 v[252:255], v[28:31], a[28:31], v[252:255]\n"
"v_perm_b32 v26, v53, v49, s56\n"
"v_perm_b32 v27, v61, v57, s56\n"
"v_mfma_f32_16x16x32_bf16 v[128:131], v[16:19], a[0:3], v[128:131]\n"
"v_perm_b32 v28, v37, v33, s57\n"
"v_perm_b32 v29, v45, v41, s57\n"
"v_mfma_f32_16x16x32_bf16 v[132:135], v[20:23], a[0:3], v[132:135]\n"
"buffer_load_dwordx4 v[100:103], v9, s[24:27], s84 offen offset:0\n"
"v_perm_b32 v30, v53, v49, s57\n"
"v_mfma_f32_16x16x32_bf16 v[160:163], v[16:19], a[4:7], v[160:163]\n"
"v_perm_b32 v31, v61, v57, s57\n"
"v_mfma_f32_16x16x32_bf16 v[164:167], v[20:23], a[4:7], v[164:167]\n"
"v_mfma_f32_16x16x32_bf16 v[192:195], v[16:19], a[8:11], v[192:195]\n"
"v_mfma_f32_16x16x32_bf16 v[196:199], v[20:23], a[8:11], v[196:199]\n"
"buffer_load_dwordx4 v[104:107], v10, s[24:27], s84 offen offset:0\n"
"v_mfma_f32_16x16x32_bf16 v[224:227], v[16:19], a[12:15], v[224:227]\n"
"ds_read_b128 a[16:19], v7 offset:16384\n"
"v_perm_b32 v16, v38, v34, s56\n"
"v_mfma_f32_16x16x32_bf16 v[228:231], v[20:23], a[12:15], v[228:231]\n"
"ds_read_b128 a[20:23], v7 offset:18432\n"
"v_perm_b32 v17, v46, v42, s56\n"
"v_mfma_f32_16x16x32_bf16 v[136:139], v[24:27], a[0:3], v[136:139]\n"
"ds_read_b128 a[24:27], v7 offset:20480\n"
"v_perm_b32 v18, v54, v50, s56\n"
"v_mfma_f32_16x16x32_bf16 v[140:143], v[28:31], a[0:3], v[140:143]\n"
"ds_read_b128 a[28:31], v7 offset:22528\n"
"buffer_load_dwordx4 v[108:111], v11, s[24:27], s84 offen offset:0\n"
"v_mfma_f32_16x16x32_bf16 v[168:171], v[24:27], a[4:7], v[168:171]\n"
"v_perm_b32 v19, v62, v58, s56\n"
"v_perm_b32 v20, v38, v34, s57\n"
"v_mfma_f32_16x16x32_bf16 v[172:175], v[28:31], a[4:7], v[172:175]\n"
"v_perm_b32 v21, v46, v42, s57\n"
"v_perm_b32 v22, v54, v50, s57\n"
"v_mfma_f32_16x16x32_bf16 v[200:203], v[24:27], a[8:11], v[200:203]\n"
"v_perm_b32 v23, v62, v58, s57\n"
"v_mfma_f32_16x16x32_bf16 v[204:207], v[28:31], a[8:11], v[204:207]\n"
"buffer_load_dwordx4 v[112:115], v12, s[24:27], s84 offen offset:0\n"
"v_mfma_f32_16x16x32_bf16 v[232:235], v[24:27], a[12:15], v[232:235]\n"
"v_perm_b32 v24, v39, v35, s56\n"
"v_perm_b32 v25, v47, v43, s56\n"
"v_mfma_f32_16x16x32_bf16 v[236:239], v[28:31], a[12:15], v[236:239]\n"
"v_perm_b32 v26, v55, v51, s56\n"
"v_perm_b32 v27, v63, v59, s56\n"
"v_mfma_f32_16x16x32_bf16 v[144:147], v[16:19], a[0:3], v[144:147]\n"
"v_perm_b32 v28, v39, v35, s57\n"
"v_perm_b32 v29, v47, v43, s57\n"
"v_mfma_f32_16x16x32_bf16 v[148:151], v[20:23], a[0:3], v[148:151]\n"
"buffer_load_dwordx4 v[116:119], v13, s[24:27], s84 offen offset:0\n"
"v_perm_b32 v30, v55, v51, s57\n"
"v_mfma_f32_16x16x32_bf16 v[176:179], v[16:19], a[4:7], v[176:179]\n"
"v_perm_b32 v31, v63, v59, s57\n"
"v_mfma_f32_16x16x32_bf16 v[180:183], v[20:23], a[4:7], v[180:183]\n"
"v_mfma_f32_16x16x32_bf16 v[208:211], v[16:19], a[8:11], v[208:211]\n"
"v_mfma_f32_16x16x32_bf16 v[212:215], v[20:23], a[8:11], v[212:215]\n"
"buffer_load_dwordx4 v[120:123], v14, s[24:27], s84 offen offset:0\n"
"v_mfma_f32_16x16x32_bf16 v[240:243], v[16:19], a[12:15], v[240:243]\n"
"s_waitcnt vmcnt(7)\n"
"v_perm_b32 v16, v68, v64, s56\n"
"v_perm_b32 v17, v76, v72, s56\n"
"v_mfma_f32_16x16x32_bf16 v[244:247], v[20:23], a[12:15], v[244:247]\n"
"v_perm_b32 v18, v84, v80, s56\n"
"v_perm_b32 v19, v92, v88, s56\n"
"v_mfma_f32_16x16x32_bf16 v[152:155], v[24:27], a[0:3], v[152:155]\n"
"v_perm_b32 v20, v68, v64, s57\n"
"v_perm_b32 v21, v76, v72, s57\n"
"buffer_load_dwordx4 v[124:127], v15, s[24:27], s84 offen offset:0\n"
"v_mfma_f32_16x16x32_bf16 v[156:159], v[28:31], a[0:3], v[156:159]\n"
"v_perm_b32 v22, v84, v80, s57\n"
"v_perm_b32 v23, v92, v88, s57\n"
"v_mfma_f32_16x16x32_bf16 v[184:187], v[24:27], a[4:7], v[184:187]\n"
"s_waitcnt lgkmcnt(0)\n"
"v_mfma_f32_16x16x32_bf16 v[188:191], v[28:31], a[4:7], v[188:191]\n"
"s_add_u32 s85, s49, 7\n"
"s_and_b32 s85, s85, 7\n"
"v_mfma_f32_16x16x32_bf16 v[216:219], v[24:27], a[8:11], v[216:219]\n"
"s_lshl_b32 s85, s85, 19\n"
"s_add_u32 s85, s85, 262144\n"
"v_mfma_f32_16x16x32_bf16 v[220:223], v[28:31], a[8:11], v[220:223]\n"
"buffer_load_dwordx4 v[32:35], v8, s[24:27], s85 offen offset:0\n"
"v_mfma_f32_16x16x32_bf16 v[248:251], v[24:27], a[12:15], v[248:251]\n"
"v_perm_b32 v24, v69, v65, s56\n"
"v_perm_b32 v25, v77, v73, s56\n"
"v_mfma_f32_16x16x32_bf16 v[252:255], v[28:31], a[12:15], v[252:255]\n"
"v_perm_b32 v26, v85, v81, s56\n"
"v_perm_b32 v27, v93, v89, s56\n"
"v_mfma_f32_16x16x32_bf16 v[128:131], v[16:19], a[16:19], v[128:131]\n"
"v_perm_b32 v28, v69, v65, s57\n"
"v_perm_b32 v29, v77, v73, s57\n"
"v_mfma_f32_16x16x32_bf16 v[132:135], v[20:23], a[16:19], v[132:135]\n"
"buffer_load_dwordx4 v[36:39], v9, s[24:27], s85 offen offset:0\n"
"v_perm_b32 v30, v85, v81, s57\n"
"v_mfma_f32_16x16x32_bf16 v[160:163], v[16:19], a[20:23], v[160:163]\n"
"v_perm_b32 v31, v93, v89, s57\n"
"v_mfma_f32_16x16x32_bf16 v[164:167], v[20:23], a[20:23], v[164:167]\n"
"v_mfma_f32_16x16x32_bf16 v[192:195], v[16:19], a[24:27], v[192:195]\n"
"v_mfma_f32_16x16x32_bf16 v[196:199], v[20:23], a[24:27], v[196:199]\n"
"buffer_load_dwordx4 v[40:43], v10, s[24:27], s85 offen offset:0\n"
"v_mfma_f32_16x16x32_bf16 v[224:227], v[16:19], a[28:31], v[224:227]\n"
"ds_read_b128 a[0:3], v6 offset:24576\n"
"v_perm_b32 v16, v70, v66, s56\n"
"v_mfma_f32_16x16x32_bf16 v[228:231], v[20:23], a[28:31], v[228:231]\n"
"ds_read_b128 a[4:7], v6 offset:26624\n"
"v_perm_b32 v17, v78, v74, s56\n"
"v_mfma_f32_16x16x32_bf16 v[136:139], v[24:27], a[16:19], v[136:139]\n"
"ds_read_b128 a[8:11], v6 offset:28672\n"
"v_perm_b32 v18, v86, v82, s56\n"
"v_mfma_f32_16x16x32_bf16 v[140:143], v[28:31], a[16:19], v[140:143]\n"
"ds_read_b128 a[12:15], v6 offset:30720\n"
"buffer_load_dwordx4 v[44:47], v11, s[24:27], s85 offen offset:0\n"
"v_mfma_f32_16x16x32_bf16 v[168:171], v[24:27], a[20:23], v[168:171]\n"
"v_perm_b32 v19, v94, v90, s56\n"
"v_perm_b32 v20, v70, v66, s57\n"
"v_mfma_f32_16x16x32_bf16 v[172:175], v[28:31], a[20:23], v[172:175]\n"
"v_perm_b32 v21, v78, v74, s57\n"
"v_perm_b32 v22, v86, v82, s57\n"
"v_mfma_f32_16x16x32_bf16 v[200:203], v[24:27], a[24:27], v[200:203]\n"
"v_perm_b32 v23, v94, v90, s57\n"
"v_mfma_f32_16x16x32_bf16 v[204:207], v[28:31], a[24:27], v[204:207]\n"
"buffer_load_dwordx4 v[48:51], v12, s[24:27], s85 offen offset:0\n"
"v_mfma_f32_16x16x32_bf16 v[232:235], v[24:27], a[28:31], v[232:235]\n"
"v_perm_b32 v24, v71, v67, s56\n"
"v_perm_b32 v25, v79, v75, s56\n"
"v_mfma_f32_16x16x32_bf16 v[236:239], v[28:31], a[28:31], v[236:239]\n"
"v_perm_b32 v26, v87, v83, s56\n"
"v_perm_b32 v27, v95, v91, s56\n"
"v_mfma_f32_16x16x32_bf16 v[144:147], v[16:19], a[16:19], v[144:147]\n"
"v_perm_b32 v28, v71, v67, s57\n"
"v_perm_b32 v29, v79, v75, s57\n"
"v_mfma_f32_16x16x32_bf16 v[148:151], v[20:23], a[16:19], v[148:151]\n"
"buffer_load_dwordx4 v[52:55], v13, s[24:27], s85 offen offset:0\n"
"v_perm_b32 v30, v87, v83, s57\n"
"v_mfma_f32_16x16x32_bf16 v[176:179], v[16:19], a[20:23], v[176:179]\n"
"v_perm_b32 v31, v95, v91, s57\n"
"v_mfma_f32_16x16x32_bf16 v[180:183], v[20:23], a[20:23], v[180:183]\n"
"v_mfma_f32_16x16x32_bf16 v[208:211], v[16:19], a[24:27], v[208:211]\n"
"v_mfma_f32_16x16x32_bf16 v[212:215], v[20:23], a[24:27], v[212:215]\n"
"buffer_load_dwordx4 v[56:59], v14, s[24:27], s85 offen offset:0\n"
"v_mfma_f32_16x16x32_bf16 v[240:243], v[16:19], a[28:31], v[240:243]\n"
"s_waitcnt vmcnt(7)\n"
"v_perm_b32 v16, v100, v96, s56\n"
"v_perm_b32 v17, v108, v104, s56\n"
"v_mfma_f32_16x16x32_bf16 v[244:247], v[20:23], a[28:31], v[244:247]\n"
"v_perm_b32 v18, v116, v112, s56\n"
"v_perm_b32 v19, v124, v120, s56\n"
"v_mfma_f32_16x16x32_bf16 v[152:155], v[24:27], a[16:19], v[152:155]\n"
"v_perm_b32 v20, v100, v96, s57\n"
"v_perm_b32 v21, v108, v104, s57\n"
"buffer_load_dwordx4 v[60:63], v15, s[24:27], s85 offen offset:0\n"
"v_mfma_f32_16x16x32_bf16 v[156:159], v[28:31], a[16:19], v[156:159]\n"
"v_perm_b32 v22, v116, v112, s57\n"
"v_perm_b32 v23, v124, v120, s57\n"
"v_mfma_f32_16x16x32_bf16 v[184:187], v[24:27], a[20:23], v[184:187]\n"
"s_waitcnt lgkmcnt(0)\n"
"s_add_u32 m0, s60, 131072\n"
"v_mfma_f32_16x16x32_bf16 v[188:191], v[28:31], a[20:23], v[188:191]\n"
"v_mfma_f32_16x16x32_bf16 v[216:219], v[24:27], a[24:27], v[216:219]\n"
"buffer_load_dwordx4 v2, s[32:35], s69 offen offset:0 nt lds\n"
"v_mfma_f32_16x16x32_bf16 v[220:223], v[28:31], a[24:27], v[220:223]\n"
"v_mfma_f32_16x16x32_bf16 v[248:251], v[24:27], a[28:31], v[248:251]\n"
"v_perm_b32 v24, v101, v97, s56\n"
"v_perm_b32 v25, v109, v105, s56\n"
"v_mfma_f32_16x16x32_bf16 v[252:255], v[28:31], a[28:31], v[252:255]\n"
"v_perm_b32 v26, v117, v113, s56\n"
"v_perm_b32 v27, v125, v121, s56\n"
"v_mfma_f32_16x16x32_bf16 v[128:131], v[16:19], a[0:3], v[128:131]\n"
"buffer_load_dwordx4 v3, s[32:35], s69 offen offset:1024 nt lds\n"
"v_perm_b32 v28, v101, v97, s57\n"
"v_mfma_f32_16x16x32_bf16 v[132:135], v[20:23], a[0:3], v[132:135]\n"
"v_perm_b32 v29, v109, v105, s57\n"
"v_perm_b32 v30, v117, v113, s57\n"
"v_mfma_f32_16x16x32_bf16 v[160:163], v[16:19], a[4:7], v[160:163]\n"
"v_perm_b32 v31, v125, v121, s57\n"
"v_mfma_f32_16x16x32_bf16 v[164:167], v[20:23], a[4:7], v[164:167]\n"
"v_mfma_f32_16x16x32_bf16 v[192:195], v[16:19], a[8:11], v[192:195]\n"
"buffer_load_dwordx4 v4, s[32:35], s69 offen offset:2048 nt lds\n"
"v_mfma_f32_16x16x32_bf16 v[196:199], v[20:23], a[8:11], v[196:199]\n"
"v_mfma_f32_16x16x32_bf16 v[224:227], v[16:19], a[12:15], v[224:227]\n"
"ds_read_b128 a[16:19], v7 offset:24576\n"
"v_perm_b32 v16, v102, v98, s56\n"
"v_mfma_f32_16x16x32_bf16 v[228:231], v[20:23], a[12:15], v[228:231]\n"
"ds_read_b128 a[20:23], v7 offset:26624\n"
"v_perm_b32 v17, v110, v106, s56\n"
"v_mfma_f32_16x16x32_bf16 v[136:139], v[24:27], a[0:3], v[136:139]\n"
"ds_read_b128 a[24:27], v7 offset:28672\n"
"buffer_load_dwordx4 v5, s[32:35], s69 offen offset:3072 nt lds\n"
"v_mfma_f32_16x16x32_bf16 v[140:143], v[28:31], a[0:3], v[140:143]\n"
"ds_read_b128 a[28:31], v7 offset:30720\n"
"v_perm_b32 v18, v118, v114, s56\n"
"v_mfma_f32_16x16x32_bf16 v[168:171], v[24:27], a[4:7], v[168:171]\n"
"v_perm_b32 v19, v126, v122, s56\n"
"v_perm_b32 v20, v102, v98, s57\n"
"v_mfma_f32_16x16x32_bf16 v[172:175], v[28:31], a[4:7], v[172:175]\n"
"v_perm_b32 v21, v110, v106, s57\n"
"v_perm_b32 v22, v118, v114, s57\n"
"v_mfma_f32_16x16x32_bf16 v[200:203], v[24:27], a[8:11], v[200:203]\n"
"v_perm_b32 v23, v126, v122, s57\n"
"v_mfma_f32_16x16x32_bf16 v[204:207], v[28:31], a[8:11], v[204:207]\n"
"v_mfma_f32_16x16x32_bf16 v[232:235], v[24:27], a[12:15], v[232:235]\n"
"v_perm_b32 v24, v103, v99, s56\n"
"v_perm_b32 v25, v111, v107, s56\n"
"v_mfma_f32_16x16x32_bf16 v[236:239], v[28:31], a[12:15], v[236:239]\n"
"v_perm_b32 v26, v119, v115, s56\n"
"v_perm_b32 v27, v127, v123, s56\n"
"v_mfma_f32_16x16x32_bf16 v[144:147], v[16:19], a[0:3], v[144:147]\n"
"v_perm_b32 v28, v103, v99, s57\n"
"v_perm_b32 v29, v111, v107, s57\n"
"v_mfma_f32_16x16x32_bf16 v[148:151], v[20:23], a[0:3], v[148:151]\n"
"v_perm_b32 v30, v119, v115, s57\n"
"v_perm_b32 v31, v127, v123, s57\n"
"v_mfma_f32_16x16x32_bf16 v[176:179], v[16:19], a[4:7], v[176:179]\n"
"v_mfma_f32_16x16x32_bf16 v[180:183], v[20:23], a[4:7], v[180:183]\n"
"v_mfma_f32_16x16x32_bf16 v[208:211], v[16:19], a[8:11], v[208:211]\n"
"v_mfma_f32_16x16x32_bf16 v[212:215], v[20:23], a[8:11], v[212:215]\n"
"v_mfma_f32_16x16x32_bf16 v[240:243], v[16:19], a[12:15], v[240:243]\n"
"s_waitcnt vmcnt(4)\n"
"v_perm_b32 v16, v36, v32, s56\n"
"v_perm_b32 v17, v44, v40, s56\n"
"v_mfma_f32_16x16x32_bf16 v[244:247], v[20:23], a[12:15], v[244:247]\n"
"v_perm_b32 v18, v52, v48, s56\n"
"v_perm_b32 v19, v60, v56, s56\n"
"v_mfma_f32_16x16x32_bf16 v[152:155], v[24:27], a[0:3], v[152:155]\n"
"v_perm_b32 v20, v36, v32, s57\n"
"v_perm_b32 v21, v44, v40, s57\n"
"v_mfma_f32_16x16x32_bf16 v[156:159], v[28:31], a[0:3], v[156:159]\n"
"v_perm_b32 v22, v52, v48, s57\n"
"v_perm_b32 v23, v60, v56, s57\n"
"v_mfma_f32_16x16x32_bf16 v[184:187], v[24:27], a[4:7], v[184:187]\n"
"s_waitcnt lgkmcnt(0)\n"
"v_mfma_f32_16x16x32_bf16 v[188:191], v[28:31], a[4:7], v[188:191]\n"
"v_mfma_f32_16x16x32_bf16 v[216:219], v[24:27], a[8:11], v[216:219]\n"
"v_mfma_f32_16x16x32_bf16 v[220:223], v[28:31], a[8:11], v[220:223]\n"
"v_mfma_f32_16x16x32_bf16 v[248:251], v[24:27], a[12:15], v[248:251]\n"
"v_perm_b32 v24, v37, v33, s56\n"
"v_perm_b32 v25, v45, v41, s56\n"
"v_mfma_f32_16x16x32_bf16 v[252:255], v[28:31], a[12:15], v[252:255]\n"
"v_perm_b32 v26, v53, v49, s56\n"
"v_perm_b32 v27, v61, v57, s56\n"
"v_mfma_f32_16x16x32_bf16 v[128:131], v[16:19], a[16:19], v[128:131]\n"
"v_perm_b32 v28, v37, v33, s57\n"
"v_perm_b32 v29, v45, v41, s57\n"
"v_mfma_f32_16x16x32_bf16 v[132:135], v[20:23], a[16:19], v[132:135]\n"
"v_perm_b32 v30, v53, v49, s57\n"
"v_perm_b32 v31, v61, v57, s57\n"
"v_mfma_f32_16x16x32_bf16 v[160:163], v[16:19], a[20:23], v[160:163]\n"
"v_mfma_f32_16x16x32_bf16 v[164:167], v[20:23], a[20:23], v[164:167]\n"
"v_mfma_f32_16x16x32_bf16 v[192:195], v[16:19], a[24:27], v[192:195]\n"
"v_mfma_f32_16x16x32_bf16 v[196:199], v[20:23], a[24:27], v[196:199]\n"
"v_mfma_f32_16x16x32_bf16 v[224:227], v[16:19], a[28:31], v[224:227]\n"
"v_perm_b32 v16, v38, v34, s56\n"
"v_perm_b32 v17, v46, v42, s56\n"
"v_mfma_f32_16x16x32_bf16 v[228:231], v[20:23], a[28:31], v[228:231]\n"
"v_perm_b32 v18, v54, v50, s56\n"
"v_perm_b32 v19, v62, v58, s56\n"
"v_mfma_f32_16x16x32_bf16 v[136:139], v[24:27], a[16:19], v[136:139]\n"
"v_perm_b32 v20, v38, v34, s57\n"
"v_perm_b32 v21, v46, v42, s57\n"
"v_mfma_f32_16x16x32_bf16 v[140:143], v[28:31], a[16:19], v[140:143]\n"
"v_perm_b32 v22, v54, v50, s57\n"
"v_perm_b32 v23, v62, v58, s57\n"
"v_mfma_f32_16x16x32_bf16 v[168:171], v[24:27], a[20:23], v[168:171]\n"
"v_mfma_f32_16x16x32_bf16 v[172:175], v[28:31], a[20:23], v[172:175]\n"
"v_mfma_f32_16x16x32_bf16 v[200:203], v[24:27], a[24:27], v[200:203]\n"
"v_mfma_f32_16x16x32_bf16 v[204:207], v[28:31], a[24:27], v[204:207]\n"
"v_mfma_f32_16x16x32_bf16 v[232:235], v[24:27], a[28:31], v[232:235]\n"
"v_perm_b32 v24, v39, v35, s56\n"
"v_perm_b32 v25, v47, v43, s56\n"
"v_mfma_f32_16x16x32_bf16 v[236:239], v[28:31], a[28:31], v[236:239]\n"
"v_perm_b32 v26, v55, v51, s56\n"
"v_perm_b32 v27, v63, v59, s56\n"
"v_mfma_f32_16x16x32_bf16 v[144:147], v[16:19], a[16:19], v[144:147]\n"
"v_perm_b32 v28, v39, v35, s57\n"
"v_perm_b32 v29, v47, v43, s57\n"
"v_mfma_f32_16x16x32_bf16 v[148:151], v[20:23], a[16:19], v[148:151]\n"
"v_perm_b32 v30, v55, v51, s57\n"
"v_perm_b32 v31, v63, v59, s57\n"
"v_mfma_f32_16x16x32_bf16 v[176:179], v[16:19], a[20:23], v[176:179]\n"
"v_mfma_f32_16x16x32_bf16 v[180:183], v[20:23], a[20:23], v[180:183]\n"
"v_mfma_f32_16x16x32_bf16 v[208:211], v[16:19], a[24:27], v[208:211]\n"
"v_mfma_f32_16x16x32_bf16 v[212:215], v[20:23], a[24:27], v[212:215]\n"
"v_mfma_f32_16x16x32_bf16 v[240:243], v[16:19], a[28:31], v[240:243]\n"
"v_mfma_f32_16x16x32_bf16 v[244:247], v[20:23], a[28:31], v[244:247]\n"
"v_mfma_f32_16x16x32_bf16 v[152:155], v[24:27], a[16:19], v[152:155]\n"
"v_mfma_f32_16x16x32_bf16 v[156:159], v[28:31], a[16:19], v[156:159]\n"
"v_mfma_f32_16x16x32_bf16 v[184:187], v[24:27], a[20:23], v[184:187]\n"
"v_mfma_f32_16x16x32_bf16 v[188:191], v[28:31], a[20:23], v[188:191]\n"
"v_mfma_f32_16x16x32_bf16 v[216:219], v[24:27], a[24:27], v[216:219]\n"
"v_mfma_f32_16x16x32_bf16 v[220:223], v[28:31], a[24:27], v[220:223]\n"
"v_mfma_f32_16x16x32_bf16 v[248:251], v[24:27], a[28:31], v[248:251]\n"
"v_mfma_f32_16x16x32_bf16 v[252:255], v[28:31], a[28:31], v[252:255]\n"
"v_pk_mul_f32 v[128:129], v[128:129], s[58:59] op_sel_hi:[1,0]\n"
"v_pk_mul_f32 v[130:131], v[130:131], s[58:59] op_sel_hi:[1,0]\n"
"v_pk_mul_f32 v[160:161], v[160:161], s[58:59] op_sel_hi:[1,0]\n"
"v_pk_mul_f32 v[162:163], v[162:163], s[58:59] op_sel_hi:[1,0]\n"
"v_pk_mul_f32 v[132:133], v[132:133], s[58:59] op_sel_hi:[1,0]\n"
"v_pk_mul_f32 v[134:135], v[134:135], s[58:59] op_sel_hi:[1,0]\n"
"v_pk_mul_f32 v[164:165], v[164:165], s[58:59] op_sel_hi:[1,0]\n"
"v_pk_mul_f32 v[166:167], v[166:167], s[58:59] op_sel_hi:[1,0]\n"
"v_pk_mul_f32 v[136:137], v[136:137], s[58:59] op_sel_hi:[1,0]\n"
"v_pk_mul_f32 v[138:139], v[138:139], s[58:59] op_sel_hi:[1,0]\n"
"v_pk_mul_f32 v[168:169], v[168:169], s[58:59] op_sel_hi:[1,0]\n"
"v_pk_mul_f32 v[170:171], v[170:171], s[58:59] op_sel_hi:[1,0]\n"
"v_pk_mul_f32 v[140:141], v[140:141], s[58:59] op_sel_hi:[1,0]\n"
"v_pk_mul_f32 v[142:143], v[142:143], s[58:59] op_sel_hi:[1,0]\n"
"v_pk_mul_f32 v[172:173], v[172:173], s[58:59] op_sel_hi:[1,0]\n"
"v_pk_mul_f32 v[174:175], v[174:175], s[58:59] op_sel_hi:[1,0]\n"
"v_pk_mul_f32 v[144:145], v[144:145], s[58:59] op_sel_hi:[1,0]\n"
"v_pk_mul_f32 v[146:147], v[146:147], s[58:59] op_sel_hi:[1,0]\n"
"v_pk_mul_f32 v[176:177], v[176:177], s[58:59] op_sel_hi:[1,0]\n"
"v_pk_mul_f32 v[178:179], v[178:179], s[58:59] op_sel_hi:[1,0]\n"
"v_pk_mul_f32 v[148:149], v[148:149], s[58:59] op_sel_hi:[1,0]\n"
"v_pk_mul_f32 v[150:151], v[150:151], s[58:59] op_sel_hi:[1,0]\n"
"v_pk_mul_f32 v[180:181], v[180:181], s[58:59] op_sel_hi:[1,0]\n"
"v_pk_mul_f32 v[182:183], v[182:183], s[58:59] op_sel_hi:[1,0]\n"
"v_pk_mul_f32 v[152:153], v[152:153], s[58:59] op_sel_hi:[1,0]\n"
"v_pk_mul_f32 v[154:155], v[154:155], s[58:59] op_sel_hi:[1,0]\n"
"v_pk_mul_f32 v[184:185], v[184:185], s[58:59] op_sel_hi:[1,0]\n"
"v_pk_mul_f32 v[186:187], v[186:187], s[58:59] op_sel_hi:[1,0]\n"
"v_pk_mul_f32 v[156:157], v[156:157], s[58:59] op_sel_hi:[1,0]\n"
"v_pk_mul_f32 v[158:159], v[158:159], s[58:59] op_sel_hi:[1,0]\n"
"v_pk_mul_f32 v[188:189], v[188:189], s[58:59] op_sel_hi:[1,0]\n"
"v_pk_mul_f32 v[190:191], v[190:191], s[58:59] op_sel_hi:[1,0]\n"
"v_cvt_pk_bf16_f32 v32, v128, v132\n"
"v_cvt_pk_bf16_f32 v33, v136, v140\n"
"v_cvt_pk_bf16_f32 v34, v144, v148\n"
"v_cvt_pk_bf16_f32 v35, v152, v156\n"
"v_cvt_pk_bf16_f32 v36, v129, v133\n"
"v_cvt_pk_bf16_f32 v37, v137, v141\n"
"v_cvt_pk_bf16_f32 v38, v145, v149\n"
"v_cvt_pk_bf16_f32 v39, v153, v157\n"
"v_cvt_pk_bf16_f32 v40, v130, v134\n"
"v_cvt_pk_bf16_f32 v41, v138, v142\n"
"v_cvt_pk_bf16_f32 v42, v146, v150\n"
"v_cvt_pk_bf16_f32 v43, v154, v158\n"
"v_cvt_pk_bf16_f32 v44, v131, v135\n"
"v_cvt_pk_bf16_f32 v45, v139, v143\n"
"v_cvt_pk_bf16_f32 v46, v147, v151\n"
"v_cvt_pk_bf16_f32 v47, v155, v159\n"
"v_cvt_pk_bf16_f32 v48, v160, v164\n"
"v_cvt_pk_bf16_f32 v49, v168, v172\n"
"v_cvt_pk_bf16_f32 v50, v176, v180\n"
"v_cvt_pk_bf16_f32 v51, v184, v188\n"
"v_cvt_pk_bf16_f32 v52, v161, v165\n"
"v_cvt_pk_bf16_f32 v53, v169, v173\n"
"v_cvt_pk_bf16_f32 v54, v177, v181\n"
"v_cvt_pk_bf16_f32 v55, v185, v189\n"
"v_cvt_pk_bf16_f32 v56, v162, v166\n"
"v_cvt_pk_bf16_f32 v57, v170, v174\n"
"v_cvt_pk_bf16_f32 v58, v178, v182\n"
"v_cvt_pk_bf16_f32 v59, v186, v190\n"
"v_cvt_pk_bf16_f32 v60, v163, v167\n"
"v_cvt_pk_bf16_f32 v61, v171, v175\n"
"v_cvt_pk_bf16_f32 v62, v179, v183\n"
"v_cvt_pk_bf16_f32 v63, v187, v191\n"
"v_bfe_u32 v180, v0, 3, 1\n"
"v_lshrrev_b32 v181, 4, v0\n"
"v_and_b32 v182, 7, v0\n"
"v_bfe_u32 v183, v0, 2, 1\n"
"v_lshl_or_b32 v183, v183, 1, v180\n"
"v_lshlrev_b32 v180, 11, v180\n"
"v_lshl_add_u32 v180, v181, 9, v180\n"
"v_lshl_add_u32 v180, v182, 6, v180\n"
"v_xor_b32 v184, 0, v183\n"
"v_lshl_add_u32 v168, v184, 4, v180\n"
"v_xor_b32 v184, 1, v183\n"
"v_lshl_add_u32 v169, v184, 4, v180\n"
"v_xor_b32 v184, 2, v183\n"
"v_lshl_add_u32 v170, v184, 4, v180\n"
"v_xor_b32 v184, 3, v183\n"
"v_lshl_add_u32 v171, v184, 4, v180\n"
"s_barrier\n"
"ds_read_b128 a[64:67], v168 offset:0\n"
"ds_read_b128 a[68:71], v168 offset:4096\n"
"ds_read_b128 a[72:75], v168 offset:8192\n"
"ds_read_b128 a[76:79], v168 offset:12288\n"
"ds_read_b128 a[80:83], v169 offset:0\n"
"ds_read_b128 a[84:87], v169 offset:4096\n"
"ds_read_b128 a[88:91], v169 offset:8192\n"
"ds_read_b128 a[92:95], v169 offset:12288\n"
"s_add_u32 m0, s60, 147456\n"
"s_nop 0\n"
"buffer_load_dwordx4 v2, s[4:7], s63 offen offset:0 nt lds\n"
"buffer_load_dwordx4 v3, s[4:7], s63 offen offset:1024 nt lds\n"
"buffer_load_dwordx4 v4, s[4:7], s63 offen offset:2048 nt lds\n"
"buffer_load_dwordx4 v5, s[4:7], s63 offen offset:3072 nt lds\n"
"s_waitcnt lgkmcnt(7)\n"
"v_mfma_f32_16x16x32_bf16 v[64:67], a[64:67], v[32:35], 0\n"
"s_waitcnt lgkmcnt(6)\n"
"v_lshrrev_b32 v180, 5, v0\n"
"v_bfe_u32 v182, v0, 4, 1\n"
"v_mfma_f32_16x16x32_bf16 v[72:75], a[68:71], v[32:35], 0\n"
"s_waitcnt lgkmcnt(5)\n"
"v_lshl_or_b32 v183, v182, 1, v180\n"
"v_bfe_u32 v184, v0, 1, 1\n"
"v_mfma_f32_16x16x32_bf16 v[80:83], a[72:75], v[32:35], 0\n"
"s_waitcnt lgkmcnt(1)\n"
"v_xor_b32 v183, v183, v184\n"
"v_lshlrev_b32 v180, 11, v180\n"
"v_mfma_f32_16x16x32_bf16 v[88:91], a[76:79], v[32:35], 0\n"
"v_lshl_add_u32 v180, v182, 8, v180\n"
"v_bfe_u32 v182, v0, 2, 2\n"
"v_mfma_f32_16x16x32_bf16 v[68:71], a[64:67], v[48:51], 0\n"
"ds_read_b128 a[64:67], v170 offset:0\n"
"v_lshl_add_u32 v180, v182, 6, v180\n"
"v_mfma_f32_16x16x32_bf16 v[76:79], a[68:71], v[48:51], 0\n"
"ds_read_b128 a[68:71], v170 offset:4096\n"
"v_and_b32 v182, 1, v0\n"
"v_mfma_f32_16x16x32_bf16 v[84:87], a[72:75], v[48:51], 0\n"
"ds_read_b128 a[72:75], v170 offset:8192\n"
"v_lshl_add_u32 v180, v182, 3, v180\n"
"v_mfma_f32_16x16x32_bf16 v[92:95], a[76:79], v[48:51], 0\n"
"ds_read_b128 a[76:79], v170 offset:12288\n"
"v_lshl_add_u32 v172, v183, 4, v180\n"
"v_mfma_f32_16x16x32_bf16 v[64:67], a[80:83], v[36:39], v[64:67]\n"
"v_xor_b32 v173, 32, v172\n"
"v_lshlrev_b32 v12, 4, v0\n"
"v_mfma_f32_16x16x32_bf16 v[72:75], a[84:87], v[36:39], v[72:75]\n"
"v_add_u32 v174, s60, v12\n"
"v_add_u32 v175, s60, v174\n"
"v_mfma_f32_16x16x32_bf16 v[80:83], a[88:91], v[36:39], v[80:83]\n"
"s_waitcnt lgkmcnt(3)\n"
"v_and_b32 v182, 15, v0\n"
"v_lshlrev_b32 v182, 4, v182\n"
"v_mfma_f32_16x16x32_bf16 v[88:91], a[92:95], v[36:39], v[88:91]\n"
"v_lshl_add_u32 v13, v181, 12, v182\n"
"v_add_u32 v14, 16384, v13\n"
"v_mfma_f32_16x16x32_bf16 v[68:71], a[80:83], v[52:55], v[68:71]\n"
"ds_read_b128 a[80:83], v171 offset:0\n"
"v_mov_b32 v176, 0x3f803f80\n"
"v_mfma_f32_16x16x32_bf16 v[76:79], a[84:87], v[52:55], v[76:79]\n"
"ds_read_b128 a[84:87], v171 offset:4096\n"
"v_mov_b32 v177, 0x3f803f80\n"
"v_mfma_f32_16x16x32_bf16 v[84:87], a[88:91], v[52:55], v[84:87]\n"
"ds_read_b128 a[88:91], v171 offset:8192\n"
"v_mov_b32 v178, 0x3f803f80\n"
"v_mfma_f32_16x16x32_bf16 v[92:95], a[92:95], v[52:55], v[92:95]\n"
"ds_read_b128 a[92:95], v171 offset:12288\n"
"v_mov_b32 v179, 0x3f803f80\n"
"v_mfma_f32_16x16x32_bf16 v[64:67], a[64:67], v[40:43], v[64:67]\n"
"s_waitcnt lgkmcnt(4)\n"
"v_mul_f32 v192, s58, v192\n"
"v_mul_f32 v196, s58, v196\n"
"v_mfma_f32_16x16x32_bf16 v[72:75], a[68:71], v[40:43], v[72:75]\n"
"v_cvt_pk_bf16_f32 v16, v192, v196\n"
"v_mul_f32 v200, s58, v200\n"
"v_mfma_f32_16x16x32_bf16 v[80:83], a[72:75], v[40:43], v[80:83]\n"
"v_mul_f32 v204, s58, v204\n"
"v_cvt_pk_bf16_f32 v17, v200, v204\n"
"v_mfma_f32_16x16x32_bf16 v[88:91], a[76:79], v[40:43], v[88:91]\n"
"v_mul_f32 v208, s58, v208\n"
"v_mul_f32 v212, s58, v212\n"
"v_mfma_f32_16x16x32_bf16 v[68:71], a[64:67], v[56:59], v[68:71]\n"
"v_cvt_pk_bf16_f32 v18, v208, v212\n"
"v_mul_f32 v216, s58, v216\n"
"v_mfma_f32_16x16x32_bf16 v[76:79], a[68:71], v[56:59], v[76:79]\n"
"v_mul_f32 v220, s58, v220\n"
"v_cvt_pk_bf16_f32 v19, v216, v220\n"
"v_mfma_f32_16x16x32_bf16 v[84:87], a[72:75], v[56:59], v[84:87]\n"
"v_mul_f32 v193, s58, v193\n"
"v_mul_f32 v197, s58, v197\n"
"v_mfma_f32_16x16x32_bf16 v[92:95], a[76:79], v[56:59], v[92:95]\n"
"s_waitcnt lgkmcnt(0)\n"
"v_cvt_pk_bf16_f32 v20, v193, v197\n"
"v_mul_f32 v201, s58, v201\n"
"v_mfma_f32_16x16x32_bf16 v[64:67], a[80:83], v[44:47], v[64:67]\n"
"v_mul_f32 v205, s58, v205\n"
"v_cvt_pk_bf16_f32 v21, v201, v205\n"
"v_mfma_f32_16x16x32_bf16 v[72:75], a[84:87], v[44:47], v[72:75]\n"
"v_mul_f32 v209, s58, v209\n"
"v_mul_f32 v213, s58, v213\n"
"v_mfma_f32_16x16x32_bf16 v[80:83], a[88:91], v[44:47], v[80:83]\n"
"v_cvt_pk_bf16_f32 v22, v209, v213\n"
"v_mul_f32 v217, s58, v217\n"
"v_mfma_f32_16x16x32_bf16 v[88:91], a[92:95], v[44:47], v[88:91]\n"
"v_mul_f32 v221, s58, v221\n"
"v_cvt_pk_bf16_f32 v23, v217, v221\n"
"v_mfma_f32_16x16x32_bf16 v[68:71], a[80:83], v[60:63], v[68:71]\n"
"v_mul_f32 v194, s58, v194\n"
"v_mul_f32 v198, s58, v198\n"
"v_mfma_f32_16x16x32_bf16 v[76:79], a[84:87], v[60:63], v[76:79]\n"
"v_cvt_pk_bf16_f32 v24, v194, v198\n"
"v_mul_f32 v202, s58, v202\n"
"v_mfma_f32_16x16x32_bf16 v[84:87], a[88:91], v[60:63], v[84:87]\n"
"v_mul_f32 v206, s58, v206\n"
"v_cvt_pk_bf16_f32 v25, v202, v206\n"
"v_mfma_f32_16x16x32_bf16 v[92:95], a[92:95], v[60:63], v[92:95]\n"
"v_mul_f32 v210, s58, v210\n"
"v_mul_f32 v214, s58, v214\n"
"s_barrier\n"
".LBbar0:\n"
"ds_read_b128 a[64:67], v168 offset:16384\n"
"ds_read_b128 a[68:71], v168 offset:20480\n"
"ds_read_b128 a[72:75], v168 offset:24576\n"
"ds_read_b128 a[76:79], v168 offset:28672\n"
"ds_read_b128 a[80:83], v169 offset:16384\n"
"ds_read_b128 a[84:87], v169 offset:20480\n"
"ds_read_b128 a[88:91], v169 offset:24576\n"
"ds_read_b128 a[92:95], v169 offset:28672\n"
"v_add_u32 v191, 0, v174\n"
"ds_write_b128 v191, a[144:147] offset:0\n"
"ds_write_b128 v191, a[148:151] offset:1024\n"
"ds_write_b128 v191, a[152:155] offset:2048\n"
"ds_write_b128 v191, a[156:159] offset:3072\n"
"v_max3_f32 v188, v64, v65, v66\n"
"v_max3_f32 v189, v68, v69, v70\n"
"v_max3_f32 v188, v188, v67, v72\n"
"v_max3_f32 v189, v189, v71, v76\n"
"v_max3_f32 v188, v188, v73, v74\n"
"v_max3_f32 v189, v189, v77, v78\n"
"v_max3_f32 v188, v188, v75, v80\n"
"v_max3_f32 v189, v189, v79, v84\n"
"v_max3_f32 v188, v188, v81, v82\n"
"v_max3_f32 v189, v189, v85, v86\n"
"v_max3_f32 v188, v188, v83, v88\n"
"v_max3_f32 v189, v189, v87, v92\n"
"v_max3_f32 v188, v188, v89, v90\n"
"v_max3_f32 v189, v189, v93, v94\n"
"v_max_f32 v188, v188, v91\n"
"v_max_f32 v189, v189, v95\n"
"s_nop 0\n"
"s_nop 0\n"
"v_permlane32_swap_b32 v188, v189\n"
"v_max_f32 v188, v188, v189\n"
"v_mov_b32 v189, v188\n"
"s_nop 0\n"
"s_nop 0\n"
"v_permlane16_swap_b32 v188, v189\n"
"v_max_f32 v188, v188, v189\n"
"v_mov_b32 v190, v188\n"
"s_nop 0\n"
"s_nop 0\n"
"v_permlane32_swap_b32 v188, v190\n"
"v_sub_f32 v128, 0, v188\n"
"v_sub_f32 v129, 0, v188\n"
"v_sub_f32 v130, 0, v188\n"
"v_sub_f32 v131, 0, v188\n"
"v_sub_f32 v64, v64, v188\n"
"v_sub_f32 v65, v65, v188\n"
"v_sub_f32 v66, v66, v188\n"
"v_sub_f32 v67, v67, v188\n"
"v_sub_f32 v72, v72, v188\n"
"v_sub_f32 v73, v73, v188\n"
"v_sub_f32 v74, v74, v188\n"
"v_sub_f32 v75, v75, v188\n"
"v_sub_f32 v80, v80, v188\n"
"v_sub_f32 v81, v81, v188\n"
"v_sub_f32 v82, v82, v188\n"
"v_sub_f32 v83, v83, v188\n"
"v_sub_f32 v88, v88, v188\n"
"v_sub_f32 v89, v89, v188\n"
"v_sub_f32 v90, v90, v188\n"
"v_sub_f32 v91, v91, v188\n"
"v_sub_f32 v132, 0, v190\n"
"v_sub_f32 v133, 0, v190\n"
"v_sub_f32 v134, 0, v190\n"
"v_sub_f32 v135, 0, v190\n"
"v_sub_f32 v68, v68, v190\n"
"v_sub_f32 v69, v69, v190\n"
"v_sub_f32 v70, v70, v190\n"
"v_sub_f32 v71, v71, v190\n"
"v_sub_f32 v76, v76, v190\n"
"v_sub_f32 v77, v77, v190\n"
"v_sub_f32 v78, v78, v190\n"
"v_sub_f32 v79, v79, v190\n"
"v_sub_f32 v84, v84, v190\n"
"v_sub_f32 v85, v85, v190\n"
"v_sub_f32 v86, v86, v190\n"
"v_sub_f32 v87, v87, v190\n"
"v_sub_f32 v92, v92, v190\n"
"v_sub_f32 v93, v93, v190\n"
"v_sub_f32 v94, v94, v190\n"
"v_sub_f32 v95, v95, v190\n"
"s_waitcnt lgkmcnt(0)\n"
"v_mfma_f32_16x16x32_bf16 v[96:99], a[64:67], v[32:35], v[128:131]\n"
"v_exp_f32 v64, v64\n"
"v_mfma_f32_16x16x32_bf16 v[104:107], a[68:71], v[32:35], v[128:131]\n"
"v_exp_f32 v65, v65\n"
"v_mfma_f32_16x16x32_bf16 v[112:115], a[72:75], v[32:35], v[128:131]\n"
"v_exp_f32 v66, v66\n"
"v_mfma_f32_16x16x32_bf16 v[120:123], a[76:79], v[32:35], v[128:131]\n"
"v_exp_f32 v67, v67\n"
"v_mfma_f32_16x16x32_bf16 v[100:103], a[64:67], v[48:51], v[132:135]\n"
"ds_read_b128 a[64:67], v170 offset:16384\n"
"v_cvt_pk_bf16_f32 v144, v64, v65\n"
"v_mfma_f32_16x16x32_bf16 v[108:111], a[68:71], v[48:51], v[132:135]\n"
"ds_read_b128 a[68:71], v170 offset:20480\n"
"v_cvt_pk_bf16_f32 v26, v210, v214\n"
"v_exp_f32 v72, v72\n"
"v_mfma_f32_16x16x32_bf16 v[116:119], a[72:75], v[48:51], v[132:135]\n"
"ds_read_b128 a[72:75], v170 offset:24576\n"
"v_mul_f32 v218, s58, v218\n"
"v_mfma_f32_16x16x32_bf16 v[124:127], a[76:79], v[48:51], v[132:135]\n"
"ds_read_b128 a[76:79], v170 offset:28672\n"
"v_mul_f32 v222, s58, v222\n"
"v_exp_f32 v73, v73\n"
"v_mfma_f32_16x16x32_bf16 v[96:99], a[80:83], v[36:39], v[96:99]\n"
"v_cvt_pk_bf16_f32 v145, v66, v67\n"
"v_cvt_pk_bf16_f32 v27, v218, v222\n"
"v_exp_f32 v74, v74\n"
"v_mfma_f32_16x16x32_bf16 v[104:107], a[84:87], v[36:39], v[104:107]\n"
"v_exp_f32 v75, v75\n"
"v_mfma_f32_16x16x32_bf16 v[112:115], a[88:91], v[36:39], v[112:115]\n"
"v_cvt_pk_bf16_f32 v146, v72, v73\n"
"v_mul_f32 v195, s58, v195\n"
"v_mfma_f32_16x16x32_bf16 v[120:123], a[92:95], v[36:39], v[120:123]\n"
"v_exp_f32 v68, v68\n"
"v_mfma_f32_16x16x32_bf16 v[100:103], a[80:83], v[52:55], v[100:103]\n"
"ds_read_b128 a[80:83], v171 offset:16384\n"
"v_mul_f32 v199, s58, v199\n"
"v_mfma_f32_16x16x32_bf16 v[108:111], a[84:87], v[52:55], v[108:111]\n"
"ds_read_b128 a[84:87], v171 offset:20480\n"
"v_cvt_pk_bf16_f32 v28, v195, v199\n"
"v_exp_f32 v69, v69\n"
"v_mfma_f32_16x16x32_bf16 v[116:119], a[88:91], v[52:55], v[116:119]\n"
"ds_read_b128 a[88:91], v171 offset:24576\n"
"v_cvt_pk_bf16_f32 v147, v74, v75\n"
"v_exp_f32 v70, v70\n"
"v_mfma_f32_16x16x32_bf16 v[124:127], a[92:95], v[52:55], v[124:127]\n"
"s_waitcnt lgkmcnt(3)\n"
"ds_read_b128 a[92:95], v171 offset:28672\n"
"v_mul_f32 v203, s58, v203\n"
"v_mfma_f32_16x16x32_bf16 v[96:99], a[64:67], v[40:43], v[96:99]\n"
"ds_read_b64_tr_b16 a[96:97], v172 offset:32768\n"
"v_add_u32 v168, 0x10000, v168\n"
"v_exp_f32 v71, v71\n"
"v_mfma_f32_16x16x32_bf16 v[104:107], a[68:71], v[40:43], v[104:107]\n"
"ds_read_b64_tr_b16 a[98:99], v172 offset:36864\n"
"v_add_u32 v169, 0x10000, v169\n"
"v_cvt_pk_bf16_f32 v148, v68, v69\n"
"v_mfma_f32_16x16x32_bf16 v[112:115], a[72:75], v[40:43], v[112:115]\n"
"ds_read_b64_tr_b16 a[100:101], v173 offset:32768\n"
"v_add_u32 v170, 0x10000, v170\n"
"v_exp_f32 v76, v76\n"
"v_mfma_f32_16x16x32_bf16 v[120:123], a[76:79], v[40:43], v[120:123]\n"
"ds_read_b64_tr_b16 a[102:103], v173 offset:36864\n"
"v_add_u32 v171, 0x10000, v171\n"
"v_exp_f32 v77, v77\n"
"v_mfma_f32_16x16x32_bf16 v[100:103], a[64:67], v[56:59], v[100:103]\n"
"ds_read_b64_tr_b16 a[104:105], v172 offset:33280\n"
"v_cvt_pk_bf16_f32 v149, v70, v71\n"
"v_exp_f32 v78, v78\n"
"v_mfma_f32_16x16x32_bf16 v[108:111], a[68:71], v[56:59], v[108:111]\n"
"ds_read_b64_tr_b16 a[106:107], v172 offset:37376\n"
"v_mul_f32 v207, s58, v207\n"
"v_exp_f32 v79, v79\n"
"v_mfma_f32_16x16x32_bf16 v[116:119], a[72:75], v[56:59], v[116:119]\n"
"ds_read_b64_tr_b16 a[108:109], v173 offset:33280\n"
"v_cvt_pk_bf16_f32 v150, v76, v77\n"
"v_mfma_f32_16x16x32_bf16 v[124:127], a[76:79], v[56:59], v[124:127]\n"
"s_waitcnt lgkmcnt(3)\n"
"ds_read_b64_tr_b16 a[110:111], v173 offset:37376\n"
"v_cvt_pk_bf16_f32 v29, v203, v207\n"
"v_exp_f32 v80, v80\n"
"v_mfma_f32_16x16x32_bf16 v[96:99], a[80:83], v[44:47], v[96:99]\n"
"ds_read_b64_tr_b16 a[112:113], v172 offset:33792\n"
"v_mul_f32 v211, s58, v211\n"
"v_exp_f32 v81, v81\n"
"v_mfma_f32_16x16x32_bf16 v[104:107], a[84:87], v[44:47], v[104:107]\n"
"ds_read_b64_tr_b16 a[114:115], v172 offset:37888\n"
"v_cvt_pk_bf16_f32 v151, v78, v79\n"
"v_mfma_f32_16x16x32_bf16 v[112:115], a[88:91], v[44:47], v[112:115]\n"
"ds_read_b64_tr_b16 a[116:117], v173 offset:33792\n"
"v_mul_f32 v215, s58, v215\n"
"v_exp_f32 v82, v82\n"
"v_mfma_f32_16x16x32_bf16 v[120:123], a[92:95], v[44:47], v[120:123]\n"
"ds_read_b64_tr_b16 a[118:119], v173 offset:37888\n"
"v_cvt_pk_bf16_f32 v30, v211, v215\n"
"v_exp_f32 v83, v83\n"
"v_mfma_f32_16x16x32_bf16 v[100:103], a[80:83], v[60:63], v[100:103]\n"
"ds_read_b64_tr_b16 a[120:121], v172 offset:34304\n"
"v_cvt_pk_bf16_f32 v152, v80, v81\n"
"v_mfma_f32_16x16x32_bf16 v[108:111], a[84:87], v[60:63], v[108:111]\n"
"ds_read_b64_tr_b16 a[122:123], v172 offset:38400\n"
"v_mul_f32 v219, s58, v219\n"
"v_exp_f32 v88, v88\n"
"v_mfma_f32_16x16x32_bf16 v[116:119], a[88:91], v[60:63], v[116:119]\n"
"ds_read_b64_tr_b16 a[124:125], v173 offset:34304\n"
"v_mul_f32 v223, s58, v223\n"
"v_exp_f32 v89, v89\n"
"v_mfma_f32_16x16x32_bf16 v[124:127], a[92:95], v[60:63], v[124:127]\n"
"ds_read_b64_tr_b16 a[126:127], v173 offset:38400\n"
"v_cvt_pk_bf16_f32 v153, v82, v83\n"
"s_barrier\n"
".LBbar1:\n"
"v_mfma_f32_16x16x32_bf16 a[0:3], a[96:99], v[144:147], 0\n"
"ds_read_b128 a[64:67], v168 offset:0\n"
"v_and_b32 v184, 7, v0\n"
"v_exp_f32 v90, v90\n"
"v_mfma_f32_16x16x32_bf16 a[8:11], a[100:103], v[144:147], 0\n"
"s_waitcnt lgkmcnt(3)\n"
"ds_read_b128 a[68:71], v168 offset:4096\n"
"s_lshl_b32 s80, s53, 2\n"
"v_mfma_f32_16x16x32_bf16 a[16:19], a[104:107], v[144:147], 0\n"
"ds_read_b128 a[72:75], v168 offset:8192\n"
"s_add_u32 s80, s80, s48\n"
"v_exp_f32 v91, v91\n"
"v_mfma_f32_16x16x32_bf16 a[24:27], a[108:111], v[144:147], 0\n"
"ds_read_b128 a[76:79], v168 offset:12288\n"
"v_cvt_pk_bf16_f32 v154, v88, v89\n"
"v_mfma_f32_16x16x32_bf16 a[4:7], a[96:99], v[148:151], 0\n"
"ds_read_b64_tr_b16 a[96:97], v172 offset:40960\n"
"s_lshl_b32 s80, s80, 7\n"
"v_exp_f32 v84, v84\n"
"v_mfma_f32_16x16x32_bf16 a[12:15], a[100:103], v[148:151], 0\n"
"ds_read_b64_tr_b16 a[98:99], v172 offset:45056\n"
"s_add_u32 s80, s80, 524288\n"
"v_mfma_f32_16x16x32_bf16 a[20:23], a[104:107], v[148:151], 0\n"
"ds_read_b64_tr_b16 a[100:101], v173 offset:40960\n"
"v_lshl_add_u32 v185, v184, 2, s80\n"
"v_exp_f32 v85, v85\n"
"v_mfma_f32_16x16x32_bf16 a[28:31], a[108:111], v[148:151], 0\n"
"ds_read_b64_tr_b16 a[102:103], v173 offset:45056\n"
"buffer_load_dword v10, v185, s[40:43], 0 offen offset:0 sc1\n"
"v_cvt_pk_bf16_f32 v155, v90, v91\n"
"v_mfma_f32_16x16x32_bf16 v[160:163], v[176:179], v[144:147], 0\n"
"ds_read_b64_tr_b16 a[104:105], v172 offset:41472\n"
"s_lshl_b32 s80, s53, 2\n"
"v_exp_f32 v86, v86\n"
"v_mfma_f32_16x16x32_bf16 a[32:35], a[112:115], v[144:147], 0\n"
"ds_read_b64_tr_b16 a[106:107], v172 offset:45568\n"
"s_add_u32 s80, s80, s48\n"
"v_mfma_f32_16x16x32_bf16 a[40:43], a[116:119], v[144:147], 0\n"
"ds_read_b64_tr_b16 a[108:109], v173 offset:41472\n"
"s_lshl_b32 s80, s80, 7\n"
"v_exp_f32 v87, v87\n"
"v_mfma_f32_16x16x32_bf16 a[48:51], a[120:123], v[144:147], 0\n"
"s_waitcnt lgkmcnt(4)\n"
"ds_read_b64_tr_b16 a[110:111], v173 offset:45568\n"
"v_cvt_pk_bf16_f32 v156, v84, v85\n"
"v_mfma_f32_16x16x32_bf16 a[56:59], a[124:127], v[144:147], 0\n"
"v_add_u32 v191, 16384, v174\n"
"s_add_u32 s80, s80, 524352\n"
"v_exp_f32 v92, v92\n"
"v_mfma_f32_16x16x32_bf16 a[36:39], a[112:115], v[148:151], 0\n"
"ds_write_b128 v191, a[128:131] offset:0\n"
"v_exp_f32 v93, v93\n"
"v_mfma_f32_16x16x32_bf16 a[44:47], a[116:119], v[148:151], 0\n"
"ds_read_b64_tr_b16 a[112:113], v172 offset:41984\n"
"v_cvt_pk_bf16_f32 v157, v86, v87\n"
"v_mfma_f32_16x16x32_bf16 a[52:55], a[120:123], v[148:151], 0\n"
"ds_read_b64_tr_b16 a[114:115], v172 offset:46080\n"
"v_max3_f32 v188, v96, v97, v98\n"
"v_mfma_f32_16x16x32_bf16 a[60:63], a[124:127], v[148:151], 0\n"
"ds_write_b128 v191, a[132:135] offset:1024\n"
"v_exp_f32 v94, v94\n"
"v_mfma_f32_16x16x32_bf16 v[164:167], v[176:179], v[148:151], 0\n"
"ds_read_b64_tr_b16 a[116:117], v173 offset:41984\n"
"v_max3_f32 v189, v100, v101, v102\n"
"v_exp_f32 v95, v95\n"
"v_mfma_f32_16x16x32_bf16 a[0:3], a[96:99], v[152:155], a[0:3]\n"
"s_waitcnt lgkmcnt(4)\n"
"ds_read_b64_tr_b16 a[118:119], v173 offset:46080\n"
"v_cvt_pk_bf16_f32 v158, v92, v93\n"
"v_mfma_f32_16x16x32_bf16 a[8:11], a[100:103], v[152:155], a[8:11]\n"
"ds_read_b64_tr_b16 a[120:121], v172 offset:42496\n"
"v_cvt_pk_bf16_f32 v159, v94, v95\n"
"ds_read_b64_tr_b16 a[122:123], v172 offset:46592\n"
"v_mfma_f32_16x16x32_bf16 a[16:19], a[104:107], v[152:155], a[16:19]\n"
"ds_write_b128 v191, a[136:139] offset:2048\n"
"ds_read_b64_tr_b16 a[124:125], v173 offset:42496\n"
"ds_read_b64_tr_b16 a[126:127], v173 offset:46592\n"
"v_mfma_f32_16x16x32_bf16 a[24:27], a[108:111], v[152:155], a[24:27]\n"
"v_max3_f32 v188, v188, v99, v104\n"
"v_max3_f32 v189, v189, v103, v108\n"
"v_mfma_f32_16x16x32_bf16 a[4:7], a[96:99], v[156:159], a[4:7]\n"
"ds_read_b128 a[80:83], v169 offset:0\n"
"v_max3_f32 v188, v188, v105, v106\n"
"v_mfma_f32_16x16x32_bf16 a[12:15], a[100:103], v[156:159], a[12:15]\n"
"ds_read_b128 a[84:87], v169 offset:4096\n"
"v_max3_f32 v189, v189, v109, v110\n"
"v_mfma_f32_16x16x32_bf16 a[20:23], a[104:107], v[156:159], a[20:23]\n"
"ds_read_b128 a[88:91], v169 offset:8192\n"
"v_max3_f32 v188, v188, v107, v112\n"
"v_mfma_f32_16x16x32_bf16 a[28:31], a[108:111], v[156:159], a[28:31]\n"
"ds_read_b128 a[92:95], v169 offset:12288\n"
"v_max3_f32 v189, v189, v111, v116\n"
"v_mfma_f32_16x16x32_bf16 v[160:163], v[176:179], v[152:155], v[160:163]\n"
"s_waitcnt lgkmcnt(5)\n"
"ds_write_b128 v191, a[140:143] offset:3072\n"
"v_mfma_f32_16x16x32_bf16 a[32:35], a[112:115], v[152:155], a[32:35]\n"
"ds_read_b64_tr_b16 a[96:97], v172 offset:49152\n"
"v_max3_f32 v188, v188, v113, v114\n"
"v_mfma_f32_16x16x32_bf16 a[40:43], a[116:119], v[152:155], a[40:43]\n"
"ds_read_b64_tr_b16 a[98:99], v172 offset:53248\n"
"v_max3_f32 v189, v189, v117, v118\n"
"v_mfma_f32_16x16x32_bf16 a[48:51], a[120:123], v[152:155], a[48:51]\n"
"s_waitcnt lgkmcnt(5)\n"
"ds_read_b64_tr_b16 a[100:101], v173 offset:49152\n"
"v_max3_f32 v188, v188, v115, v120\n"
"v_mfma_f32_16x16x32_bf16 a[56:59], a[124:127], v[152:155], a[56:59]\n"
"ds_read_b64_tr_b16 a[102:103], v173 offset:53248\n"
"v_max3_f32 v189, v189, v119, v124\n"
"v_mfma_f32_16x16x32_bf16 a[36:39], a[112:115], v[156:159], a[36:39]\n"
"ds_read_b64_tr_b16 a[104:105], v172 offset:49664\n"
"v_max3_f32 v188, v188, v121, v122\n"
"v_mfma_f32_16x16x32_bf16 a[44:47], a[116:119], v[156:159], a[44:47]\n"
"ds_read_b64_tr_b16 a[106:107], v172 offset:53760\n"
"v_max3_f32 v189, v189, v125, v126\n"
"v_mfma_f32_16x16x32_bf16 a[52:55], a[120:123], v[156:159], a[52:55]\n"
"ds_read_b64_tr_b16 a[108:109], v173 offset:49664\n"
"v_max3_f32 v190, v188, v123, v127\n"
"v_mfma_f32_16x16x32_bf16 a[60:63], a[124:127], v[156:159], a[60:63]\n"
"ds_read_b64_tr_b16 a[110:111], v173 offset:53760\n"
"v_max_f32 v190, v190, v189\n"
"v_mfma_f32_16x16x32_bf16 v[164:167], v[176:179], v[156:159], v[164:167]\n"
"v_cmp_lt_f32 vcc, s59, v190\n"
"v_lshl_add_u32 v186, v184, 3, s80\n"
"buffer_load_dword v11, v186, s[40:43], 0 offen offset:4 sc1\n"
"s_cbranch_vccz .LBskip1\n"
"s_swappc_b64 s[70:71], s[74:75]\n"
".LBskip1:\n"
"v_mfma_f32_16x16x32_bf16 v[64:67], a[64:67], v[32:35], v[128:131]\n"
"v_exp_f32 v96, v96\n"
"v_mfma_f32_16x16x32_bf16 v[72:75], a[68:71], v[32:35], v[128:131]\n"
"v_exp_f32 v97, v97\n"
"v_mfma_f32_16x16x32_bf16 v[80:83], a[72:75], v[32:35], v[128:131]\n"
"v_exp_f32 v98, v98\n"
"v_mfma_f32_16x16x32_bf16 v[88:91], a[76:79], v[32:35], v[128:131]\n"
"v_exp_f32 v99, v99\n"
"v_mfma_f32_16x16x32_bf16 v[68:71], a[64:67], v[48:51], v[132:135]\n"
"ds_read_b128 a[64:67], v170 offset:0\n"
"v_cvt_pk_bf16_f32 v144, v96, v97\n"
"v_mfma_f32_16x16x32_bf16 v[76:79], a[68:71], v[48:51], v[132:135]\n"
"ds_read_b128 a[68:71], v170 offset:4096\n"
"v_cvt_pk_bf16_f32 v31, v219, v223\n"
"v_exp_f32 v104, v104\n"
"v_mfma_f32_16x16x32_bf16 v[84:87], a[72:75], v[48:51], v[132:135]\n"
"ds_read_b128 a[72:75], v170 offset:8192\n"
"v_mov_b32 v192, v16\n"
"v_mfma_f32_16x16x32_bf16 v[92:95], a[76:79], v[48:51], v[132:135]\n"
"ds_read_b128 a[76:79], v170 offset:12288\n"
"v_mov_b32 v193, v17\n"
"v_exp_f32 v105, v105\n"
"v_mfma_f32_16x16x32_bf16 v[64:67], a[80:83], v[36:39], v[64:67]\n"
"v_cvt_pk_bf16_f32 v145, v98, v99\n"
"v_mov_b32 v194, v18\n"
"v_exp_f32 v106, v106\n"
"v_mfma_f32_16x16x32_bf16 v[72:75], a[84:87], v[36:39], v[72:75]\n"
"s_waitcnt lgkmcnt(3)\n"
"v_exp_f32 v107, v107\n"
"v_mfma_f32_16x16x32_bf16 v[80:83], a[88:91], v[36:39], v[80:83]\n"
"v_cvt_pk_bf16_f32 v146, v104, v105\n"
"v_mov_b32 v195, v19\n"
"v_mfma_f32_16x16x32_bf16 v[88:91], a[92:95], v[36:39], v[88:91]\n"
"v_exp_f32 v100, v100\n"
"v_mfma_f32_16x16x32_bf16 v[68:71], a[80:83], v[52:55], v[68:71]\n"
"ds_read_b128 a[80:83], v171 offset:0\n"
"v_mov_b32 v196, v20\n"
"v_mfma_f32_16x16x32_bf16 v[76:79], a[84:87], v[52:55], v[76:79]\n"
"ds_read_b128 a[84:87], v171 offset:4096\n"
"v_mov_b32 v197, v21\n"
"v_exp_f32 v101, v101\n"
"v_mfma_f32_16x16x32_bf16 v[84:87], a[88:91], v[52:55], v[84:87]\n"
"ds_read_b128 a[88:91], v171 offset:8192\n"
"v_cvt_pk_bf16_f32 v147, v106, v107\n"
"v_exp_f32 v102, v102\n"
"v_mfma_f32_16x16x32_bf16 v[92:95], a[92:95], v[52:55], v[92:95]\n"
"ds_read_b128 a[92:95], v171 offset:12288\n"
"v_mov_b32 v198, v22\n"
"v_mfma_f32_16x16x32_bf16 v[64:67], a[64:67], v[40:43], v[64:67]\n"
"s_waitcnt lgkmcnt(4)\n"
"ds_read_b64_tr_b16 a[112:113], v172 offset:50176\n"
"v_mov_b32 v199, v23\n"
"v_exp_f32 v103, v103\n"
"v_mfma_f32_16x16x32_bf16 v[72:75], a[68:71], v[40:43], v[72:75]\n"
"ds_read_b64_tr_b16 a[114:115], v172 offset:54272\n"
"v_cvt_pk_bf16_f32 v148, v100, v101\n"
"v_exp_f32 v108, v108\n"
"v_mfma_f32_16x16x32_bf16 v[80:83], a[72:75], v[40:43], v[80:83]\n"
"ds_read_b64_tr_b16 a[116:117], v173 offset:50176\n"
"v_mov_b32 v200, v24\n"
"v_exp_f32 v109, v109\n"
"v_mfma_f32_16x16x32_bf16 v[88:91], a[76:79], v[40:43], v[88:91]\n"
"ds_read_b64_tr_b16 a[118:119], v173 offset:54272\n"
"v_cvt_pk_bf16_f32 v149, v102, v103\n"
"v_mfma_f32_16x16x32_bf16 v[68:71], a[64:67], v[56:59], v[68:71]\n"
"ds_read_b64_tr_b16 a[120:121], v172 offset:50688\n"
"v_mov_b32 v201, v25\n"
"v_exp_f32 v110, v110\n"
"v_mfma_f32_16x16x32_bf16 v[76:79], a[68:71], v[56:59], v[76:79]\n"
"ds_read_b64_tr_b16 a[122:123], v172 offset:54784\n"
"v_mov_b32 v202, v26\n"
"v_exp_f32 v111, v111\n"
"v_mfma_f32_16x16x32_bf16 v[84:87], a[72:75], v[56:59], v[84:87]\n"
"ds_read_b64_tr_b16 a[124:125], v173 offset:50688\n"
"v_cvt_pk_bf16_f32 v150, v108, v109\n"
"v_mfma_f32_16x16x32_bf16 v[92:95], a[76:79], v[56:59], v[92:95]\n"
"s_waitcnt lgkmcnt(4)\n"
"ds_read_b64_tr_b16 a[126:127], v173 offset:54784\n"
"v_mov_b32 v203, v27\n"
"v_exp_f32 v112, v112\n"
"v_mfma_f32_16x16x32_bf16 v[64:67], a[80:83], v[44:47], v[64:67]\n"
"v_exp_f32 v113, v113\n"
"v_mfma_f32_16x16x32_bf16 v[72:75], a[84:87], v[44:47], v[72:75]\n"
"v_cvt_pk_bf16_f32 v151, v110, v111\n"
"v_mov_b32 v204, v28\n"
"v_mfma_f32_16x16x32_bf16 v[80:83], a[88:91], v[44:47], v[80:83]\n"
"v_exp_f32 v114, v114\n"
"v_mfma_f32_16x16x32_bf16 v[88:91], a[92:95], v[44:47], v[88:91]\n"
"v_exp_f32 v115, v115\n"
"v_mfma_f32_16x16x32_bf16 v[68:71], a[80:83], v[60:63], v[68:71]\n"
"v_cvt_pk_bf16_f32 v152, v112, v113\n"
"v_mov_b32 v205, v29\n"
"v_mfma_f32_16x16x32_bf16 v[76:79], a[84:87], v[60:63], v[76:79]\n"
"v_exp_f32 v120, v120\n"
"v_mfma_f32_16x16x32_bf16 v[84:87], a[88:91], v[60:63], v[84:87]\n"
"v_exp_f32 v121, v121\n"
"v_mfma_f32_16x16x32_bf16 v[92:95], a[92:95], v[60:63], v[92:95]\n"
"v_cvt_pk_bf16_f32 v153, v114, v115\n"
"v_mov_b32 v206, v30\n"
"s_barrier\n"
".LBbar2:\n"
"v_mfma_f32_16x16x32_bf16 a[0:3], a[96:99], v[144:147], a[0:3]\n"
"ds_read_b128 a[64:67], v168 offset:16384\n"
"buffer_load_dwordx4 a[144:147], v2, s[8:11], s62 offen offset:0 nt\n"
"v_exp_f32 v122, v122\n"
"v_mfma_f32_16x16x32_bf16 a[8:11], a[100:103], v[144:147], a[8:11]\n"
"ds_read_b128 a[68:71], v168 offset:20480\n"
"v_mov_b32 v207, v31\n"
"v_mfma_f32_16x16x32_bf16 a[16:19], a[104:107], v[144:147], a[16:19]\n"
"ds_read_b128 a[72:75], v168 offset:24576\n"
"v_mul_f32 v224, s58, v224\n"
"v_exp_f32 v123, v123\n"
"v_mfma_f32_16x16x32_bf16 a[24:27], a[108:111], v[144:147], a[24:27]\n"
"ds_read_b128 a[76:79], v168 offset:28672\n"
"v_cvt_pk_bf16_f32 v154, v120, v121\n"
"v_mfma_f32_16x16x32_bf16 a[4:7], a[96:99], v[148:151], a[4:7]\n"
"ds_read_b64_tr_b16 a[96:97], v172 offset:57344\n"
"buffer_load_dwordx4 a[148:151], v3, s[8:11], s62 offen offset:1024 nt\n"
"v_exp_f32 v116, v116\n"
"v_mfma_f32_16x16x32_bf16 a[12:15], a[100:103], v[148:151], a[12:15]\n"
"ds_read_b64_tr_b16 a[98:99], v172 offset:61440\n"
"v_mul_f32 v228, s58, v228\n"
"v_mfma_f32_16x16x32_bf16 a[20:23], a[104:107], v[148:151], a[20:23]\n"
"ds_read_b64_tr_b16 a[100:101], v173 offset:57344\n"
"v_cvt_pk_bf16_f32 v208, v224, v228\n"
"v_exp_f32 v117, v117\n"
"v_mfma_f32_16x16x32_bf16 a[28:31], a[108:111], v[148:151], a[28:31]\n"
"ds_read_b64_tr_b16 a[102:103], v173 offset:61440\n"
"v_cvt_pk_bf16_f32 v155, v122, v123\n"
"v_mfma_f32_16x16x32_bf16 v[160:163], v[176:179], v[144:147], v[160:163]\n"
"ds_read_b64_tr_b16 a[104:105], v172 offset:57856\n"
"buffer_load_dwordx4 a[152:155], v4, s[8:11], s62 offen offset:2048 nt\n"
"v_exp_f32 v118, v118\n"
"v_mfma_f32_16x16x32_bf16 a[32:35], a[112:115], v[144:147], a[32:35]\n"
"s_waitcnt lgkmcnt(4)\n"
"ds_read_b64_tr_b16 a[106:107], v172 offset:61952\n"
"v_mul_f32 v232, s58, v232\n"
"v_mfma_f32_16x16x32_bf16 a[40:43], a[116:119], v[144:147], a[40:43]\n"
"ds_read_b64_tr_b16 a[108:109], v173 offset:57856\n"
"v_mul_f32 v236, s58, v236\n"
"v_exp_f32 v119, v119\n"
"v_mfma_f32_16x16x32_bf16 a[48:51], a[120:123], v[144:147], a[48:51]\n"
"ds_read_b64_tr_b16 a[110:111], v173 offset:61952\n"
"v_cvt_pk_bf16_f32 v156, v116, v117\n"
"v_mfma_f32_16x16x32_bf16 a[56:59], a[124:127], v[144:147], a[56:59]\n"
"buffer_load_dwordx4 a[156:159], v5, s[8:11], s62 offen offset:3072 nt\n"
"v_add_u32 v191, 32768, v174\n"
"v_exp_f32 v124, v124\n"
"v_mfma_f32_16x16x32_bf16 a[36:39], a[112:115], v[148:151], a[36:39]\n"
"ds_write_b128 v191, a[160:163] offset:0\n"
"v_exp_f32 v125, v125\n"
"v_mfma_f32_16x16x32_bf16 a[44:47], a[116:119], v[148:151], a[44:47]\n"
"ds_read_b64_tr_b16 a[112:113], v172 offset:58368\n"
"v_cvt_pk_bf16_f32 v157, v118, v119\n"
"v_mfma_f32_16x16x32_bf16 a[52:55], a[120:123], v[148:151], a[52:55]\n"
"ds_read_b64_tr_b16 a[114:115], v172 offset:62464\n"
"v_max3_f32 v188, v64, v65, v66\n"
"v_mfma_f32_16x16x32_bf16 a[60:63], a[124:127], v[148:151], a[60:63]\n"
"ds_write_b128 v191, a[164:167] offset:1024\n"
"v_exp_f32 v126, v126\n"
"v_mfma_f32_16x16x32_bf16 v[164:167], v[176:179], v[148:151], v[164:167]\n"
"s_waitcnt lgkmcnt(4)\n"
"ds_read_b64_tr_b16 a[116:117], v173 offset:58368\n"
"v_max3_f32 v189, v68, v69, v70\n"
"v_exp_f32 v127, v127\n"
"v_mfma_f32_16x16x32_bf16 a[0:3], a[96:99], v[152:155], a[0:3]\n"
"ds_read_b64_tr_b16 a[118:119], v173 offset:62464\n"
"v_cvt_pk_bf16_f32 v158, v124, v125\n"
"v_mfma_f32_16x16x32_bf16 a[8:11], a[100:103], v[152:155], a[8:11]\n"
"ds_read_b64_tr_b16 a[120:121], v172 offset:58880\n"
"v_cvt_pk_bf16_f32 v159, v126, v127\n"
"ds_read_b64_tr_b16 a[122:123], v172 offset:62976\n"
"v_mfma_f32_16x16x32_bf16 a[16:19], a[104:107], v[152:155], a[16:19]\n"
"ds_write_b128 v191, a[168:171] offset:2048\n"
"ds_read_b64_tr_b16 a[124:125], v173 offset:58880\n"
"ds_read_b64_tr_b16 a[126:127], v173 offset:62976\n"
"v_mfma_f32_16x16x32_bf16 a[24:27], a[108:111], v[152:155], a[24:27]\n"
"v_add_u32 v172, 0x10000, v172\n"
"v_add_u32 v173, 0x10000, v173\n"
"v_mfma_f32_16x16x32_bf16 a[4:7], a[96:99], v[156:159], a[4:7]\n"
"ds_read_b128 a[80:83], v169 offset:16384\n"
"v_max3_f32 v188, v188, v67, v72\n"
"v_mfma_f32_16x16x32_bf16 a[12:15], a[100:103], v[156:159], a[12:15]\n"
"ds_read_b128 a[84:87], v169 offset:20480\n"
"v_max3_f32 v189, v189, v71, v76\n"
"v_mfma_f32_16x16x32_bf16 a[20:23], a[104:107], v[156:159], a[20:23]\n"
"ds_read_b128 a[88:91], v169 offset:24576\n"
"v_max3_f32 v188, v188, v73, v74\n"
"v_mfma_f32_16x16x32_bf16 a[28:31], a[108:111], v[156:159], a[28:31]\n"
"ds_read_b128 a[92:95], v169 offset:28672\n"
"v_max3_f32 v189, v189, v77, v78\n"
"v_mfma_f32_16x16x32_bf16 v[160:163], v[176:179], v[152:155], v[160:163]\n"
"s_waitcnt lgkmcnt(5)\n"
"ds_write_b128 v191, a[172:175] offset:3072\n"
"v_mfma_f32_16x16x32_bf16 a[32:35], a[112:115], v[152:155], a[32:35]\n"
"ds_read_b64_tr_b16 a[96:97], v172 offset:32768\n"
"v_max3_f32 v188, v188, v75, v80\n"
"v_mfma_f32_16x16x32_bf16 a[40:43], a[116:119], v[152:155], a[40:43]\n"
"ds_read_b64_tr_b16 a[98:99], v172 offset:36864\n"
"v_max3_f32 v189, v189, v79, v84\n"
"v_mfma_f32_16x16x32_bf16 a[48:51], a[120:123], v[152:155], a[48:51]\n"
"s_waitcnt lgkmcnt(5)\n"
"ds_read_b64_tr_b16 a[100:101], v173 offset:32768\n"
"v_max3_f32 v188, v188, v81, v82\n"
"v_mfma_f32_16x16x32_bf16 a[56:59], a[124:127], v[152:155], a[56:59]\n"
"ds_read_b64_tr_b16 a[102:103], v173 offset:36864\n"
"v_max3_f32 v189, v189, v85, v86\n"
"v_mfma_f32_16x16x32_bf16 a[36:39], a[112:115], v[156:159], a[36:39]\n"
"ds_read_b64_tr_b16 a[104:105], v172 offset:33280\n"
"v_max3_f32 v188, v188, v83, v88\n"
"v_mfma_f32_16x16x32_bf16 a[44:47], a[116:119], v[156:159], a[44:47]\n"
"ds_read_b64_tr_b16 a[106:107], v172 offset:37376\n"
"v_max3_f32 v189, v189, v87, v92\n"
"v_mfma_f32_16x16x32_bf16 a[52:55], a[120:123], v[156:159], a[52:55]\n"
"ds_read_b64_tr_b16 a[108:109], v173 offset:33280\n"
"v_max3_f32 v188, v188, v89, v90\n"
"v_mfma_f32_16x16x32_bf16 a[60:63], a[124:127], v[156:159], a[60:63]\n"
"ds_read_b64_tr_b16 a[110:111], v173 offset:37376\n"
"v_max3_f32 v189, v189, v93, v94\n"
"v_mfma_f32_16x16x32_bf16 v[164:167], v[176:179], v[156:159], v[164:167]\n"
"v_max3_f32 v190, v188, v91, v95\n"
"v_max_f32 v190, v190, v189\n"
"v_cmp_lt_f32 vcc, s59, v190\n"
"s_nop 1\n"
"s_cbranch_vccz .LBskip2\n"
"s_swappc_b64 s[70:71], s[72:73]\n"
".LBskip2:\n"
"v_mfma_f32_16x16x32_bf16 v[96:99], a[64:67], v[32:35], v[128:131]\n"
"v_add_u32 v191, 65536, v174\n"
"v_cvt_pk_bf16_f32 v209, v232, v236\n"
"v_exp_f32 v64, v64\n"
"v_mfma_f32_16x16x32_bf16 v[104:107], a[68:71], v[32:35], v[128:131]\n"
"ds_write_b128 v191, a[176:179] offset:0\n"
"v_exp_f32 v65, v65\n"
"v_mfma_f32_16x16x32_bf16 v[112:115], a[72:75], v[32:35], v[128:131]\n"
"v_exp_f32 v66, v66\n"
"v_mfma_f32_16x16x32_bf16 v[120:123], a[76:79], v[32:35], v[128:131]\n"
"v_exp_f32 v67, v67\n"
"v_mfma_f32_16x16x32_bf16 v[100:103], a[64:67], v[48:51], v[132:135]\n"
"ds_write_b128 v191, a[180:183] offset:1024\n"
"v_mfma_f32_16x16x32_bf16 v[108:111], a[68:71], v[48:51], v[132:135]\n"
"ds_read_b128 a[64:67], v170 offset:16384\n"
"v_cvt_pk_bf16_f32 v144, v64, v65\n"
"v_exp_f32 v72, v72\n"
"v_mfma_f32_16x16x32_bf16 v[116:119], a[72:75], v[48:51], v[132:135]\n"
"ds_read_b128 a[68:71], v170 offset:20480\n"
"v_mul_f32 v240, s58, v240\n"
"v_mfma_f32_16x16x32_bf16 v[124:127], a[76:79], v[48:51], v[132:135]\n"
"ds_read_b128 a[72:75], v170 offset:24576\n"
"v_mul_f32 v244, s58, v244\n"
"v_exp_f32 v73, v73\n"
"v_mfma_f32_16x16x32_bf16 v[96:99], a[80:83], v[36:39], v[96:99]\n"
"ds_write_b128 v191, a[184:187] offset:2048\n"
"v_cvt_pk_bf16_f32 v145, v66, v67\n"
"v_mfma_f32_16x16x32_bf16 v[104:107], a[84:87], v[36:39], v[104:107]\n"
"s_waitcnt lgkmcnt(4)\n"
"ds_read_b128 a[76:79], v170 offset:28672\n"
"v_cvt_pk_bf16_f32 v210, v240, v244\n"
"v_exp_f32 v74, v74\n"
"v_mfma_f32_16x16x32_bf16 v[112:115], a[88:91], v[36:39], v[112:115]\n"
"v_exp_f32 v75, v75\n"
"v_mfma_f32_16x16x32_bf16 v[120:123], a[92:95], v[36:39], v[120:123]\n"
"v_cvt_pk_bf16_f32 v146, v72, v73\n"
"v_mul_f32 v248, s58, v248\n"
"v_exp_f32 v68, v68\n"
"v_mfma_f32_16x16x32_bf16 v[100:103], a[80:83], v[52:55], v[100:103]\n"
"ds_write_b128 v191, a[188:191] offset:3072\n"
"v_mfma_f32_16x16x32_bf16 v[108:111], a[84:87], v[52:55], v[108:111]\n"
"ds_read_b128 a[80:83], v171 offset:16384\n"
"v_mul_f32 v252, s58, v252\n"
"v_exp_f32 v69, v69\n"
"v_mfma_f32_16x16x32_bf16 v[116:119], a[88:91], v[52:55], v[116:119]\n"
"ds_read_b128 a[84:87], v171 offset:20480\n"
"v_cvt_pk_bf16_f32 v147, v74, v75\n"
"v_exp_f32 v70, v70\n"
"v_mfma_f32_16x16x32_bf16 v[124:127], a[92:95], v[52:55], v[124:127]\n"
"s_waitcnt lgkmcnt(3)\n"
"ds_read_b128 a[88:91], v171 offset:24576\n"
"v_cvt_pk_bf16_f32 v211, v248, v252\n"
"v_mfma_f32_16x16x32_bf16 v[96:99], a[64:67], v[40:43], v[96:99]\n"
"ds_read_b128 a[92:95], v171 offset:28672\n"
"v_mul_f32 v225, s58, v225\n"
"v_exp_f32 v71, v71\n"
"v_mfma_f32_16x16x32_bf16 v[104:107], a[68:71], v[40:43], v[104:107]\n"
"ds_read_b64_tr_b16 a[112:113], v172 offset:33792\n"
"v_cvt_pk_bf16_f32 v148, v68, v69\n"
"v_exp_f32 v76, v76\n"
"v_mfma_f32_16x16x32_bf16 v[112:115], a[72:75], v[40:43], v[112:115]\n"
"ds_read_b64_tr_b16 a[114:115], v172 offset:37888\n"
"v_mul_f32 v229, s58, v229\n"
"v_exp_f32 v77, v77\n"
"v_mfma_f32_16x16x32_bf16 v[120:123], a[76:79], v[40:43], v[120:123]\n"
"ds_read_b64_tr_b16 a[116:117], v173 offset:33792\n"
"v_cvt_pk_bf16_f32 v149, v70, v71\n"
"v_mfma_f32_16x16x32_bf16 v[100:103], a[64:67], v[56:59], v[100:103]\n"
"ds_read_b64_tr_b16 a[118:119], v173 offset:37888\n"
"v_cvt_pk_bf16_f32 v212, v225, v229\n"
"v_exp_f32 v78, v78\n"
"v_mfma_f32_16x16x32_bf16 v[108:111], a[68:71], v[56:59], v[108:111]\n"
"ds_read_b64_tr_b16 a[120:121], v172 offset:34304\n"
"v_mul_f32 v233, s58, v233\n"
"v_exp_f32 v79, v79\n"
"v_mfma_f32_16x16x32_bf16 v[116:119], a[72:75], v[56:59], v[116:119]\n"
"ds_read_b64_tr_b16 a[122:123], v172 offset:38400\n"
"v_cvt_pk_bf16_f32 v150, v76, v77\n"
"v_mfma_f32_16x16x32_bf16 v[124:127], a[76:79], v[56:59], v[124:127]\n"
"s_waitcnt lgkmcnt(4)\n"
"ds_read_b64_tr_b16 a[124:125], v173 offset:34304\n"
"v_mul_f32 v237, s58, v237\n"
"v_exp_f32 v80, v80\n"
"v_mfma_f32_16x16x32_bf16 v[96:99], a[80:83], v[44:47], v[96:99]\n"
"ds_read_b64_tr_b16 a[126:127], v173 offset:38400\n"
"v_cvt_pk_bf16_f32 v213, v233, v237\n"
"v_exp_f32 v81, v81\n"
"v_mfma_f32_16x16x32_bf16 v[104:107], a[84:87], v[44:47], v[104:107]\n"
"v_cvt_pk_bf16_f32 v151, v78, v79\n"
"v_mul_f32 v241, s58, v241\n"
"v_mfma_f32_16x16x32_bf16 v[112:115], a[88:91], v[44:47], v[112:115]\n"
"v_exp_f32 v82, v82\n"
"v_mfma_f32_16x16x32_bf16 v[120:123], a[92:95], v[44:47], v[120:123]\n"
"v_exp_f32 v83, v83\n"
"v_mfma_f32_16x16x32_bf16 v[100:103], a[80:83], v[60:63], v[100:103]\n"
"v_cvt_pk_bf16_f32 v152, v80, v81\n"
"v_mul_f32 v245, s58, v245\n"
"v_mfma_f32_16x16x32_bf16 v[108:111], a[84:87], v[60:63], v[108:111]\n"
"v_exp_f32 v88, v88\n"
"v_mfma_f32_16x16x32_bf16 v[116:119], a[88:91], v[60:63], v[116:119]\n"
"v_exp_f32 v89, v89\n"
"v_mfma_f32_16x16x32_bf16 v[124:127], a[92:95], v[60:63], v[124:127]\n"
"v_cvt_pk_bf16_f32 v153, v82, v83\n"
"v_cvt_pk_bf16_f32 v214, v241, v245\n"
"s_barrier\n"
".LBbar3:\n"
"v_mfma_f32_16x16x32_bf16 a[0:3], a[96:99], v[144:147], a[0:3]\n"
"ds_read_b128 a[64:67], v168 offset:49152\n"
"buffer_load_dwordx4 a[128:131], v2, s[4:7], s64 offen offset:0 nt\n"
"v_exp_f32 v90, v90\n"
"v_mfma_f32_16x16x32_bf16 a[8:11], a[100:103], v[144:147], a[8:11]\n"
"ds_read_b128 a[68:71], v168 offset:53248\n"
"v_mul_f32 v249, s58, v249\n"
"v_mfma_f32_16x16x32_bf16 a[16:19], a[104:107], v[144:147], a[16:19]\n"
"ds_read_b128 a[72:75], v168 offset:57344\n"
"v_mul_f32 v253, s58, v253\n"
"v_exp_f32 v91, v91\n"
"v_mfma_f32_16x16x32_bf16 a[24:27], a[108:111], v[144:147], a[24:27]\n"
"ds_read_b128 a[76:79], v168 offset:61440\n"
"v_cvt_pk_bf16_f32 v154, v88, v89\n"
"v_mfma_f32_16x16x32_bf16 a[4:7], a[96:99], v[148:151], a[4:7]\n"
"ds_read_b64_tr_b16 a[96:97], v172 offset:40960\n"
"buffer_load_dwordx4 a[132:135], v3, s[4:7], s64 offen offset:1024 nt\n"
"v_exp_f32 v84, v84\n"
"v_mfma_f32_16x16x32_bf16 a[12:15], a[100:103], v[148:151], a[12:15]\n"
"ds_read_b64_tr_b16 a[98:99], v172 offset:45056\n"
"v_cvt_pk_bf16_f32 v215, v249, v253\n"
"v_mfma_f32_16x16x32_bf16 a[20:23], a[104:107], v[148:151], a[20:23]\n"
"ds_read_b64_tr_b16 a[100:101], v173 offset:40960\n"
"v_mul_f32 v226, s58, v226\n"
"v_exp_f32 v85, v85\n"
"v_mfma_f32_16x16x32_bf16 a[28:31], a[108:111], v[148:151], a[28:31]\n"
"ds_read_b64_tr_b16 a[102:103], v173 offset:45056\n"
"v_cvt_pk_bf16_f32 v155, v90, v91\n"
"v_mfma_f32_16x16x32_bf16 v[160:163], v[176:179], v[144:147], v[160:163]\n"
"ds_read_b64_tr_b16 a[104:105], v172 offset:41472\n"
"buffer_load_dwordx4 a[136:139], v4, s[4:7], s64 offen offset:2048 nt\n"
"v_exp_f32 v86, v86\n"
"v_mfma_f32_16x16x32_bf16 a[32:35], a[112:115], v[144:147], a[32:35]\n"
"s_waitcnt lgkmcnt(4)\n"
"ds_read_b64_tr_b16 a[106:107], v172 offset:45568\n"
"v_mul_f32 v230, s58, v230\n"
"v_mfma_f32_16x16x32_bf16 a[40:43], a[116:119], v[144:147], a[40:43]\n"
"ds_read_b64_tr_b16 a[108:109], v173 offset:41472\n"
"v_cvt_pk_bf16_f32 v216, v226, v230\n"
"v_exp_f32 v87, v87\n"
"v_mfma_f32_16x16x32_bf16 a[48:51], a[120:123], v[144:147], a[48:51]\n"
"ds_read_b64_tr_b16 a[110:111], v173 offset:45568\n"
"v_cvt_pk_bf16_f32 v156, v84, v85\n"
"v_mfma_f32_16x16x32_bf16 a[56:59], a[124:127], v[144:147], a[56:59]\n"
"buffer_load_dwordx4 a[140:143], v5, s[4:7], s64 offen offset:3072 nt\n"
"v_add_u32 v191, 49152, v174\n"
"v_exp_f32 v92, v92\n"
"v_mfma_f32_16x16x32_bf16 a[36:39], a[112:115], v[148:151], a[36:39]\n"
"ds_write_b128 v191, a[192:195] offset:0\n"
"v_exp_f32 v93, v93\n"
"v_mfma_f32_16x16x32_bf16 a[44:47], a[116:119], v[148:151], a[44:47]\n"
"ds_read_b64_tr_b16 a[112:113], v172 offset:41984\n"
"v_cvt_pk_bf16_f32 v157, v86, v87\n"
"v_mfma_f32_16x16x32_bf16 a[52:55], a[120:123], v[148:151], a[52:55]\n"
"ds_read_b64_tr_b16 a[114:115], v172 offset:46080\n"
"v_max3_f32 v188, v96, v97, v98\n"
"v_mfma_f32_16x16x32_bf16 a[60:63], a[124:127], v[148:151], a[60:63]\n"
"ds_write_b128 v191, a[196:199] offset:1024\n"
"v_exp_f32 v94, v94\n"
"v_mfma_f32_16x16x32_bf16 v[164:167], v[176:179], v[148:151], v[164:167]\n"
"s_waitcnt lgkmcnt(4)\n"
"ds_read_b64_tr_b16 a[116:117], v173 offset:41984\n"
"v_max3_f32 v189, v100, v101, v102\n"
"v_exp_f32 v95, v95\n"
"v_mfma_f32_16x16x32_bf16 a[0:3], a[96:99], v[152:155], a[0:3]\n"
"ds_read_b64_tr_b16 a[118:119], v173 offset:46080\n"
"v_cvt_pk_bf16_f32 v158, v92, v93\n"
"v_mfma_f32_16x16x32_bf16 a[8:11], a[100:103], v[152:155], a[8:11]\n"
"ds_read_b64_tr_b16 a[120:121], v172 offset:42496\n"
"v_cvt_pk_bf16_f32 v159, v94, v95\n"
"ds_read_b64_tr_b16 a[122:123], v172 offset:46592\n"
"v_mfma_f32_16x16x32_bf16 a[16:19], a[104:107], v[152:155], a[16:19]\n"
"ds_write_b128 v191, a[200:203] offset:2048\n"
"ds_read_b64_tr_b16 a[124:125], v173 offset:42496\n"
"ds_read_b64_tr_b16 a[126:127], v173 offset:46592\n"
"v_mfma_f32_16x16x32_bf16 a[24:27], a[108:111], v[152:155], a[24:27]\n"
"v_add_u32 v172, 0xffff0000, v172\n"
"v_add_u32 v173, 0xffff0000, v173\n"
"v_mfma_f32_16x16x32_bf16 a[4:7], a[96:99], v[156:159], a[4:7]\n"
"ds_read_b128 a[80:83], v169 offset:49152\n"
"v_max3_f32 v188, v188, v99, v104\n"
"v_mfma_f32_16x16x32_bf16 a[12:15], a[100:103], v[156:159], a[12:15]\n"
"ds_read_b128 a[84:87], v169 offset:53248\n"
"v_max3_f32 v189, v189, v103, v108\n"
"v_mfma_f32_16x16x32_bf16 a[20:23], a[104:107], v[156:159], a[20:23]\n"
"ds_read_b128 a[88:91], v169 offset:57344\n"
"v_max3_f32 v188, v188, v105, v106\n"
"v_mfma_f32_16x16x32_bf16 a[28:31], a[108:111], v[156:159], a[28:31]\n"
"ds_read_b128 a[92:95], v169 offset:61440\n"
"v_max3_f32 v189, v189, v109, v110\n"
"v_mfma_f32_16x16x32_bf16 v[160:163], v[176:179], v[152:155], v[160:163]\n"
"s_waitcnt lgkmcnt(5)\n"
"ds_write_b128 v191, a[204:207] offset:3072\n"
"v_mfma_f32_16x16x32_bf16 a[32:35], a[112:115], v[152:155], a[32:35]\n"
"ds_read_b64_tr_b16 a[96:97], v172 offset:0\n"
"v_max3_f32 v188, v188, v107, v112\n"
"v_mfma_f32_16x16x32_bf16 a[40:43], a[116:119], v[152:155], a[40:43]\n"
"ds_read_b64_tr_b16 a[98:99], v172 offset:4096\n"
"v_max3_f32 v189, v189, v111, v116\n"
"v_mfma_f32_16x16x32_bf16 a[48:51], a[120:123], v[152:155], a[48:51]\n"
"s_waitcnt lgkmcnt(5)\n"
"ds_read_b64_tr_b16 a[100:101], v173 offset:0\n"
"v_max3_f32 v188, v188, v113, v114\n"
"v_mfma_f32_16x16x32_bf16 a[56:59], a[124:127], v[152:155], a[56:59]\n"
"ds_read_b64_tr_b16 a[102:103], v173 offset:4096\n"
"v_max3_f32 v189, v189, v117, v118\n"
"v_mfma_f32_16x16x32_bf16 a[36:39], a[112:115], v[156:159], a[36:39]\n"
"ds_read_b64_tr_b16 a[104:105], v172 offset:512\n"
"v_max3_f32 v188, v188, v115, v120\n"
"v_mfma_f32_16x16x32_bf16 a[44:47], a[116:119], v[156:159], a[44:47]\n"
"ds_read_b64_tr_b16 a[106:107], v172 offset:4608\n"
"v_max3_f32 v189, v189, v119, v124\n"
"v_mfma_f32_16x16x32_bf16 a[52:55], a[120:123], v[156:159], a[52:55]\n"
"ds_read_b64_tr_b16 a[108:109], v173 offset:512\n"
"v_max3_f32 v188, v188, v121, v122\n"
"v_mfma_f32_16x16x32_bf16 a[60:63], a[124:127], v[156:159], a[60:63]\n"
"ds_read_b64_tr_b16 a[110:111], v173 offset:4608\n"
"v_max3_f32 v189, v189, v125, v126\n"
"v_mfma_f32_16x16x32_bf16 v[164:167], v[176:179], v[156:159], v[164:167]\n"
"v_max3_f32 v190, v188, v123, v127\n"
"v_max_f32 v190, v190, v189\n"
"v_cmp_lt_f32 vcc, s59, v190\n"
"s_nop 1\n"
"s_cbranch_vccz .LBskip3\n"
"s_swappc_b64 s[70:71], s[74:75]\n"
".LBskip3:\n"
"v_mfma_f32_16x16x32_bf16 v[64:67], a[64:67], v[32:35], v[128:131]\n"
"v_add_u32 v191, 81920, v174\n"
"v_mul_f32 v234, s58, v234\n"
"v_exp_f32 v96, v96\n"
"v_mfma_f32_16x16x32_bf16 v[72:75], a[68:71], v[32:35], v[128:131]\n"
"ds_write_b128 v191, a[208:211] offset:0\n"
"v_exp_f32 v97, v97\n"
"v_mfma_f32_16x16x32_bf16 v[80:83], a[72:75], v[32:35], v[128:131]\n"
"v_exp_f32 v98, v98\n"
"v_mfma_f32_16x16x32_bf16 v[88:91], a[76:79], v[32:35], v[128:131]\n"
"v_exp_f32 v99, v99\n"
"v_mfma_f32_16x16x32_bf16 v[68:71], a[64:67], v[48:51], v[132:135]\n"
"ds_write_b128 v191, a[212:215] offset:1024\n"
"v_mfma_f32_16x16x32_bf16 v[76:79], a[68:71], v[48:51], v[132:135]\n"
"ds_read_b128 a[64:67], v170 offset:49152\n"
"v_cvt_pk_bf16_f32 v144, v96, v97\n"
"v_exp_f32 v104, v104\n"
"v_mfma_f32_16x16x32_bf16 v[84:87], a[72:75], v[48:51], v[132:135]\n"
"ds_read_b128 a[68:71], v170 offset:53248\n"
"v_mul_f32 v238, s58, v238\n"
"v_mfma_f32_16x16x32_bf16 v[92:95], a[76:79], v[48:51], v[132:135]\n"
"ds_read_b128 a[72:75], v170 offset:57344\n"
"v_cvt_pk_bf16_f32 v217, v234, v238\n"
"v_exp_f32 v105, v105\n"
"v_mfma_f32_16x16x32_bf16 v[64:67], a[80:83], v[36:39], v[64:67]\n"
"ds_write_b128 v191, a[216:219] offset:2048\n"
"v_cvt_pk_bf16_f32 v145, v98, v99\n"
"v_mfma_f32_16x16x32_bf16 v[72:75], a[84:87], v[36:39], v[72:75]\n"
"s_waitcnt lgkmcnt(4)\n"
"ds_read_b128 a[76:79], v170 offset:61440\n"
"v_mul_f32 v242, s58, v242\n"
"v_exp_f32 v106, v106\n"
"v_mfma_f32_16x16x32_bf16 v[80:83], a[88:91], v[36:39], v[80:83]\n"
"v_exp_f32 v107, v107\n"
"v_mfma_f32_16x16x32_bf16 v[88:91], a[92:95], v[36:39], v[88:91]\n"
"v_cvt_pk_bf16_f32 v146, v104, v105\n"
"v_mul_f32 v246, s58, v246\n"
"v_exp_f32 v100, v100\n"
"v_mfma_f32_16x16x32_bf16 v[68:71], a[80:83], v[52:55], v[68:71]\n"
"ds_write_b128 v191, a[220:223] offset:3072\n"
"v_mfma_f32_16x16x32_bf16 v[76:79], a[84:87], v[52:55], v[76:79]\n"
"ds_read_b128 a[80:83], v171 offset:49152\n"
"v_cvt_pk_bf16_f32 v218, v242, v246\n"
"v_exp_f32 v101, v101\n"
"v_mfma_f32_16x16x32_bf16 v[84:87], a[88:91], v[52:55], v[84:87]\n"
"ds_read_b128 a[84:87], v171 offset:53248\n"
"v_cvt_pk_bf16_f32 v147, v106, v107\n"
"v_exp_f32 v102, v102\n"
"v_mfma_f32_16x16x32_bf16 v[92:95], a[92:95], v[52:55], v[92:95]\n"
"s_waitcnt lgkmcnt(3)\n"
"ds_read_b128 a[88:91], v171 offset:57344\n"
"v_mul_f32 v250, s58, v250\n"
"v_mfma_f32_16x16x32_bf16 v[64:67], a[64:67], v[40:43], v[64:67]\n"
"ds_read_b128 a[92:95], v171 offset:61440\n"
"v_add_u32 v168, 0xffff0000, v168\n"
"v_exp_f32 v103, v103\n"
"v_mfma_f32_16x16x32_bf16 v[72:75], a[68:71], v[40:43], v[72:75]\n"
"ds_read_b64_tr_b16 a[112:113], v172 offset:1024\n"
"v_add_u32 v169, 0xffff0000, v169\n"
"v_cvt_pk_bf16_f32 v148, v100, v101\n"
"v_mfma_f32_16x16x32_bf16 v[80:83], a[72:75], v[40:43], v[80:83]\n"
"ds_read_b64_tr_b16 a[114:115], v172 offset:5120\n"
"v_add_u32 v170, 0xffff0000, v170\n"
"v_exp_f32 v108, v108\n"
"v_mfma_f32_16x16x32_bf16 v[88:91], a[76:79], v[40:43], v[88:91]\n"
"ds_read_b64_tr_b16 a[116:117], v173 offset:1024\n"
"v_add_u32 v171, 0xffff0000, v171\n"
"v_exp_f32 v109, v109\n"
"v_mfma_f32_16x16x32_bf16 v[68:71], a[64:67], v[56:59], v[68:71]\n"
"ds_read_b64_tr_b16 a[118:119], v173 offset:5120\n"
"v_cvt_pk_bf16_f32 v149, v102, v103\n"
"v_exp_f32 v110, v110\n"
"v_mfma_f32_16x16x32_bf16 v[76:79], a[68:71], v[56:59], v[76:79]\n"
"ds_read_b64_tr_b16 a[120:121], v172 offset:1536\n"
"v_mul_f32 v254, s58, v254\n"
"v_exp_f32 v111, v111\n"
"v_mfma_f32_16x16x32_bf16 v[84:87], a[72:75], v[56:59], v[84:87]\n"
"ds_read_b64_tr_b16 a[122:123], v172 offset:5632\n"
"v_cvt_pk_bf16_f32 v150, v108, v109\n"
"v_mfma_f32_16x16x32_bf16 v[92:95], a[76:79], v[56:59], v[92:95]\n"
"s_waitcnt lgkmcnt(3)\n"
"ds_read_b64_tr_b16 a[124:125], v173 offset:1536\n"
"v_cvt_pk_bf16_f32 v219, v250, v254\n"
"v_exp_f32 v112, v112\n"
"v_mfma_f32_16x16x32_bf16 v[64:67], a[80:83], v[44:47], v[64:67]\n"
"ds_read_b64_tr_b16 a[126:127], v173 offset:5632\n"
"v_mul_f32 v227, s58, v227\n"
"v_exp_f32 v113, v113\n"
"v_mfma_f32_16x16x32_bf16 v[72:75], a[84:87], v[44:47], v[72:75]\n"
"v_cvt_pk_bf16_f32 v151, v110, v111\n"
"v_mul_f32 v231, s58, v231\n"
"v_mfma_f32_16x16x32_bf16 v[80:83], a[88:91], v[44:47], v[80:83]\n"
"v_exp_f32 v114, v114\n"
"v_mfma_f32_16x16x32_bf16 v[88:91], a[92:95], v[44:47], v[88:91]\n"
"v_exp_f32 v115, v115\n"
"v_mfma_f32_16x16x32_bf16 v[68:71], a[80:83], v[60:63], v[68:71]\n"
"v_cvt_pk_bf16_f32 v152, v112, v113\n"
"v_cvt_pk_bf16_f32 v220, v227, v231\n"
"v_mfma_f32_16x16x32_bf16 v[76:79], a[84:87], v[60:63], v[76:79]\n"
"v_exp_f32 v120, v120\n"
"v_mfma_f32_16x16x32_bf16 v[84:87], a[88:91], v[60:63], v[84:87]\n"
"v_exp_f32 v121, v121\n"
"v_mfma_f32_16x16x32_bf16 v[92:95], a[92:95], v[60:63], v[92:95]\n"
"v_cvt_pk_bf16_f32 v153, v114, v115\n"
"v_mul_f32 v235, s58, v235\n"
"s_barrier\n"
".LBbar4:\n"
"v_mfma_f32_16x16x32_bf16 a[0:3], a[96:99], v[144:147], a[0:3]\n"
"ds_read_b128 a[64:67], v168 offset:16384\n"
"v_and_b32 v184, 7, v0\n"
"v_exp_f32 v122, v122\n"
"v_mfma_f32_16x16x32_bf16 a[8:11], a[100:103], v[144:147], a[8:11]\n"
"ds_read_b128 a[68:71], v168 offset:20480\n"
"s_lshr_b32 s80, s53, 1\n"
"v_mfma_f32_16x16x32_bf16 a[16:19], a[104:107], v[144:147], a[16:19]\n"
"ds_read_b128 a[72:75], v168 offset:24576\n"
"s_lshl_b32 s80, s80, 7\n"
"v_exp_f32 v123, v123\n"
"v_mfma_f32_16x16x32_bf16 a[24:27], a[108:111], v[144:147], a[24:27]\n"
"ds_read_b128 a[76:79], v168 offset:28672\n"
"v_cvt_pk_bf16_f32 v154, v120, v121\n"
"v_mfma_f32_16x16x32_bf16 a[4:7], a[96:99], v[148:151], a[4:7]\n"
"ds_read_b64_tr_b16 a[96:97], v172 offset:8192\n"
"s_and_b32 s81, s53, 1\n"
"v_exp_f32 v116, v116\n"
"v_mfma_f32_16x16x32_bf16 a[12:15], a[100:103], v[148:151], a[12:15]\n"
"ds_read_b64_tr_b16 a[98:99], v172 offset:12288\n"
"s_lshl_b32 s81, s81, 2\n"
"v_mfma_f32_16x16x32_bf16 a[20:23], a[104:107], v[148:151], a[20:23]\n"
"ds_read_b64_tr_b16 a[100:101], v173 offset:8192\n"
"s_add_u32 s81, s81, s48\n"
"v_exp_f32 v117, v117\n"
"v_mfma_f32_16x16x32_bf16 a[28:31], a[108:111], v[148:151], a[28:31]\n"
"ds_read_b64_tr_b16 a[102:103], v173 offset:12288\n"
"v_cvt_pk_bf16_f32 v155, v122, v123\n"
"v_mfma_f32_16x16x32_bf16 v[160:163], v[176:179], v[144:147], v[160:163]\n"
"ds_read_b64_tr_b16 a[104:105], v172 offset:8704\n"
"s_lshl_b32 s81, s81, 3\n"
"v_exp_f32 v118, v118\n"
"v_mfma_f32_16x16x32_bf16 a[32:35], a[112:115], v[144:147], a[32:35]\n"
"s_waitcnt lgkmcnt(4)\n"
"ds_read_b64_tr_b16 a[106:107], v172 offset:12800\n"
"s_add_u32 s80, s80, s81\n"
"v_mfma_f32_16x16x32_bf16 a[40:43], a[116:119], v[144:147], a[40:43]\n"
"ds_read_b64_tr_b16 a[108:109], v173 offset:8704\n"
"s_add_u32 s80, s80, 524352\n"
"v_exp_f32 v119, v119\n"
"v_mfma_f32_16x16x32_bf16 a[48:51], a[120:123], v[144:147], a[48:51]\n"
"ds_read_b64_tr_b16 a[110:111], v173 offset:12800\n"
"v_cvt_pk_bf16_f32 v156, v116, v117\n"
"v_mfma_f32_16x16x32_bf16 a[56:59], a[124:127], v[144:147], a[56:59]\n"
"v_add_u32 v191, 98304, v174\n"
"v_lshl_add_u32 v185, v184, 9, s80\n"
"v_exp_f32 v124, v124\n"
"v_mfma_f32_16x16x32_bf16 a[36:39], a[112:115], v[148:151], a[36:39]\n"
"ds_write_b128 v191, a[224:227] offset:0\n"
"v_exp_f32 v125, v125\n"
"v_mfma_f32_16x16x32_bf16 a[44:47], a[116:119], v[148:151], a[44:47]\n"
"ds_read_b64_tr_b16 a[112:113], v172 offset:9216\n"
"v_cvt_pk_bf16_f32 v157, v118, v119\n"
"v_mfma_f32_16x16x32_bf16 a[52:55], a[120:123], v[148:151], a[52:55]\n"
"ds_read_b64_tr_b16 a[114:115], v172 offset:13312\n"
"v_max3_f32 v188, v64, v65, v66\n"
"v_mfma_f32_16x16x32_bf16 a[60:63], a[124:127], v[148:151], a[60:63]\n"
"ds_write_b128 v191, a[228:231] offset:1024\n"
"v_exp_f32 v126, v126\n"
"v_mfma_f32_16x16x32_bf16 v[164:167], v[176:179], v[148:151], v[164:167]\n"
"s_waitcnt lgkmcnt(4)\n"
"ds_read_b64_tr_b16 a[116:117], v173 offset:9216\n"
"v_max3_f32 v189, v68, v69, v70\n"
"v_exp_f32 v127, v127\n"
"v_mfma_f32_16x16x32_bf16 a[0:3], a[96:99], v[152:155], a[0:3]\n"
"ds_read_b64_tr_b16 a[118:119], v173 offset:13312\n"
"v_cvt_pk_bf16_f32 v158, v124, v125\n"
"v_mfma_f32_16x16x32_bf16 a[8:11], a[100:103], v[152:155], a[8:11]\n"
"ds_read_b64_tr_b16 a[120:121], v172 offset:9728\n"
"v_cvt_pk_bf16_f32 v159, v126, v127\n"
"ds_read_b64_tr_b16 a[122:123], v172 offset:13824\n"
"v_mfma_f32_16x16x32_bf16 a[16:19], a[104:107], v[152:155], a[16:19]\n"
"ds_write_b128 v191, a[232:235] offset:2048\n"
"ds_read_b64_tr_b16 a[124:125], v173 offset:9728\n"
"ds_read_b64_tr_b16 a[126:127], v173 offset:13824\n"
"v_mfma_f32_16x16x32_bf16 a[24:27], a[108:111], v[152:155], a[24:27]\n"
"v_max3_f32 v188, v188, v67, v72\n"
"v_max3_f32 v189, v189, v71, v76\n"
"v_mfma_f32_16x16x32_bf16 a[4:7], a[96:99], v[156:159], a[4:7]\n"
"ds_read_b128 a[80:83], v169 offset:16384\n"
"v_max3_f32 v188, v188, v73, v74\n"
"v_mfma_f32_16x16x32_bf16 a[12:15], a[100:103], v[156:159], a[12:15]\n"
"ds_read_b128 a[84:87], v169 offset:20480\n"
"v_max3_f32 v189, v189, v77, v78\n"
"v_mfma_f32_16x16x32_bf16 a[20:23], a[104:107], v[156:159], a[20:23]\n"
"ds_read_b128 a[88:91], v169 offset:24576\n"
"v_max3_f32 v188, v188, v75, v80\n"
"v_mfma_f32_16x16x32_bf16 a[28:31], a[108:111], v[156:159], a[28:31]\n"
"ds_read_b128 a[92:95], v169 offset:28672\n"
"v_max3_f32 v189, v189, v79, v84\n"
"v_mfma_f32_16x16x32_bf16 v[160:163], v[176:179], v[152:155], v[160:163]\n"
"s_waitcnt lgkmcnt(5)\n"
"ds_write_b128 v191, a[236:239] offset:3072\n"
"v_mfma_f32_16x16x32_bf16 a[32:35], a[112:115], v[152:155], a[32:35]\n"
"ds_read_b64_tr_b16 a[96:97], v172 offset:32768\n"
"v_max3_f32 v188, v188, v81, v82\n"
"v_mfma_f32_16x16x32_bf16 a[40:43], a[116:119], v[152:155], a[40:43]\n"
"ds_read_b64_tr_b16 a[98:99], v172 offset:36864\n"
"v_max3_f32 v189, v189, v85, v86\n"
"v_mfma_f32_16x16x32_bf16 a[48:51], a[120:123], v[152:155], a[48:51]\n"
"s_waitcnt lgkmcnt(5)\n"
"ds_read_b64_tr_b16 a[100:101], v173 offset:32768\n"
"v_max3_f32 v188, v188, v83, v88\n"
"v_mfma_f32_16x16x32_bf16 a[56:59], a[124:127], v[152:155], a[56:59]\n"
"ds_read_b64_tr_b16 a[102:103], v173 offset:36864\n"
"v_max3_f32 v189, v189, v87, v92\n"
"v_mfma_f32_16x16x32_bf16 a[36:39], a[112:115], v[156:159], a[36:39]\n"
"ds_read_b64_tr_b16 a[104:105], v172 offset:33280\n"
"v_max3_f32 v188, v188, v89, v90\n"
"v_mfma_f32_16x16x32_bf16 a[44:47], a[116:119], v[156:159], a[44:47]\n"
"ds_read_b64_tr_b16 a[106:107], v172 offset:37376\n"
"v_max3_f32 v189, v189, v93, v94\n"
"v_mfma_f32_16x16x32_bf16 a[52:55], a[120:123], v[156:159], a[52:55]\n"
"ds_read_b64_tr_b16 a[108:109], v173 offset:33280\n"
"v_max3_f32 v190, v188, v91, v95\n"
"v_mfma_f32_16x16x32_bf16 a[60:63], a[124:127], v[156:159], a[60:63]\n"
"ds_read_b64_tr_b16 a[110:111], v173 offset:37376\n"
"v_max_f32 v190, v190, v189\n"
"v_mfma_f32_16x16x32_bf16 v[164:167], v[176:179], v[156:159], v[164:167]\n"
"v_cmp_lt_f32 vcc, s59, v190\n"
"s_waitcnt vmcnt(9)\n"
"v_add_u32 v10, 1, v10\n"
"v_and_b32 v10, 0xffff, v10\n"
"s_waitcnt vmcnt(8)\n"
"v_add_u32 v11, 1, v11\n"
"v_and_b32 v11, 0xffff, v11\n"
"buffer_store_dword v10, v185, s[40:43], 0 offen offset:0 sc1\n"
"s_cbranch_vccz .LBskip4\n"
"s_swappc_b64 s[70:71], s[72:73]\n"
".LBskip4:\n"
"v_mfma_f32_16x16x32_bf16 v[96:99], a[64:67], v[32:35], v[128:131]\n"
"v_add_u32 v191, 114688, v174\n"
"v_mul_f32 v239, s58, v239\n"
"v_exp_f32 v64, v64\n"
"v_mfma_f32_16x16x32_bf16 v[104:107], a[68:71], v[32:35], v[128:131]\n"
"ds_write_b128 v191, a[240:243] offset:0\n"
"v_exp_f32 v65, v65\n"
"v_mfma_f32_16x16x32_bf16 v[112:115], a[72:75], v[32:35], v[128:131]\n"
"v_exp_f32 v66, v66\n"
"v_mfma_f32_16x16x32_bf16 v[120:123], a[76:79], v[32:35], v[128:131]\n"
"v_exp_f32 v67, v67\n"
"v_mfma_f32_16x16x32_bf16 v[100:103], a[64:67], v[48:51], v[132:135]\n"
"ds_write_b128 v191, a[244:247] offset:1024\n"
"v_mfma_f32_16x16x32_bf16 v[108:111], a[68:71], v[48:51], v[132:135]\n"
"ds_read_b128 a[64:67], v170 offset:16384\n"
"v_cvt_pk_bf16_f32 v144, v64, v65\n"
"v_exp_f32 v72, v72\n"
"v_mfma_f32_16x16x32_bf16 v[116:119], a[72:75], v[48:51], v[132:135]\n"
"ds_read_b128 a[68:71], v170 offset:20480\n"
"v_cvt_pk_bf16_f32 v221, v235, v239\n"
"v_mfma_f32_16x16x32_bf16 v[124:127], a[76:79], v[48:51], v[132:135]\n"
"ds_read_b128 a[72:75], v170 offset:24576\n"
"v_mul_f32 v243, s58, v243\n"
"v_exp_f32 v73, v73\n"
"v_mfma_f32_16x16x32_bf16 v[96:99], a[80:83], v[36:39], v[96:99]\n"
"ds_write_b128 v191, a[248:251] offset:2048\n"
"v_cvt_pk_bf16_f32 v145, v66, v67\n"
"v_mfma_f32_16x16x32_bf16 v[104:107], a[84:87], v[36:39], v[104:107]\n"
"s_waitcnt lgkmcnt(4)\n"
"ds_read_b128 a[76:79], v170 offset:28672\n"
"v_mul_f32 v247, s58, v247\n"
"v_exp_f32 v74, v74\n"
"v_mfma_f32_16x16x32_bf16 v[112:115], a[88:91], v[36:39], v[112:115]\n"
"v_exp_f32 v75, v75\n"
"v_mfma_f32_16x16x32_bf16 v[120:123], a[92:95], v[36:39], v[120:123]\n"
"v_cvt_pk_bf16_f32 v146, v72, v73\n"
"v_cvt_pk_bf16_f32 v222, v243, v247\n"
"v_exp_f32 v68, v68\n"
"v_mfma_f32_16x16x32_bf16 v[100:103], a[80:83], v[52:55], v[100:103]\n"
"ds_write_b128 v191, a[252:255] offset:3072\n"
"v_mfma_f32_16x16x32_bf16 v[108:111], a[84:87], v[52:55], v[108:111]\n"
"ds_read_b128 a[80:83], v171 offset:16384\n"
"v_mul_f32 v251, s58, v251\n"
"v_exp_f32 v69, v69\n"
"v_mfma_f32_16x16x32_bf16 v[116:119], a[88:91], v[52:55], v[116:119]\n"
"ds_read_b128 a[84:87], v171 offset:20480\n"
"v_cvt_pk_bf16_f32 v147, v74, v75\n"
"v_exp_f32 v70, v70\n"
"v_mfma_f32_16x16x32_bf16 v[124:127], a[92:95], v[52:55], v[124:127]\n"
"s_waitcnt lgkmcnt(3)\n"
"ds_read_b128 a[88:91], v171 offset:24576\n"
"v_mul_f32 v255, s58, v255\n"
"v_mfma_f32_16x16x32_bf16 v[96:99], a[64:67], v[40:43], v[96:99]\n"
"ds_read_b128 a[92:95], v171 offset:28672\n"
"v_add_u32 v168, 0x10000, v168\n"
"v_exp_f32 v71, v71\n"
"v_mfma_f32_16x16x32_bf16 v[104:107], a[68:71], v[40:43], v[104:107]\n"
"ds_read_b64_tr_b16 a[112:113], v172 offset:33792\n"
"v_add_u32 v169, 0x10000, v169\n"
"v_cvt_pk_bf16_f32 v148, v68, v69\n"
"v_mfma_f32_16x16x32_bf16 v[112:115], a[72:75], v[40:43], v[112:115]\n"
"ds_read_b64_tr_b16 a[114:115], v172 offset:37888\n"
"v_add_u32 v170, 0x10000, v170\n"
"v_exp_f32 v76, v76\n"
"v_mfma_f32_16x16x32_bf16 v[120:123], a[76:79], v[40:43], v[120:123]\n"
"ds_read_b64_tr_b16 a[116:117], v173 offset:33792\n"
"v_add_u32 v171, 0x10000, v171\n"
"v_exp_f32 v77, v77\n"
"v_mfma_f32_16x16x32_bf16 v[100:103], a[64:67], v[56:59], v[100:103]\n"
"ds_read_b64_tr_b16 a[118:119], v173 offset:37888\n"
"v_cvt_pk_bf16_f32 v149, v70, v71\n"
"v_exp_f32 v78, v78\n"
"v_mfma_f32_16x16x32_bf16 v[108:111], a[68:71], v[56:59], v[108:111]\n"
"ds_read_b64_tr_b16 a[120:121], v172 offset:34304\n"
"v_cvt_pk_bf16_f32 v223, v251, v255\n"
"v_exp_f32 v79, v79\n"
"v_mfma_f32_16x16x32_bf16 v[116:119], a[72:75], v[56:59], v[116:119]\n"
"ds_read_b64_tr_b16 a[122:123], v172 offset:38400\n"
"v_cvt_pk_bf16_f32 v150, v76, v77\n"
"v_mfma_f32_16x16x32_bf16 v[124:127], a[76:79], v[56:59], v[124:127]\n"
"s_waitcnt lgkmcnt(4)\n"
"ds_read_b64_tr_b16 a[124:125], v173 offset:34304\n"
"v_exp_f32 v80, v80\n"
"v_mfma_f32_16x16x32_bf16 v[96:99], a[80:83], v[44:47], v[96:99]\n"
"ds_read_b64_tr_b16 a[126:127], v173 offset:38400\n"
"v_exp_f32 v81, v81\n"
"v_mfma_f32_16x16x32_bf16 v[104:107], a[84:87], v[44:47], v[104:107]\n"
"v_cvt_pk_bf16_f32 v151, v78, v79\n"
"v_mfma_f32_16x16x32_bf16 v[112:115], a[88:91], v[44:47], v[112:115]\n"
"v_exp_f32 v82, v82\n"
"v_mfma_f32_16x16x32_bf16 v[120:123], a[92:95], v[44:47], v[120:123]\n"
"v_exp_f32 v83, v83\n"
"v_mfma_f32_16x16x32_bf16 v[100:103], a[80:83], v[60:63], v[100:103]\n"
"v_cvt_pk_bf16_f32 v152, v80, v81\n"
"v_mfma_f32_16x16x32_bf16 v[108:111], a[84:87], v[60:63], v[108:111]\n"
"v_exp_f32 v88, v88\n"
"v_mfma_f32_16x16x32_bf16 v[116:119], a[88:91], v[60:63], v[116:119]\n"
"v_exp_f32 v89, v89\n"
"v_mfma_f32_16x16x32_bf16 v[124:127], a[92:95], v[60:63], v[124:127]\n"
"v_cvt_pk_bf16_f32 v153, v82, v83\n"
"s_barrier\n"
".LBbar5:\n"
"v_mfma_f32_16x16x32_bf16 a[0:3], a[96:99], v[144:147], a[0:3]\n"
"ds_read_b128 a[64:67], v168 offset:0\n"
"s_add_u32 m0, s60, 0\n"
"v_exp_f32 v90, v90\n"
"v_mfma_f32_16x16x32_bf16 a[8:11], a[100:103], v[144:147], a[8:11]\n"
"ds_read_b128 a[68:71], v168 offset:4096\n"
"buffer_load_dwordx4 v2, s[8:11], s63 offen offset:0 nt lds\n"
"v_mfma_f32_16x16x32_bf16 a[16:19], a[104:107], v[144:147], a[16:19]\n"
"ds_read_b128 a[72:75], v168 offset:8192\n"
"v_exp_f32 v91, v91\n"
"v_mfma_f32_16x16x32_bf16 a[24:27], a[108:111], v[144:147], a[24:27]\n"
"ds_read_b128 a[76:79], v168 offset:12288\n"
"v_cvt_pk_bf16_f32 v154, v88, v89\n"
"v_mfma_f32_16x16x32_bf16 a[4:7], a[96:99], v[148:151], a[4:7]\n"
"ds_read_b64_tr_b16 a[96:97], v172 offset:40960\n"
"v_exp_f32 v84, v84\n"
"v_mfma_f32_16x16x32_bf16 a[12:15], a[100:103], v[148:151], a[12:15]\n"
"ds_read_b64_tr_b16 a[98:99], v172 offset:45056\n"
"buffer_load_dwordx4 v3, s[8:11], s63 offen offset:1024 nt lds\n"
"v_mfma_f32_16x16x32_bf16 a[20:23], a[104:107], v[148:151], a[20:23]\n"
"ds_read_b64_tr_b16 a[100:101], v173 offset:40960\n"
"v_exp_f32 v85, v85\n"
"v_mfma_f32_16x16x32_bf16 a[28:31], a[108:111], v[148:151], a[28:31]\n"
"ds_read_b64_tr_b16 a[102:103], v173 offset:45056\n"
"v_cvt_pk_bf16_f32 v155, v90, v91\n"
"v_mfma_f32_16x16x32_bf16 v[160:163], v[176:179], v[144:147], v[160:163]\n"
"ds_read_b64_tr_b16 a[104:105], v172 offset:41472\n"
"v_exp_f32 v86, v86\n"
"v_mfma_f32_16x16x32_bf16 a[32:35], a[112:115], v[144:147], a[32:35]\n"
"s_waitcnt lgkmcnt(4)\n"
"ds_read_b64_tr_b16 a[106:107], v172 offset:45568\n"
"buffer_load_dwordx4 v4, s[8:11], s63 offen offset:2048 nt lds\n"
"v_mfma_f32_16x16x32_bf16 a[40:43], a[116:119], v[144:147], a[40:43]\n"
"ds_read_b64_tr_b16 a[108:109], v173 offset:41472\n"
"v_exp_f32 v87, v87\n"
"v_mfma_f32_16x16x32_bf16 a[48:51], a[120:123], v[144:147], a[48:51]\n"
"ds_read_b64_tr_b16 a[110:111], v173 offset:45568\n"
"v_cvt_pk_bf16_f32 v156, v84, v85\n"
"v_mfma_f32_16x16x32_bf16 a[56:59], a[124:127], v[144:147], a[56:59]\n"
"v_exp_f32 v92, v92\n"
"v_mfma_f32_16x16x32_bf16 a[36:39], a[112:115], v[148:151], a[36:39]\n"
"ds_read_b64_tr_b16 a[112:113], v172 offset:41984\n"
"buffer_load_dwordx4 v5, s[8:11], s63 offen offset:3072 nt lds\n"
"v_exp_f32 v93, v93\n"
"v_mfma_f32_16x16x32_bf16 a[44:47], a[116:119], v[148:151], a[44:47]\n"
"ds_read_b64_tr_b16 a[114:115], v172 offset:46080\n"
"v_cvt_pk_bf16_f32 v157, v86, v87\n"
"v_mfma_f32_16x16x32_bf16 a[52:55], a[120:123], v[148:151], a[52:55]\n"
"ds_read_b64_tr_b16 a[116:117], v173 offset:41984\n"
"v_max3_f32 v188, v96, v97, v98\n"
"v_mfma_f32_16x16x32_bf16 a[60:63], a[124:127], v[148:151], a[60:63]\n"
"ds_read_b64_tr_b16 a[118:119], v173 offset:46080\n"
"v_max3_f32 v189, v100, v101, v102\n"
"v_exp_f32 v94, v94\n"
"v_mfma_f32_16x16x32_bf16 v[164:167], v[176:179], v[148:151], v[164:167]\n"
"s_waitcnt lgkmcnt(4)\n"
"ds_read_b64_tr_b16 a[120:121], v172 offset:42496\n"
"v_max3_f32 v188, v188, v99, v104\n"
"v_exp_f32 v95, v95\n"
"v_mfma_f32_16x16x32_bf16 a[0:3], a[96:99], v[152:155], a[0:3]\n"
"ds_read_b64_tr_b16 a[122:123], v172 offset:46592\n"
"v_cvt_pk_bf16_f32 v158, v92, v93\n"
"v_mfma_f32_16x16x32_bf16 a[8:11], a[100:103], v[152:155], a[8:11]\n"
"ds_read_b64_tr_b16 a[124:125], v173 offset:42496\n"
"v_cvt_pk_bf16_f32 v159, v94, v95\n"
"v_mfma_f32_16x16x32_bf16 a[16:19], a[104:107], v[152:155], a[16:19]\n"
"ds_read_b64_tr_b16 a[126:127], v173 offset:46592\n"
"v_max3_f32 v189, v189, v103, v108\n"
"v_mfma_f32_16x16x32_bf16 a[24:27], a[108:111], v[152:155], a[24:27]\n"
"v_max3_f32 v188, v188, v105, v106\n"
"v_max3_f32 v189, v189, v109, v110\n"
"v_mfma_f32_16x16x32_bf16 a[4:7], a[96:99], v[156:159], a[4:7]\n"
"ds_read_b128 a[80:83], v169 offset:0\n"
"v_max3_f32 v188, v188, v107, v112\n"
"v_mfma_f32_16x16x32_bf16 a[12:15], a[100:103], v[156:159], a[12:15]\n"
"ds_read_b128 a[84:87], v169 offset:4096\n"
"v_max3_f32 v189, v189, v111, v116\n"
"v_mfma_f32_16x16x32_bf16 a[20:23], a[104:107], v[156:159], a[20:23]\n"
"ds_read_b128 a[88:91], v169 offset:8192\n"
"v_max3_f32 v188, v188, v113, v114\n"
"v_mfma_f32_16x16x32_bf16 a[28:31], a[108:111], v[156:159], a[28:31]\n"
"ds_read_b128 a[92:95], v169 offset:12288\n"
"v_max3_f32 v189, v189, v117, v118\n"
"v_mfma_f32_16x16x32_bf16 v[160:163], v[176:179], v[152:155], v[160:163]\n"
"s_waitcnt lgkmcnt(4)\n"
"ds_read_b64_tr_b16 a[96:97], v172 offset:49152\n"
"v_max3_f32 v188, v188, v115, v120\n"
"v_mfma_f32_16x16x32_bf16 a[32:35], a[112:115], v[152:155], a[32:35]\n"
"ds_read_b64_tr_b16 a[98:99], v172 offset:53248\n"
"v_max3_f32 v189, v189, v119, v124\n"
"v_mfma_f32_16x16x32_bf16 a[40:43], a[116:119], v[152:155], a[40:43]\n"
"ds_read_b64_tr_b16 a[100:101], v173 offset:49152\n"
"v_max3_f32 v188, v188, v121, v122\n"
"v_mfma_f32_16x16x32_bf16 a[48:51], a[120:123], v[152:155], a[48:51]\n"
"ds_read_b64_tr_b16 a[102:103], v173 offset:53248\n"
"v_max3_f32 v189, v189, v125, v126\n"
"v_mfma_f32_16x16x32_bf16 a[56:59], a[124:127], v[152:155], a[56:59]\n"
"ds_read_b64_tr_b16 a[104:105], v172 offset:49664\n"
"v_max3_f32 v190, v188, v123, v127\n"
"v_mfma_f32_16x16x32_bf16 a[36:39], a[112:115], v[156:159], a[36:39]\n"
"ds_read_b64_tr_b16 a[106:107], v172 offset:53760\n"
"v_max_f32 v190, v190, v189\n"
"v_mfma_f32_16x16x32_bf16 a[44:47], a[116:119], v[156:159], a[44:47]\n"
"ds_read_b64_tr_b16 a[108:109], v173 offset:49664\n"
"v_cmp_lt_f32 vcc, s59, v190\n"
"v_mfma_f32_16x16x32_bf16 a[52:55], a[120:123], v[156:159], a[52:55]\n"
"ds_read_b64_tr_b16 a[110:111], v173 offset:53760\n"
"v_mfma_f32_16x16x32_bf16 a[60:63], a[124:127], v[156:159], a[60:63]\n"
"v_mfma_f32_16x16x32_bf16 v[164:167], v[176:179], v[156:159], v[164:167]\n"
"s_cbranch_vccz .LBskip5\n"
"s_swappc_b64 s[70:71], s[74:75]\n"
".LBskip5:\n"
"v_mfma_f32_16x16x32_bf16 v[64:67], a[64:67], v[32:35], v[128:131]\n"
"v_exp_f32 v96, v96\n"
"v_mfma_f32_16x16x32_bf16 v[72:75], a[68:71], v[32:35], v[128:131]\n"
"v_exp_f32 v97, v97\n"
"v_mfma_f32_16x16x32_bf16 v[80:83], a[72:75], v[32:35], v[128:131]\n"
"v_exp_f32 v98, v98\n"
"v_mfma_f32_16x16x32_bf16 v[88:91], a[76:79], v[32:35], v[128:131]\n"
"v_exp_f32 v99, v99\n"
"v_mfma_f32_16x16x32_bf16 v[68:71], a[64:67], v[48:51], v[132:135]\n"
"ds_read_b128 a[64:67], v170 offset:0\n"
"v_cvt_pk_bf16_f32 v144, v96, v97\n"
"v_mfma_f32_16x16x32_bf16 v[76:79], a[68:71], v[48:51], v[132:135]\n"
"ds_read_b128 a[68:71], v170 offset:4096\n"
"v_exp_f32 v104, v104\n"
"v_mfma_f32_16x16x32_bf16 v[84:87], a[72:75], v[48:51], v[132:135]\n"
"ds_read_b128 a[72:75], v170 offset:8192\n"
"v_mfma_f32_16x16x32_bf16 v[92:95], a[76:79], v[48:51], v[132:135]\n"
"s_waitcnt lgkmcnt(3)\n"
"ds_read_b128 a[76:79], v170 offset:12288\n"
"v_exp_f32 v105, v105\n"
"v_mfma_f32_16x16x32_bf16 v[64:67], a[80:83], v[36:39], v[64:67]\n"
"s_add_u32 m0, s60, 16384\n"
"v_cvt_pk_bf16_f32 v145, v98, v99\n"
"v_exp_f32 v106, v106\n"
"v_mfma_f32_16x16x32_bf16 v[72:75], a[84:87], v[36:39], v[72:75]\n"
"buffer_load_dwordx4 v2, s[4:7], s65 offen offset:0 nt lds\n"
"v_mfma_f32_16x16x32_bf16 v[80:83], a[88:91], v[36:39], v[80:83]\n"
"v_exp_f32 v107, v107\n"
"v_mfma_f32_16x16x32_bf16 v[88:91], a[92:95], v[36:39], v[88:91]\n"
"v_cvt_pk_bf16_f32 v146, v104, v105\n"
"v_exp_f32 v100, v100\n"
"v_mfma_f32_16x16x32_bf16 v[68:71], a[80:83], v[52:55], v[68:71]\n"
"ds_read_b128 a[80:83], v171 offset:0\n"
"v_mfma_f32_16x16x32_bf16 v[76:79], a[84:87], v[52:55], v[76:79]\n"
"ds_read_b128 a[84:87], v171 offset:4096\n"
"buffer_load_dwordx4 v3, s[4:7], s65 offen offset:1024 nt lds\n"
"v_exp_f32 v101, v101\n"
"v_mfma_f32_16x16x32_bf16 v[84:87], a[88:91], v[52:55], v[84:87]\n"
"ds_read_b128 a[88:91], v171 offset:8192\n"
"v_cvt_pk_bf16_f32 v147, v106, v107\n"
"v_exp_f32 v102, v102\n"
"v_mfma_f32_16x16x32_bf16 v[92:95], a[92:95], v[52:55], v[92:95]\n"
"s_waitcnt lgkmcnt(3)\n"
"ds_read_b128 a[92:95], v171 offset:12288\n"
"v_mfma_f32_16x16x32_bf16 v[64:67], a[64:67], v[40:43], v[64:67]\n"
"ds_read_b64_tr_b16 a[112:113], v172 offset:50176\n"
"v_exp_f32 v103, v103\n"
"v_mfma_f32_16x16x32_bf16 v[72:75], a[68:71], v[40:43], v[72:75]\n"
"ds_read_b64_tr_b16 a[114:115], v172 offset:54272\n"
"buffer_load_dwordx4 v4, s[4:7], s65 offen offset:2048 nt lds\n"
"v_cvt_pk_bf16_f32 v148, v100, v101\n"
"v_mfma_f32_16x16x32_bf16 v[80:83], a[72:75], v[40:43], v[80:83]\n"
"ds_read_b64_tr_b16 a[116:117], v173 offset:50176\n"
"v_exp_f32 v108, v108\n"
"v_mfma_f32_16x16x32_bf16 v[88:91], a[76:79], v[40:43], v[88:91]\n"
"ds_read_b64_tr_b16 a[118:119], v173 offset:54272\n"
"v_exp_f32 v109, v109\n"
"v_mfma_f32_16x16x32_bf16 v[68:71], a[64:67], v[56:59], v[68:71]\n"
"ds_read_b64_tr_b16 a[120:121], v172 offset:50688\n"
"v_cvt_pk_bf16_f32 v149, v102, v103\n"
"v_exp_f32 v110, v110\n"
"v_mfma_f32_16x16x32_bf16 v[76:79], a[68:71], v[56:59], v[76:79]\n"
"ds_read_b64_tr_b16 a[122:123], v172 offset:54784\n"
"buffer_load_dwordx4 v5, s[4:7], s65 offen offset:3072 nt lds\n"
"v_exp_f32 v111, v111\n"
"v_mfma_f32_16x16x32_bf16 v[84:87], a[72:75], v[56:59], v[84:87]\n"
"ds_read_b64_tr_b16 a[124:125], v173 offset:50688\n"
"v_cvt_pk_bf16_f32 v150, v108, v109\n"
"v_mfma_f32_16x16x32_bf16 v[92:95], a[76:79], v[56:59], v[92:95]\n"
"s_waitcnt lgkmcnt(4)\n"
"ds_read_b64_tr_b16 a[126:127], v173 offset:54784\n"
"v_exp_f32 v112, v112\n"
"v_mfma_f32_16x16x32_bf16 v[64:67], a[80:83], v[44:47], v[64:67]\n"
"v_exp_f32 v113, v113\n"
"v_mfma_f32_16x16x32_bf16 v[72:75], a[84:87], v[44:47], v[72:75]\n"
"v_cvt_pk_bf16_f32 v151, v110, v111\n"
"v_mfma_f32_16x16x32_bf16 v[80:83], a[88:91], v[44:47], v[80:83]\n"
"v_exp_f32 v114, v114\n"
"v_mfma_f32_16x16x32_bf16 v[88:91], a[92:95], v[44:47], v[88:91]\n"
"v_exp_f32 v115, v115\n"
"v_mfma_f32_16x16x32_bf16 v[68:71], a[80:83], v[60:63], v[68:71]\n"
"v_cvt_pk_bf16_f32 v152, v112, v113\n"
"v_mfma_f32_16x16x32_bf16 v[76:79], a[84:87], v[60:63], v[76:79]\n"
"v_exp_f32 v120, v120\n"
"v_mfma_f32_16x16x32_bf16 v[84:87], a[88:91], v[60:63], v[84:87]\n"
"v_exp_f32 v121, v121\n"
"v_mfma_f32_16x16x32_bf16 v[92:95], a[92:95], v[60:63], v[92:95]\n"
"v_cvt_pk_bf16_f32 v153, v114, v115\n"
"s_barrier\n"
".LBbar6:\n"
"v_mfma_f32_16x16x32_bf16 a[0:3], a[96:99], v[144:147], a[0:3]\n"
"ds_read_b128 a[64:67], v168 offset:16384\n"
"s_add_u32 m0, s60, 32768\n"
"v_exp_f32 v122, v122\n"
"v_mfma_f32_16x16x32_bf16 a[8:11], a[100:103], v[144:147], a[8:11]\n"
"ds_read_b128 a[68:71], v168 offset:20480\n"
"buffer_load_dwordx4 v2, s[8:11], s64 offen offset:0 nt lds\n"
"v_mfma_f32_16x16x32_bf16 a[16:19], a[104:107], v[144:147], a[16:19]\n"
"ds_read_b128 a[72:75], v168 offset:24576\n"
"v_exp_f32 v123, v123\n"
"v_mfma_f32_16x16x32_bf16 a[24:27], a[108:111], v[144:147], a[24:27]\n"
"ds_read_b128 a[76:79], v168 offset:28672\n"
"v_cvt_pk_bf16_f32 v154, v120, v121\n"
"v_mfma_f32_16x16x32_bf16 a[4:7], a[96:99], v[148:151], a[4:7]\n"
"ds_read_b64_tr_b16 a[96:97], v172 offset:57344\n"
"v_exp_f32 v116, v116\n"
"v_mfma_f32_16x16x32_bf16 a[12:15], a[100:103], v[148:151], a[12:15]\n"
"ds_read_b64_tr_b16 a[98:99], v172 offset:61440\n"
"buffer_load_dwordx4 v3, s[8:11], s64 offen offset:1024 nt lds\n"
"v_mfma_f32_16x16x32_bf16 a[20:23], a[104:107], v[148:151], a[20:23]\n"
"ds_read_b64_tr_b16 a[100:101], v173 offset:57344\n"
"v_exp_f32 v117, v117\n"
"v_mfma_f32_16x16x32_bf16 a[28:31], a[108:111], v[148:151], a[28:31]\n"
"ds_read_b64_tr_b16 a[102:103], v173 offset:61440\n"
"v_cvt_pk_bf16_f32 v155, v122, v123\n"
"v_mfma_f32_16x16x32_bf16 v[160:163], v[176:179], v[144:147], v[160:163]\n"
"ds_read_b64_tr_b16 a[104:105], v172 offset:57856\n"
"v_exp_f32 v118, v118\n"
"v_mfma_f32_16x16x32_bf16 a[32:35], a[112:115], v[144:147], a[32:35]\n"
"s_waitcnt lgkmcnt(4)\n"
"ds_read_b64_tr_b16 a[106:107], v172 offset:61952\n"
"buffer_load_dwordx4 v4, s[8:11], s64 offen offset:2048 nt lds\n"
"v_mfma_f32_16x16x32_bf16 a[40:43], a[116:119], v[144:147], a[40:43]\n"
"ds_read_b64_tr_b16 a[108:109], v173 offset:57856\n"
"v_exp_f32 v119, v119\n"
"v_mfma_f32_16x16x32_bf16 a[48:51], a[120:123], v[144:147], a[48:51]\n"
"ds_read_b64_tr_b16 a[110:111], v173 offset:61952\n"
"v_cvt_pk_bf16_f32 v156, v116, v117\n"
"v_mfma_f32_16x16x32_bf16 a[56:59], a[124:127], v[144:147], a[56:59]\n"
"v_exp_f32 v124, v124\n"
"v_mfma_f32_16x16x32_bf16 a[36:39], a[112:115], v[148:151], a[36:39]\n"
"ds_read_b64_tr_b16 a[112:113], v172 offset:58368\n"
"buffer_load_dwordx4 v5, s[8:11], s64 offen offset:3072 nt lds\n"
"v_exp_f32 v125, v125\n"
"v_mfma_f32_16x16x32_bf16 a[44:47], a[116:119], v[148:151], a[44:47]\n"
"ds_read_b64_tr_b16 a[114:115], v172 offset:62464\n"
"v_cvt_pk_bf16_f32 v157, v118, v119\n"
"v_mfma_f32_16x16x32_bf16 a[52:55], a[120:123], v[148:151], a[52:55]\n"
"ds_read_b64_tr_b16 a[116:117], v173 offset:58368\n"
"v_max3_f32 v188, v64, v65, v66\n"
"v_mfma_f32_16x16x32_bf16 a[60:63], a[124:127], v[148:151], a[60:63]\n"
"ds_read_b64_tr_b16 a[118:119], v173 offset:62464\n"
"v_max3_f32 v189, v68, v69, v70\n"
"v_exp_f32 v126, v126\n"
"v_mfma_f32_16x16x32_bf16 v[164:167], v[176:179], v[148:151], v[164:167]\n"
"s_waitcnt lgkmcnt(4)\n"
"ds_read_b64_tr_b16 a[120:121], v172 offset:58880\n"
"v_max3_f32 v188, v188, v67, v72\n"
"v_exp_f32 v127, v127\n"
"v_mfma_f32_16x16x32_bf16 a[0:3], a[96:99], v[152:155], a[0:3]\n"
"ds_read_b64_tr_b16 a[122:123], v172 offset:62976\n"
"v_cvt_pk_bf16_f32 v158, v124, v125\n"
"v_mfma_f32_16x16x32_bf16 a[8:11], a[100:103], v[152:155], a[8:11]\n"
"ds_read_b64_tr_b16 a[124:125], v173 offset:58880\n"
"v_cvt_pk_bf16_f32 v159, v126, v127\n"
"v_mfma_f32_16x16x32_bf16 a[16:19], a[104:107], v[152:155], a[16:19]\n"
"ds_read_b64_tr_b16 a[126:127], v173 offset:62976\n"
"v_add_u32 v172, 0x10000, v172\n"
"v_mfma_f32_16x16x32_bf16 a[24:27], a[108:111], v[152:155], a[24:27]\n"
"v_add_u32 v173, 0x10000, v173\n"
"v_max3_f32 v189, v189, v71, v76\n"
"v_mfma_f32_16x16x32_bf16 a[4:7], a[96:99], v[156:159], a[4:7]\n"
"ds_read_b128 a[80:83], v169 offset:16384\n"
"v_max3_f32 v188, v188, v73, v74\n"
"v_mfma_f32_16x16x32_bf16 a[12:15], a[100:103], v[156:159], a[12:15]\n"
"ds_read_b128 a[84:87], v169 offset:20480\n"
"v_max3_f32 v189, v189, v77, v78\n"
"v_mfma_f32_16x16x32_bf16 a[20:23], a[104:107], v[156:159], a[20:23]\n"
"ds_read_b128 a[88:91], v169 offset:24576\n"
"v_max3_f32 v188, v188, v75, v80\n"
"v_mfma_f32_16x16x32_bf16 a[28:31], a[108:111], v[156:159], a[28:31]\n"
"ds_read_b128 a[92:95], v169 offset:28672\n"
"v_max3_f32 v189, v189, v79, v84\n"
"v_mfma_f32_16x16x32_bf16 v[160:163], v[176:179], v[152:155], v[160:163]\n"
"s_waitcnt lgkmcnt(4)\n"
"ds_read_b64_tr_b16 a[96:97], v172 offset:32768\n"
"v_max3_f32 v188, v188, v81, v82\n"
"v_mfma_f32_16x16x32_bf16 a[32:35], a[112:115], v[152:155], a[32:35]\n"
"ds_read_b64_tr_b16 a[98:99], v172 offset:36864\n"
"v_max3_f32 v189, v189, v85, v86\n"
"v_mfma_f32_16x16x32_bf16 a[40:43], a[116:119], v[152:155], a[40:43]\n"
"ds_read_b64_tr_b16 a[100:101], v173 offset:32768\n"
"v_max3_f32 v188, v188, v83, v88\n"
"v_mfma_f32_16x16x32_bf16 a[48:51], a[120:123], v[152:155], a[48:51]\n"
"ds_read_b64_tr_b16 a[102:103], v173 offset:36864\n"
"v_max3_f32 v189, v189, v87, v92\n"
"v_mfma_f32_16x16x32_bf16 a[56:59], a[124:127], v[152:155], a[56:59]\n"
"ds_read_b64_tr_b16 a[104:105], v172 offset:33280\n"
"v_max3_f32 v188, v188, v89, v90\n"
"v_mfma_f32_16x16x32_bf16 a[36:39], a[112:115], v[156:159], a[36:39]\n"
"ds_read_b64_tr_b16 a[106:107], v172 offset:37376\n"
"v_max3_f32 v189, v189, v93, v94\n"
"v_mfma_f32_16x16x32_bf16 a[44:47], a[116:119], v[156:159], a[44:47]\n"
"ds_read_b64_tr_b16 a[108:109], v173 offset:33280\n"
"v_max3_f32 v190, v188, v91, v95\n"
"v_mfma_f32_16x16x32_bf16 a[52:55], a[120:123], v[156:159], a[52:55]\n"
"ds_read_b64_tr_b16 a[110:111], v173 offset:37376\n"
"v_max_f32 v190, v190, v189\n"
"v_mfma_f32_16x16x32_bf16 a[60:63], a[124:127], v[156:159], a[60:63]\n"
"v_cmp_lt_f32 vcc, s59, v190\n"
"v_mfma_f32_16x16x32_bf16 v[164:167], v[176:179], v[156:159], v[164:167]\n"
"s_nop 0\n"
"s_cbranch_vccz .LBskip6\n"
"s_swappc_b64 s[70:71], s[72:73]\n"
".LBskip6:\n"
"v_mfma_f32_16x16x32_bf16 v[96:99], a[64:67], v[32:35], v[128:131]\n"
"v_exp_f32 v64, v64\n"
"v_mfma_f32_16x16x32_bf16 v[104:107], a[68:71], v[32:35], v[128:131]\n"
"v_exp_f32 v65, v65\n"
"v_mfma_f32_16x16x32_bf16 v[112:115], a[72:75], v[32:35], v[128:131]\n"
"v_exp_f32 v66, v66\n"
"v_mfma_f32_16x16x32_bf16 v[120:123], a[76:79], v[32:35], v[128:131]\n"
"v_exp_f32 v67, v67\n"
"v_mfma_f32_16x16x32_bf16 v[100:103], a[64:67], v[48:51], v[132:135]\n"
"ds_read_b128 a[64:67], v170 offset:16384\n"
"v_cvt_pk_bf16_f32 v144, v64, v65\n"
"v_mfma_f32_16x16x32_bf16 v[108:111], a[68:71], v[48:51], v[132:135]\n"
"ds_read_b128 a[68:71], v170 offset:20480\n"
"v_exp_f32 v72, v72\n"
"v_mfma_f32_16x16x32_bf16 v[116:119], a[72:75], v[48:51], v[132:135]\n"
"ds_read_b128 a[72:75], v170 offset:24576\n"
"v_mfma_f32_16x16x32_bf16 v[124:127], a[76:79], v[48:51], v[132:135]\n"
"s_waitcnt lgkmcnt(3)\n"
"ds_read_b128 a[76:79], v170 offset:28672\n"
"v_exp_f32 v73, v73\n"
"v_mfma_f32_16x16x32_bf16 v[96:99], a[80:83], v[36:39], v[96:99]\n"
"s_add_u32 m0, s60, 65536\n"
"v_cvt_pk_bf16_f32 v145, v66, v67\n"
"v_exp_f32 v74, v74\n"
"v_mfma_f32_16x16x32_bf16 v[104:107], a[84:87], v[36:39], v[104:107]\n"
"buffer_load_dwordx4 v2, s[4:7], s66 offen offset:0 nt lds\n"
"v_mfma_f32_16x16x32_bf16 v[112:115], a[88:91], v[36:39], v[112:115]\n"
"v_exp_f32 v75, v75\n"
"v_mfma_f32_16x16x32_bf16 v[120:123], a[92:95], v[36:39], v[120:123]\n"
"v_cvt_pk_bf16_f32 v146, v72, v73\n"
"v_exp_f32 v68, v68\n"
"v_mfma_f32_16x16x32_bf16 v[100:103], a[80:83], v[52:55], v[100:103]\n"
"ds_read_b128 a[80:83], v171 offset:16384\n"
"v_mfma_f32_16x16x32_bf16 v[108:111], a[84:87], v[52:55], v[108:111]\n"
"ds_read_b128 a[84:87], v171 offset:20480\n"
"buffer_load_dwordx4 v3, s[4:7], s66 offen offset:1024 nt lds\n"
"v_exp_f32 v69, v69\n"
"v_mfma_f32_16x16x32_bf16 v[116:119], a[88:91], v[52:55], v[116:119]\n"
"ds_read_b128 a[88:91], v171 offset:24576\n"
"v_cvt_pk_bf16_f32 v147, v74, v75\n"
"v_exp_f32 v70, v70\n"
"v_mfma_f32_16x16x32_bf16 v[124:127], a[92:95], v[52:55], v[124:127]\n"
"s_waitcnt lgkmcnt(3)\n"
"ds_read_b128 a[92:95], v171 offset:28672\n"
"v_mfma_f32_16x16x32_bf16 v[96:99], a[64:67], v[40:43], v[96:99]\n"
"ds_read_b64_tr_b16 a[112:113], v172 offset:33792\n"
"v_exp_f32 v71, v71\n"
"v_mfma_f32_16x16x32_bf16 v[104:107], a[68:71], v[40:43], v[104:107]\n"
"ds_read_b64_tr_b16 a[114:115], v172 offset:37888\n"
"buffer_load_dwordx4 v4, s[4:7], s66 offen offset:2048 nt lds\n"
"v_cvt_pk_bf16_f32 v148, v68, v69\n"
"v_mfma_f32_16x16x32_bf16 v[112:115], a[72:75], v[40:43], v[112:115]\n"
"ds_read_b64_tr_b16 a[116:117], v173 offset:33792\n"
"v_exp_f32 v76, v76\n"
"v_mfma_f32_16x16x32_bf16 v[120:123], a[76:79], v[40:43], v[120:123]\n"
"ds_read_b64_tr_b16 a[118:119], v173 offset:37888\n"
"v_exp_f32 v77, v77\n"
"v_mfma_f32_16x16x32_bf16 v[100:103], a[64:67], v[56:59], v[100:103]\n"
"ds_read_b64_tr_b16 a[120:121], v172 offset:34304\n"
"v_cvt_pk_bf16_f32 v149, v70, v71\n"
"v_exp_f32 v78, v78\n"
"v_mfma_f32_16x16x32_bf16 v[108:111], a[68:71], v[56:59], v[108:111]\n"
"ds_read_b64_tr_b16 a[122:123], v172 offset:38400\n"
"buffer_load_dwordx4 v5, s[4:7], s66 offen offset:3072 nt lds\n"
"v_exp_f32 v79, v79\n"
"v_mfma_f32_16x16x32_bf16 v[116:119], a[72:75], v[56:59], v[116:119]\n"
"ds_read_b64_tr_b16 a[124:125], v173 offset:34304\n"
"v_cvt_pk_bf16_f32 v150, v76, v77\n"
"v_mfma_f32_16x16x32_bf16 v[124:127], a[76:79], v[56:59], v[124:127]\n"
"s_waitcnt lgkmcnt(4)\n"
"ds_read_b64_tr_b16 a[126:127], v173 offset:38400\n"
"v_exp_f32 v80, v80\n"
"v_mfma_f32_16x16x32_bf16 v[96:99], a[80:83], v[44:47], v[96:99]\n"
"v_exp_f32 v81, v81\n"
"v_mfma_f32_16x16x32_bf16 v[104:107], a[84:87], v[44:47], v[104:107]\n"
"v_cvt_pk_bf16_f32 v151, v78, v79\n"
"v_mfma_f32_16x16x32_bf16 v[112:115], a[88:91], v[44:47], v[112:115]\n"
"v_exp_f32 v82, v82\n"
"v_mfma_f32_16x16x32_bf16 v[120:123], a[92:95], v[44:47], v[120:123]\n"
"v_exp_f32 v83, v83\n"
"v_mfma_f32_16x16x32_bf16 v[100:103], a[80:83], v[60:63], v[100:103]\n"
"v_cvt_pk_bf16_f32 v152, v80, v81\n"
"v_mfma_f32_16x16x32_bf16 v[108:111], a[84:87], v[60:63], v[108:111]\n"
"v_exp_f32 v88, v88\n"
"v_mfma_f32_16x16x32_bf16 v[116:119], a[88:91], v[60:63], v[116:119]\n"
"v_exp_f32 v89, v89\n"
"v_mfma_f32_16x16x32_bf16 v[124:127], a[92:95], v[60:63], v[124:127]\n"
"v_cvt_pk_bf16_f32 v153, v82, v83\n"
"s_barrier\n"
".LBbar7:\n"
"v_mfma_f32_16x16x32_bf16 a[0:3], a[96:99], v[144:147], a[0:3]\n"
"ds_read_b128 a[64:67], v168 offset:49152\n"
"s_add_u32 m0, s60, 81920\n"
"v_exp_f32 v90, v90\n"
"v_mfma_f32_16x16x32_bf16 a[8:11], a[100:103], v[144:147], a[8:11]\n"
"ds_read_b128 a[68:71], v168 offset:53248\n"
"buffer_load_dwordx4 v2, s[8:11], s65 offen offset:0 nt lds\n"
"v_mfma_f32_16x16x32_bf16 a[16:19], a[104:107], v[144:147], a[16:19]\n"
"ds_read_b128 a[72:75], v168 offset:57344\n"
"v_exp_f32 v91, v91\n"
"v_mfma_f32_16x16x32_bf16 a[24:27], a[108:111], v[144:147], a[24:27]\n"
"ds_read_b128 a[76:79], v168 offset:61440\n"
"v_cvt_pk_bf16_f32 v154, v88, v89\n"
"v_mfma_f32_16x16x32_bf16 a[4:7], a[96:99], v[148:151], a[4:7]\n"
"ds_read_b64_tr_b16 a[96:97], v172 offset:40960\n"
"v_exp_f32 v84, v84\n"
"v_mfma_f32_16x16x32_bf16 a[12:15], a[100:103], v[148:151], a[12:15]\n"
"ds_read_b64_tr_b16 a[98:99], v172 offset:45056\n"
"buffer_load_dwordx4 v3, s[8:11], s65 offen offset:1024 nt lds\n"
"v_mfma_f32_16x16x32_bf16 a[20:23], a[104:107], v[148:151], a[20:23]\n"
"ds_read_b64_tr_b16 a[100:101], v173 offset:40960\n"
"v_exp_f32 v85, v85\n"
"v_mfma_f32_16x16x32_bf16 a[28:31], a[108:111], v[148:151], a[28:31]\n"
"ds_read_b64_tr_b16 a[102:103], v173 offset:45056\n"
"v_cvt_pk_bf16_f32 v155, v90, v91\n"
"v_mfma_f32_16x16x32_bf16 v[160:163], v[176:179], v[144:147], v[160:163]\n"
"ds_read_b64_tr_b16 a[104:105], v172 offset:41472\n"
"v_exp_f32 v86, v86\n"
"v_mfma_f32_16x16x32_bf16 a[32:35], a[112:115], v[144:147], a[32:35]\n"
"s_waitcnt lgkmcnt(4)\n"
"ds_read_b64_tr_b16 a[106:107], v172 offset:45568\n"
"buffer_load_dwordx4 v4, s[8:11], s65 offen offset:2048 nt lds\n"
"v_mfma_f32_16x16x32_bf16 a[40:43], a[116:119], v[144:147], a[40:43]\n"
"ds_read_b64_tr_b16 a[108:109], v173 offset:41472\n"
"v_exp_f32 v87, v87\n"
"v_mfma_f32_16x16x32_bf16 a[48:51], a[120:123], v[144:147], a[48:51]\n"
"ds_read_b64_tr_b16 a[110:111], v173 offset:45568\n"
"v_cvt_pk_bf16_f32 v156, v84, v85\n"
"v_mfma_f32_16x16x32_bf16 a[56:59], a[124:127], v[144:147], a[56:59]\n"
"v_add_u32 v191, 49152, v174\n"
"v_exp_f32 v92, v92\n"
"v_mfma_f32_16x16x32_bf16 a[36:39], a[112:115], v[148:151], a[36:39]\n"
"s_waitcnt vmcnt(27)\n"
"ds_write_b128 v191, a[144:147] offset:0\n"
"v_exp_f32 v93, v93\n"
"v_mfma_f32_16x16x32_bf16 a[44:47], a[116:119], v[148:151], a[44:47]\n"
"ds_read_b64_tr_b16 a[112:113], v172 offset:41984\n"
"buffer_load_dwordx4 v5, s[8:11], s65 offen offset:3072 nt lds\n"
"v_mfma_f32_16x16x32_bf16 a[52:55], a[120:123], v[148:151], a[52:55]\n"
"ds_read_b64_tr_b16 a[114:115], v172 offset:46080\n"
"v_cvt_pk_bf16_f32 v157, v86, v87\n"
"v_mfma_f32_16x16x32_bf16 a[60:63], a[124:127], v[148:151], a[60:63]\n"
"s_waitcnt vmcnt(27)\n"
"ds_write_b128 v191, a[148:151] offset:1024\n"
"v_exp_f32 v94, v94\n"
"v_mfma_f32_16x16x32_bf16 v[164:167], v[176:179], v[148:151], v[164:167]\n"
"s_waitcnt lgkmcnt(3)\n"
"ds_read_b64_tr_b16 a[116:117], v173 offset:41984\n"
"v_max3_f32 v188, v96, v97, v98\n"
"v_exp_f32 v95, v95\n"
"v_mfma_f32_16x16x32_bf16 a[0:3], a[96:99], v[152:155], a[0:3]\n"
"ds_read_b64_tr_b16 a[118:119], v173 offset:46080\n"
"v_cvt_pk_bf16_f32 v158, v92, v93\n"
"v_mfma_f32_16x16x32_bf16 a[8:11], a[100:103], v[152:155], a[8:11]\n"
"ds_read_b64_tr_b16 a[120:121], v172 offset:42496\n"
"v_cvt_pk_bf16_f32 v159, v94, v95\n"
"ds_read_b64_tr_b16 a[122:123], v172 offset:46592\n"
"v_mfma_f32_16x16x32_bf16 a[16:19], a[104:107], v[152:155], a[16:19]\n"
"s_waitcnt vmcnt(26)\n"
"ds_write_b128 v191, a[152:155] offset:2048\n"
"ds_read_b64_tr_b16 a[124:125], v173 offset:42496\n"
"ds_read_b64_tr_b16 a[126:127], v173 offset:46592\n"
"v_mfma_f32_16x16x32_bf16 a[24:27], a[108:111], v[152:155], a[24:27]\n"
"v_add_u32 v172, 0x10000, v172\n"
"v_add_u32 v173, 0x10000, v173\n"
"v_mfma_f32_16x16x32_bf16 a[4:7], a[96:99], v[156:159], a[4:7]\n"
"ds_read_b128 a[80:83], v169 offset:49152\n"
"v_max3_f32 v189, v100, v101, v102\n"
"v_mfma_f32_16x16x32_bf16 a[12:15], a[100:103], v[156:159], a[12:15]\n"
"ds_read_b128 a[84:87], v169 offset:53248\n"
"v_max3_f32 v188, v188, v99, v104\n"
"v_mfma_f32_16x16x32_bf16 a[20:23], a[104:107], v[156:159], a[20:23]\n"
"ds_read_b128 a[88:91], v169 offset:57344\n"
"v_max3_f32 v189, v189, v103, v108\n"
"v_mfma_f32_16x16x32_bf16 a[28:31], a[108:111], v[156:159], a[28:31]\n"
"ds_read_b128 a[92:95], v169 offset:61440\n"
"v_max3_f32 v188, v188, v105, v106\n"
"v_mfma_f32_16x16x32_bf16 v[160:163], v[176:179], v[152:155], v[160:163]\n"
"s_waitcnt vmcnt(25)\n"
"ds_write_b128 v191, a[156:159] offset:3072\n"
"s_waitcnt lgkmcnt(5)\n"
"v_mfma_f32_16x16x32_bf16 a[32:35], a[112:115], v[152:155], a[32:35]\n"
"ds_read_b64_tr_b16 a[96:97], v172 offset:0\n"
"v_max3_f32 v189, v189, v109, v110\n"
"v_mfma_f32_16x16x32_bf16 a[40:43], a[116:119], v[152:155], a[40:43]\n"
"ds_read_b64_tr_b16 a[98:99], v172 offset:4096\n"
"v_max3_f32 v188, v188, v107, v112\n"
"v_mfma_f32_16x16x32_bf16 a[48:51], a[120:123], v[152:155], a[48:51]\n"
"ds_read_b64_tr_b16 a[100:101], v173 offset:0\n"
"v_max3_f32 v189, v189, v111, v116\n"
"v_mfma_f32_16x16x32_bf16 a[56:59], a[124:127], v[152:155], a[56:59]\n"
"ds_read_b64_tr_b16 a[102:103], v173 offset:4096\n"
"v_max3_f32 v188, v188, v113, v114\n"
"v_mfma_f32_16x16x32_bf16 a[36:39], a[112:115], v[156:159], a[36:39]\n"
"ds_read_b64_tr_b16 a[104:105], v172 offset:512\n"
"v_max3_f32 v189, v189, v117, v118\n"
"v_mfma_f32_16x16x32_bf16 a[44:47], a[116:119], v[156:159], a[44:47]\n"
"ds_read_b64_tr_b16 a[106:107], v172 offset:4608\n"
"v_max3_f32 v188, v188, v115, v120\n"
"v_mfma_f32_16x16x32_bf16 a[52:55], a[120:123], v[156:159], a[52:55]\n"
"ds_read_b64_tr_b16 a[108:109], v173 offset:512\n"
"v_max3_f32 v189, v189, v119, v124\n"
"v_mfma_f32_16x16x32_bf16 a[60:63], a[124:127], v[156:159], a[60:63]\n"
"ds_read_b64_tr_b16 a[110:111], v173 offset:4608\n"
"v_max3_f32 v188, v188, v121, v122\n"
"v_mfma_f32_16x16x32_bf16 v[164:167], v[176:179], v[156:159], v[164:167]\n"
"v_max3_f32 v189, v189, v125, v126\n"
"v_max3_f32 v190, v188, v123, v127\n"
"v_max_f32 v190, v190, v189\n"
"v_cmp_lt_f32 vcc, s59, v190\n"
"s_nop 1\n"
"s_cbranch_vccz .LBskip7\n"
"s_swappc_b64 s[70:71], s[74:75]\n"
".LBskip7:\n"
"v_mfma_f32_16x16x32_bf16 v[64:67], a[64:67], v[192:195], 0\n"
"v_exp_f32 v96, v96\n"
"v_mfma_f32_16x16x32_bf16 v[72:75], a[68:71], v[192:195], 0\n"
"v_exp_f32 v97, v97\n"
"v_mfma_f32_16x16x32_bf16 v[80:83], a[72:75], v[192:195], 0\n"
"v_exp_f32 v98, v98\n"
"v_mfma_f32_16x16x32_bf16 v[88:91], a[76:79], v[192:195], 0\n"
"v_exp_f32 v99, v99\n"
"v_mfma_f32_16x16x32_bf16 v[68:71], a[64:67], v[208:211], 0\n"
"ds_read_b128 a[64:67], v170 offset:49152\n"
"v_cvt_pk_bf16_f32 v144, v96, v97\n"
"v_mfma_f32_16x16x32_bf16 v[76:79], a[68:71], v[208:211], 0\n"
"ds_read_b128 a[68:71], v170 offset:53248\n"
"v_exp_f32 v104, v104\n"
"v_mfma_f32_16x16x32_bf16 v[84:87], a[72:75], v[208:211], 0\n"
"ds_read_b128 a[72:75], v170 offset:57344\n"
"v_mfma_f32_16x16x32_bf16 v[92:95], a[76:79], v[208:211], 0\n"
"s_waitcnt lgkmcnt(3)\n"
"ds_read_b128 a[76:79], v170 offset:61440\n"
"v_exp_f32 v105, v105\n"
"v_mfma_f32_16x16x32_bf16 v[64:67], a[80:83], v[196:199], v[64:67]\n"
"v_cvt_pk_bf16_f32 v145, v98, v99\n"
"v_exp_f32 v106, v106\n"
"v_mfma_f32_16x16x32_bf16 v[72:75], a[84:87], v[196:199], v[72:75]\n"
"v_exp_f32 v107, v107\n"
"v_mfma_f32_16x16x32_bf16 v[80:83], a[88:91], v[196:199], v[80:83]\n"
"v_cvt_pk_bf16_f32 v146, v104, v105\n"
"v_mfma_f32_16x16x32_bf16 v[88:91], a[92:95], v[196:199], v[88:91]\n"
"v_exp_f32 v100, v100\n"
"v_mfma_f32_16x16x32_bf16 v[68:71], a[80:83], v[212:215], v[68:71]\n"
"ds_read_b128 a[80:83], v171 offset:49152\n"
"v_mfma_f32_16x16x32_bf16 v[76:79], a[84:87], v[212:215], v[76:79]\n"
"ds_read_b128 a[84:87], v171 offset:53248\n"
"v_exp_f32 v101, v101\n"
"v_mfma_f32_16x16x32_bf16 v[84:87], a[88:91], v[212:215], v[84:87]\n"
"ds_read_b128 a[88:91], v171 offset:57344\n"
"v_cvt_pk_bf16_f32 v147, v106, v107\n"
"v_exp_f32 v102, v102\n"
"v_mfma_f32_16x16x32_bf16 v[92:95], a[92:95], v[212:215], v[92:95]\n"
"s_waitcnt lgkmcnt(3)\n"
"ds_read_b128 a[92:95], v171 offset:61440\n"
"v_mfma_f32_16x16x32_bf16 v[64:67], a[64:67], v[200:203], v[64:67]\n"
"ds_read_b64_tr_b16 a[112:113], v172 offset:1024\n"
"v_add_u32 v168, 0x10000, v168\n"
"v_exp_f32 v103, v103\n"
"v_mfma_f32_16x16x32_bf16 v[72:75], a[68:71], v[200:203], v[72:75]\n"
"ds_read_b64_tr_b16 a[114:115], v172 offset:5120\n"
"v_add_u32 v169, 0x10000, v169\n"
"v_cvt_pk_bf16_f32 v148, v100, v101\n"
"v_mfma_f32_16x16x32_bf16 v[80:83], a[72:75], v[200:203], v[80:83]\n"
"ds_read_b64_tr_b16 a[116:117], v173 offset:1024\n"
"v_add_u32 v170, 0x10000, v170\n"
"v_exp_f32 v108, v108\n"
"v_mfma_f32_16x16x32_bf16 v[88:91], a[76:79], v[200:203], v[88:91]\n"
"ds_read_b64_tr_b16 a[118:119], v173 offset:5120\n"
"v_add_u32 v171, 0x10000, v171\n"
"v_exp_f32 v109, v109\n"
"v_mfma_f32_16x16x32_bf16 v[68:71], a[64:67], v[216:219], v[68:71]\n"
"ds_read_b64_tr_b16 a[120:121], v172 offset:1536\n"
"v_cvt_pk_bf16_f32 v149, v102, v103\n"
"v_exp_f32 v110, v110\n"
"v_mfma_f32_16x16x32_bf16 v[76:79], a[68:71], v[216:219], v[76:79]\n"
"ds_read_b64_tr_b16 a[122:123], v172 offset:5632\n"
"v_exp_f32 v111, v111\n"
"v_mfma_f32_16x16x32_bf16 v[84:87], a[72:75], v[216:219], v[84:87]\n"
"ds_read_b64_tr_b16 a[124:125], v173 offset:1536\n"
"v_cvt_pk_bf16_f32 v150, v108, v109\n"
"v_mfma_f32_16x16x32_bf16 v[92:95], a[76:79], v[216:219], v[92:95]\n"
"s_waitcnt lgkmcnt(4)\n"
"ds_read_b64_tr_b16 a[126:127], v173 offset:5632\n"
"v_exp_f32 v112, v112\n"
"v_mfma_f32_16x16x32_bf16 v[64:67], a[80:83], v[204:207], v[64:67]\n"
"v_exp_f32 v113, v113\n"
"v_mfma_f32_16x16x32_bf16 v[72:75], a[84:87], v[204:207], v[72:75]\n"
"v_cvt_pk_bf16_f32 v151, v110, v111\n"
"v_mfma_f32_16x16x32_bf16 v[80:83], a[88:91], v[204:207], v[80:83]\n"
"v_exp_f32 v114, v114\n"
"v_mfma_f32_16x16x32_bf16 v[88:91], a[92:95], v[204:207], v[88:91]\n"
"v_exp_f32 v115, v115\n"
"v_mfma_f32_16x16x32_bf16 v[68:71], a[80:83], v[220:223], v[68:71]\n"
"v_cvt_pk_bf16_f32 v152, v112, v113\n"
"v_mfma_f32_16x16x32_bf16 v[76:79], a[84:87], v[220:223], v[76:79]\n"
"v_exp_f32 v120, v120\n"
"v_mfma_f32_16x16x32_bf16 v[84:87], a[88:91], v[220:223], v[84:87]\n"
"v_exp_f32 v121, v121\n"
"v_mfma_f32_16x16x32_bf16 v[92:95], a[92:95], v[220:223], v[92:95]\n"
"v_cvt_pk_bf16_f32 v153, v114, v115\n"
"s_barrier\n"
"s_mov_b32 s61, 1\n"
".LBbar8:\n"
"v_mfma_f32_16x16x32_bf16 a[0:3], a[96:99], v[144:147], a[0:3]\n"
"ds_read_b128 a[64:67], v168 offset:16384\n"
"v_exp_f32 v122, v122\n"
"v_mfma_f32_16x16x32_bf16 a[8:11], a[100:103], v[144:147], a[8:11]\n"
"ds_read_b128 a[68:71], v168 offset:20480\n"
"v_mfma_f32_16x16x32_bf16 a[16:19], a[104:107], v[144:147], a[16:19]\n"
"ds_read_b128 a[72:75], v168 offset:24576\n"
"v_exp_f32 v123, v123\n"
"v_mfma_f32_16x16x32_bf16 a[24:27], a[108:111], v[144:147], a[24:27]\n"
"ds_read_b128 a[76:79], v168 offset:28672\n"
"v_cvt_pk_bf16_f32 v154, v120, v121\n"
"v_mfma_f32_16x16x32_bf16 a[4:7], a[96:99], v[148:151], a[4:7]\n"
"ds_read_b64_tr_b16 a[96:97], v172 offset:8192\n"
"v_exp_f32 v116, v116\n"
"v_mfma_f32_16x16x32_bf16 a[12:15], a[100:103], v[148:151], a[12:15]\n"
"ds_read_b64_tr_b16 a[98:99], v172 offset:12288\n"
"v_mfma_f32_16x16x32_bf16 a[20:23], a[104:107], v[148:151], a[20:23]\n"
"ds_read_b64_tr_b16 a[100:101], v173 offset:8192\n"
"v_exp_f32 v117, v117\n"
"v_mfma_f32_16x16x32_bf16 a[28:31], a[108:111], v[148:151], a[28:31]\n"
"ds_read_b64_tr_b16 a[102:103], v173 offset:12288\n"
"v_cvt_pk_bf16_f32 v155, v122, v123\n"
"v_mfma_f32_16x16x32_bf16 v[160:163], v[176:179], v[144:147], v[160:163]\n"
"ds_read_b64_tr_b16 a[104:105], v172 offset:8704\n"
"v_exp_f32 v118, v118\n"
"v_mfma_f32_16x16x32_bf16 a[32:35], a[112:115], v[144:147], a[32:35]\n"
"s_waitcnt lgkmcnt(5)\n"
"ds_read_b64_tr_b16 a[106:107], v172 offset:12800\n"
"v_mfma_f32_16x16x32_bf16 a[40:43], a[116:119], v[144:147], a[40:43]\n"
"ds_read_b64_tr_b16 a[108:109], v173 offset:8704\n"
"v_exp_f32 v119, v119\n"
"v_mfma_f32_16x16x32_bf16 a[48:51], a[120:123], v[144:147], a[48:51]\n"
"ds_read_b64_tr_b16 a[110:111], v173 offset:12800\n"
"v_cvt_pk_bf16_f32 v156, v116, v117\n"
"v_mfma_f32_16x16x32_bf16 a[56:59], a[124:127], v[144:147], a[56:59]\n"
"v_add_u32 v191, 98304, v174\n"
"v_exp_f32 v124, v124\n"
"v_mfma_f32_16x16x32_bf16 a[36:39], a[112:115], v[148:151], a[36:39]\n"
"s_waitcnt vmcnt(24)\n"
"ds_write_b128 v191, a[128:131] offset:0\n"
"v_exp_f32 v125, v125\n"
"v_mfma_f32_16x16x32_bf16 a[44:47], a[116:119], v[148:151], a[44:47]\n"
"ds_read_b64_tr_b16 a[112:113], v172 offset:9216\n"
"v_cvt_pk_bf16_f32 v157, v118, v119\n"
"v_mfma_f32_16x16x32_bf16 a[52:55], a[120:123], v[148:151], a[52:55]\n"
"ds_read_b64_tr_b16 a[114:115], v172 offset:13312\n"
"v_max3_f32 v188, v64, v65, v66\n"
"v_mfma_f32_16x16x32_bf16 a[60:63], a[124:127], v[148:151], a[60:63]\n"
"s_waitcnt vmcnt(23)\n"
"ds_write_b128 v191, a[132:135] offset:1024\n"
"v_exp_f32 v126, v126\n"
"v_mfma_f32_16x16x32_bf16 v[164:167], v[176:179], v[148:151], v[164:167]\n"
"s_waitcnt lgkmcnt(4)\n"
"ds_read_b64_tr_b16 a[116:117], v173 offset:9216\n"
"v_max3_f32 v189, v68, v69, v70\n"
"v_exp_f32 v127, v127\n"
"v_mfma_f32_16x16x32_bf16 a[0:3], a[96:99], v[152:155], a[0:3]\n"
"ds_read_b64_tr_b16 a[118:119], v173 offset:13312\n"
"v_cvt_pk_bf16_f32 v158, v124, v125\n"
"v_mfma_f32_16x16x32_bf16 a[8:11], a[100:103], v[152:155], a[8:11]\n"
"ds_read_b64_tr_b16 a[120:121], v172 offset:9728\n"
"v_cvt_pk_bf16_f32 v159, v126, v127\n"
"ds_read_b64_tr_b16 a[122:123], v172 offset:13824\n"
"v_mfma_f32_16x16x32_bf16 a[16:19], a[104:107], v[152:155], a[16:19]\n"
"s_waitcnt vmcnt(22)\n"
"ds_write_b128 v191, a[136:139] offset:2048\n"
"ds_read_b64_tr_b16 a[124:125], v173 offset:9728\n"
"ds_read_b64_tr_b16 a[126:127], v173 offset:13824\n"
"v_mfma_f32_16x16x32_bf16 a[24:27], a[108:111], v[152:155], a[24:27]\n"
"v_add_u32 v172, 0xfffe0000, v172\n"
"v_add_u32 v173, 0xfffe0000, v173\n"
"v_mfma_f32_16x16x32_bf16 a[4:7], a[96:99], v[156:159], a[4:7]\n"
"ds_read_b128 a[80:83], v169 offset:16384\n"
"v_max3_f32 v188, v188, v67, v72\n"
"v_mfma_f32_16x16x32_bf16 a[12:15], a[100:103], v[156:159], a[12:15]\n"
"ds_read_b128 a[84:87], v169 offset:20480\n"
"v_max3_f32 v189, v189, v71, v76\n"
"v_mfma_f32_16x16x32_bf16 a[20:23], a[104:107], v[156:159], a[20:23]\n"
"ds_read_b128 a[88:91], v169 offset:24576\n"
"v_max3_f32 v188, v188, v73, v74\n"
"v_mfma_f32_16x16x32_bf16 a[28:31], a[108:111], v[156:159], a[28:31]\n"
"ds_read_b128 a[92:95], v169 offset:28672\n"
"v_max3_f32 v189, v189, v77, v78\n"
"v_mfma_f32_16x16x32_bf16 v[160:163], v[176:179], v[152:155], v[160:163]\n"
"s_waitcnt vmcnt(21)\n"
"ds_write_b128 v191, a[140:143] offset:3072\n"
"s_waitcnt lgkmcnt(5)\n"
"v_mfma_f32_16x16x32_bf16 a[32:35], a[112:115], v[152:155], a[32:35]\n"
"ds_read_b64_tr_b16 a[96:97], v172 offset:49152\n"
"v_max3_f32 v188, v188, v75, v80\n"
"v_mfma_f32_16x16x32_bf16 a[40:43], a[116:119], v[152:155], a[40:43]\n"
"ds_read_b64_tr_b16 a[98:99], v172 offset:53248\n"
"v_max3_f32 v189, v189, v79, v84\n"
"v_mfma_f32_16x16x32_bf16 a[48:51], a[120:123], v[152:155], a[48:51]\n"
"ds_read_b64_tr_b16 a[100:101], v173 offset:49152\n"
"v_max3_f32 v188, v188, v81, v82\n"
"v_mfma_f32_16x16x32_bf16 a[56:59], a[124:127], v[152:155], a[56:59]\n"
"ds_read_b64_tr_b16 a[102:103], v173 offset:53248\n"
"v_max3_f32 v189, v189, v85, v86\n"
"v_mfma_f32_16x16x32_bf16 a[36:39], a[112:115], v[156:159], a[36:39]\n"
"ds_read_b64_tr_b16 a[104:105], v172 offset:49664\n"
"v_max3_f32 v188, v188, v83, v88\n"
"v_mfma_f32_16x16x32_bf16 a[44:47], a[116:119], v[156:159], a[44:47]\n"
"ds_read_b64_tr_b16 a[106:107], v172 offset:53760\n"
"v_max3_f32 v189, v189, v87, v92\n"
"v_mfma_f32_16x16x32_bf16 a[52:55], a[120:123], v[156:159], a[52:55]\n"
"ds_read_b64_tr_b16 a[108:109], v173 offset:49664\n"
"v_max3_f32 v188, v188, v89, v90\n"
"v_mfma_f32_16x16x32_bf16 a[60:63], a[124:127], v[156:159], a[60:63]\n"
"ds_read_b64_tr_b16 a[110:111], v173 offset:53760\n"
"v_max3_f32 v189, v189, v93, v94\n"
"v_mfma_f32_16x16x32_bf16 v[164:167], v[176:179], v[156:159], v[164:167]\n"
"v_max_f32 v188, v188, v91\n"
"v_max_f32 v189, v189, v95\n"
"s_nop 0\n"
"s_nop 0\n"
"v_permlane32_swap_b32 v188, v189\n"
"v_max_f32 v188, v188, v189\n"
"v_mov_b32 v189, v188\n"
"s_nop 0\n"
"s_nop 0\n"
"v_permlane16_swap_b32 v188, v189\n"
"v_max_f32 v188, v188, v189\n"
"v_mov_b32 v190, v188\n"
"s_nop 0\n"
"s_nop 0\n"
"v_permlane32_swap_b32 v188, v190\n"
"v_sub_f32 v128, 0, v188\n"
"v_sub_f32 v129, 0, v188\n"
"v_sub_f32 v130, 0, v188\n"
"v_sub_f32 v131, 0, v188\n"
"v_sub_f32 v64, v64, v188\n"
"v_sub_f32 v65, v65, v188\n"
"v_sub_f32 v66, v66, v188\n"
"v_sub_f32 v67, v67, v188\n"
"v_sub_f32 v72, v72, v188\n"
"v_sub_f32 v73, v73, v188\n"
"v_sub_f32 v74, v74, v188\n"
"v_sub_f32 v75, v75, v188\n"
"v_sub_f32 v80, v80, v188\n"
"v_sub_f32 v81, v81, v188\n"
"v_sub_f32 v82, v82, v188\n"
"v_sub_f32 v83, v83, v188\n"
"v_sub_f32 v88, v88, v188\n"
"v_sub_f32 v89, v89, v188\n"
"v_sub_f32 v90, v90, v188\n"
"v_sub_f32 v91, v91, v188\n"
"v_sub_f32 v132, 0, v190\n"
"v_sub_f32 v133, 0, v190\n"
"v_sub_f32 v134, 0, v190\n"
"v_sub_f32 v135, 0, v190\n"
"v_sub_f32 v68, v68, v190\n"
"v_sub_f32 v69, v69, v190\n"
"v_sub_f32 v70, v70, v190\n"
"v_sub_f32 v71, v71, v190\n"
"v_sub_f32 v76, v76, v190\n"
"v_sub_f32 v77, v77, v190\n"
"v_sub_f32 v78, v78, v190\n"
"v_sub_f32 v79, v79, v190\n"
"v_sub_f32 v84, v84, v190\n"
"v_sub_f32 v85, v85, v190\n"
"v_sub_f32 v86, v86, v190\n"
"v_sub_f32 v87, v87, v190\n"
"v_sub_f32 v92, v92, v190\n"
"v_sub_f32 v93, v93, v190\n"
"v_sub_f32 v94, v94, v190\n"
"v_sub_f32 v95, v95, v190\n"
"v_mfma_f32_16x16x32_bf16 v[96:99], a[64:67], v[192:195], v[128:131]\n"
"v_exp_f32 v64, v64\n"
"v_mfma_f32_16x16x32_bf16 v[104:107], a[68:71], v[192:195], v[128:131]\n"
"v_exp_f32 v65, v65\n"
"v_mfma_f32_16x16x32_bf16 v[112:115], a[72:75], v[192:195], v[128:131]\n"
"v_exp_f32 v66, v66\n"
"v_mfma_f32_16x16x32_bf16 v[120:123], a[76:79], v[192:195], v[128:131]\n"
"v_exp_f32 v67, v67\n"
"v_mfma_f32_16x16x32_bf16 v[100:103], a[64:67], v[208:211], v[132:135]\n"
"ds_read_b128 a[64:67], v170 offset:16384\n"
"v_cvt_pk_bf16_f32 v144, v64, v65\n"
"v_mfma_f32_16x16x32_bf16 v[108:111], a[68:71], v[208:211], v[132:135]\n"
"ds_read_b128 a[68:71], v170 offset:20480\n"
"v_exp_f32 v72, v72\n"
"v_mfma_f32_16x16x32_bf16 v[116:119], a[72:75], v[208:211], v[132:135]\n"
"ds_read_b128 a[72:75], v170 offset:24576\n"
"v_mfma_f32_16x16x32_bf16 v[124:127], a[76:79], v[208:211], v[132:135]\n"
"s_waitcnt lgkmcnt(3)\n"
"ds_read_b128 a[76:79], v170 offset:28672\n"
"v_exp_f32 v73, v73\n"
"v_mfma_f32_16x16x32_bf16 v[96:99], a[80:83], v[196:199], v[96:99]\n"
"s_add_u32 m0, s60, 114688\n"
"v_cvt_pk_bf16_f32 v145, v66, v67\n"
"v_exp_f32 v74, v74\n"
"v_mfma_f32_16x16x32_bf16 v[104:107], a[84:87], v[196:199], v[104:107]\n"
"buffer_load_dwordx4 v2, s[4:7], s67 offen offset:0 nt lds\n"
"v_mfma_f32_16x16x32_bf16 v[112:115], a[88:91], v[196:199], v[112:115]\n"
"v_exp_f32 v75, v75\n"
"v_mfma_f32_16x16x32_bf16 v[120:123], a[92:95], v[196:199], v[120:123]\n"
"v_cvt_pk_bf16_f32 v146, v72, v73\n"
"v_exp_f32 v68, v68\n"
"v_mfma_f32_16x16x32_bf16 v[100:103], a[80:83], v[212:215], v[100:103]\n"
"ds_read_b128 a[80:83], v171 offset:16384\n"
"v_mfma_f32_16x16x32_bf16 v[108:111], a[84:87], v[212:215], v[108:111]\n"
"ds_read_b128 a[84:87], v171 offset:20480\n"
"buffer_load_dwordx4 v3, s[4:7], s67 offen offset:1024 nt lds\n"
"v_exp_f32 v69, v69\n"
"v_mfma_f32_16x16x32_bf16 v[116:119], a[88:91], v[212:215], v[116:119]\n"
"ds_read_b128 a[88:91], v171 offset:24576\n"
"v_cvt_pk_bf16_f32 v147, v74, v75\n"
"v_exp_f32 v70, v70\n"
"v_mfma_f32_16x16x32_bf16 v[124:127], a[92:95], v[212:215], v[124:127]\n"
"s_waitcnt lgkmcnt(3)\n"
"ds_read_b128 a[92:95], v171 offset:28672\n"
"v_mfma_f32_16x16x32_bf16 v[96:99], a[64:67], v[200:203], v[96:99]\n"
"ds_read_b64_tr_b16 a[112:113], v172 offset:50176\n"
"v_add_u32 v168, 0xffff0000, v168\n"
"v_exp_f32 v71, v71\n"
"v_mfma_f32_16x16x32_bf16 v[104:107], a[68:71], v[200:203], v[104:107]\n"
"ds_read_b64_tr_b16 a[114:115], v172 offset:54272\n"
"buffer_load_dwordx4 v4, s[4:7], s67 offen offset:2048 nt lds\n"
"v_cvt_pk_bf16_f32 v148, v68, v69\n"
"v_mfma_f32_16x16x32_bf16 v[112:115], a[72:75], v[200:203], v[112:115]\n"
"ds_read_b64_tr_b16 a[116:117], v173 offset:50176\n"
"v_add_u32 v169, 0xffff0000, v169\n"
"v_exp_f32 v76, v76\n"
"v_mfma_f32_16x16x32_bf16 v[120:123], a[76:79], v[200:203], v[120:123]\n"
"ds_read_b64_tr_b16 a[118:119], v173 offset:54272\n"
"v_add_u32 v170, 0xffff0000, v170\n"
"v_exp_f32 v77, v77\n"
"v_mfma_f32_16x16x32_bf16 v[100:103], a[64:67], v[216:219], v[100:103]\n"
"ds_read_b64_tr_b16 a[120:121], v172 offset:50688\n"
"v_add_u32 v171, 0xffff0000, v171\n"
"v_cvt_pk_bf16_f32 v149, v70, v71\n"
"v_mfma_f32_16x16x32_bf16 v[108:111], a[68:71], v[216:219], v[108:111]\n"
"ds_read_b64_tr_b16 a[122:123], v172 offset:54784\n"
"buffer_load_dwordx4 v5, s[4:7], s67 offen offset:3072 nt lds\n"
"v_exp_f32 v78, v78\n"
"v_mfma_f32_16x16x32_bf16 v[116:119], a[72:75], v[216:219], v[116:119]\n"
"ds_read_b64_tr_b16 a[124:125], v173 offset:50688\n"
"v_exp_f32 v79, v79\n"
"v_mfma_f32_16x16x32_bf16 v[124:127], a[76:79], v[216:219], v[124:127]\n"
"s_waitcnt lgkmcnt(3)\n"
"ds_read_b64_tr_b16 a[126:127], v173 offset:54784\n"
"v_cvt_pk_bf16_f32 v150, v76, v77\n"
"v_exp_f32 v80, v80\n"
"v_mfma_f32_16x16x32_bf16 v[96:99], a[80:83], v[204:207], v[96:99]\n"
"v_exp_f32 v81, v81\n"
"v_mfma_f32_16x16x32_bf16 v[104:107], a[84:87], v[204:207], v[104:107]\n"
"v_cvt_pk_bf16_f32 v151, v78, v79\n"
"v_mfma_f32_16x16x32_bf16 v[112:115], a[88:91], v[204:207], v[112:115]\n"
"v_exp_f32 v82, v82\n"
"v_mfma_f32_16x16x32_bf16 v[120:123], a[92:95], v[204:207], v[120:123]\n"
"v_exp_f32 v83, v83\n"
"v_mfma_f32_16x16x32_bf16 v[100:103], a[80:83], v[220:223], v[100:103]\n"
"v_cvt_pk_bf16_f32 v152, v80, v81\n"
"v_mfma_f32_16x16x32_bf16 v[108:111], a[84:87], v[220:223], v[108:111]\n"
"v_exp_f32 v88, v88\n"
"v_mfma_f32_16x16x32_bf16 v[116:119], a[88:91], v[220:223], v[116:119]\n"
"v_exp_f32 v89, v89\n"
"v_mfma_f32_16x16x32_bf16 v[124:127], a[92:95], v[220:223], v[124:127]\n"
"v_cvt_pk_bf16_f32 v153, v82, v83\n"
"s_waitcnt vmcnt(20)\n"
"s_barrier\n"
".LBbar9:\n"
"v_mfma_f32_16x16x32_bf16 a[160:163], a[96:99], v[144:147], 0\n"
"ds_read_b128 a[64:67], v168 offset:32768\n"
"s_add_u32 m0, s60, 131072\n"
"v_exp_f32 v90, v90\n"
"v_mfma_f32_16x16x32_bf16 a[168:171], a[100:103], v[144:147], 0\n"
"ds_read_b128 a[68:71], v168 offset:36864\n"
"buffer_load_dwordx4 v2, s[8:11], s66 offen offset:0 nt lds\n"
"v_mfma_f32_16x16x32_bf16 a[176:179], a[104:107], v[144:147], 0\n"
"ds_read_b128 a[72:75], v168 offset:40960\n"
"v_exp_f32 v91, v91\n"
"v_mfma_f32_16x16x32_bf16 a[184:187], a[108:111], v[144:147], 0\n"
"ds_read_b128 a[76:79], v168 offset:45056\n"
"v_cvt_pk_bf16_f32 v154, v88, v89\n"
"v_mfma_f32_16x16x32_bf16 a[164:167], a[96:99], v[148:151], 0\n"
"ds_read_b64_tr_b16 a[96:97], v172 offset:57344\n"
"v_exp_f32 v84, v84\n"
"v_mfma_f32_16x16x32_bf16 a[172:175], a[100:103], v[148:151], 0\n"
"ds_read_b64_tr_b16 a[98:99], v172 offset:61440\n"
"buffer_load_dwordx4 v3, s[8:11], s66 offen offset:1024 nt lds\n"
"v_mfma_f32_16x16x32_bf16 a[180:183], a[104:107], v[148:151], 0\n"
"ds_read_b64_tr_b16 a[100:101], v173 offset:57344\n"
"v_exp_f32 v85, v85\n"
"v_mfma_f32_16x16x32_bf16 a[188:191], a[108:111], v[148:151], 0\n"
"ds_read_b64_tr_b16 a[102:103], v173 offset:61440\n"
"v_cvt_pk_bf16_f32 v155, v90, v91\n"
"v_mfma_f32_16x16x32_bf16 v[136:139], v[176:179], v[144:147], 0\n"
"ds_read_b64_tr_b16 a[104:105], v172 offset:57856\n"
"v_exp_f32 v86, v86\n"
"v_mfma_f32_16x16x32_bf16 a[192:195], a[112:115], v[144:147], 0\n"
"ds_read_b64_tr_b16 a[106:107], v172 offset:61952\n"
"buffer_load_dwordx4 v4, s[8:11], s66 offen offset:2048 nt lds\n"
"v_mfma_f32_16x16x32_bf16 a[200:203], a[116:119], v[144:147], 0\n"
"s_waitcnt lgkmcnt(4)\n"
"ds_read_b64_tr_b16 a[108:109], v173 offset:57856\n"
"v_exp_f32 v87, v87\n"
"v_mfma_f32_16x16x32_bf16 a[208:211], a[120:123], v[144:147], 0\n"
"ds_read_b64_tr_b16 a[110:111], v173 offset:61952\n"
"v_cvt_pk_bf16_f32 v156, v84, v85\n"
"v_mfma_f32_16x16x32_bf16 a[216:219], a[124:127], v[144:147], 0\n"
"v_exp_f32 v92, v92\n"
"v_mfma_f32_16x16x32_bf16 a[196:199], a[112:115], v[148:151], 0\n"
"ds_read_b64_tr_b16 a[112:113], v172 offset:58368\n"
"buffer_load_dwordx4 v5, s[8:11], s66 offen offset:3072 nt lds\n"
"v_exp_f32 v93, v93\n"
"v_mfma_f32_16x16x32_bf16 a[204:207], a[116:119], v[148:151], 0\n"
"ds_read_b64_tr_b16 a[114:115], v172 offset:62464\n"
"v_cvt_pk_bf16_f32 v157, v86, v87\n"
"v_mfma_f32_16x16x32_bf16 a[212:215], a[120:123], v[148:151], 0\n"
"ds_read_b64_tr_b16 a[116:117], v173 offset:58368\n"
"v_max3_f32 v188, v96, v97, v98\n"
"v_mfma_f32_16x16x32_bf16 a[220:223], a[124:127], v[148:151], 0\n"
"ds_read_b64_tr_b16 a[118:119], v173 offset:62464\n"
"v_max3_f32 v189, v100, v101, v102\n"
"v_exp_f32 v94, v94\n"
"v_mfma_f32_16x16x32_bf16 v[140:143], v[176:179], v[148:151], 0\n"
"ds_read_b64_tr_b16 a[120:121], v172 offset:58880\n"
"v_max3_f32 v188, v188, v99, v104\n"
"v_exp_f32 v95, v95\n"
"v_mfma_f32_16x16x32_bf16 a[160:163], a[96:99], v[152:155], a[160:163]\n"
"s_waitcnt lgkmcnt(4)\n"
"ds_read_b64_tr_b16 a[122:123], v172 offset:62976\n"
"v_cvt_pk_bf16_f32 v158, v92, v93\n"
"v_mfma_f32_16x16x32_bf16 a[168:171], a[100:103], v[152:155], a[168:171]\n"
"ds_read_b64_tr_b16 a[124:125], v173 offset:58880\n"
"v_cvt_pk_bf16_f32 v159, v94, v95\n"
"v_mfma_f32_16x16x32_bf16 a[176:179], a[104:107], v[152:155], a[176:179]\n"
"ds_read_b64_tr_b16 a[126:127], v173 offset:62976\n"
"v_max3_f32 v189, v189, v103, v108\n"
"v_mfma_f32_16x16x32_bf16 a[184:187], a[108:111], v[152:155], a[184:187]\n"
"v_max3_f32 v188, v188, v105, v106\n"
"v_max3_f32 v189, v189, v109, v110\n"
"v_mfma_f32_16x16x32_bf16 a[164:167], a[96:99], v[156:159], a[164:167]\n"
"ds_read_b128 a[80:83], v169 offset:32768\n"
"v_max3_f32 v188, v188, v107, v112\n"
"v_mfma_f32_16x16x32_bf16 a[172:175], a[100:103], v[156:159], a[172:175]\n"
"ds_read_b128 a[84:87], v169 offset:36864\n"
"v_max3_f32 v189, v189, v111, v116\n"
"v_mfma_f32_16x16x32_bf16 a[180:183], a[104:107], v[156:159], a[180:183]\n"
"ds_read_b128 a[88:91], v169 offset:40960\n"
"v_max3_f32 v188, v188, v113, v114\n"
"v_mfma_f32_16x16x32_bf16 a[188:191], a[108:111], v[156:159], a[188:191]\n"
"ds_read_b128 a[92:95], v169 offset:45056\n"
"v_max3_f32 v189, v189, v117, v118\n"
"v_mfma_f32_16x16x32_bf16 v[136:139], v[176:179], v[152:155], v[136:139]\n"
"s_waitcnt lgkmcnt(4)\n"
"ds_read_b64_tr_b16 a[96:97], v172 offset:0\n"
"v_max3_f32 v188, v188, v115, v120\n"
"v_mfma_f32_16x16x32_bf16 a[192:195], a[112:115], v[152:155], a[192:195]\n"
"ds_read_b64_tr_b16 a[98:99], v172 offset:4096\n"
"v_max3_f32 v189, v189, v119, v124\n"
"v_mfma_f32_16x16x32_bf16 a[200:203], a[116:119], v[152:155], a[200:203]\n"
"ds_read_b64_tr_b16 a[100:101], v173 offset:0\n"
"v_max3_f32 v188, v188, v121, v122\n"
"v_mfma_f32_16x16x32_bf16 a[208:211], a[120:123], v[152:155], a[208:211]\n"
"ds_read_b64_tr_b16 a[102:103], v173 offset:4096\n"
"v_max3_f32 v189, v189, v125, v126\n"
"v_mfma_f32_16x16x32_bf16 a[216:219], a[124:127], v[152:155], a[216:219]\n"
"ds_read_b64_tr_b16 a[104:105], v172 offset:512\n"
"v_max3_f32 v190, v188, v123, v127\n"
"v_mfma_f32_16x16x32_bf16 a[196:199], a[112:115], v[156:159], a[196:199]\n"
"ds_read_b64_tr_b16 a[106:107], v172 offset:4608\n"
"v_max_f32 v190, v190, v189\n"
"v_mfma_f32_16x16x32_bf16 a[204:207], a[116:119], v[156:159], a[204:207]\n"
"ds_read_b64_tr_b16 a[108:109], v173 offset:512\n"
"v_cmp_lt_f32 vcc, s59, v190\n"
"v_mfma_f32_16x16x32_bf16 a[212:215], a[120:123], v[156:159], a[212:215]\n"
"ds_read_b64_tr_b16 a[110:111], v173 offset:4608\n"
"v_mfma_f32_16x16x32_bf16 a[220:223], a[124:127], v[156:159], a[220:223]\n"
"v_rcp_f32 v6, v160\n"
"v_mfma_f32_16x16x32_bf16 v[140:143], v[176:179], v[156:159], v[140:143]\n"
"v_rcp_f32 v7, v164\n"
"s_cbranch_vccz .LBskip9\n"
"s_swappc_b64 s[70:71], s[74:75]\n"
".LBskip9:\n"
"v_mfma_f32_16x16x32_bf16 v[64:67], a[64:67], v[192:195], v[128:131]\n"
"v_exp_f32 v96, v96\n"
"v_mfma_f32_16x16x32_bf16 v[72:75], a[68:71], v[192:195], v[128:131]\n"
"v_exp_f32 v97, v97\n"
"v_mfma_f32_16x16x32_bf16 v[80:83], a[72:75], v[192:195], v[128:131]\n"
"v_exp_f32 v98, v98\n"
"v_mfma_f32_16x16x32_bf16 v[88:91], a[76:79], v[192:195], v[128:131]\n"
"v_exp_f32 v99, v99\n"
"v_mfma_f32_16x16x32_bf16 v[68:71], a[64:67], v[208:211], v[132:135]\n"
"ds_read_b128 a[64:67], v170 offset:32768\n"
"v_cvt_pk_bf16_f32 v144, v96, v97\n"
"v_mfma_f32_16x16x32_bf16 v[76:79], a[68:71], v[208:211], v[132:135]\n"
"ds_read_b128 a[68:71], v170 offset:36864\n"
"v_accvgpr_read_b32 v16, a0\n"
"v_exp_f32 v104, v104\n"
"v_mfma_f32_16x16x32_bf16 v[84:87], a[72:75], v[208:211], v[132:135]\n"
"ds_read_b128 a[72:75], v170 offset:40960\n"
"v_accvgpr_read_b32 v17, a1\n"
"v_mfma_f32_16x16x32_bf16 v[92:95], a[76:79], v[208:211], v[132:135]\n"
"s_waitcnt lgkmcnt(3)\n"
"ds_read_b128 a[76:79], v170 offset:45056\n"
"v_accvgpr_read_b32 v18, a2\n"
"v_exp_f32 v105, v105\n"
"v_mfma_f32_16x16x32_bf16 v[64:67], a[80:83], v[196:199], v[64:67]\n"
"s_add_u32 m0, s60, 147456\n"
"v_cvt_pk_bf16_f32 v145, v98, v99\n"
"v_exp_f32 v106, v106\n"
"v_mfma_f32_16x16x32_bf16 v[72:75], a[84:87], v[196:199], v[72:75]\n"
"buffer_load_dwordx4 v2, s[4:7], s68 offen offset:0 nt lds\n"
"v_accvgpr_read_b32 v19, a3\n"
"v_mfma_f32_16x16x32_bf16 v[80:83], a[88:91], v[196:199], v[80:83]\n"
"v_exp_f32 v107, v107\n"
"v_mfma_f32_16x16x32_bf16 v[88:91], a[92:95], v[196:199], v[88:91]\n"
"v_cvt_pk_bf16_f32 v146, v104, v105\n"
"v_accvgpr_read_b32 v20, a8\n"
"v_exp_f32 v100, v100\n"
"v_mfma_f32_16x16x32_bf16 v[68:71], a[80:83], v[212:215], v[68:71]\n"
"ds_read_b128 a[80:83], v171 offset:32768\n"
"v_accvgpr_read_b32 v21, a9\n"
"v_mfma_f32_16x16x32_bf16 v[76:79], a[84:87], v[212:215], v[76:79]\n"
"ds_read_b128 a[84:87], v171 offset:36864\n"
"buffer_load_dwordx4 v3, s[4:7], s68 offen offset:1024 nt lds\n"
"v_exp_f32 v101, v101\n"
"v_mfma_f32_16x16x32_bf16 v[84:87], a[88:91], v[212:215], v[84:87]\n"
"ds_read_b128 a[88:91], v171 offset:40960\n"
"v_cvt_pk_bf16_f32 v147, v106, v107\n"
"v_exp_f32 v102, v102\n"
"v_mfma_f32_16x16x32_bf16 v[92:95], a[92:95], v[212:215], v[92:95]\n"
"s_waitcnt lgkmcnt(3)\n"
"ds_read_b128 a[92:95], v171 offset:45056\n"
"v_accvgpr_read_b32 v22, a10\n"
"v_mfma_f32_16x16x32_bf16 v[64:67], a[64:67], v[200:203], v[64:67]\n"
"ds_read_b64_tr_b16 a[112:113], v172 offset:1024\n"
"v_add_u32 v168, 0xffff0000, v168\n"
"v_exp_f32 v103, v103\n"
"v_mfma_f32_16x16x32_bf16 v[72:75], a[68:71], v[200:203], v[72:75]\n"
"ds_read_b64_tr_b16 a[114:115], v172 offset:5120\n"
"buffer_load_dwordx4 v4, s[4:7], s68 offen offset:2048 nt lds\n"
"v_cvt_pk_bf16_f32 v148, v100, v101\n"
"v_mfma_f32_16x16x32_bf16 v[80:83], a[72:75], v[200:203], v[80:83]\n"
"ds_read_b64_tr_b16 a[116:117], v173 offset:1024\n"
"v_add_u32 v169, 0xffff0000, v169\n"
"v_exp_f32 v108, v108\n"
"v_mfma_f32_16x16x32_bf16 v[88:91], a[76:79], v[200:203], v[88:91]\n"
"ds_read_b64_tr_b16 a[118:119], v173 offset:5120\n"
"v_add_u32 v170, 0xffff0000, v170\n"
"v_exp_f32 v109, v109\n"
"v_mfma_f32_16x16x32_bf16 v[68:71], a[64:67], v[216:219], v[68:71]\n"
"ds_read_b64_tr_b16 a[120:121], v172 offset:1536\n"
"v_add_u32 v171, 0xffff0000, v171\n"
"v_cvt_pk_bf16_f32 v149, v102, v103\n"
"v_mfma_f32_16x16x32_bf16 v[76:79], a[68:71], v[216:219], v[76:79]\n"
"ds_read_b64_tr_b16 a[122:123], v172 offset:5632\n"
"buffer_load_dwordx4 v5, s[4:7], s68 offen offset:3072 nt lds\n"
"v_exp_f32 v110, v110\n"
"v_mfma_f32_16x16x32_bf16 v[84:87], a[72:75], v[216:219], v[84:87]\n"
"ds_read_b64_tr_b16 a[124:125], v173 offset:1536\n"
"v_accvgpr_read_b32 v23, a11\n"
"v_exp_f32 v111, v111\n"
"v_mfma_f32_16x16x32_bf16 v[92:95], a[76:79], v[216:219], v[92:95]\n"
"s_waitcnt lgkmcnt(3)\n"
"ds_read_b64_tr_b16 a[126:127], v173 offset:5632\n"
"v_cvt_pk_bf16_f32 v150, v108, v109\n"
"v_exp_f32 v112, v112\n"
"v_mfma_f32_16x16x32_bf16 v[64:67], a[80:83], v[204:207], v[64:67]\n"
"v_exp_f32 v113, v113\n"
"v_mfma_f32_16x16x32_bf16 v[72:75], a[84:87], v[204:207], v[72:75]\n"
"v_cvt_pk_bf16_f32 v151, v110, v111\n"
"v_mul_f32 v16, v16, v6\n"
"v_mfma_f32_16x16x32_bf16 v[80:83], a[88:91], v[204:207], v[80:83]\n"
"v_exp_f32 v114, v114\n"
"v_mfma_f32_16x16x32_bf16 v[88:91], a[92:95], v[204:207], v[88:91]\n"
"v_exp_f32 v115, v115\n"
"v_mfma_f32_16x16x32_bf16 v[68:71], a[80:83], v[220:223], v[68:71]\n"
"v_cvt_pk_bf16_f32 v152, v112, v113\n"
"v_mul_f32 v17, v17, v6\n"
"v_mfma_f32_16x16x32_bf16 v[76:79], a[84:87], v[220:223], v[76:79]\n"
"v_exp_f32 v120, v120\n"
"v_mfma_f32_16x16x32_bf16 v[84:87], a[88:91], v[220:223], v[84:87]\n"
"v_exp_f32 v121, v121\n"
"v_mfma_f32_16x16x32_bf16 v[92:95], a[92:95], v[220:223], v[92:95]\n"
"v_cvt_pk_bf16_f32 v153, v114, v115\n"
"v_mul_f32 v18, v18, v6\n"
"s_waitcnt vmcnt(20)\n"
"s_barrier\n"
".LBbar10:\n"
"v_mfma_f32_16x16x32_bf16 a[160:163], a[96:99], v[144:147], a[160:163]\n"
"ds_read_b128 a[64:67], v168 offset:16384\n"
"v_mul_f32 v19, v19, v6\n"
"v_exp_f32 v122, v122\n"
"v_mfma_f32_16x16x32_bf16 a[168:171], a[100:103], v[144:147], a[168:171]\n"
"ds_read_b128 a[68:71], v168 offset:20480\n"
"v_mul_f32 v20, v20, v6\n"
"v_mfma_f32_16x16x32_bf16 a[176:179], a[104:107], v[144:147], a[176:179]\n"
"ds_read_b128 a[72:75], v168 offset:24576\n"
"v_mul_f32 v21, v21, v6\n"
"v_exp_f32 v123, v123\n"
"v_mfma_f32_16x16x32_bf16 a[184:187], a[108:111], v[144:147], a[184:187]\n"
"ds_read_b128 a[76:79], v168 offset:28672\n"
"v_cvt_pk_bf16_f32 v154, v120, v121\n"
"v_mfma_f32_16x16x32_bf16 a[164:167], a[96:99], v[148:151], a[164:167]\n"
"ds_read_b64_tr_b16 a[96:97], v172 offset:8192\n"
"v_mul_f32 v22, v22, v6\n"
"v_exp_f32 v116, v116\n"
"v_mfma_f32_16x16x32_bf16 a[172:175], a[100:103], v[148:151], a[172:175]\n"
"ds_read_b64_tr_b16 a[98:99], v172 offset:12288\n"
"v_mul_f32 v23, v23, v6\n"
"v_mfma_f32_16x16x32_bf16 a[180:183], a[104:107], v[148:151], a[180:183]\n"
"ds_read_b64_tr_b16 a[100:101], v173 offset:8192\n"
"v_cvt_pk_bf16_f32 v32, v16, v17\n"
"v_exp_f32 v117, v117\n"
"v_mfma_f32_16x16x32_bf16 a[188:191], a[108:111], v[148:151], a[188:191]\n"
"ds_read_b64_tr_b16 a[102:103], v173 offset:12288\n"
"v_cvt_pk_bf16_f32 v155, v122, v123\n"
"v_mfma_f32_16x16x32_bf16 v[136:139], v[176:179], v[144:147], v[136:139]\n"
"ds_read_b64_tr_b16 a[104:105], v172 offset:8704\n"
"v_cvt_pk_bf16_f32 v33, v18, v19\n"
"v_exp_f32 v118, v118\n"
"v_mfma_f32_16x16x32_bf16 a[192:195], a[112:115], v[144:147], a[192:195]\n"
"ds_read_b64_tr_b16 a[106:107], v172 offset:12800\n"
"v_cvt_pk_bf16_f32 v34, v20, v21\n"
"v_mfma_f32_16x16x32_bf16 a[200:203], a[116:119], v[144:147], a[200:203]\n"
"s_waitcnt lgkmcnt(4)\n"
"ds_read_b64_tr_b16 a[108:109], v173 offset:8704\n"
"v_cvt_pk_bf16_f32 v35, v22, v23\n"
"v_exp_f32 v119, v119\n"
"v_mfma_f32_16x16x32_bf16 a[208:211], a[120:123], v[144:147], a[208:211]\n"
"ds_read_b64_tr_b16 a[110:111], v173 offset:12800\n"
"v_cvt_pk_bf16_f32 v156, v116, v117\n"
"v_mfma_f32_16x16x32_bf16 a[216:219], a[124:127], v[144:147], a[216:219]\n"
"v_exp_f32 v124, v124\n"
"v_mfma_f32_16x16x32_bf16 a[196:199], a[112:115], v[148:151], a[196:199]\n"
"ds_read_b64_tr_b16 a[112:113], v172 offset:9216\n"
"v_accvgpr_read_b32 v24, a4\n"
"v_exp_f32 v125, v125\n"
"v_mfma_f32_16x16x32_bf16 a[204:207], a[116:119], v[148:151], a[204:207]\n"
"ds_read_b64_tr_b16 a[114:115], v172 offset:13312\n"
"v_cvt_pk_bf16_f32 v157, v118, v119\n"
"v_mfma_f32_16x16x32_bf16 a[212:215], a[120:123], v[148:151], a[212:215]\n"
"ds_read_b64_tr_b16 a[116:117], v173 offset:9216\n"
"v_max3_f32 v188, v64, v65, v66\n"
"v_mfma_f32_16x16x32_bf16 a[220:223], a[124:127], v[148:151], a[220:223]\n"
"ds_read_b64_tr_b16 a[118:119], v173 offset:13312\n"
"v_max3_f32 v189, v68, v69, v70\n"
"v_exp_f32 v126, v126\n"
"v_mfma_f32_16x16x32_bf16 v[140:143], v[176:179], v[148:151], v[140:143]\n"
"ds_read_b64_tr_b16 a[120:121], v172 offset:9728\n"
"v_max3_f32 v188, v188, v67, v72\n"
"v_exp_f32 v127, v127\n"
"v_mfma_f32_16x16x32_bf16 a[160:163], a[96:99], v[152:155], a[160:163]\n"
"s_waitcnt lgkmcnt(4)\n"
"ds_read_b64_tr_b16 a[122:123], v172 offset:13824\n"
"v_cvt_pk_bf16_f32 v158, v124, v125\n"
"v_mfma_f32_16x16x32_bf16 a[168:171], a[100:103], v[152:155], a[168:171]\n"
"ds_read_b64_tr_b16 a[124:125], v173 offset:9728\n"
"v_cvt_pk_bf16_f32 v159, v126, v127\n"
"v_mfma_f32_16x16x32_bf16 a[176:179], a[104:107], v[152:155], a[176:179]\n"
"ds_read_b64_tr_b16 a[126:127], v173 offset:13824\n"
"v_max3_f32 v189, v189, v71, v76\n"
"v_mfma_f32_16x16x32_bf16 a[184:187], a[108:111], v[152:155], a[184:187]\n"
"v_max3_f32 v188, v188, v73, v74\n"
"v_max3_f32 v189, v189, v77, v78\n"
"v_mfma_f32_16x16x32_bf16 a[164:167], a[96:99], v[156:159], a[164:167]\n"
"ds_read_b128 a[80:83], v169 offset:16384\n"
"v_max3_f32 v188, v188, v75, v80\n"
"v_mfma_f32_16x16x32_bf16 a[172:175], a[100:103], v[156:159], a[172:175]\n"
"ds_read_b128 a[84:87], v169 offset:20480\n"
"v_max3_f32 v189, v189, v79, v84\n"
"v_mfma_f32_16x16x32_bf16 a[180:183], a[104:107], v[156:159], a[180:183]\n"
"ds_read_b128 a[88:91], v169 offset:24576\n"
"v_max3_f32 v188, v188, v81, v82\n"
"v_mfma_f32_16x16x32_bf16 a[188:191], a[108:111], v[156:159], a[188:191]\n"
"ds_read_b128 a[92:95], v169 offset:28672\n"
"v_max3_f32 v189, v189, v85, v86\n"
"v_mfma_f32_16x16x32_bf16 v[136:139], v[176:179], v[152:155], v[136:139]\n"
"s_waitcnt lgkmcnt(4)\n"
"ds_read_b64_tr_b16 a[96:97], v172 offset:32768\n"
"v_max3_f32 v188, v188, v83, v88\n"
"v_mfma_f32_16x16x32_bf16 a[192:195], a[112:115], v[152:155], a[192:195]\n"
"ds_read_b64_tr_b16 a[98:99], v172 offset:36864\n"
"v_max3_f32 v189, v189, v87, v92\n"
"v_mfma_f32_16x16x32_bf16 a[200:203], a[116:119], v[152:155], a[200:203]\n"
"ds_read_b64_tr_b16 a[100:101], v173 offset:32768\n"
"v_max3_f32 v188, v188, v89, v90\n"
"v_mfma_f32_16x16x32_bf16 a[208:211], a[120:123], v[152:155], a[208:211]\n"
"ds_read_b64_tr_b16 a[102:103], v173 offset:36864\n"
"v_max3_f32 v189, v189, v93, v94\n"
"v_mfma_f32_16x16x32_bf16 a[216:219], a[124:127], v[152:155], a[216:219]\n"
"ds_read_b64_tr_b16 a[104:105], v172 offset:33280\n"
"v_max3_f32 v190, v188, v91, v95\n"
"v_mfma_f32_16x16x32_bf16 a[196:199], a[112:115], v[156:159], a[196:199]\n"
"ds_read_b64_tr_b16 a[106:107], v172 offset:37376\n"
"v_max_f32 v190, v190, v189\n"
"v_mfma_f32_16x16x32_bf16 a[204:207], a[116:119], v[156:159], a[204:207]\n"
"ds_read_b64_tr_b16 a[108:109], v173 offset:33280\n"
"v_cmp_lt_f32 vcc, s59, v190\n"
"v_mfma_f32_16x16x32_bf16 a[212:215], a[120:123], v[156:159], a[212:215]\n"
"ds_read_b64_tr_b16 a[110:111], v173 offset:37376\n"
"v_accvgpr_read_b32 v25, a5\n"
"v_mfma_f32_16x16x32_bf16 a[220:223], a[124:127], v[156:159], a[220:223]\n"
"v_accvgpr_read_b32 v26, a6\n"
"v_accvgpr_read_b32 v27, a7\n"
"v_mfma_f32_16x16x32_bf16 v[140:143], v[176:179], v[156:159], v[140:143]\n"
"v_accvgpr_read_b32 v28, a12\n"
"v_accvgpr_read_b32 v29, a13\n"
"s_cbranch_vccz .LBskip10\n"
"s_swappc_b64 s[70:71], s[72:73]\n"
".LBskip10:\n"
"v_mfma_f32_16x16x32_bf16 v[96:99], a[64:67], v[192:195], v[128:131]\n"
"v_exp_f32 v64, v64\n"
"v_mfma_f32_16x16x32_bf16 v[104:107], a[68:71], v[192:195], v[128:131]\n"
"v_exp_f32 v65, v65\n"
"v_mfma_f32_16x16x32_bf16 v[112:115], a[72:75], v[192:195], v[128:131]\n"
"v_exp_f32 v66, v66\n"
"v_mfma_f32_16x16x32_bf16 v[120:123], a[76:79], v[192:195], v[128:131]\n"
"v_exp_f32 v67, v67\n"
"v_mfma_f32_16x16x32_bf16 v[100:103], a[64:67], v[208:211], v[132:135]\n"
"ds_read_b128 a[64:67], v170 offset:16384\n"
"v_cvt_pk_bf16_f32 v144, v64, v65\n"
"v_mfma_f32_16x16x32_bf16 v[108:111], a[68:71], v[208:211], v[132:135]\n"
"ds_read_b128 a[68:71], v170 offset:20480\n"
"v_accvgpr_read_b32 v30, a14\n"
"v_exp_f32 v72, v72\n"
"v_mfma_f32_16x16x32_bf16 v[116:119], a[72:75], v[208:211], v[132:135]\n"
"ds_read_b128 a[72:75], v170 offset:24576\n"
"v_accvgpr_read_b32 v31, a15\n"
"v_mfma_f32_16x16x32_bf16 v[124:127], a[76:79], v[208:211], v[132:135]\n"
"s_waitcnt lgkmcnt(3)\n"
"ds_read_b128 a[76:79], v170 offset:28672\n"
"v_mul_f32 v24, v24, v7\n"
"v_exp_f32 v73, v73\n"
"v_mfma_f32_16x16x32_bf16 v[96:99], a[80:83], v[196:199], v[96:99]\n"
"s_add_u32 m0, s60, 49152\n"
"v_cvt_pk_bf16_f32 v145, v66, v67\n"
"v_exp_f32 v74, v74\n"
"v_mfma_f32_16x16x32_bf16 v[104:107], a[84:87], v[196:199], v[104:107]\n"
"buffer_load_dwordx4 v2, s[8:11], s67 offen offset:0 nt lds\n"
"v_mul_f32 v25, v25, v7\n"
"v_mfma_f32_16x16x32_bf16 v[112:115], a[88:91], v[196:199], v[112:115]\n"
"v_exp_f32 v75, v75\n"
"v_mfma_f32_16x16x32_bf16 v[120:123], a[92:95], v[196:199], v[120:123]\n"
"v_cvt_pk_bf16_f32 v146, v72, v73\n"
"v_mul_f32 v26, v26, v7\n"
"v_exp_f32 v68, v68\n"
"v_mfma_f32_16x16x32_bf16 v[100:103], a[80:83], v[212:215], v[100:103]\n"
"ds_read_b128 a[80:83], v171 offset:16384\n"
"v_mul_f32 v27, v27, v7\n"
"v_mfma_f32_16x16x32_bf16 v[108:111], a[84:87], v[212:215], v[108:111]\n"
"ds_read_b128 a[84:87], v171 offset:20480\n"
"buffer_load_dwordx4 v3, s[8:11], s67 offen offset:1024 nt lds\n"
"v_exp_f32 v69, v69\n"
"v_mfma_f32_16x16x32_bf16 v[116:119], a[88:91], v[212:215], v[116:119]\n"
"ds_read_b128 a[88:91], v171 offset:24576\n"
"v_cvt_pk_bf16_f32 v147, v74, v75\n"
"v_exp_f32 v70, v70\n"
"v_mfma_f32_16x16x32_bf16 v[124:127], a[92:95], v[212:215], v[124:127]\n"
"s_waitcnt lgkmcnt(3)\n"
"ds_read_b128 a[92:95], v171 offset:28672\n"
"v_mul_f32 v28, v28, v7\n"
"v_mfma_f32_16x16x32_bf16 v[96:99], a[64:67], v[200:203], v[96:99]\n"
"ds_read_b64_tr_b16 a[112:113], v172 offset:33792\n"
"v_add_u32 v168, 0x10000, v168\n"
"v_exp_f32 v71, v71\n"
"v_mfma_f32_16x16x32_bf16 v[104:107], a[68:71], v[200:203], v[104:107]\n"
"ds_read_b64_tr_b16 a[114:115], v172 offset:37888\n"
"buffer_load_dwordx4 v4, s[8:11], s67 offen offset:2048 nt lds\n"
"v_cvt_pk_bf16_f32 v148, v68, v69\n"
"v_mfma_f32_16x16x32_bf16 v[112:115], a[72:75], v[200:203], v[112:115]\n"
"ds_read_b64_tr_b16 a[116:117], v173 offset:33792\n"
"v_add_u32 v169, 0x10000, v169\n"
"v_exp_f32 v76, v76\n"
"v_mfma_f32_16x16x32_bf16 v[120:123], a[76:79], v[200:203], v[120:123]\n"
"ds_read_b64_tr_b16 a[118:119], v173 offset:37888\n"
"v_add_u32 v170, 0x10000, v170\n"
"v_exp_f32 v77, v77\n"
"v_mfma_f32_16x16x32_bf16 v[100:103], a[64:67], v[216:219], v[100:103]\n"
"ds_read_b64_tr_b16 a[120:121], v172 offset:34304\n"
"v_add_u32 v171, 0x10000, v171\n"
"v_cvt_pk_bf16_f32 v149, v70, v71\n"
"v_mfma_f32_16x16x32_bf16 v[108:111], a[68:71], v[216:219], v[108:111]\n"
"ds_read_b64_tr_b16 a[122:123], v172 offset:38400\n"
"buffer_load_dwordx4 v5, s[8:11], s67 offen offset:3072 nt lds\n"
"v_exp_f32 v78, v78\n"
"v_mfma_f32_16x16x32_bf16 v[116:119], a[72:75], v[216:219], v[116:119]\n"
"ds_read_b64_tr_b16 a[124:125], v173 offset:34304\n"
"v_mul_f32 v29, v29, v7\n"
"v_exp_f32 v79, v79\n"
"v_mfma_f32_16x16x32_bf16 v[124:127], a[76:79], v[216:219], v[124:127]\n"
"s_waitcnt lgkmcnt(3)\n"
"ds_read_b64_tr_b16 a[126:127], v173 offset:38400\n"
"v_cvt_pk_bf16_f32 v150, v76, v77\n"
"v_exp_f32 v80, v80\n"
"v_mfma_f32_16x16x32_bf16 v[96:99], a[80:83], v[204:207], v[96:99]\n"
"v_exp_f32 v81, v81\n"
"v_mfma_f32_16x16x32_bf16 v[104:107], a[84:87], v[204:207], v[104:107]\n"
"v_cvt_pk_bf16_f32 v151, v78, v79\n"
"v_mul_f32 v30, v30, v7\n"
"v_mfma_f32_16x16x32_bf16 v[112:115], a[88:91], v[204:207], v[112:115]\n"
"v_exp_f32 v82, v82\n"
"v_mfma_f32_16x16x32_bf16 v[120:123], a[92:95], v[204:207], v[120:123]\n"
"v_exp_f32 v83, v83\n"
"v_mfma_f32_16x16x32_bf16 v[100:103], a[80:83], v[220:223], v[100:103]\n"
"v_cvt_pk_bf16_f32 v152, v80, v81\n"
"v_mul_f32 v31, v31, v7\n"
"v_mfma_f32_16x16x32_bf16 v[108:111], a[84:87], v[220:223], v[108:111]\n"
"v_exp_f32 v88, v88\n"
"v_mfma_f32_16x16x32_bf16 v[116:119], a[88:91], v[220:223], v[116:119]\n"
"v_exp_f32 v89, v89\n"
"v_mfma_f32_16x16x32_bf16 v[124:127], a[92:95], v[220:223], v[124:127]\n"
"v_cvt_pk_bf16_f32 v153, v82, v83\n"
"v_cvt_pk_bf16_f32 v36, v24, v25\n"
"s_waitcnt vmcnt(16)\n"
"s_barrier\n"
".LBbar11:\n"
"v_mfma_f32_16x16x32_bf16 a[160:163], a[96:99], v[144:147], a[160:163]\n"
"ds_read_b128 a[64:67], v168 offset:0\n"
"s_add_u32 m0, s60, 98304\n"
"v_exp_f32 v90, v90\n"
"v_mfma_f32_16x16x32_bf16 a[168:171], a[100:103], v[144:147], a[168:171]\n"
"ds_read_b128 a[68:71], v168 offset:4096\n"
"buffer_load_dwordx4 v2, s[4:7], s69 offen offset:0 nt lds\n"
"v_mfma_f32_16x16x32_bf16 a[176:179], a[104:107], v[144:147], a[176:179]\n"
"ds_read_b128 a[72:75], v168 offset:8192\n"
"v_cvt_pk_bf16_f32 v37, v26, v27\n"
"v_exp_f32 v91, v91\n"
"v_mfma_f32_16x16x32_bf16 a[184:187], a[108:111], v[144:147], a[184:187]\n"
"ds_read_b128 a[76:79], v168 offset:12288\n"
"v_cvt_pk_bf16_f32 v154, v88, v89\n"
"v_mfma_f32_16x16x32_bf16 a[164:167], a[96:99], v[148:151], a[164:167]\n"
"ds_read_b64_tr_b16 a[96:97], v172 offset:40960\n"
"v_cvt_pk_bf16_f32 v38, v28, v29\n"
"v_exp_f32 v84, v84\n"
"v_mfma_f32_16x16x32_bf16 a[172:175], a[100:103], v[148:151], a[172:175]\n"
"ds_read_b64_tr_b16 a[98:99], v172 offset:45056\n"
"buffer_load_dwordx4 v3, s[4:7], s69 offen offset:1024 nt lds\n"
"v_mfma_f32_16x16x32_bf16 a[180:183], a[104:107], v[148:151], a[180:183]\n"
"ds_read_b64_tr_b16 a[100:101], v173 offset:40960\n"
"v_cvt_pk_bf16_f32 v39, v30, v31\n"
"v_exp_f32 v85, v85\n"
"v_mfma_f32_16x16x32_bf16 a[188:191], a[108:111], v[148:151], a[188:191]\n"
"ds_read_b64_tr_b16 a[102:103], v173 offset:45056\n"
"v_cvt_pk_bf16_f32 v155, v90, v91\n"
"v_mfma_f32_16x16x32_bf16 v[136:139], v[176:179], v[144:147], v[136:139]\n"
"ds_read_b64_tr_b16 a[104:105], v172 offset:41472\n"
"v_accvgpr_read_b32 v16, a16\n"
"v_exp_f32 v86, v86\n"
"v_mfma_f32_16x16x32_bf16 a[192:195], a[112:115], v[144:147], a[192:195]\n"
"ds_read_b64_tr_b16 a[106:107], v172 offset:45568\n"
"buffer_load_dwordx4 v4, s[4:7], s69 offen offset:2048 nt lds\n"
"v_mfma_f32_16x16x32_bf16 a[200:203], a[116:119], v[144:147], a[200:203]\n"
"s_waitcnt lgkmcnt(4)\n"
"ds_read_b64_tr_b16 a[108:109], v173 offset:41472\n"
"v_accvgpr_read_b32 v17, a17\n"
"v_exp_f32 v87, v87\n"
"v_mfma_f32_16x16x32_bf16 a[208:211], a[120:123], v[144:147], a[208:211]\n"
"ds_read_b64_tr_b16 a[110:111], v173 offset:45568\n"
"v_cvt_pk_bf16_f32 v156, v84, v85\n"
"v_mfma_f32_16x16x32_bf16 a[216:219], a[124:127], v[144:147], a[216:219]\n"
"v_exp_f32 v92, v92\n"
"v_mfma_f32_16x16x32_bf16 a[196:199], a[112:115], v[148:151], a[196:199]\n"
"ds_read_b64_tr_b16 a[112:113], v172 offset:41984\n"
"buffer_load_dwordx4 v5, s[4:7], s69 offen offset:3072 nt lds\n"
"v_exp_f32 v93, v93\n"
"v_mfma_f32_16x16x32_bf16 a[204:207], a[116:119], v[148:151], a[204:207]\n"
"ds_read_b64_tr_b16 a[114:115], v172 offset:46080\n"
"v_cvt_pk_bf16_f32 v157, v86, v87\n"
"v_mfma_f32_16x16x32_bf16 a[212:215], a[120:123], v[148:151], a[212:215]\n"
"ds_read_b64_tr_b16 a[116:117], v173 offset:41984\n"
"v_max3_f32 v188, v96, v97, v98\n"
"v_mfma_f32_16x16x32_bf16 a[220:223], a[124:127], v[148:151], a[220:223]\n"
"ds_read_b64_tr_b16 a[118:119], v173 offset:46080\n"
"v_max3_f32 v189, v100, v101, v102\n"
"v_exp_f32 v94, v94\n"
"v_mfma_f32_16x16x32_bf16 v[140:143], v[176:179], v[148:151], v[140:143]\n"
"ds_read_b64_tr_b16 a[120:121], v172 offset:42496\n"
"v_max3_f32 v188, v188, v99, v104\n"
"v_exp_f32 v95, v95\n"
"v_mfma_f32_16x16x32_bf16 a[160:163], a[96:99], v[152:155], a[160:163]\n"
"s_waitcnt lgkmcnt(4)\n"
"ds_read_b64_tr_b16 a[122:123], v172 offset:46592\n"
"v_cvt_pk_bf16_f32 v158, v92, v93\n"
"v_mfma_f32_16x16x32_bf16 a[168:171], a[100:103], v[152:155], a[168:171]\n"
"ds_read_b64_tr_b16 a[124:125], v173 offset:42496\n"
"v_cvt_pk_bf16_f32 v159, v94, v95\n"
"v_mfma_f32_16x16x32_bf16 a[176:179], a[104:107], v[152:155], a[176:179]\n"
"ds_read_b64_tr_b16 a[126:127], v173 offset:46592\n"
"v_add_u32 v172, 0x10000, v172\n"
"v_mfma_f32_16x16x32_bf16 a[184:187], a[108:111], v[152:155], a[184:187]\n"
"v_add_u32 v173, 0x10000, v173\n"
"v_max3_f32 v189, v189, v103, v108\n"
"v_mfma_f32_16x16x32_bf16 a[164:167], a[96:99], v[156:159], a[164:167]\n"
"ds_read_b128 a[80:83], v169 offset:0\n"
"v_max3_f32 v188, v188, v105, v106\n"
"v_mfma_f32_16x16x32_bf16 a[172:175], a[100:103], v[156:159], a[172:175]\n"
"ds_read_b128 a[84:87], v169 offset:4096\n"
"v_max3_f32 v189, v189, v109, v110\n"
"v_mfma_f32_16x16x32_bf16 a[180:183], a[104:107], v[156:159], a[180:183]\n"
"ds_read_b128 a[88:91], v169 offset:8192\n"
"v_max3_f32 v188, v188, v107, v112\n"
"v_mfma_f32_16x16x32_bf16 a[188:191], a[108:111], v[156:159], a[188:191]\n"
"ds_read_b128 a[92:95], v169 offset:12288\n"
"v_max3_f32 v189, v189, v111, v116\n"
"v_mfma_f32_16x16x32_bf16 v[136:139], v[176:179], v[152:155], v[136:139]\n"
"s_waitcnt lgkmcnt(4)\n"
"ds_read_b64_tr_b16 a[96:97], v172 offset:16384\n"
"v_max3_f32 v188, v188, v113, v114\n"
"v_mfma_f32_16x16x32_bf16 a[192:195], a[112:115], v[152:155], a[192:195]\n"
"ds_read_b64_tr_b16 a[98:99], v172 offset:20480\n"
"v_max3_f32 v189, v189, v117, v118\n"
"v_mfma_f32_16x16x32_bf16 a[200:203], a[116:119], v[152:155], a[200:203]\n"
"ds_read_b64_tr_b16 a[100:101], v173 offset:16384\n"
"v_max3_f32 v188, v188, v115, v120\n"
"v_mfma_f32_16x16x32_bf16 a[208:211], a[120:123], v[152:155], a[208:211]\n"
"ds_read_b64_tr_b16 a[102:103], v173 offset:20480\n"
"v_max3_f32 v189, v189, v119, v124\n"
"v_mfma_f32_16x16x32_bf16 a[216:219], a[124:127], v[152:155], a[216:219]\n"
"ds_read_b64_tr_b16 a[104:105], v172 offset:16896\n"
"v_max3_f32 v188, v188, v121, v122\n"
"v_mfma_f32_16x16x32_bf16 a[196:199], a[112:115], v[156:159], a[196:199]\n"
"ds_read_b64_tr_b16 a[106:107], v172 offset:20992\n"
"v_max3_f32 v189, v189, v125, v126\n"
"v_mfma_f32_16x16x32_bf16 a[204:207], a[116:119], v[156:159], a[204:207]\n"
"ds_read_b64_tr_b16 a[108:109], v173 offset:16896\n"
"v_max3_f32 v190, v188, v123, v127\n"
"v_mfma_f32_16x16x32_bf16 a[212:215], a[120:123], v[156:159], a[212:215]\n"
"ds_read_b64_tr_b16 a[110:111], v173 offset:20992\n"
"v_max_f32 v190, v190, v189\n"
"v_mfma_f32_16x16x32_bf16 a[220:223], a[124:127], v[156:159], a[220:223]\n"
"v_cmp_lt_f32 vcc, s59, v190\n"
"v_accvgpr_read_b32 v18, a18\n"
"v_mfma_f32_16x16x32_bf16 v[140:143], v[176:179], v[156:159], v[140:143]\n"
"v_accvgpr_read_b32 v19, a19\n"
"v_accvgpr_read_b32 v20, a24\n"
"s_cbranch_vccz .LBskip11\n"
"s_swappc_b64 s[70:71], s[74:75]\n"
".LBskip11:\n"
"v_mfma_f32_16x16x32_bf16 v[64:67], a[64:67], v[192:195], v[128:131]\n"
"v_exp_f32 v96, v96\n"
"v_mfma_f32_16x16x32_bf16 v[72:75], a[68:71], v[192:195], v[128:131]\n"
"v_exp_f32 v97, v97\n"
"v_mfma_f32_16x16x32_bf16 v[80:83], a[72:75], v[192:195], v[128:131]\n"
"v_exp_f32 v98, v98\n"
"v_mfma_f32_16x16x32_bf16 v[88:91], a[76:79], v[192:195], v[128:131]\n"
"v_exp_f32 v99, v99\n"
"v_mfma_f32_16x16x32_bf16 v[68:71], a[64:67], v[208:211], v[132:135]\n"
"ds_read_b128 a[64:67], v170 offset:0\n"
"v_cvt_pk_bf16_f32 v144, v96, v97\n"
"v_mfma_f32_16x16x32_bf16 v[76:79], a[68:71], v[208:211], v[132:135]\n"
"ds_read_b128 a[68:71], v170 offset:4096\n"
"v_accvgpr_read_b32 v21, a25\n"
"v_exp_f32 v104, v104\n"
"v_mfma_f32_16x16x32_bf16 v[84:87], a[72:75], v[208:211], v[132:135]\n"
"ds_read_b128 a[72:75], v170 offset:8192\n"
"v_accvgpr_read_b32 v22, a26\n"
"v_mfma_f32_16x16x32_bf16 v[92:95], a[76:79], v[208:211], v[132:135]\n"
"s_waitcnt lgkmcnt(3)\n"
"ds_read_b128 a[76:79], v170 offset:12288\n"
"v_accvgpr_read_b32 v23, a27\n"
"v_exp_f32 v105, v105\n"
"v_mfma_f32_16x16x32_bf16 v[64:67], a[80:83], v[196:199], v[64:67]\n"
"v_cvt_pk_bf16_f32 v145, v98, v99\n"
"v_mul_f32 v16, v16, v6\n"
"v_exp_f32 v106, v106\n"
"v_mfma_f32_16x16x32_bf16 v[72:75], a[84:87], v[196:199], v[72:75]\n"
"v_exp_f32 v107, v107\n"
"v_mfma_f32_16x16x32_bf16 v[80:83], a[88:91], v[196:199], v[80:83]\n"
"v_cvt_pk_bf16_f32 v146, v104, v105\n"
"v_mul_f32 v17, v17, v6\n"
"v_mfma_f32_16x16x32_bf16 v[88:91], a[92:95], v[196:199], v[88:91]\n"
"v_exp_f32 v100, v100\n"
"v_mfma_f32_16x16x32_bf16 v[68:71], a[80:83], v[212:215], v[68:71]\n"
"ds_read_b128 a[80:83], v171 offset:0\n"
"v_mul_f32 v18, v18, v6\n"
"v_mfma_f32_16x16x32_bf16 v[76:79], a[84:87], v[212:215], v[76:79]\n"
"ds_read_b128 a[84:87], v171 offset:4096\n"
"v_mul_f32 v19, v19, v6\n"
"v_exp_f32 v101, v101\n"
"v_mfma_f32_16x16x32_bf16 v[84:87], a[88:91], v[212:215], v[84:87]\n"
"ds_read_b128 a[88:91], v171 offset:8192\n"
"v_cvt_pk_bf16_f32 v147, v106, v107\n"
"v_exp_f32 v102, v102\n"
"v_mfma_f32_16x16x32_bf16 v[92:95], a[92:95], v[212:215], v[92:95]\n"
"s_waitcnt lgkmcnt(3)\n"
"ds_read_b128 a[92:95], v171 offset:12288\n"
"v_mul_f32 v20, v20, v6\n"
"v_mfma_f32_16x16x32_bf16 v[64:67], a[64:67], v[200:203], v[64:67]\n"
"ds_read_b64_tr_b16 a[112:113], v172 offset:17408\n"
"v_mul_f32 v21, v21, v6\n"
"v_exp_f32 v103, v103\n"
"v_mfma_f32_16x16x32_bf16 v[72:75], a[68:71], v[200:203], v[72:75]\n"
"ds_read_b64_tr_b16 a[114:115], v172 offset:21504\n"
"v_cvt_pk_bf16_f32 v148, v100, v101\n"
"v_exp_f32 v108, v108\n"
"v_mfma_f32_16x16x32_bf16 v[80:83], a[72:75], v[200:203], v[80:83]\n"
"ds_read_b64_tr_b16 a[116:117], v173 offset:17408\n"
"v_mul_f32 v22, v22, v6\n"
"v_exp_f32 v109, v109\n"
"v_mfma_f32_16x16x32_bf16 v[88:91], a[76:79], v[200:203], v[88:91]\n"
"ds_read_b64_tr_b16 a[118:119], v173 offset:21504\n"
"v_cvt_pk_bf16_f32 v149, v102, v103\n"
"v_mfma_f32_16x16x32_bf16 v[68:71], a[64:67], v[216:219], v[68:71]\n"
"ds_read_b64_tr_b16 a[120:121], v172 offset:17920\n"
"v_mul_f32 v23, v23, v6\n"
"v_exp_f32 v110, v110\n"
"v_mfma_f32_16x16x32_bf16 v[76:79], a[68:71], v[216:219], v[76:79]\n"
"ds_read_b64_tr_b16 a[122:123], v172 offset:22016\n"
"v_cvt_pk_bf16_f32 v40, v16, v17\n"
"v_exp_f32 v111, v111\n"
"v_mfma_f32_16x16x32_bf16 v[84:87], a[72:75], v[216:219], v[84:87]\n"
"ds_read_b64_tr_b16 a[124:125], v173 offset:17920\n"
"v_cvt_pk_bf16_f32 v150, v108, v109\n"
"v_mfma_f32_16x16x32_bf16 v[92:95], a[76:79], v[216:219], v[92:95]\n"
"s_waitcnt lgkmcnt(4)\n"
"ds_read_b64_tr_b16 a[126:127], v173 offset:22016\n"
"v_cvt_pk_bf16_f32 v41, v18, v19\n"
"v_exp_f32 v112, v112\n"
"v_mfma_f32_16x16x32_bf16 v[64:67], a[80:83], v[204:207], v[64:67]\n"
"v_exp_f32 v113, v113\n"
"v_mfma_f32_16x16x32_bf16 v[72:75], a[84:87], v[204:207], v[72:75]\n"
"v_cvt_pk_bf16_f32 v151, v110, v111\n"
"v_cvt_pk_bf16_f32 v42, v20, v21\n"
"v_mfma_f32_16x16x32_bf16 v[80:83], a[88:91], v[204:207], v[80:83]\n"
"v_exp_f32 v114, v114\n"
"v_mfma_f32_16x16x32_bf16 v[88:91], a[92:95], v[204:207], v[88:91]\n"
"v_exp_f32 v115, v115\n"
"v_mfma_f32_16x16x32_bf16 v[68:71], a[80:83], v[220:223], v[68:71]\n"
"v_cvt_pk_bf16_f32 v152, v112, v113\n"
"v_cvt_pk_bf16_f32 v43, v22, v23\n"
"v_mfma_f32_16x16x32_bf16 v[76:79], a[84:87], v[220:223], v[76:79]\n"
"v_exp_f32 v120, v120\n"
"v_mfma_f32_16x16x32_bf16 v[84:87], a[88:91], v[220:223], v[84:87]\n"
"v_exp_f32 v121, v121\n"
"v_mfma_f32_16x16x32_bf16 v[92:95], a[92:95], v[220:223], v[92:95]\n"
"v_cvt_pk_bf16_f32 v153, v114, v115\n"
"v_accvgpr_read_b32 v24, a20\n"
"s_waitcnt vmcnt(12)\n"
"s_barrier\n"
".LBbar12:\n"
"v_mfma_f32_16x16x32_bf16 a[160:163], a[96:99], v[144:147], a[160:163]\n"
"ds_read_b128 a[64:67], v168 offset:49152\n"
"s_add_u32 m0, s60, 32768\n"
"v_exp_f32 v122, v122\n"
"v_mfma_f32_16x16x32_bf16 a[168:171], a[100:103], v[144:147], a[168:171]\n"
"ds_read_b128 a[68:71], v168 offset:53248\n"
"buffer_load_dwordx4 v2, s[8:11], s68 offen offset:0 nt lds\n"
"v_mfma_f32_16x16x32_bf16 a[176:179], a[104:107], v[144:147], a[176:179]\n"
"ds_read_b128 a[72:75], v168 offset:57344\n"
"v_accvgpr_read_b32 v25, a21\n"
"v_exp_f32 v123, v123\n"
"v_mfma_f32_16x16x32_bf16 a[184:187], a[108:111], v[144:147], a[184:187]\n"
"ds_read_b128 a[76:79], v168 offset:61440\n"
"v_cvt_pk_bf16_f32 v154, v120, v121\n"
"v_mfma_f32_16x16x32_bf16 a[164:167], a[96:99], v[148:151], a[164:167]\n"
"ds_read_b64_tr_b16 a[96:97], v172 offset:24576\n"
"v_accvgpr_read_b32 v26, a22\n"
"v_exp_f32 v116, v116\n"
"v_mfma_f32_16x16x32_bf16 a[172:175], a[100:103], v[148:151], a[172:175]\n"
"ds_read_b64_tr_b16 a[98:99], v172 offset:28672\n"
"buffer_load_dwordx4 v3, s[8:11], s68 offen offset:1024 nt lds\n"
"v_mfma_f32_16x16x32_bf16 a[180:183], a[104:107], v[148:151], a[180:183]\n"
"ds_read_b64_tr_b16 a[100:101], v173 offset:24576\n"
"v_accvgpr_read_b32 v27, a23\n"
"v_exp_f32 v117, v117\n"
"v_mfma_f32_16x16x32_bf16 a[188:191], a[108:111], v[148:151], a[188:191]\n"
"ds_read_b64_tr_b16 a[102:103], v173 offset:28672\n"
"v_cvt_pk_bf16_f32 v155, v122, v123\n"
"v_mfma_f32_16x16x32_bf16 v[136:139], v[176:179], v[144:147], v[136:139]\n"
"ds_read_b64_tr_b16 a[104:105], v172 offset:25088\n"
"v_accvgpr_read_b32 v28, a28\n"
"v_exp_f32 v118, v118\n"
"v_mfma_f32_16x16x32_bf16 a[192:195], a[112:115], v[144:147], a[192:195]\n"
"s_waitcnt lgkmcnt(4)\n"
"ds_read_b64_tr_b16 a[106:107], v172 offset:29184\n"
"buffer_load_dwordx4 v4, s[8:11], s68 offen offset:2048 nt lds\n"
"v_mfma_f32_16x16x32_bf16 a[200:203], a[116:119], v[144:147], a[200:203]\n"
"ds_read_b64_tr_b16 a[108:109], v173 offset:25088\n"
"v_accvgpr_read_b32 v29, a29\n"
"v_exp_f32 v119, v119\n"
"v_mfma_f32_16x16x32_bf16 a[208:211], a[120:123], v[144:147], a[208:211]\n"
"ds_read_b64_tr_b16 a[110:111], v173 offset:29184\n"
"v_cvt_pk_bf16_f32 v156, v116, v117\n"
"v_mfma_f32_16x16x32_bf16 a[216:219], a[124:127], v[144:147], a[216:219]\n"
"v_exp_f32 v124, v124\n"
"v_mfma_f32_16x16x32_bf16 a[196:199], a[112:115], v[148:151], a[196:199]\n"
"ds_read_b64_tr_b16 a[112:113], v172 offset:25600\n"
"buffer_load_dwordx4 v5, s[8:11], s68 offen offset:3072 nt lds\n"
"v_exp_f32 v125, v125\n"
"v_mfma_f32_16x16x32_bf16 a[204:207], a[116:119], v[148:151], a[204:207]\n"
"ds_read_b64_tr_b16 a[114:115], v172 offset:29696\n"
"v_cvt_pk_bf16_f32 v157, v118, v119\n"
"v_mfma_f32_16x16x32_bf16 a[212:215], a[120:123], v[148:151], a[212:215]\n"
"ds_read_b64_tr_b16 a[116:117], v173 offset:25600\n"
"v_max3_f32 v188, v64, v65, v66\n"
"v_mfma_f32_16x16x32_bf16 a[220:223], a[124:127], v[148:151], a[220:223]\n"
"ds_read_b64_tr_b16 a[118:119], v173 offset:29696\n"
"v_max3_f32 v189, v68, v69, v70\n"
"v_exp_f32 v126, v126\n"
"v_mfma_f32_16x16x32_bf16 v[140:143], v[176:179], v[148:151], v[140:143]\n"
"s_waitcnt lgkmcnt(4)\n"
"ds_read_b64_tr_b16 a[120:121], v172 offset:26112\n"
"v_max3_f32 v188, v188, v67, v72\n"
"v_exp_f32 v127, v127\n"
"v_mfma_f32_16x16x32_bf16 a[160:163], a[96:99], v[152:155], a[160:163]\n"
"ds_read_b64_tr_b16 a[122:123], v172 offset:30208\n"
"v_cvt_pk_bf16_f32 v158, v124, v125\n"
"v_mfma_f32_16x16x32_bf16 a[168:171], a[100:103], v[152:155], a[168:171]\n"
"ds_read_b64_tr_b16 a[124:125], v173 offset:26112\n"
"v_cvt_pk_bf16_f32 v159, v126, v127\n"
"v_mfma_f32_16x16x32_bf16 a[176:179], a[104:107], v[152:155], a[176:179]\n"
"ds_read_b64_tr_b16 a[126:127], v173 offset:30208\n"
"v_add_u32 v172, 0x10000, v172\n"
"v_mfma_f32_16x16x32_bf16 a[184:187], a[108:111], v[152:155], a[184:187]\n"
"v_add_u32 v173, 0x10000, v173\n"
"v_max3_f32 v189, v189, v71, v76\n"
"v_mfma_f32_16x16x32_bf16 a[164:167], a[96:99], v[156:159], a[164:167]\n"
"ds_read_b128 a[80:83], v169 offset:49152\n"
"v_max3_f32 v188, v188, v73, v74\n"
"v_mfma_f32_16x16x32_bf16 a[172:175], a[100:103], v[156:159], a[172:175]\n"
"ds_read_b128 a[84:87], v169 offset:53248\n"
"v_max3_f32 v189, v189, v77, v78\n"
"v_mfma_f32_16x16x32_bf16 a[180:183], a[104:107], v[156:159], a[180:183]\n"
"ds_read_b128 a[88:91], v169 offset:57344\n"
"v_max3_f32 v188, v188, v75, v80\n"
"v_mfma_f32_16x16x32_bf16 a[188:191], a[108:111], v[156:159], a[188:191]\n"
"ds_read_b128 a[92:95], v169 offset:61440\n"
"v_max3_f32 v189, v189, v79, v84\n"
"v_mfma_f32_16x16x32_bf16 v[136:139], v[176:179], v[152:155], v[136:139]\n"
"s_waitcnt lgkmcnt(4)\n"
"ds_read_b64_tr_b16 a[96:97], v172 offset:0\n"
"v_max3_f32 v188, v188, v81, v82\n"
"v_mfma_f32_16x16x32_bf16 a[192:195], a[112:115], v[152:155], a[192:195]\n"
"ds_read_b64_tr_b16 a[98:99], v172 offset:4096\n"
"v_max3_f32 v189, v189, v85, v86\n"
"v_mfma_f32_16x16x32_bf16 a[200:203], a[116:119], v[152:155], a[200:203]\n"
"ds_read_b64_tr_b16 a[100:101], v173 offset:0\n"
"v_max3_f32 v188, v188, v83, v88\n"
"v_mfma_f32_16x16x32_bf16 a[208:211], a[120:123], v[152:155], a[208:211]\n"
"ds_read_b64_tr_b16 a[102:103], v173 offset:4096\n"
"v_max3_f32 v189, v189, v87, v92\n"
"v_mfma_f32_16x16x32_bf16 a[216:219], a[124:127], v[152:155], a[216:219]\n"
"ds_read_b64_tr_b16 a[104:105], v172 offset:512\n"
"v_max3_f32 v188, v188, v89, v90\n"
"v_mfma_f32_16x16x32_bf16 a[196:199], a[112:115], v[156:159], a[196:199]\n"
"ds_read_b64_tr_b16 a[106:107], v172 offset:4608\n"
"v_max3_f32 v189, v189, v93, v94\n"
"v_mfma_f32_16x16x32_bf16 a[204:207], a[116:119], v[156:159], a[204:207]\n"
"ds_read_b64_tr_b16 a[108:109], v173 offset:512\n"
"v_max3_f32 v190, v188, v91, v95\n"
"v_mfma_f32_16x16x32_bf16 a[212:215], a[120:123], v[156:159], a[212:215]\n"
"ds_read_b64_tr_b16 a[110:111], v173 offset:4608\n"
"v_max_f32 v190, v190, v189\n"
"v_mfma_f32_16x16x32_bf16 a[220:223], a[124:127], v[156:159], a[220:223]\n"
"v_cmp_lt_f32 vcc, s59, v190\n"
"v_accvgpr_read_b32 v30, a30\n"
"v_mfma_f32_16x16x32_bf16 v[140:143], v[176:179], v[156:159], v[140:143]\n"
"v_accvgpr_read_b32 v31, a31\n"
"v_mul_f32 v24, v24, v7\n"
"s_cbranch_vccz .LBskip12\n"
"s_swappc_b64 s[70:71], s[72:73]\n"
".LBskip12:\n"
"v_mfma_f32_16x16x32_bf16 v[96:99], a[64:67], v[192:195], v[128:131]\n"
"v_exp_f32 v64, v64\n"
"v_mfma_f32_16x16x32_bf16 v[104:107], a[68:71], v[192:195], v[128:131]\n"
"v_exp_f32 v65, v65\n"
"v_mfma_f32_16x16x32_bf16 v[112:115], a[72:75], v[192:195], v[128:131]\n"
"v_exp_f32 v66, v66\n"
"v_mfma_f32_16x16x32_bf16 v[120:123], a[76:79], v[192:195], v[128:131]\n"
"v_exp_f32 v67, v67\n"
"v_mfma_f32_16x16x32_bf16 v[100:103], a[64:67], v[208:211], v[132:135]\n"
"ds_read_b128 a[64:67], v170 offset:49152\n"
"v_cvt_pk_bf16_f32 v144, v64, v65\n"
"v_mfma_f32_16x16x32_bf16 v[108:111], a[68:71], v[208:211], v[132:135]\n"
"ds_read_b128 a[68:71], v170 offset:53248\n"
"v_mul_f32 v25, v25, v7\n"
"v_exp_f32 v72, v72\n"
"v_mfma_f32_16x16x32_bf16 v[116:119], a[72:75], v[208:211], v[132:135]\n"
"ds_read_b128 a[72:75], v170 offset:57344\n"
"v_mul_f32 v26, v26, v7\n"
"v_mfma_f32_16x16x32_bf16 v[124:127], a[76:79], v[208:211], v[132:135]\n"
"s_waitcnt lgkmcnt(3)\n"
"ds_read_b128 a[76:79], v170 offset:61440\n"
"v_mul_f32 v27, v27, v7\n"
"v_exp_f32 v73, v73\n"
"v_mfma_f32_16x16x32_bf16 v[96:99], a[80:83], v[196:199], v[96:99]\n"
"s_add_u32 m0, s60, 65536\n"
"v_cvt_pk_bf16_f32 v145, v66, v67\n"
"v_exp_f32 v74, v74\n"
"v_mfma_f32_16x16x32_bf16 v[104:107], a[84:87], v[196:199], v[104:107]\n"
"buffer_load_dwordx4 v2, s[8:11], s69 offen offset:0 nt lds\n"
"v_mul_f32 v28, v28, v7\n"
"v_mfma_f32_16x16x32_bf16 v[112:115], a[88:91], v[196:199], v[112:115]\n"
"v_exp_f32 v75, v75\n"
"v_mfma_f32_16x16x32_bf16 v[120:123], a[92:95], v[196:199], v[120:123]\n"
"v_cvt_pk_bf16_f32 v146, v72, v73\n"
"v_mul_f32 v29, v29, v7\n"
"v_exp_f32 v68, v68\n"
"v_mfma_f32_16x16x32_bf16 v[100:103], a[80:83], v[212:215], v[100:103]\n"
"ds_read_b128 a[80:83], v171 offset:49152\n"
"v_mul_f32 v30, v30, v7\n"
"v_mfma_f32_16x16x32_bf16 v[108:111], a[84:87], v[212:215], v[108:111]\n"
"ds_read_b128 a[84:87], v171 offset:53248\n"
"buffer_load_dwordx4 v3, s[8:11], s69 offen offset:1024 nt lds\n"
"v_exp_f32 v69, v69\n"
"v_mfma_f32_16x16x32_bf16 v[116:119], a[88:91], v[212:215], v[116:119]\n"
"ds_read_b128 a[88:91], v171 offset:57344\n"
"v_cvt_pk_bf16_f32 v147, v74, v75\n"
"v_exp_f32 v70, v70\n"
"v_mfma_f32_16x16x32_bf16 v[124:127], a[92:95], v[212:215], v[124:127]\n"
"s_waitcnt lgkmcnt(3)\n"
"ds_read_b128 a[92:95], v171 offset:61440\n"
"v_mul_f32 v31, v31, v7\n"
"v_mfma_f32_16x16x32_bf16 v[96:99], a[64:67], v[200:203], v[96:99]\n"
"ds_read_b64_tr_b16 a[112:113], v172 offset:1024\n"
"v_add_u32 v168, 0x10000, v168\n"
"v_exp_f32 v71, v71\n"
"v_mfma_f32_16x16x32_bf16 v[104:107], a[68:71], v[200:203], v[104:107]\n"
"ds_read_b64_tr_b16 a[114:115], v172 offset:5120\n"
"buffer_load_dwordx4 v4, s[8:11], s69 offen offset:2048 nt lds\n"
"v_cvt_pk_bf16_f32 v148, v68, v69\n"
"v_mfma_f32_16x16x32_bf16 v[112:115], a[72:75], v[200:203], v[112:115]\n"
"ds_read_b64_tr_b16 a[116:117], v173 offset:1024\n"
"v_add_u32 v169, 0x10000, v169\n"
"v_exp_f32 v76, v76\n"
"v_mfma_f32_16x16x32_bf16 v[120:123], a[76:79], v[200:203], v[120:123]\n"
"ds_read_b64_tr_b16 a[118:119], v173 offset:5120\n"
"v_add_u32 v170, 0x10000, v170\n"
"v_exp_f32 v77, v77\n"
"v_mfma_f32_16x16x32_bf16 v[100:103], a[64:67], v[216:219], v[100:103]\n"
"ds_read_b64_tr_b16 a[120:121], v172 offset:1536\n"
"v_add_u32 v171, 0x10000, v171\n"
"v_cvt_pk_bf16_f32 v149, v70, v71\n"
"v_mfma_f32_16x16x32_bf16 v[108:111], a[68:71], v[216:219], v[108:111]\n"
"ds_read_b64_tr_b16 a[122:123], v172 offset:5632\n"
"buffer_load_dwordx4 v5, s[8:11], s69 offen offset:3072 nt lds\n"
"v_exp_f32 v78, v78\n"
"v_mfma_f32_16x16x32_bf16 v[116:119], a[72:75], v[216:219], v[116:119]\n"
"ds_read_b64_tr_b16 a[124:125], v173 offset:1536\n"
"v_cvt_pk_bf16_f32 v44, v24, v25\n"
"v_exp_f32 v79, v79\n"
"v_mfma_f32_16x16x32_bf16 v[124:127], a[76:79], v[216:219], v[124:127]\n"
"s_waitcnt lgkmcnt(3)\n"
"ds_read_b64_tr_b16 a[126:127], v173 offset:5632\n"
"v_cvt_pk_bf16_f32 v150, v76, v77\n"
"v_exp_f32 v80, v80\n"
"v_mfma_f32_16x16x32_bf16 v[96:99], a[80:83], v[204:207], v[96:99]\n"
"v_exp_f32 v81, v81\n"
"v_mfma_f32_16x16x32_bf16 v[104:107], a[84:87], v[204:207], v[104:107]\n"
"v_cvt_pk_bf16_f32 v151, v78, v79\n"
"v_cvt_pk_bf16_f32 v45, v26, v27\n"
"v_mfma_f32_16x16x32_bf16 v[112:115], a[88:91], v[204:207], v[112:115]\n"
"v_exp_f32 v82, v82\n"
"v_mfma_f32_16x16x32_bf16 v[120:123], a[92:95], v[204:207], v[120:123]\n"
"v_exp_f32 v83, v83\n"
"v_mfma_f32_16x16x32_bf16 v[100:103], a[80:83], v[220:223], v[100:103]\n"
"v_cvt_pk_bf16_f32 v152, v80, v81\n"
"v_cvt_pk_bf16_f32 v46, v28, v29\n"
"v_mfma_f32_16x16x32_bf16 v[108:111], a[84:87], v[220:223], v[108:111]\n"
"v_exp_f32 v88, v88\n"
"v_mfma_f32_16x16x32_bf16 v[116:119], a[88:91], v[220:223], v[116:119]\n"
"v_exp_f32 v89, v89\n"
"v_mfma_f32_16x16x32_bf16 v[124:127], a[92:95], v[220:223], v[124:127]\n"
"v_cvt_pk_bf16_f32 v153, v82, v83\n"
"v_cvt_pk_bf16_f32 v47, v30, v31\n"
"s_waitcnt vmcnt(12)\n"
"s_barrier\n"
".LBbar13:\n"
"v_mfma_f32_16x16x32_bf16 a[160:163], a[96:99], v[144:147], a[160:163]\n"
"ds_read_b128 a[64:67], v168 offset:16384\n"
"v_accvgpr_read_b32 v16, a32\n"
"v_exp_f32 v90, v90\n"
"v_mfma_f32_16x16x32_bf16 a[168:171], a[100:103], v[144:147], a[168:171]\n"
"ds_read_b128 a[68:71], v168 offset:20480\n"
"v_accvgpr_read_b32 v17, a33\n"
"v_mfma_f32_16x16x32_bf16 a[176:179], a[104:107], v[144:147], a[176:179]\n"
"ds_read_b128 a[72:75], v168 offset:24576\n"
"v_accvgpr_read_b32 v18, a34\n"
"v_exp_f32 v91, v91\n"
"v_mfma_f32_16x16x32_bf16 a[184:187], a[108:111], v[144:147], a[184:187]\n"
"ds_read_b128 a[76:79], v168 offset:28672\n"
"v_cvt_pk_bf16_f32 v154, v88, v89\n"
"v_mfma_f32_16x16x32_bf16 a[164:167], a[96:99], v[148:151], a[164:167]\n"
"ds_read_b64_tr_b16 a[96:97], v172 offset:8192\n"
"v_accvgpr_read_b32 v19, a35\n"
"v_exp_f32 v84, v84\n"
"v_mfma_f32_16x16x32_bf16 a[172:175], a[100:103], v[148:151], a[172:175]\n"
"ds_read_b64_tr_b16 a[98:99], v172 offset:12288\n"
"v_accvgpr_read_b32 v20, a40\n"
"v_mfma_f32_16x16x32_bf16 a[180:183], a[104:107], v[148:151], a[180:183]\n"
"ds_read_b64_tr_b16 a[100:101], v173 offset:8192\n"
"v_accvgpr_read_b32 v21, a41\n"
"v_exp_f32 v85, v85\n"
"v_mfma_f32_16x16x32_bf16 a[188:191], a[108:111], v[148:151], a[188:191]\n"
"ds_read_b64_tr_b16 a[102:103], v173 offset:12288\n"
"v_cvt_pk_bf16_f32 v155, v90, v91\n"
"v_mfma_f32_16x16x32_bf16 v[136:139], v[176:179], v[144:147], v[136:139]\n"
"ds_read_b64_tr_b16 a[104:105], v172 offset:8704\n"
"v_accvgpr_read_b32 v22, a42\n"
"v_exp_f32 v86, v86\n"
"v_mfma_f32_16x16x32_bf16 a[192:195], a[112:115], v[144:147], a[192:195]\n"
"ds_read_b64_tr_b16 a[106:107], v172 offset:12800\n"
"v_accvgpr_read_b32 v23, a43\n"
"v_mfma_f32_16x16x32_bf16 a[200:203], a[116:119], v[144:147], a[200:203]\n"
"s_waitcnt lgkmcnt(4)\n"
"ds_read_b64_tr_b16 a[108:109], v173 offset:8704\n"
"v_mul_f32 v16, v16, v6\n"
"v_exp_f32 v87, v87\n"
"v_mfma_f32_16x16x32_bf16 a[208:211], a[120:123], v[144:147], a[208:211]\n"
"ds_read_b64_tr_b16 a[110:111], v173 offset:12800\n"
"v_cvt_pk_bf16_f32 v156, v84, v85\n"
"v_mfma_f32_16x16x32_bf16 a[216:219], a[124:127], v[144:147], a[216:219]\n"
"v_exp_f32 v92, v92\n"
"v_mfma_f32_16x16x32_bf16 a[196:199], a[112:115], v[148:151], a[196:199]\n"
"ds_read_b64_tr_b16 a[112:113], v172 offset:9216\n"
"v_mul_f32 v17, v17, v6\n"
"v_exp_f32 v93, v93\n"
"v_mfma_f32_16x16x32_bf16 a[204:207], a[116:119], v[148:151], a[204:207]\n"
"ds_read_b64_tr_b16 a[114:115], v172 offset:13312\n"
"v_cvt_pk_bf16_f32 v157, v86, v87\n"
"v_mfma_f32_16x16x32_bf16 a[212:215], a[120:123], v[148:151], a[212:215]\n"
"ds_read_b64_tr_b16 a[116:117], v173 offset:9216\n"
"v_max3_f32 v188, v96, v97, v98\n"
"v_mfma_f32_16x16x32_bf16 a[220:223], a[124:127], v[148:151], a[220:223]\n"
"ds_read_b64_tr_b16 a[118:119], v173 offset:13312\n"
"v_max3_f32 v189, v100, v101, v102\n"
"v_exp_f32 v94, v94\n"
"v_mfma_f32_16x16x32_bf16 v[140:143], v[176:179], v[148:151], v[140:143]\n"
"ds_read_b64_tr_b16 a[120:121], v172 offset:9728\n"
"v_max3_f32 v188, v188, v99, v104\n"
"v_exp_f32 v95, v95\n"
"v_mfma_f32_16x16x32_bf16 a[160:163], a[96:99], v[152:155], a[160:163]\n"
"s_waitcnt lgkmcnt(4)\n"
"ds_read_b64_tr_b16 a[122:123], v172 offset:13824\n"
"v_cvt_pk_bf16_f32 v158, v92, v93\n"
"v_mfma_f32_16x16x32_bf16 a[168:171], a[100:103], v[152:155], a[168:171]\n"
"ds_read_b64_tr_b16 a[124:125], v173 offset:9728\n"
"v_cvt_pk_bf16_f32 v159, v94, v95\n"
"v_mfma_f32_16x16x32_bf16 a[176:179], a[104:107], v[152:155], a[176:179]\n"
"ds_read_b64_tr_b16 a[126:127], v173 offset:13824\n"
"v_add_u32 v172, 0xfffe0000, v172\n"
"v_mfma_f32_16x16x32_bf16 a[184:187], a[108:111], v[152:155], a[184:187]\n"
"v_add_u32 v173, 0xfffe0000, v173\n"
"v_max3_f32 v189, v189, v103, v108\n"
"v_mfma_f32_16x16x32_bf16 a[164:167], a[96:99], v[156:159], a[164:167]\n"
"ds_read_b128 a[80:83], v169 offset:16384\n"
"v_max3_f32 v188, v188, v105, v106\n"
"v_mfma_f32_16x16x32_bf16 a[172:175], a[100:103], v[156:159], a[172:175]\n"
"ds_read_b128 a[84:87], v169 offset:20480\n"
"v_max3_f32 v189, v189, v109, v110\n"
"v_mfma_f32_16x16x32_bf16 a[180:183], a[104:107], v[156:159], a[180:183]\n"
"ds_read_b128 a[88:91], v169 offset:24576\n"
"v_max3_f32 v188, v188, v107, v112\n"
"v_mfma_f32_16x16x32_bf16 a[188:191], a[108:111], v[156:159], a[188:191]\n"
"ds_read_b128 a[92:95], v169 offset:28672\n"
"v_max3_f32 v189, v189, v111, v116\n"
"v_mfma_f32_16x16x32_bf16 v[136:139], v[176:179], v[152:155], v[136:139]\n"
"s_waitcnt lgkmcnt(4)\n"
"ds_read_b64_tr_b16 a[96:97], v172 offset:49152\n"
"v_max3_f32 v188, v188, v113, v114\n"
"v_mfma_f32_16x16x32_bf16 a[192:195], a[112:115], v[152:155], a[192:195]\n"
"ds_read_b64_tr_b16 a[98:99], v172 offset:53248\n"
"v_max3_f32 v189, v189, v117, v118\n"
"v_mfma_f32_16x16x32_bf16 a[200:203], a[116:119], v[152:155], a[200:203]\n"
"ds_read_b64_tr_b16 a[100:101], v173 offset:49152\n"
"v_max3_f32 v188, v188, v115, v120\n"
"v_mfma_f32_16x16x32_bf16 a[208:211], a[120:123], v[152:155], a[208:211]\n"
"ds_read_b64_tr_b16 a[102:103], v173 offset:53248\n"
"v_max3_f32 v189, v189, v119, v124\n"
"v_mfma_f32_16x16x32_bf16 a[216:219], a[124:127], v[152:155], a[216:219]\n"
"ds_read_b64_tr_b16 a[104:105], v172 offset:49664\n"
"v_max3_f32 v188, v188, v121, v122\n"
"v_mfma_f32_16x16x32_bf16 a[196:199], a[112:115], v[156:159], a[196:199]\n"
"ds_read_b64_tr_b16 a[106:107], v172 offset:53760\n"
"v_max3_f32 v189, v189, v125, v126\n"
"v_mfma_f32_16x16x32_bf16 a[204:207], a[116:119], v[156:159], a[204:207]\n"
"ds_read_b64_tr_b16 a[108:109], v173 offset:49664\n"
"v_max3_f32 v190, v188, v123, v127\n"
"v_mfma_f32_16x16x32_bf16 a[212:215], a[120:123], v[156:159], a[212:215]\n"
"ds_read_b64_tr_b16 a[110:111], v173 offset:53760\n"
"v_max_f32 v190, v190, v189\n"
"v_mfma_f32_16x16x32_bf16 a[220:223], a[124:127], v[156:159], a[220:223]\n"
"v_cmp_lt_f32 vcc, s59, v190\n"
"v_mul_f32 v18, v18, v6\n"
"v_mfma_f32_16x16x32_bf16 v[140:143], v[176:179], v[156:159], v[140:143]\n"
"v_mul_f32 v19, v19, v6\n"
"v_mul_f32 v20, v20, v6\n"
"s_cbranch_vccz .LBskip13\n"
"s_swappc_b64 s[70:71], s[74:75]\n"
".LBskip13:\n"
"v_mfma_f32_16x16x32_bf16 v[64:67], a[64:67], v[192:195], v[128:131]\n"
"v_exp_f32 v96, v96\n"
"v_mfma_f32_16x16x32_bf16 v[72:75], a[68:71], v[192:195], v[128:131]\n"
"v_exp_f32 v97, v97\n"
"v_mfma_f32_16x16x32_bf16 v[80:83], a[72:75], v[192:195], v[128:131]\n"
"v_exp_f32 v98, v98\n"
"v_mfma_f32_16x16x32_bf16 v[88:91], a[76:79], v[192:195], v[128:131]\n"
"v_exp_f32 v99, v99\n"
"v_mfma_f32_16x16x32_bf16 v[68:71], a[64:67], v[208:211], v[132:135]\n"
"ds_read_b128 a[64:67], v170 offset:16384\n"
"v_cvt_pk_bf16_f32 v144, v96, v97\n"
"v_mfma_f32_16x16x32_bf16 v[76:79], a[68:71], v[208:211], v[132:135]\n"
"ds_read_b128 a[68:71], v170 offset:20480\n"
"v_mul_f32 v21, v21, v6\n"
"v_exp_f32 v104, v104\n"
"v_mfma_f32_16x16x32_bf16 v[84:87], a[72:75], v[208:211], v[132:135]\n"
"ds_read_b128 a[72:75], v170 offset:24576\n"
"v_mul_f32 v22, v22, v6\n"
"v_mfma_f32_16x16x32_bf16 v[92:95], a[76:79], v[208:211], v[132:135]\n"
"s_waitcnt lgkmcnt(3)\n"
"ds_read_b128 a[76:79], v170 offset:28672\n"
"v_mul_f32 v23, v23, v6\n"
"v_exp_f32 v105, v105\n"
"v_mfma_f32_16x16x32_bf16 v[64:67], a[80:83], v[196:199], v[64:67]\n"
"s_add_u32 s84, s51, 0\n"
"s_and_b32 s84, s84, 15\n"
"v_cvt_pk_bf16_f32 v145, v98, v99\n"
"v_mfma_f32_16x16x32_bf16 v[72:75], a[84:87], v[196:199], v[72:75]\n"
"s_lshl_b32 s84, s84, 15\n"
"v_cvt_pk_bf16_f32 v48, v16, v17\n"
"v_exp_f32 v106, v106\n"
"v_mfma_f32_16x16x32_bf16 v[80:83], a[88:91], v[196:199], v[80:83]\n"
"buffer_load_dwordx4 v[224:227], v13, s[36:39], s84 offen offset:0\n"
"v_cvt_pk_bf16_f32 v49, v18, v19\n"
"v_exp_f32 v107, v107\n"
"v_mfma_f32_16x16x32_bf16 v[88:91], a[92:95], v[196:199], v[88:91]\n"
"v_cvt_pk_bf16_f32 v146, v104, v105\n"
"v_cvt_pk_bf16_f32 v50, v20, v21\n"
"v_exp_f32 v100, v100\n"
"v_mfma_f32_16x16x32_bf16 v[68:71], a[80:83], v[212:215], v[68:71]\n"
"ds_read_b128 a[80:83], v171 offset:16384\n"
"v_cvt_pk_bf16_f32 v51, v22, v23\n"
"v_mfma_f32_16x16x32_bf16 v[76:79], a[84:87], v[212:215], v[76:79]\n"
"ds_read_b128 a[84:87], v171 offset:20480\n"
"v_accvgpr_read_b32 v24, a36\n"
"v_exp_f32 v101, v101\n"
"v_mfma_f32_16x16x32_bf16 v[84:87], a[88:91], v[212:215], v[84:87]\n"
"ds_read_b128 a[88:91], v171 offset:24576\n"
"buffer_load_dwordx4 v[228:231], v13, s[36:39], s84 offen offset:1024\n"
"v_cvt_pk_bf16_f32 v147, v106, v107\n"
"v_mfma_f32_16x16x32_bf16 v[92:95], a[92:95], v[212:215], v[92:95]\n"
"s_waitcnt lgkmcnt(3)\n"
"ds_read_b128 a[92:95], v171 offset:28672\n"
"v_accvgpr_read_b32 v25, a37\n"
"v_exp_f32 v102, v102\n"
"v_mfma_f32_16x16x32_bf16 v[64:67], a[64:67], v[200:203], v[64:67]\n"
"ds_read_b64_tr_b16 a[112:113], v172 offset:50176\n"
"v_add_u32 v168, 0xffff0000, v168\n"
"v_exp_f32 v103, v103\n"
"v_mfma_f32_16x16x32_bf16 v[72:75], a[68:71], v[200:203], v[72:75]\n"
"ds_read_b64_tr_b16 a[114:115], v172 offset:54272\n"
"v_add_u32 v169, 0xffff0000, v169\n"
"v_cvt_pk_bf16_f32 v148, v100, v101\n"
"v_mfma_f32_16x16x32_bf16 v[80:83], a[72:75], v[200:203], v[80:83]\n"
"ds_read_b64_tr_b16 a[116:117], v173 offset:50176\n"
"buffer_load_dwordx4 v[232:235], v13, s[36:39], s84 offen offset:2048\n"
"v_exp_f32 v108, v108\n"
"v_mfma_f32_16x16x32_bf16 v[88:91], a[76:79], v[200:203], v[88:91]\n"
"ds_read_b64_tr_b16 a[118:119], v173 offset:54272\n"
"v_add_u32 v170, 0xffff0000, v170\n"
"v_exp_f32 v109, v109\n"
"v_mfma_f32_16x16x32_bf16 v[68:71], a[64:67], v[216:219], v[68:71]\n"
"ds_read_b64_tr_b16 a[120:121], v172 offset:50688\n"
"v_add_u32 v171, 0xffff0000, v171\n"
"v_cvt_pk_bf16_f32 v149, v102, v103\n"
"v_mfma_f32_16x16x32_bf16 v[76:79], a[68:71], v[216:219], v[76:79]\n"
"ds_read_b64_tr_b16 a[122:123], v172 offset:54784\n"
"v_accvgpr_read_b32 v26, a38\n"
"v_exp_f32 v110, v110\n"
"v_mfma_f32_16x16x32_bf16 v[84:87], a[72:75], v[216:219], v[84:87]\n"
"ds_read_b64_tr_b16 a[124:125], v173 offset:50688\n"
"buffer_load_dwordx4 v[236:239], v13, s[36:39], s84 offen offset:3072\n"
"v_exp_f32 v111, v111\n"
"v_mfma_f32_16x16x32_bf16 v[92:95], a[76:79], v[216:219], v[92:95]\n"
"s_waitcnt lgkmcnt(3)\n"
"ds_read_b64_tr_b16 a[126:127], v173 offset:54784\n"
"v_cvt_pk_bf16_f32 v150, v108, v109\n"
"v_exp_f32 v112, v112\n"
"v_mfma_f32_16x16x32_bf16 v[64:67], a[80:83], v[204:207], v[64:67]\n"
"v_exp_f32 v113, v113\n"
"v_mfma_f32_16x16x32_bf16 v[72:75], a[84:87], v[204:207], v[72:75]\n"
"v_cvt_pk_bf16_f32 v151, v110, v111\n"
"v_accvgpr_read_b32 v27, a39\n"
"v_mfma_f32_16x16x32_bf16 v[80:83], a[88:91], v[204:207], v[80:83]\n"
"buffer_load_dwordx4 v[240:243], v14, s[36:39], s84 offen offset:0\n"
"v_accvgpr_read_b32 v28, a44\n"
"v_exp_f32 v114, v114\n"
"v_mfma_f32_16x16x32_bf16 v[88:91], a[92:95], v[204:207], v[88:91]\n"
"v_exp_f32 v115, v115\n"
"v_mfma_f32_16x16x32_bf16 v[68:71], a[80:83], v[220:223], v[68:71]\n"
"v_cvt_pk_bf16_f32 v152, v112, v113\n"
"v_accvgpr_read_b32 v29, a45\n"
"v_mfma_f32_16x16x32_bf16 v[76:79], a[84:87], v[220:223], v[76:79]\n"
"v_exp_f32 v120, v120\n"
"v_mfma_f32_16x16x32_bf16 v[84:87], a[88:91], v[220:223], v[84:87]\n"
"buffer_load_dwordx4 v[244:247], v14, s[36:39], s84 offen offset:1024\n"
"v_accvgpr_read_b32 v30, a46\n"
"v_exp_f32 v121, v121\n"
"v_mfma_f32_16x16x32_bf16 v[92:95], a[92:95], v[220:223], v[92:95]\n"
"v_cvt_pk_bf16_f32 v153, v114, v115\n"
"v_accvgpr_read_b32 v31, a47\n"
"buffer_load_dwordx4 v[248:251], v14, s[36:39], s84 offen offset:2048\n"
"buffer_load_dwordx4 v[252:255], v14, s[36:39], s84 offen offset:3072\n"
"v_mul_f32 v24, v24, v7\n"
"v_mul_f32 v25, v25, v7\n"
"v_mul_f32 v26, v26, v7\n"
"v_mul_f32 v27, v27, v7\n"
"v_mul_f32 v28, v28, v7\n"
"v_mul_f32 v29, v29, v7\n"
"v_mul_f32 v30, v30, v7\n"
"v_mul_f32 v31, v31, v7\n"
"v_cvt_pk_bf16_f32 v52, v24, v25\n"
"v_cvt_pk_bf16_f32 v53, v26, v27\n"
"v_cvt_pk_bf16_f32 v54, v28, v29\n"
"v_cvt_pk_bf16_f32 v55, v30, v31\n"
"v_accvgpr_read_b32 v16, a48\n"
"v_accvgpr_read_b32 v17, a49\n"
"v_accvgpr_read_b32 v18, a50\n"
"v_accvgpr_read_b32 v19, a51\n"
"v_accvgpr_read_b32 v20, a56\n"
"v_accvgpr_read_b32 v21, a57\n"
"v_accvgpr_read_b32 v22, a58\n"
"v_accvgpr_read_b32 v23, a59\n"
"v_mul_f32 v16, v16, v6\n"
"v_mul_f32 v17, v17, v6\n"
"v_mul_f32 v18, v18, v6\n"
"v_mul_f32 v19, v19, v6\n"
"v_mul_f32 v20, v20, v6\n"
"v_mul_f32 v21, v21, v6\n"
"v_mul_f32 v22, v22, v6\n"
"v_mul_f32 v23, v23, v6\n"
"v_cvt_pk_bf16_f32 v56, v16, v17\n"
"v_cvt_pk_bf16_f32 v57, v18, v19\n"
"v_cvt_pk_bf16_f32 v58, v20, v21\n"
"v_cvt_pk_bf16_f32 v59, v22, v23\n"
"v_accvgpr_read_b32 v24, a52\n"
"v_accvgpr_read_b32 v25, a53\n"
"v_accvgpr_read_b32 v26, a54\n"
"v_accvgpr_read_b32 v27, a55\n"
"v_accvgpr_read_b32 v28, a60\n"
"v_accvgpr_read_b32 v29, a61\n"
"v_accvgpr_read_b32 v30, a62\n"
"v_accvgpr_read_b32 v31, a63\n"
"v_mul_f32 v24, v24, v7\n"
"v_mul_f32 v25, v25, v7\n"
"v_mul_f32 v26, v26, v7\n"
"v_mul_f32 v27, v27, v7\n"
"v_mul_f32 v28, v28, v7\n"
"v_mul_f32 v29, v29, v7\n"
"v_mul_f32 v30, v30, v7\n"
"v_mul_f32 v31, v31, v7\n"
"v_cvt_pk_bf16_f32 v60, v24, v25\n"
"v_cvt_pk_bf16_f32 v61, v26, v27\n"
"v_cvt_pk_bf16_f32 v62, v28, v29\n"
"v_cvt_pk_bf16_f32 v63, v30, v31\n"
"s_waitcnt vmcnt(12)\n"
"s_barrier\n"
".LBbar14:\n"
"v_mfma_f32_16x16x32_bf16 a[160:163], a[96:99], v[144:147], a[160:163]\n"
"ds_read_b128 a[64:67], v168 offset:32768\n"
"v_exp_f32 v122, v122\n"
"v_mfma_f32_16x16x32_bf16 a[168:171], a[100:103], v[144:147], a[168:171]\n"
"ds_read_b128 a[68:71], v168 offset:36864\n"
"v_mfma_f32_16x16x32_bf16 a[176:179], a[104:107], v[144:147], a[176:179]\n"
"ds_read_b128 a[72:75], v168 offset:40960\n"
"v_exp_f32 v123, v123\n"
"v_mfma_f32_16x16x32_bf16 a[184:187], a[108:111], v[144:147], a[184:187]\n"
"ds_read_b128 a[76:79], v168 offset:45056\n"
"v_cvt_pk_bf16_f32 v154, v120, v121\n"
"v_mfma_f32_16x16x32_bf16 a[164:167], a[96:99], v[148:151], a[164:167]\n"
"ds_read_b64_tr_b16 a[96:97], v172 offset:57344\n"
"v_exp_f32 v116, v116\n"
"v_mfma_f32_16x16x32_bf16 a[172:175], a[100:103], v[148:151], a[172:175]\n"
"ds_read_b64_tr_b16 a[98:99], v172 offset:61440\n"
"v_mfma_f32_16x16x32_bf16 a[180:183], a[104:107], v[148:151], a[180:183]\n"
"ds_read_b64_tr_b16 a[100:101], v173 offset:57344\n"
"v_exp_f32 v117, v117\n"
"v_mfma_f32_16x16x32_bf16 a[188:191], a[108:111], v[148:151], a[188:191]\n"
"ds_read_b64_tr_b16 a[102:103], v173 offset:61440\n"
"v_cvt_pk_bf16_f32 v155, v122, v123\n"
"v_mfma_f32_16x16x32_bf16 v[136:139], v[176:179], v[144:147], v[136:139]\n"
"ds_read_b64_tr_b16 a[104:105], v172 offset:57856\n"
"v_exp_f32 v118, v118\n"
"v_mfma_f32_16x16x32_bf16 a[192:195], a[112:115], v[144:147], a[192:195]\n"
"ds_read_b64_tr_b16 a[106:107], v172 offset:61952\n"
"v_mfma_f32_16x16x32_bf16 a[200:203], a[116:119], v[144:147], a[200:203]\n"
"s_waitcnt lgkmcnt(5)\n"
"ds_read_b64_tr_b16 a[108:109], v173 offset:57856\n"
"v_exp_f32 v119, v119\n"
"v_mfma_f32_16x16x32_bf16 a[208:211], a[120:123], v[144:147], a[208:211]\n"
"ds_read_b64_tr_b16 a[110:111], v173 offset:61952\n"
"v_cvt_pk_bf16_f32 v156, v116, v117\n"
"v_mfma_f32_16x16x32_bf16 a[216:219], a[124:127], v[144:147], a[216:219]\n"
"ds_write_b128 v175, v[32:35] offset:0\n"
"v_exp_f32 v124, v124\n"
"v_mfma_f32_16x16x32_bf16 a[196:199], a[112:115], v[148:151], a[196:199]\n"
"ds_read_b64_tr_b16 a[112:113], v172 offset:58368\n"
"v_exp_f32 v125, v125\n"
"v_mfma_f32_16x16x32_bf16 a[204:207], a[116:119], v[148:151], a[204:207]\n"
"ds_read_b64_tr_b16 a[114:115], v172 offset:62464\n"
"v_cvt_pk_bf16_f32 v157, v118, v119\n"
"v_mfma_f32_16x16x32_bf16 a[212:215], a[120:123], v[148:151], a[212:215]\n"
"ds_read_b64_tr_b16 a[116:117], v173 offset:58368\n"
"v_max3_f32 v188, v64, v65, v66\n"
"v_mfma_f32_16x16x32_bf16 a[220:223], a[124:127], v[148:151], a[220:223]\n"
"ds_read_b64_tr_b16 a[118:119], v173 offset:62464\n"
"v_max3_f32 v189, v68, v69, v70\n"
"v_exp_f32 v126, v126\n"
"v_mfma_f32_16x16x32_bf16 v[140:143], v[176:179], v[148:151], v[140:143]\n"
"s_waitcnt lgkmcnt(4)\n"
"ds_read_b64_tr_b16 a[120:121], v172 offset:58880\n"
"v_max3_f32 v188, v188, v67, v72\n"
"v_exp_f32 v127, v127\n"
"v_mfma_f32_16x16x32_bf16 a[160:163], a[96:99], v[152:155], a[160:163]\n"
"ds_read_b64_tr_b16 a[122:123], v172 offset:62976\n"
"v_cvt_pk_bf16_f32 v158, v124, v125\n"
"v_mfma_f32_16x16x32_bf16 a[168:171], a[100:103], v[152:155], a[168:171]\n"
"ds_read_b64_tr_b16 a[124:125], v173 offset:58880\n"
"v_cvt_pk_bf16_f32 v159, v126, v127\n"
"v_mfma_f32_16x16x32_bf16 a[176:179], a[104:107], v[152:155], a[176:179]\n"
"ds_read_b64_tr_b16 a[126:127], v173 offset:62976\n"
"v_max3_f32 v189, v189, v71, v76\n"
"v_mfma_f32_16x16x32_bf16 a[184:187], a[108:111], v[152:155], a[184:187]\n"
"ds_write_b128 v175, v[36:39] offset:1024\n"
"v_mfma_f32_16x16x32_bf16 a[164:167], a[96:99], v[156:159], a[164:167]\n"
"ds_read_b128 a[80:83], v169 offset:32768\n"
"v_max3_f32 v188, v188, v73, v74\n"
"v_mfma_f32_16x16x32_bf16 a[172:175], a[100:103], v[156:159], a[172:175]\n"
"ds_read_b128 a[84:87], v169 offset:36864\n"
"v_max3_f32 v189, v189, v77, v78\n"
"v_mfma_f32_16x16x32_bf16 a[180:183], a[104:107], v[156:159], a[180:183]\n"
"ds_read_b128 a[88:91], v169 offset:40960\n"
"v_max3_f32 v188, v188, v75, v80\n"
"v_mfma_f32_16x16x32_bf16 a[188:191], a[108:111], v[156:159], a[188:191]\n"
"ds_read_b128 a[92:95], v169 offset:45056\n"
"v_max3_f32 v189, v189, v79, v84\n"
"v_mfma_f32_16x16x32_bf16 v[136:139], v[176:179], v[152:155], v[136:139]\n"
"s_waitcnt lgkmcnt(5)\n"
"ds_read_b64_tr_b16 a[96:97], v172 offset:32768\n"
"v_max3_f32 v188, v188, v81, v82\n"
"v_mfma_f32_16x16x32_bf16 a[192:195], a[112:115], v[152:155], a[192:195]\n"
"ds_read_b64_tr_b16 a[98:99], v172 offset:36864\n"
"v_max3_f32 v189, v189, v85, v86\n"
"v_mfma_f32_16x16x32_bf16 a[200:203], a[116:119], v[152:155], a[200:203]\n"
"ds_read_b64_tr_b16 a[100:101], v173 offset:32768\n"
"v_max3_f32 v188, v188, v83, v88\n"
"v_mfma_f32_16x16x32_bf16 a[208:211], a[120:123], v[152:155], a[208:211]\n"
"ds_read_b64_tr_b16 a[102:103], v173 offset:36864\n"
"v_max3_f32 v189, v189, v87, v92\n"
"v_mfma_f32_16x16x32_bf16 a[216:219], a[124:127], v[152:155], a[216:219]\n"
"ds_read_b64_tr_b16 a[104:105], v172 offset:33280\n"
"v_max3_f32 v188, v188, v89, v90\n"
"v_mfma_f32_16x16x32_bf16 a[196:199], a[112:115], v[156:159], a[196:199]\n"
"ds_read_b64_tr_b16 a[106:107], v172 offset:37376\n"
"v_max3_f32 v189, v189, v93, v94\n"
"v_mfma_f32_16x16x32_bf16 a[204:207], a[116:119], v[156:159], a[204:207]\n"
"ds_read_b64_tr_b16 a[108:109], v173 offset:33280\n"
"v_max3_f32 v190, v188, v91, v95\n"
"v_mfma_f32_16x16x32_bf16 a[212:215], a[120:123], v[156:159], a[212:215]\n"
"ds_read_b64_tr_b16 a[110:111], v173 offset:37376\n"
"v_max_f32 v190, v190, v189\n"
"v_mfma_f32_16x16x32_bf16 a[220:223], a[124:127], v[156:159], a[220:223]\n"
"ds_write_b128 v175, v[40:43] offset:2048\n"
"v_mfma_f32_16x16x32_bf16 v[140:143], v[176:179], v[156:159], v[140:143]\n"
"ds_write_b128 v175, v[44:47] offset:3072\n"
"v_cmp_lt_f32 vcc, s59, v190\n"
"ds_write_b128 v175, v[48:51] offset:4096\n"
"ds_write_b128 v175, v[52:55] offset:5120\n"
"ds_write_b128 v175, v[56:59] offset:6144\n"
"ds_write_b128 v175, v[60:63] offset:7168\n"
"s_cbranch_vccz .LBskip14\n"
"s_swappc_b64 s[70:71], s[72:73]\n"
".LBskip14:\n"
"v_mfma_f32_16x16x32_bf16 v[96:99], a[64:67], v[192:195], v[128:131]\n"
"v_exp_f32 v64, v64\n"
"v_mfma_f32_16x16x32_bf16 v[104:107], a[68:71], v[192:195], v[128:131]\n"
"v_exp_f32 v65, v65\n"
"v_mfma_f32_16x16x32_bf16 v[112:115], a[72:75], v[192:195], v[128:131]\n"
"v_exp_f32 v66, v66\n"
"v_mfma_f32_16x16x32_bf16 v[120:123], a[76:79], v[192:195], v[128:131]\n"
"v_exp_f32 v67, v67\n"
"v_mfma_f32_16x16x32_bf16 v[100:103], a[64:67], v[208:211], v[132:135]\n"
"ds_read_b128 a[64:67], v170 offset:32768\n"
"v_cvt_pk_bf16_f32 v144, v64, v65\n"
"v_mfma_f32_16x16x32_bf16 v[108:111], a[68:71], v[208:211], v[132:135]\n"
"ds_read_b128 a[68:71], v170 offset:36864\n"
"v_exp_f32 v72, v72\n"
"v_mfma_f32_16x16x32_bf16 v[116:119], a[72:75], v[208:211], v[132:135]\n"
"ds_read_b128 a[72:75], v170 offset:40960\n"
"v_mfma_f32_16x16x32_bf16 v[124:127], a[76:79], v[208:211], v[132:135]\n"
"s_waitcnt lgkmcnt(3)\n"
"ds_read_b128 a[76:79], v170 offset:45056\n"
"v_exp_f32 v73, v73\n"
"v_mfma_f32_16x16x32_bf16 v[96:99], a[80:83], v[196:199], v[96:99]\n"
"s_add_u32 s85, s51, 1\n"
"s_and_b32 s85, s85, 15\n"
"v_cvt_pk_bf16_f32 v145, v66, v67\n"
"v_mfma_f32_16x16x32_bf16 v[104:107], a[84:87], v[196:199], v[104:107]\n"
"s_lshl_b32 s85, s85, 15\n"
"v_exp_f32 v74, v74\n"
"v_mfma_f32_16x16x32_bf16 v[112:115], a[88:91], v[196:199], v[112:115]\n"
"buffer_load_dwordx4 v[32:35], v13, s[36:39], s85 offen offset:0\n"
"v_exp_f32 v75, v75\n"
"v_mfma_f32_16x16x32_bf16 v[120:123], a[92:95], v[196:199], v[120:123]\n"
"v_cvt_pk_bf16_f32 v146, v72, v73\n"
"v_exp_f32 v68, v68\n"
"v_mfma_f32_16x16x32_bf16 v[100:103], a[80:83], v[212:215], v[100:103]\n"
"ds_read_b128 a[80:83], v171 offset:32768\n"
"v_mfma_f32_16x16x32_bf16 v[108:111], a[84:87], v[212:215], v[108:111]\n"
"ds_read_b128 a[84:87], v171 offset:36864\n"
"v_exp_f32 v69, v69\n"
"v_mfma_f32_16x16x32_bf16 v[116:119], a[88:91], v[212:215], v[116:119]\n"
"ds_read_b128 a[88:91], v171 offset:40960\n"
"buffer_load_dwordx4 v[36:39], v13, s[36:39], s85 offen offset:1024\n"
"v_cvt_pk_bf16_f32 v147, v74, v75\n"
"v_mfma_f32_16x16x32_bf16 v[124:127], a[92:95], v[212:215], v[124:127]\n"
"s_waitcnt lgkmcnt(3)\n"
"ds_read_b128 a[92:95], v171 offset:45056\n"
"v_exp_f32 v70, v70\n"
"v_mfma_f32_16x16x32_bf16 v[96:99], a[64:67], v[200:203], v[96:99]\n"
"ds_read_b64_tr_b16 a[112:113], v172 offset:33792\n"
"v_exp_f32 v71, v71\n"
"v_mfma_f32_16x16x32_bf16 v[104:107], a[68:71], v[200:203], v[104:107]\n"
"ds_read_b64_tr_b16 a[114:115], v172 offset:37888\n"
"v_cvt_pk_bf16_f32 v148, v68, v69\n"
"v_exp_f32 v76, v76\n"
"v_mfma_f32_16x16x32_bf16 v[112:115], a[72:75], v[200:203], v[112:115]\n"
"ds_read_b64_tr_b16 a[116:117], v173 offset:33792\n"
"buffer_load_dwordx4 v[40:43], v13, s[36:39], s85 offen offset:2048\n"
"v_exp_f32 v77, v77\n"
"v_mfma_f32_16x16x32_bf16 v[120:123], a[76:79], v[200:203], v[120:123]\n"
"ds_read_b64_tr_b16 a[118:119], v173 offset:37888\n"
"v_cvt_pk_bf16_f32 v149, v70, v71\n"
"v_mfma_f32_16x16x32_bf16 v[100:103], a[64:67], v[216:219], v[100:103]\n"
"ds_read_b64_tr_b16 a[120:121], v172 offset:34304\n"
"v_exp_f32 v78, v78\n"
"v_mfma_f32_16x16x32_bf16 v[108:111], a[68:71], v[216:219], v[108:111]\n"
"ds_read_b64_tr_b16 a[122:123], v172 offset:38400\n"
"v_exp_f32 v79, v79\n"
"v_mfma_f32_16x16x32_bf16 v[116:119], a[72:75], v[216:219], v[116:119]\n"
"ds_read_b64_tr_b16 a[124:125], v173 offset:34304\n"
"buffer_load_dwordx4 v[44:47], v13, s[36:39], s85 offen offset:3072\n"
"v_mfma_f32_16x16x32_bf16 v[124:127], a[76:79], v[216:219], v[124:127]\n"
"s_waitcnt lgkmcnt(4)\n"
"ds_read_b64_tr_b16 a[126:127], v173 offset:38400\n"
"v_cvt_pk_bf16_f32 v150, v76, v77\n"
"v_exp_f32 v80, v80\n"
"v_mfma_f32_16x16x32_bf16 v[96:99], a[80:83], v[204:207], v[96:99]\n"
"v_exp_f32 v81, v81\n"
"v_mfma_f32_16x16x32_bf16 v[104:107], a[84:87], v[204:207], v[104:107]\n"
"v_cvt_pk_bf16_f32 v151, v78, v79\n"
"v_mfma_f32_16x16x32_bf16 v[112:115], a[88:91], v[204:207], v[112:115]\n"
"buffer_load_dwordx4 v[48:51], v14, s[36:39], s85 offen offset:0\n"
"v_exp_f32 v82, v82\n"
"v_mfma_f32_16x16x32_bf16 v[120:123], a[92:95], v[204:207], v[120:123]\n"
"v_exp_f32 v83, v83\n"
"v_mfma_f32_16x16x32_bf16 v[100:103], a[80:83], v[220:223], v[100:103]\n"
"v_cvt_pk_bf16_f32 v152, v80, v81\n"
"v_mfma_f32_16x16x32_bf16 v[108:111], a[84:87], v[220:223], v[108:111]\n"
"v_exp_f32 v88, v88\n"
"v_mfma_f32_16x16x32_bf16 v[116:119], a[88:91], v[220:223], v[116:119]\n"
"buffer_load_dwordx4 v[52:55], v14, s[36:39], s85 offen offset:1024\n"
"v_exp_f32 v89, v89\n"
"v_mfma_f32_16x16x32_bf16 v[124:127], a[92:95], v[220:223], v[124:127]\n"
"v_cvt_pk_bf16_f32 v153, v82, v83\n"
"buffer_load_dwordx4 v[56:59], v14, s[36:39], s85 offen offset:2048\n"
"buffer_load_dwordx4 v[60:63], v14, s[36:39], s85 offen offset:3072\n"
"s_barrier\n"
".LBbar15:\n"
"v_mfma_f32_16x16x32_bf16 a[160:163], a[96:99], v[144:147], a[160:163]\n"
"v_exp_f32 v90, v90\n"
"v_mfma_f32_16x16x32_bf16 a[168:171], a[100:103], v[144:147], a[168:171]\n"
"v_exp_f32 v91, v91\n"
"v_mfma_f32_16x16x32_bf16 a[176:179], a[104:107], v[144:147], a[176:179]\n"
"v_cvt_pk_bf16_f32 v154, v88, v89\n"
"s_add_u32 s84, s51, 2\n"
"v_mfma_f32_16x16x32_bf16 a[184:187], a[108:111], v[144:147], a[184:187]\n"
"v_exp_f32 v84, v84\n"
"v_mfma_f32_16x16x32_bf16 a[164:167], a[96:99], v[148:151], a[164:167]\n"
"ds_read_b64_tr_b16 a[96:97], v172 offset:40960\n"
"s_and_b32 s84, s84, 15\n"
"v_mfma_f32_16x16x32_bf16 a[172:175], a[100:103], v[148:151], a[172:175]\n"
"ds_read_b64_tr_b16 a[98:99], v172 offset:45056\n"
"s_lshl_b32 s84, s84, 15\n"
"v_mfma_f32_16x16x32_bf16 a[180:183], a[104:107], v[148:151], a[180:183]\n"
"ds_read_b64_tr_b16 a[100:101], v173 offset:40960\n"
"buffer_load_dwordx4 v[192:195], v13, s[36:39], s84 offen offset:0\n"
"v_exp_f32 v85, v85\n"
"v_mfma_f32_16x16x32_bf16 a[188:191], a[108:111], v[148:151], a[188:191]\n"
"ds_read_b64_tr_b16 a[102:103], v173 offset:45056\n"
"v_cvt_pk_bf16_f32 v155, v90, v91\n"
"v_mfma_f32_16x16x32_bf16 v[136:139], v[176:179], v[144:147], v[136:139]\n"
"ds_read_b64_tr_b16 a[104:105], v172 offset:41472\n"
"v_mov_b32 v2, v10\n"
"v_exp_f32 v86, v86\n"
"v_mfma_f32_16x16x32_bf16 a[192:195], a[112:115], v[144:147], a[192:195]\n"
"s_waitcnt lgkmcnt(4)\n"
"ds_read_b64_tr_b16 a[106:107], v172 offset:45568\n"
"v_mov_b32 v3, v11\n"
"v_mfma_f32_16x16x32_bf16 a[200:203], a[116:119], v[144:147], a[200:203]\n"
"ds_read_b64_tr_b16 a[108:109], v173 offset:41472\n"
"buffer_load_dwordx4 v[196:199], v13, s[36:39], s84 offen offset:1024\n"
"v_exp_f32 v87, v87\n"
"v_mfma_f32_16x16x32_bf16 a[208:211], a[120:123], v[144:147], a[208:211]\n"
"ds_read_b64_tr_b16 a[110:111], v173 offset:45568\n"
"v_cvt_pk_bf16_f32 v156, v84, v85\n"
"v_mfma_f32_16x16x32_bf16 a[216:219], a[124:127], v[144:147], a[216:219]\n"
"v_exp_f32 v92, v92\n"
"v_mfma_f32_16x16x32_bf16 a[196:199], a[112:115], v[148:151], a[196:199]\n"
"ds_read_b64_tr_b16 a[112:113], v172 offset:41984\n"
"v_exp_f32 v93, v93\n"
"v_mfma_f32_16x16x32_bf16 a[204:207], a[116:119], v[148:151], a[204:207]\n"
"ds_read_b64_tr_b16 a[114:115], v172 offset:46080\n"
"buffer_load_dwordx4 v[200:203], v13, s[36:39], s84 offen offset:2048\n"
"v_mfma_f32_16x16x32_bf16 a[212:215], a[120:123], v[148:151], a[212:215]\n"
"ds_read_b64_tr_b16 a[116:117], v173 offset:41984\n"
"v_cvt_pk_bf16_f32 v157, v86, v87\n"
"v_mfma_f32_16x16x32_bf16 a[220:223], a[124:127], v[148:151], a[220:223]\n"
"ds_read_b64_tr_b16 a[118:119], v173 offset:46080\n"
"v_max3_f32 v188, v96, v97, v98\n"
"v_exp_f32 v94, v94\n"
"v_mfma_f32_16x16x32_bf16 v[140:143], v[176:179], v[148:151], v[140:143]\n"
"s_waitcnt lgkmcnt(4)\n"
"ds_read_b64_tr_b16 a[120:121], v172 offset:42496\n"
"v_max3_f32 v189, v100, v101, v102\n"
"v_exp_f32 v95, v95\n"
"v_mfma_f32_16x16x32_bf16 a[160:163], a[96:99], v[152:155], a[160:163]\n"
"ds_read_b64_tr_b16 a[122:123], v172 offset:46592\n"
"buffer_load_dwordx4 v[204:207], v13, s[36:39], s84 offen offset:3072\n"
"v_mfma_f32_16x16x32_bf16 a[168:171], a[100:103], v[152:155], a[168:171]\n"
"ds_read_b64_tr_b16 a[124:125], v173 offset:42496\n"
"v_cvt_pk_bf16_f32 v158, v92, v93\n"
"v_mfma_f32_16x16x32_bf16 a[176:179], a[104:107], v[152:155], a[176:179]\n"
"ds_read_b64_tr_b16 a[126:127], v173 offset:46592\n"
"v_cvt_pk_bf16_f32 v159, v94, v95\n"
"v_mfma_f32_16x16x32_bf16 a[184:187], a[108:111], v[152:155], a[184:187]\n"
"v_max3_f32 v188, v188, v99, v104\n"
"v_max3_f32 v189, v189, v103, v108\n"
"v_mfma_f32_16x16x32_bf16 a[164:167], a[96:99], v[156:159], a[164:167]\n"
"buffer_load_dwordx4 v[208:211], v14, s[36:39], s84 offen offset:0\n"
"v_max3_f32 v188, v188, v105, v106\n"
"v_mfma_f32_16x16x32_bf16 a[172:175], a[100:103], v[156:159], a[172:175]\n"
"v_max3_f32 v189, v189, v109, v110\n"
"v_max3_f32 v188, v188, v107, v112\n"
"v_mfma_f32_16x16x32_bf16 a[180:183], a[104:107], v[156:159], a[180:183]\n"
"v_max3_f32 v189, v189, v111, v116\n"
"v_max3_f32 v188, v188, v113, v114\n"
"v_mfma_f32_16x16x32_bf16 a[188:191], a[108:111], v[156:159], a[188:191]\n"
"v_max3_f32 v189, v189, v117, v118\n"
"v_max3_f32 v188, v188, v115, v120\n"
"v_mfma_f32_16x16x32_bf16 v[136:139], v[176:179], v[152:155], v[136:139]\n"
"s_waitcnt lgkmcnt(0)\n"
"buffer_load_dwordx4 v[212:215], v14, s[36:39], s84 offen offset:1024\n"
"v_max3_f32 v189, v189, v119, v124\n"
"v_mfma_f32_16x16x32_bf16 a[192:195], a[112:115], v[152:155], a[192:195]\n"
"v_max3_f32 v188, v188, v121, v122\n"
"v_max3_f32 v189, v189, v125, v126\n"
"v_mfma_f32_16x16x32_bf16 a[200:203], a[116:119], v[152:155], a[200:203]\n"
"v_max3_f32 v190, v188, v123, v127\n"
"v_max_f32 v190, v190, v189\n"
"v_mfma_f32_16x16x32_bf16 a[208:211], a[120:123], v[152:155], a[208:211]\n"
"v_cmp_lt_f32 vcc, s59, v190\n"
"v_mfma_f32_16x16x32_bf16 a[216:219], a[124:127], v[152:155], a[216:219]\n"
"buffer_load_dwordx4 v[216:219], v14, s[36:39], s84 offen offset:2048\n"
"v_mfma_f32_16x16x32_bf16 a[196:199], a[112:115], v[156:159], a[196:199]\n"
"v_mfma_f32_16x16x32_bf16 a[204:207], a[116:119], v[156:159], a[204:207]\n"
"v_mfma_f32_16x16x32_bf16 a[212:215], a[120:123], v[156:159], a[212:215]\n"
"v_mfma_f32_16x16x32_bf16 a[220:223], a[124:127], v[156:159], a[220:223]\n"
"buffer_load_dwordx4 v[220:223], v14, s[36:39], s84 offen offset:3072\n"
"v_mfma_f32_16x16x32_bf16 v[140:143], v[176:179], v[156:159], v[140:143]\n"
"s_cbranch_vccz .LBskip15\n"
"s_swappc_b64 s[70:71], s[74:75]\n"
".LBskip15:\n"
"v_exp_f32 v96, v96\n"
"v_exp_f32 v97, v97\n"
"v_exp_f32 v98, v98\n"
"v_exp_f32 v99, v99\n"
"v_cvt_pk_bf16_f32 v144, v96, v97\n"
"v_exp_f32 v104, v104\n"
"v_exp_f32 v105, v105\n"
"v_cvt_pk_bf16_f32 v145, v98, v99\n"
"v_exp_f32 v106, v106\n"
"v_exp_f32 v107, v107\n"
"v_cvt_pk_bf16_f32 v146, v104, v105\n"
"v_exp_f32 v100, v100\n"
"v_exp_f32 v101, v101\n"
"v_cvt_pk_bf16_f32 v147, v106, v107\n"
"v_exp_f32 v102, v102\n"
"v_exp_f32 v103, v103\n"
"v_cvt_pk_bf16_f32 v148, v100, v101\n"
"v_exp_f32 v108, v108\n"
"v_exp_f32 v109, v109\n"
"v_cvt_pk_bf16_f32 v149, v102, v103\n"
"v_exp_f32 v110, v110\n"
"v_exp_f32 v111, v111\n"
"v_cvt_pk_bf16_f32 v150, v108, v109\n"
"v_exp_f32 v112, v112\n"
"v_exp_f32 v113, v113\n"
"v_cvt_pk_bf16_f32 v151, v110, v111\n"
"v_exp_f32 v114, v114\n"
"v_exp_f32 v115, v115\n"
"v_cvt_pk_bf16_f32 v152, v112, v113\n"
"v_exp_f32 v120, v120\n"
"v_exp_f32 v121, v121\n"
"v_cvt_pk_bf16_f32 v153, v114, v115\n"
"v_exp_f32 v122, v122\n"
"v_exp_f32 v123, v123\n"
"v_cvt_pk_bf16_f32 v154, v120, v121\n"
"v_exp_f32 v116, v116\n"
"v_exp_f32 v117, v117\n"
"v_cvt_pk_bf16_f32 v155, v122, v123\n"
"v_exp_f32 v118, v118\n"
"v_exp_f32 v119, v119\n"
"v_cvt_pk_bf16_f32 v156, v116, v117\n"
"v_exp_f32 v124, v124\n"
"v_exp_f32 v125, v125\n"
"v_cvt_pk_bf16_f32 v157, v118, v119\n"
"v_exp_f32 v126, v126\n"
"v_exp_f32 v127, v127\n"
"v_cvt_pk_bf16_f32 v158, v124, v125\n"
"v_cvt_pk_bf16_f32 v159, v126, v127\n"
"s_waitcnt vmcnt(24)\n"
"s_barrier\n"
".LBbar16:\n"
"v_add_u32 v172, 0x10000, v172\n"
"v_add_u32 v173, 0x10000, v173\n"
"ds_read_b64_tr_b16 a[96:97], v172 offset:0\n"
"ds_read_b64_tr_b16 a[98:99], v172 offset:4096\n"
"ds_read_b64_tr_b16 a[100:101], v173 offset:0\n"
"ds_read_b64_tr_b16 a[102:103], v173 offset:4096\n"
"ds_read_b64_tr_b16 a[104:105], v172 offset:8192\n"
"ds_read_b64_tr_b16 a[106:107], v172 offset:12288\n"
"ds_read_b64_tr_b16 a[108:109], v173 offset:8192\n"
"ds_read_b64_tr_b16 a[110:111], v173 offset:12288\n"
"v_mfma_f32_16x16x32_bf16 v[136:139], v[176:179], v[144:147], v[136:139]\n"
"ds_read_b64_tr_b16 a[112:113], v172 offset:512\n"
"ds_read_b64_tr_b16 a[114:115], v172 offset:4608\n"
"v_mfma_f32_16x16x32_bf16 v[140:143], v[176:179], v[148:151], v[140:143]\n"
"ds_read_b64_tr_b16 a[116:117], v173 offset:512\n"
"ds_read_b64_tr_b16 a[118:119], v173 offset:4608\n"
"v_mfma_f32_16x16x32_bf16 v[136:139], v[176:179], v[152:155], v[136:139]\n"
"ds_read_b64_tr_b16 a[120:121], v172 offset:8704\n"
"ds_read_b64_tr_b16 a[122:123], v172 offset:12800\n"
"v_mfma_f32_16x16x32_bf16 v[140:143], v[176:179], v[156:159], v[140:143]\n"
"s_waitcnt lgkmcnt(12)\n"
"ds_read_b64_tr_b16 a[124:125], v173 offset:8704\n"
"ds_read_b64_tr_b16 a[126:127], v173 offset:12800\n"
"v_mfma_f32_16x16x32_bf16 a[160:163], a[96:99], v[144:147], a[160:163]\n"
"s_waitcnt lgkmcnt(12)\n"
"v_rcp_f32 v6, v136\n"
"v_mfma_f32_16x16x32_bf16 a[168:171], a[100:103], v[144:147], a[168:171]\n"
"s_nop 0\n"
"v_rcp_f32 v7, v140\n"
"v_mfma_f32_16x16x32_bf16 a[164:167], a[96:99], v[148:151], a[164:167]\n"
"ds_read_b64_tr_b16 a[96:97], v172 offset:1024\n"
"v_mfma_f32_16x16x32_bf16 a[172:175], a[100:103], v[148:151], a[172:175]\n"
"s_waitcnt lgkmcnt(5)\n"
"ds_read_b64_tr_b16 a[98:99], v172 offset:5120\n"
"v_mfma_f32_16x16x32_bf16 a[160:163], a[104:107], v[152:155], a[160:163]\n"
"ds_read_b64_tr_b16 a[100:101], v173 offset:1024\n"
"v_mfma_f32_16x16x32_bf16 a[168:171], a[108:111], v[152:155], a[168:171]\n"
"ds_read_b64_tr_b16 a[102:103], v173 offset:5120\n"
"v_mfma_f32_16x16x32_bf16 a[164:167], a[104:107], v[156:159], a[164:167]\n"
"ds_read_b64_tr_b16 a[104:105], v172 offset:9216\n"
"v_mfma_f32_16x16x32_bf16 a[172:175], a[108:111], v[156:159], a[172:175]\n"
"ds_read_b64_tr_b16 a[106:107], v172 offset:13312\n"
"v_mfma_f32_16x16x32_bf16 a[176:179], a[112:115], v[144:147], a[176:179]\n"
"ds_read_b64_tr_b16 a[108:109], v173 offset:9216\n"
"v_mfma_f32_16x16x32_bf16 a[184:187], a[116:119], v[144:147], a[184:187]\n"
"ds_read_b64_tr_b16 a[110:111], v173 offset:13312\n"
"v_mfma_f32_16x16x32_bf16 a[180:183], a[112:115], v[148:151], a[180:183]\n"
"ds_read_b64_tr_b16 a[112:113], v172 offset:1536\n"
"v_accvgpr_read_b32 v64, a160\n"
"v_mfma_f32_16x16x32_bf16 a[188:191], a[116:119], v[148:151], a[188:191]\n"
"s_waitcnt lgkmcnt(5)\n"
"ds_read_b64_tr_b16 a[114:115], v172 offset:5632\n"
"v_accvgpr_read_b32 v65, a161\n"
"v_mfma_f32_16x16x32_bf16 a[176:179], a[120:123], v[152:155], a[176:179]\n"
"ds_read_b64_tr_b16 a[116:117], v173 offset:1536\n"
"v_accvgpr_read_b32 v66, a162\n"
"v_mfma_f32_16x16x32_bf16 a[184:187], a[124:127], v[152:155], a[184:187]\n"
"ds_read_b64_tr_b16 a[118:119], v173 offset:5632\n"
"v_accvgpr_read_b32 v67, a163\n"
"v_mfma_f32_16x16x32_bf16 a[180:183], a[120:123], v[156:159], a[180:183]\n"
"ds_read_b64_tr_b16 a[120:121], v172 offset:9728\n"
"v_accvgpr_read_b32 v68, a168\n"
"v_mfma_f32_16x16x32_bf16 a[188:191], a[124:127], v[156:159], a[188:191]\n"
"ds_read_b64_tr_b16 a[122:123], v172 offset:13824\n"
"v_accvgpr_read_b32 v69, a169\n"
"v_mfma_f32_16x16x32_bf16 a[192:195], a[96:99], v[144:147], a[192:195]\n"
"ds_read_b64_tr_b16 a[124:125], v173 offset:9728\n"
"v_accvgpr_read_b32 v70, a170\n"
"v_mfma_f32_16x16x32_bf16 a[200:203], a[100:103], v[144:147], a[200:203]\n"
"ds_read_b64_tr_b16 a[126:127], v173 offset:13824\n"
"v_accvgpr_read_b32 v71, a171\n"
"v_mfma_f32_16x16x32_bf16 a[196:199], a[96:99], v[148:151], a[196:199]\n"
"v_mul_f32 v64, v64, v6\n"
"v_mul_f32 v65, v65, v6\n"
"v_mfma_f32_16x16x32_bf16 a[204:207], a[100:103], v[148:151], a[204:207]\n"
"s_waitcnt lgkmcnt(4)\n"
"v_mul_f32 v66, v66, v6\n"
"v_mul_f32 v67, v67, v6\n"
"v_mfma_f32_16x16x32_bf16 a[192:195], a[104:107], v[152:155], a[192:195]\n"
"v_mul_f32 v68, v68, v6\n"
"v_mul_f32 v69, v69, v6\n"
"v_mfma_f32_16x16x32_bf16 a[200:203], a[108:111], v[152:155], a[200:203]\n"
"v_mul_f32 v70, v70, v6\n"
"v_mul_f32 v71, v71, v6\n"
"v_mfma_f32_16x16x32_bf16 a[196:199], a[104:107], v[156:159], a[196:199]\n"
"v_cvt_pk_bf16_f32 v180, v64, v65\n"
"v_cvt_pk_bf16_f32 v181, v66, v67\n"
"v_mfma_f32_16x16x32_bf16 a[204:207], a[108:111], v[156:159], a[204:207]\n"
"v_cvt_pk_bf16_f32 v182, v68, v69\n"
"v_cvt_pk_bf16_f32 v183, v70, v71\n"
"v_mfma_f32_16x16x32_bf16 a[208:211], a[112:115], v[144:147], a[208:211]\n"
"ds_write_b128 v175, v[180:183] offset:32768\n"
"v_mfma_f32_16x16x32_bf16 a[216:219], a[116:119], v[144:147], a[216:219]\n"
"v_accvgpr_read_b32 v72, a164\n"
"v_accvgpr_read_b32 v73, a165\n"
"v_mfma_f32_16x16x32_bf16 a[212:215], a[112:115], v[148:151], a[212:215]\n"
"v_accvgpr_read_b32 v74, a166\n"
"v_accvgpr_read_b32 v75, a167\n"
"v_mfma_f32_16x16x32_bf16 a[220:223], a[116:119], v[148:151], a[220:223]\n"
"s_waitcnt lgkmcnt(1)\n"
"v_accvgpr_read_b32 v76, a172\n"
"v_accvgpr_read_b32 v77, a173\n"
"v_mfma_f32_16x16x32_bf16 a[208:211], a[120:123], v[152:155], a[208:211]\n"
"v_accvgpr_read_b32 v78, a174\n"
"v_accvgpr_read_b32 v79, a175\n"
"v_mfma_f32_16x16x32_bf16 a[216:219], a[124:127], v[152:155], a[216:219]\n"
"v_mul_f32 v72, v72, v7\n"
"v_mul_f32 v73, v73, v7\n"
"v_mfma_f32_16x16x32_bf16 a[212:215], a[120:123], v[156:159], a[212:215]\n"
"v_mul_f32 v74, v74, v7\n"
"v_mul_f32 v75, v75, v7\n"
"v_mfma_f32_16x16x32_bf16 a[220:223], a[124:127], v[156:159], a[220:223]\n"
"v_mul_f32 v76, v76, v7\n"
"v_mul_f32 v77, v77, v7\n"
"v_mul_f32 v78, v78, v7\n"
"v_mul_f32 v79, v79, v7\n"
"v_cvt_pk_bf16_f32 v184, v72, v73\n"
"v_cvt_pk_bf16_f32 v185, v74, v75\n"
"v_cvt_pk_bf16_f32 v186, v76, v77\n"
"v_cvt_pk_bf16_f32 v187, v78, v79\n"
"ds_write_b128 v175, v[184:187] offset:33792\n"
"v_accvgpr_read_b32 v64, a176\n"
"v_accvgpr_read_b32 v65, a177\n"
"v_accvgpr_read_b32 v66, a178\n"
"v_accvgpr_read_b32 v67, a179\n"
"v_accvgpr_read_b32 v68, a184\n"
"v_accvgpr_read_b32 v69, a185\n"
"v_accvgpr_read_b32 v70, a186\n"
"v_accvgpr_read_b32 v71, a187\n"
"v_mul_f32 v64, v64, v6\n"
"v_mul_f32 v65, v65, v6\n"
"v_mul_f32 v66, v66, v6\n"
"v_mul_f32 v67, v67, v6\n"
"v_mul_f32 v68, v68, v6\n"
"v_mul_f32 v69, v69, v6\n"
"v_mul_f32 v70, v70, v6\n"
"v_mul_f32 v71, v71, v6\n"
"s_waitcnt lgkmcnt(1)\n"
"v_cvt_pk_bf16_f32 v180, v64, v65\n"
"v_cvt_pk_bf16_f32 v181, v66, v67\n"
"v_cvt_pk_bf16_f32 v182, v68, v69\n"
"v_cvt_pk_bf16_f32 v183, v70, v71\n"
"ds_write_b128 v175, v[180:183] offset:34816\n"
"v_accvgpr_read_b32 v72, a180\n"
"v_accvgpr_read_b32 v73, a181\n"
"v_accvgpr_read_b32 v74, a182\n"
"v_accvgpr_read_b32 v75, a183\n"
"v_accvgpr_read_b32 v76, a188\n"
"v_accvgpr_read_b32 v77, a189\n"
"v_accvgpr_read_b32 v78, a190\n"
"v_accvgpr_read_b32 v79, a191\n"
"v_mul_f32 v72, v72, v7\n"
"v_mul_f32 v73, v73, v7\n"
"v_mul_f32 v74, v74, v7\n"
"v_mul_f32 v75, v75, v7\n"
"v_mul_f32 v76, v76, v7\n"
"v_mul_f32 v77, v77, v7\n"
"v_mul_f32 v78, v78, v7\n"
"v_mul_f32 v79, v79, v7\n"
"s_waitcnt lgkmcnt(1)\n"
"v_cvt_pk_bf16_f32 v184, v72, v73\n"
"v_cvt_pk_bf16_f32 v185, v74, v75\n"
"v_cvt_pk_bf16_f32 v186, v76, v77\n"
"v_cvt_pk_bf16_f32 v187, v78, v79\n"
"ds_write_b128 v175, v[184:187] offset:35840\n"
"v_accvgpr_read_b32 v64, a192\n"
"v_accvgpr_read_b32 v65, a193\n"
"v_accvgpr_read_b32 v66, a194\n"
"v_accvgpr_read_b32 v67, a195\n"
"v_accvgpr_read_b32 v68, a200\n"
"v_accvgpr_read_b32 v69, a201\n"
"v_accvgpr_read_b32 v70, a202\n"
"v_accvgpr_read_b32 v71, a203\n"
"v_mul_f32 v64, v64, v6\n"
"v_mul_f32 v65, v65, v6\n"
"v_mul_f32 v66, v66, v6\n"
"v_mul_f32 v67, v67, v6\n"
"v_mul_f32 v68, v68, v6\n"
"v_mul_f32 v69, v69, v6\n"
"v_mul_f32 v70, v70, v6\n"
"v_mul_f32 v71, v71, v6\n"
"s_waitcnt lgkmcnt(1)\n"
"v_cvt_pk_bf16_f32 v180, v64, v65\n"
"v_cvt_pk_bf16_f32 v181, v66, v67\n"
"v_cvt_pk_bf16_f32 v182, v68, v69\n"
"v_cvt_pk_bf16_f32 v183, v70, v71\n"
"ds_write_b128 v175, v[180:183] offset:36864\n"
"v_accvgpr_read_b32 v72, a196\n"
"v_accvgpr_read_b32 v73, a197\n"
"v_accvgpr_read_b32 v74, a198\n"
"v_accvgpr_read_b32 v75, a199\n"
"v_accvgpr_read_b32 v76, a204\n"
"v_accvgpr_read_b32 v77, a205\n"
"v_accvgpr_read_b32 v78, a206\n"
"v_accvgpr_read_b32 v79, a207\n"
"v_mul_f32 v72, v72, v7\n"
"v_mul_f32 v73, v73, v7\n"
"v_mul_f32 v74, v74, v7\n"
"v_mul_f32 v75, v75, v7\n"
"v_mul_f32 v76, v76, v7\n"
"v_mul_f32 v77, v77, v7\n"
"v_mul_f32 v78, v78, v7\n"
"v_mul_f32 v79, v79, v7\n"
"s_waitcnt lgkmcnt(1)\n"
"v_cvt_pk_bf16_f32 v184, v72, v73\n"
"v_cvt_pk_bf16_f32 v185, v74, v75\n"
"v_cvt_pk_bf16_f32 v186, v76, v77\n"
"v_cvt_pk_bf16_f32 v187, v78, v79\n"
"ds_write_b128 v175, v[184:187] offset:37888\n"
"v_accvgpr_read_b32 v64, a208\n"
"v_accvgpr_read_b32 v65, a209\n"
"v_accvgpr_read_b32 v66, a210\n"
"v_accvgpr_read_b32 v67, a211\n"
"v_accvgpr_read_b32 v68, a216\n"
"v_accvgpr_read_b32 v69, a217\n"
"v_accvgpr_read_b32 v70, a218\n"
"v_accvgpr_read_b32 v71, a219\n"
"v_mul_f32 v64, v64, v6\n"
"v_mul_f32 v65, v65, v6\n"
"v_mul_f32 v66, v66, v6\n"
"v_mul_f32 v67, v67, v6\n"
"v_mul_f32 v68, v68, v6\n"
"v_mul_f32 v69, v69, v6\n"
"v_mul_f32 v70, v70, v6\n"
"v_mul_f32 v71, v71, v6\n"
"s_waitcnt lgkmcnt(1)\n"
"v_cvt_pk_bf16_f32 v180, v64, v65\n"
"v_cvt_pk_bf16_f32 v181, v66, v67\n"
"v_cvt_pk_bf16_f32 v182, v68, v69\n"
"v_cvt_pk_bf16_f32 v183, v70, v71\n"
"ds_write_b128 v175, v[180:183] offset:38912\n"
"v_accvgpr_read_b32 v72, a212\n"
"v_accvgpr_read_b32 v73, a213\n"
"v_accvgpr_read_b32 v74, a214\n"
"v_accvgpr_read_b32 v75, a215\n"
"v_accvgpr_read_b32 v76, a220\n"
"v_accvgpr_read_b32 v77, a221\n"
"v_accvgpr_read_b32 v78, a222\n"
"v_accvgpr_read_b32 v79, a223\n"
"v_mul_f32 v72, v72, v7\n"
"v_mul_f32 v73, v73, v7\n"
"v_mul_f32 v74, v74, v7\n"
"v_mul_f32 v75, v75, v7\n"
"v_mul_f32 v76, v76, v7\n"
"v_mul_f32 v77, v77, v7\n"
"v_mul_f32 v78, v78, v7\n"
"v_mul_f32 v79, v79, v7\n"
"s_waitcnt lgkmcnt(1)\n"
"v_cvt_pk_bf16_f32 v184, v72, v73\n"
"v_cvt_pk_bf16_f32 v185, v74, v75\n"
"v_cvt_pk_bf16_f32 v186, v76, v77\n"
"v_cvt_pk_bf16_f32 v187, v78, v79\n"
"ds_write_b128 v175, v[184:187] offset:39936\n"
"s_waitcnt vmcnt(16)\n"
".Lo:\n"
"s_add_u32 s83, s51, 0\n"
"s_and_b32 s83, s83, 15\n"
"s_lshl_b32 s83, s83, 11\n"
"v_add_u32 v0, s83, v12\n"
"ds_read_b128 a[0:3], v0 offset:0\n"
"ds_read_b128 a[4:7], v0 offset:1024\n"
"v_perm_b32 v16, v228, v224, s56\n"
"v_perm_b32 v17, v236, v232, s56\n"
"v_perm_b32 v18, v244, v240, s56\n"
"v_perm_b32 v19, v252, v248, s56\n"
"v_perm_b32 v20, v228, v224, s57\n"
"v_perm_b32 v21, v236, v232, s57\n"
"v_perm_b32 v22, v244, v240, s57\n"
"v_perm_b32 v23, v252, v248, s57\n"
"v_perm_b32 v24, v229, v225, s56\n"
"v_perm_b32 v25, v237, v233, s56\n"
"v_perm_b32 v26, v245, v241, s56\n"
"v_perm_b32 v27, v253, v249, s56\n"
"v_perm_b32 v28, v229, v225, s57\n"
"v_perm_b32 v29, v237, v233, s57\n"
"v_perm_b32 v30, v245, v241, s57\n"
"v_perm_b32 v31, v253, v249, s57\n"
"s_waitcnt lgkmcnt(2)\n"
"s_barrier\n"
"ds_read_b128 a[8:11], v0 offset:32768\n"
"ds_read_b128 a[12:15], v0 offset:33792\n"
"s_waitcnt lgkmcnt(3)\n"
"v_mfma_f32_16x16x32_bf16 v[64:67], v[16:19], a[0:3], 0\n"
"s_waitcnt lgkmcnt(2)\n"
"v_mbcnt_lo_u32_b32 v15, -1, 0\n"
"v_and_b32 v15, 7, v15\n"
"v_mfma_f32_16x16x32_bf16 v[96:99], v[16:19], a[4:7], 0\n"
"s_lshl_b32 s80, s53, 2\n"
"s_add_u32 s80, s80, s48\n"
"v_mfma_f32_16x16x32_bf16 v[68:71], v[20:23], a[0:3], 0\n"
"s_lshl_b32 s80, s80, 7\n"
"s_add_u32 s80, s80, 524352\n"
"v_mfma_f32_16x16x32_bf16 v[100:103], v[20:23], a[4:7], 0\n"
"v_lshl_add_u32 v15, v15, 3, s80\n"
"v_mfma_f32_16x16x32_bf16 v[72:75], v[24:27], a[0:3], 0\n"
"buffer_load_dword v15, v15, s[40:43], 0 offen offset:0 sc1\n"
"v_mfma_f32_16x16x32_bf16 v[104:107], v[24:27], a[4:7], 0\n"
"v_mfma_f32_16x16x32_bf16 v[76:79], v[28:31], a[0:3], 0\n"
"s_add_u32 s83, s51, 1\n"
"s_and_b32 s83, s83, 15\n"
"v_mfma_f32_16x16x32_bf16 v[108:111], v[28:31], a[4:7], 0\n"
"s_waitcnt lgkmcnt(0)\n"
"s_lshl_b32 s83, s83, 11\n"
"v_add_u32 v1, s83, v12\n"
"v_mfma_f32_16x16x32_bf16 v[128:131], v[16:19], a[8:11], 0\n"
"ds_read_b128 a[16:19], v1 offset:0\n"
"v_mfma_f32_16x16x32_bf16 v[160:163], v[16:19], a[12:15], 0\n"
"ds_read_b128 a[20:23], v1 offset:1024\n"
"v_perm_b32 v16, v230, v226, s56\n"
"v_mfma_f32_16x16x32_bf16 v[132:135], v[20:23], a[8:11], 0\n"
"ds_read_b128 a[24:27], v1 offset:32768\n"
"v_perm_b32 v17, v238, v234, s56\n"
"v_mfma_f32_16x16x32_bf16 v[164:167], v[20:23], a[12:15], 0\n"
"ds_read_b128 a[28:31], v1 offset:33792\n"
"v_perm_b32 v18, v246, v242, s56\n"
"v_mfma_f32_16x16x32_bf16 v[136:139], v[24:27], a[8:11], 0\n"
"v_perm_b32 v19, v254, v250, s56\n"
"v_perm_b32 v20, v230, v226, s57\n"
"v_mfma_f32_16x16x32_bf16 v[168:171], v[24:27], a[12:15], 0\n"
"v_perm_b32 v21, v238, v234, s57\n"
"v_perm_b32 v22, v246, v242, s57\n"
"v_mfma_f32_16x16x32_bf16 v[140:143], v[28:31], a[8:11], 0\n"
"v_perm_b32 v23, v254, v250, s57\n"
"v_perm_b32 v24, v231, v227, s56\n"
"v_mfma_f32_16x16x32_bf16 v[172:175], v[28:31], a[12:15], 0\n"
"v_perm_b32 v25, v239, v235, s56\n"
"v_perm_b32 v26, v247, v243, s56\n"
"v_mfma_f32_16x16x32_bf16 v[80:83], v[16:19], a[0:3], 0\n"
"v_perm_b32 v27, v255, v251, s56\n"
"v_perm_b32 v28, v231, v227, s57\n"
"v_mfma_f32_16x16x32_bf16 v[84:87], v[20:23], a[0:3], 0\n"
"v_perm_b32 v29, v239, v235, s57\n"
"v_perm_b32 v30, v247, v243, s57\n"
"v_mfma_f32_16x16x32_bf16 v[112:115], v[16:19], a[4:7], 0\n"
"v_perm_b32 v31, v255, v251, s57\n"
"v_mfma_f32_16x16x32_bf16 v[116:119], v[20:23], a[4:7], 0\n"
"v_mfma_f32_16x16x32_bf16 v[144:147], v[16:19], a[8:11], 0\n"
"v_mfma_f32_16x16x32_bf16 v[148:151], v[20:23], a[8:11], 0\n"
"v_mfma_f32_16x16x32_bf16 v[176:179], v[16:19], a[12:15], 0\n"
"s_waitcnt vmcnt(9)\n"
"v_perm_b32 v16, v36, v32, s56\n"
"v_perm_b32 v17, v44, v40, s56\n"
"v_mfma_f32_16x16x32_bf16 v[180:183], v[20:23], a[12:15], 0\n"
"v_perm_b32 v18, v52, v48, s56\n"
"v_perm_b32 v19, v60, v56, s56\n"
"v_mfma_f32_16x16x32_bf16 v[88:91], v[24:27], a[0:3], 0\n"
"v_perm_b32 v20, v36, v32, s57\n"
"v_perm_b32 v21, v44, v40, s57\n"
"v_mfma_f32_16x16x32_bf16 v[92:95], v[28:31], a[0:3], 0\n"
"v_perm_b32 v22, v52, v48, s57\n"
"v_perm_b32 v23, v60, v56, s57\n"
"v_mfma_f32_16x16x32_bf16 v[120:123], v[24:27], a[4:7], 0\n"
"s_waitcnt lgkmcnt(0)\n"
"v_mfma_f32_16x16x32_bf16 v[124:127], v[28:31], a[4:7], 0\n"
"s_add_u32 s85, s51, 3\n"
"s_and_b32 s85, s85, 15\n"
"v_mfma_f32_16x16x32_bf16 v[152:155], v[24:27], a[8:11], 0\n"
"s_lshl_b32 s85, s85, 15\n"
"v_mfma_f32_16x16x32_bf16 v[156:159], v[28:31], a[8:11], 0\n"
"buffer_load_dwordx4 v[224:227], v13, s[36:39], s85 offen offset:0\n"
"v_mfma_f32_16x16x32_bf16 v[184:187], v[24:27], a[12:15], 0\n"
"v_perm_b32 v24, v37, v33, s56\n"
"v_perm_b32 v25, v45, v41, s56\n"
"v_mfma_f32_16x16x32_bf16 v[188:191], v[28:31], a[12:15], 0\n"
"v_perm_b32 v26, v53, v49, s56\n"
"v_perm_b32 v27, v61, v57, s56\n"
"v_mfma_f32_16x16x32_bf16 v[64:67], v[16:19], a[16:19], v[64:67]\n"
"v_perm_b32 v28, v37, v33, s57\n"
"v_perm_b32 v29, v45, v41, s57\n"
"v_mfma_f32_16x16x32_bf16 v[68:71], v[20:23], a[16:19], v[68:71]\n"
"buffer_load_dwordx4 v[228:231], v13, s[36:39], s85 offen offset:1024\n"
"v_perm_b32 v30, v53, v49, s57\n"
"v_mfma_f32_16x16x32_bf16 v[96:99], v[16:19], a[20:23], v[96:99]\n"
"v_perm_b32 v31, v61, v57, s57\n"
"v_mfma_f32_16x16x32_bf16 v[100:103], v[20:23], a[20:23], v[100:103]\n"
"v_mfma_f32_16x16x32_bf16 v[128:131], v[16:19], a[24:27], v[128:131]\n"
"v_mfma_f32_16x16x32_bf16 v[132:135], v[20:23], a[24:27], v[132:135]\n"
"buffer_load_dwordx4 v[232:235], v13, s[36:39], s85 offen offset:2048\n"
"v_mfma_f32_16x16x32_bf16 v[160:163], v[16:19], a[28:31], v[160:163]\n"
"v_perm_b32 v16, v38, v34, s56\n"
"v_perm_b32 v17, v46, v42, s56\n"
"v_mfma_f32_16x16x32_bf16 v[164:167], v[20:23], a[28:31], v[164:167]\n"
"v_perm_b32 v18, v54, v50, s56\n"
"v_perm_b32 v19, v62, v58, s56\n"
"v_mfma_f32_16x16x32_bf16 v[72:75], v[24:27], a[16:19], v[72:75]\n"
"v_perm_b32 v20, v38, v34, s57\n"
"v_perm_b32 v21, v46, v42, s57\n"
"v_mfma_f32_16x16x32_bf16 v[76:79], v[28:31], a[16:19], v[76:79]\n"
"buffer_load_dwordx4 v[236:239], v13, s[36:39], s85 offen offset:3072\n"
"v_perm_b32 v22, v54, v50, s57\n"
"v_mfma_f32_16x16x32_bf16 v[104:107], v[24:27], a[20:23], v[104:107]\n"
"v_perm_b32 v23, v62, v58, s57\n"
"s_add_u32 s83, s51, 2\n"
"v_mfma_f32_16x16x32_bf16 v[108:111], v[28:31], a[20:23], v[108:111]\n"
"s_and_b32 s83, s83, 15\n"
"s_lshl_b32 s83, s83, 11\n"
"v_mfma_f32_16x16x32_bf16 v[136:139], v[24:27], a[24:27], v[136:139]\n"
"v_add_u32 v0, s83, v12\n"
"v_mfma_f32_16x16x32_bf16 v[140:143], v[28:31], a[24:27], v[140:143]\n"
"ds_read_b128 a[0:3], v0 offset:0\n"
"buffer_load_dwordx4 v[240:243], v14, s[36:39], s85 offen offset:0\n"
"v_mfma_f32_16x16x32_bf16 v[168:171], v[24:27], a[28:31], v[168:171]\n"
"ds_read_b128 a[4:7], v0 offset:1024\n"
"v_perm_b32 v24, v39, v35, s56\n"
"v_mfma_f32_16x16x32_bf16 v[172:175], v[28:31], a[28:31], v[172:175]\n"
"ds_read_b128 a[8:11], v0 offset:32768\n"
"v_perm_b32 v25, v47, v43, s56\n"
"v_mfma_f32_16x16x32_bf16 v[80:83], v[16:19], a[16:19], v[80:83]\n"
"ds_read_b128 a[12:15], v0 offset:33792\n"
"v_perm_b32 v26, v55, v51, s56\n"
"v_mfma_f32_16x16x32_bf16 v[84:87], v[20:23], a[16:19], v[84:87]\n"
"buffer_load_dwordx4 v[244:247], v14, s[36:39], s85 offen offset:1024\n"
"v_perm_b32 v27, v63, v59, s56\n"
"v_mfma_f32_16x16x32_bf16 v[112:115], v[16:19], a[20:23], v[112:115]\n"
"v_perm_b32 v28, v39, v35, s57\n"
"v_perm_b32 v29, v47, v43, s57\n"
"v_mfma_f32_16x16x32_bf16 v[116:119], v[20:23], a[20:23], v[116:119]\n"
"v_perm_b32 v30, v55, v51, s57\n"
"v_perm_b32 v31, v63, v59, s57\n"
"v_mfma_f32_16x16x32_bf16 v[144:147], v[16:19], a[24:27], v[144:147]\n"
"v_mfma_f32_16x16x32_bf16 v[148:151], v[20:23], a[24:27], v[148:151]\n"
"buffer_load_dwordx4 v[248:251], v14, s[36:39], s85 offen offset:2048\n"
"v_mfma_f32_16x16x32_bf16 v[176:179], v[16:19], a[28:31], v[176:179]\n"
"s_waitcnt vmcnt(8)\n"
"v_perm_b32 v16, v196, v192, s56\n"
"v_perm_b32 v17, v204, v200, s56\n"
"v_mfma_f32_16x16x32_bf16 v[180:183], v[20:23], a[28:31], v[180:183]\n"
"v_perm_b32 v18, v212, v208, s56\n"
"v_perm_b32 v19, v220, v216, s56\n"
"v_mfma_f32_16x16x32_bf16 v[88:91], v[24:27], a[16:19], v[88:91]\n"
"v_perm_b32 v20, v196, v192, s57\n"
"v_perm_b32 v21, v204, v200, s57\n"
"buffer_load_dwordx4 v[252:255], v14, s[36:39], s85 offen offset:3072\n"
"v_mfma_f32_16x16x32_bf16 v[92:95], v[28:31], a[16:19], v[92:95]\n"
"v_perm_b32 v22, v212, v208, s57\n"
"v_perm_b32 v23, v220, v216, s57\n"
"v_mfma_f32_16x16x32_bf16 v[120:123], v[24:27], a[20:23], v[120:123]\n"
"s_waitcnt lgkmcnt(0)\n"
"v_mfma_f32_16x16x32_bf16 v[124:127], v[28:31], a[20:23], v[124:127]\n"
"s_add_u32 s84, s51, 4\n"
"s_and_b32 s84, s84, 15\n"
"v_mfma_f32_16x16x32_bf16 v[152:155], v[24:27], a[24:27], v[152:155]\n"
"s_lshl_b32 s84, s84, 15\n"
"v_mfma_f32_16x16x32_bf16 v[156:159], v[28:31], a[24:27], v[156:159]\n"
"buffer_load_dwordx4 v[32:35], v13, s[36:39], s84 offen offset:0\n"
"v_mfma_f32_16x16x32_bf16 v[184:187], v[24:27], a[28:31], v[184:187]\n"
"v_perm_b32 v24, v197, v193, s56\n"
"v_perm_b32 v25, v205, v201, s56\n"
"v_mfma_f32_16x16x32_bf16 v[188:191], v[28:31], a[28:31], v[188:191]\n"
"v_perm_b32 v26, v213, v209, s56\n"
"v_perm_b32 v27, v221, v217, s56\n"
"v_mfma_f32_16x16x32_bf16 v[64:67], v[16:19], a[0:3], v[64:67]\n"
"v_perm_b32 v28, v197, v193, s57\n"
"v_perm_b32 v29, v205, v201, s57\n"
"v_mfma_f32_16x16x32_bf16 v[68:71], v[20:23], a[0:3], v[68:71]\n"
"buffer_load_dwordx4 v[36:39], v13, s[36:39], s84 offen offset:1024\n"
"v_perm_b32 v30, v213, v209, s57\n"
"v_mfma_f32_16x16x32_bf16 v[96:99], v[16:19], a[4:7], v[96:99]\n"
"v_perm_b32 v31, v221, v217, s57\n"
"v_mfma_f32_16x16x32_bf16 v[100:103], v[20:23], a[4:7], v[100:103]\n"
"v_mfma_f32_16x16x32_bf16 v[128:131], v[16:19], a[8:11], v[128:131]\n"
"v_mfma_f32_16x16x32_bf16 v[132:135], v[20:23], a[8:11], v[132:135]\n"
"buffer_load_dwordx4 v[40:43], v13, s[36:39], s84 offen offset:2048\n"
"v_mfma_f32_16x16x32_bf16 v[160:163], v[16:19], a[12:15], v[160:163]\n"
"v_perm_b32 v16, v198, v194, s56\n"
"v_perm_b32 v17, v206, v202, s56\n"
"v_mfma_f32_16x16x32_bf16 v[164:167], v[20:23], a[12:15], v[164:167]\n"
"v_perm_b32 v18, v214, v210, s56\n"
"v_perm_b32 v19, v222, v218, s56\n"
"v_mfma_f32_16x16x32_bf16 v[72:75], v[24:27], a[0:3], v[72:75]\n"
"v_perm_b32 v20, v198, v194, s57\n"
"v_perm_b32 v21, v206, v202, s57\n"
"v_mfma_f32_16x16x32_bf16 v[76:79], v[28:31], a[0:3], v[76:79]\n"
"buffer_load_dwordx4 v[44:47], v13, s[36:39], s84 offen offset:3072\n"
"v_perm_b32 v22, v214, v210, s57\n"
"v_mfma_f32_16x16x32_bf16 v[104:107], v[24:27], a[4:7], v[104:107]\n"
"v_perm_b32 v23, v222, v218, s57\n"
"s_add_u32 s83, s51, 3\n"
"v_mfma_f32_16x16x32_bf16 v[108:111], v[28:31], a[4:7], v[108:111]\n"
"s_and_b32 s83, s83, 15\n"
"s_lshl_b32 s83, s83, 11\n"
"v_mfma_f32_16x16x32_bf16 v[136:139], v[24:27], a[8:11], v[136:139]\n"
"v_add_u32 v1, s83, v12\n"
"v_mfma_f32_16x16x32_bf16 v[140:143], v[28:31], a[8:11], v[140:143]\n"
"ds_read_b128 a[16:19], v1 offset:0\n"
"buffer_load_dwordx4 v[48:51], v14, s[36:39], s84 offen offset:0\n"
"v_mfma_f32_16x16x32_bf16 v[168:171], v[24:27], a[12:15], v[168:171]\n"
"ds_read_b128 a[20:23], v1 offset:1024\n"
"v_perm_b32 v24, v199, v195, s56\n"
"v_mfma_f32_16x16x32_bf16 v[172:175], v[28:31], a[12:15], v[172:175]\n"
"ds_read_b128 a[24:27], v1 offset:32768\n"
"v_perm_b32 v25, v207, v203, s56\n"
"v_mfma_f32_16x16x32_bf16 v[80:83], v[16:19], a[0:3], v[80:83]\n"
"ds_read_b128 a[28:31], v1 offset:33792\n"
"v_perm_b32 v26, v215, v211, s56\n"
"v_mfma_f32_16x16x32_bf16 v[84:87], v[20:23], a[0:3], v[84:87]\n"
"buffer_load_dwordx4 v[52:55], v14, s[36:39], s84 offen offset:1024\n"
"v_perm_b32 v27, v223, v219, s56\n"
"v_mfma_f32_16x16x32_bf16 v[112:115], v[16:19], a[4:7], v[112:115]\n"
"v_perm_b32 v28, v199, v195, s57\n"
"v_perm_b32 v29, v207, v203, s57\n"
"v_mfma_f32_16x16x32_bf16 v[116:119], v[20:23], a[4:7], v[116:119]\n"
"v_perm_b32 v30, v215, v211, s57\n"
"v_perm_b32 v31, v223, v219, s57\n"
"v_mfma_f32_16x16x32_bf16 v[144:147], v[16:19], a[8:11], v[144:147]\n"
"v_mfma_f32_16x16x32_bf16 v[148:151], v[20:23], a[8:11], v[148:151]\n"
"buffer_load_dwordx4 v[56:59], v14, s[36:39], s84 offen offset:2048\n"
"v_mfma_f32_16x16x32_bf16 v[176:179], v[16:19], a[12:15], v[176:179]\n"
"s_waitcnt vmcnt(7)\n"
"v_perm_b32 v16, v228, v224, s56\n"
"v_perm_b32 v17, v236, v232, s56\n"
"v_mfma_f32_16x16x32_bf16 v[180:183], v[20:23], a[12:15], v[180:183]\n"
"v_perm_b32 v18, v244, v240, s56\n"
"v_perm_b32 v19, v252, v248, s56\n"
"v_mfma_f32_16x16x32_bf16 v[88:91], v[24:27], a[0:3], v[88:91]\n"
"v_perm_b32 v20, v228, v224, s57\n"
"v_perm_b32 v21, v236, v232, s57\n"
"buffer_load_dwordx4 v[60:63], v14, s[36:39], s84 offen offset:3072\n"
"v_mfma_f32_16x16x32_bf16 v[92:95], v[28:31], a[0:3], v[92:95]\n"
"v_perm_b32 v22, v244, v240, s57\n"
"v_perm_b32 v23, v252, v248, s57\n"
"v_mfma_f32_16x16x32_bf16 v[120:123], v[24:27], a[4:7], v[120:123]\n"
"s_waitcnt lgkmcnt(0)\n"
"v_mfma_f32_16x16x32_bf16 v[124:127], v[28:31], a[4:7], v[124:127]\n"
"s_add_u32 s85, s51, 5\n"
"s_and_b32 s85, s85, 15\n"
"v_mfma_f32_16x16x32_bf16 v[152:155], v[24:27], a[8:11], v[152:155]\n"
"s_lshl_b32 s85, s85, 15\n"
"v_mfma_f32_16x16x32_bf16 v[156:159], v[28:31], a[8:11], v[156:159]\n"
"buffer_load_dwordx4 v[192:195], v13, s[36:39], s85 offen offset:0\n"
"v_mfma_f32_16x16x32_bf16 v[184:187], v[24:27], a[12:15], v[184:187]\n"
"v_perm_b32 v24, v229, v225, s56\n"
"v_perm_b32 v25, v237, v233, s56\n"
"v_mfma_f32_16x16x32_bf16 v[188:191], v[28:31], a[12:15], v[188:191]\n"
"v_perm_b32 v26, v245, v241, s56\n"
"v_perm_b32 v27, v253, v249, s56\n"
"v_mfma_f32_16x16x32_bf16 v[64:67], v[16:19], a[16:19], v[64:67]\n"
"v_perm_b32 v28, v229, v225, s57\n"
"v_perm_b32 v29, v237, v233, s57\n"
"v_mfma_f32_16x16x32_bf16 v[68:71], v[20:23], a[16:19], v[68:71]\n"
"buffer_load_dwordx4 v[196:199], v13, s[36:39], s85 offen offset:1024\n"
"v_perm_b32 v30, v245, v241, s57\n"
"v_mfma_f32_16x16x32_bf16 v[96:99], v[16:19], a[20:23], v[96:99]\n"
"v_perm_b32 v31, v253, v249, s57\n"
"v_mfma_f32_16x16x32_bf16 v[100:103], v[20:23], a[20:23], v[100:103]\n"
"v_mfma_f32_16x16x32_bf16 v[128:131], v[16:19], a[24:27], v[128:131]\n"
"v_mfma_f32_16x16x32_bf16 v[132:135], v[20:23], a[24:27], v[132:135]\n"
"buffer_load_dwordx4 v[200:203], v13, s[36:39], s85 offen offset:2048\n"
"v_mfma_f32_16x16x32_bf16 v[160:163], v[16:19], a[28:31], v[160:163]\n"
"v_perm_b32 v16, v230, v226, s56\n"
"v_perm_b32 v17, v238, v234, s56\n"
"v_mfma_f32_16x16x32_bf16 v[164:167], v[20:23], a[28:31], v[164:167]\n"
"v_perm_b32 v18, v246, v242, s56\n"
"v_perm_b32 v19, v254, v250, s56\n"
"v_mfma_f32_16x16x32_bf16 v[72:75], v[24:27], a[16:19], v[72:75]\n"
"v_perm_b32 v20, v230, v226, s57\n"
"v_perm_b32 v21, v238, v234, s57\n"
"v_mfma_f32_16x16x32_bf16 v[76:79], v[28:31], a[16:19], v[76:79]\n"
"buffer_load_dwordx4 v[204:207], v13, s[36:39], s85 offen offset:3072\n"
"v_perm_b32 v22, v246, v242, s57\n"
"v_mfma_f32_16x16x32_bf16 v[104:107], v[24:27], a[20:23], v[104:107]\n"
"v_perm_b32 v23, v254, v250, s57\n"
"s_add_u32 s83, s51, 4\n"
"v_mfma_f32_16x16x32_bf16 v[108:111], v[28:31], a[20:23], v[108:111]\n"
"s_and_b32 s83, s83, 15\n"
"s_lshl_b32 s83, s83, 11\n"
"v_mfma_f32_16x16x32_bf16 v[136:139], v[24:27], a[24:27], v[136:139]\n"
"v_add_u32 v0, s83, v12\n"
"v_mfma_f32_16x16x32_bf16 v[140:143], v[28:31], a[24:27], v[140:143]\n"
"ds_read_b128 a[0:3], v0 offset:0\n"
"buffer_load_dwordx4 v[208:211], v14, s[36:39], s85 offen offset:0\n"
"v_mfma_f32_16x16x32_bf16 v[168:171], v[24:27], a[28:31], v[168:171]\n"
"ds_read_b128 a[4:7], v0 offset:1024\n"
"v_perm_b32 v24, v231, v227, s56\n"
"v_mfma_f32_16x16x32_bf16 v[172:175], v[28:31], a[28:31], v[172:175]\n"
"ds_read_b128 a[8:11], v0 offset:32768\n"
"v_perm_b32 v25, v239, v235, s56\n"
"v_mfma_f32_16x16x32_bf16 v[80:83], v[16:19], a[16:19], v[80:83]\n"
"ds_read_b128 a[12:15], v0 offset:33792\n"
"v_perm_b32 v26, v247, v243, s56\n"
"v_mfma_f32_16x16x32_bf16 v[84:87], v[20:23], a[16:19], v[84:87]\n"
"buffer_load_dwordx4 v[212:215], v14, s[36:39], s85 offen offset:1024\n"
"v_perm_b32 v27, v255, v251, s56\n"
"v_mfma_f32_16x16x32_bf16 v[112:115], v[16:19], a[20:23], v[112:115]\n"
"v_perm_b32 v28, v231, v227, s57\n"
"v_perm_b32 v29, v239, v235, s57\n"
"v_mfma_f32_16x16x32_bf16 v[116:119], v[20:23], a[20:23], v[116:119]\n"
"v_perm_b32 v30, v247, v243, s57\n"
"v_perm_b32 v31, v255, v251, s57\n"
"v_mfma_f32_16x16x32_bf16 v[144:147], v[16:19], a[24:27], v[144:147]\n"
"v_mfma_f32_16x16x32_bf16 v[148:151], v[20:23], a[24:27], v[148:151]\n"
"buffer_load_dwordx4 v[216:219], v14, s[36:39], s85 offen offset:2048\n"
"v_mfma_f32_16x16x32_bf16 v[176:179], v[16:19], a[28:31], v[176:179]\n"
"s_waitcnt vmcnt(7)\n"
"v_perm_b32 v16, v36, v32, s56\n"
"v_perm_b32 v17, v44, v40, s56\n"
"v_mfma_f32_16x16x32_bf16 v[180:183], v[20:23], a[28:31], v[180:183]\n"
"v_perm_b32 v18, v52, v48, s56\n"
"v_perm_b32 v19, v60, v56, s56\n"
"v_mfma_f32_16x16x32_bf16 v[88:91], v[24:27], a[16:19], v[88:91]\n"
"v_perm_b32 v20, v36, v32, s57\n"
"v_perm_b32 v21, v44, v40, s57\n"
"buffer_load_dwordx4 v[220:223], v14, s[36:39], s85 offen offset:3072\n"
"v_mfma_f32_16x16x32_bf16 v[92:95], v[28:31], a[16:19], v[92:95]\n"
"v_perm_b32 v22, v52, v48, s57\n"
"v_perm_b32 v23, v60, v56, s57\n"
"v_mfma_f32_16x16x32_bf16 v[120:123], v[24:27], a[20:23], v[120:123]\n"
"s_waitcnt lgkmcnt(0)\n"
"v_mfma_f32_16x16x32_bf16 v[124:127], v[28:31], a[20:23], v[124:127]\n"
"s_add_u32 s84, s51, 6\n"
"s_and_b32 s84, s84, 15\n"
"v_mfma_f32_16x16x32_bf16 v[152:155], v[24:27], a[24:27], v[152:155]\n"
"s_lshl_b32 s84, s84, 15\n"
"v_mfma_f32_16x16x32_bf16 v[156:159], v[28:31], a[24:27], v[156:159]\n"
"buffer_load_dwordx4 v[224:227], v13, s[36:39], s84 offen offset:0\n"
"v_mfma_f32_16x16x32_bf16 v[184:187], v[24:27], a[28:31], v[184:187]\n"
"v_perm_b32 v24, v37, v33, s56\n"
"v_perm_b32 v25, v45, v41, s56\n"
"v_mfma_f32_16x16x32_bf16 v[188:191], v[28:31], a[28:31], v[188:191]\n"
"v_perm_b32 v26, v53, v49, s56\n"
"v_perm_b32 v27, v61, v57, s56\n"
"v_mfma_f32_16x16x32_bf16 v[64:67], v[16:19], a[0:3], v[64:67]\n"
"v_perm_b32 v28, v37, v33, s57\n"
"v_perm_b32 v29, v45, v41, s57\n"
"v_mfma_f32_16x16x32_bf16 v[68:71], v[20:23], a[0:3], v[68:71]\n"
"buffer_load_dwordx4 v[228:231], v13, s[36:39], s84 offen offset:1024\n"
"v_perm_b32 v30, v53, v49, s57\n"
"v_mfma_f32_16x16x32_bf16 v[96:99], v[16:19], a[4:7], v[96:99]\n"
"v_perm_b32 v31, v61, v57, s57\n"
"v_mfma_f32_16x16x32_bf16 v[100:103], v[20:23], a[4:7], v[100:103]\n"
"v_mfma_f32_16x16x32_bf16 v[128:131], v[16:19], a[8:11], v[128:131]\n"
"v_mfma_f32_16x16x32_bf16 v[132:135], v[20:23], a[8:11], v[132:135]\n"
"buffer_load_dwordx4 v[232:235], v13, s[36:39], s84 offen offset:2048\n"
"v_mfma_f32_16x16x32_bf16 v[160:163], v[16:19], a[12:15], v[160:163]\n"
"v_perm_b32 v16, v38, v34, s56\n"
"v_perm_b32 v17, v46, v42, s56\n"
"v_mfma_f32_16x16x32_bf16 v[164:167], v[20:23], a[12:15], v[164:167]\n"
"v_perm_b32 v18, v54, v50, s56\n"
"v_perm_b32 v19, v62, v58, s56\n"
"v_mfma_f32_16x16x32_bf16 v[72:75], v[24:27], a[0:3], v[72:75]\n"
"v_perm_b32 v20, v38, v34, s57\n"
"v_perm_b32 v21, v46, v42, s57\n"
"v_mfma_f32_16x16x32_bf16 v[76:79], v[28:31], a[0:3], v[76:79]\n"
"buffer_load_dwordx4 v[236:239], v13, s[36:39], s84 offen offset:3072\n"
"v_perm_b32 v22, v54, v50, s57\n"
"v_mfma_f32_16x16x32_bf16 v[104:107], v[24:27], a[4:7], v[104:107]\n"
"v_perm_b32 v23, v62, v58, s57\n"
"s_add_u32 s83, s51, 5\n"
"v_mfma_f32_16x16x32_bf16 v[108:111], v[28:31], a[4:7], v[108:111]\n"
"s_and_b32 s83, s83, 15\n"
"s_lshl_b32 s83, s83, 11\n"
"v_mfma_f32_16x16x32_bf16 v[136:139], v[24:27], a[8:11], v[136:139]\n"
"v_add_u32 v1, s83, v12\n"
"v_mfma_f32_16x16x32_bf16 v[140:143], v[28:31], a[8:11], v[140:143]\n"
"ds_read_b128 a[16:19], v1 offset:0\n"
"buffer_load_dwordx4 v[240:243], v14, s[36:39], s84 offen offset:0\n"
"v_mfma_f32_16x16x32_bf16 v[168:171], v[24:27], a[12:15], v[168:171]\n"
"ds_read_b128 a[20:23], v1 offset:1024\n"
"v_perm_b32 v24, v39, v35, s56\n"
"v_mfma_f32_16x16x32_bf16 v[172:175], v[28:31], a[12:15], v[172:175]\n"
"ds_read_b128 a[24:27], v1 offset:32768\n"
"v_perm_b32 v25, v47, v43, s56\n"
"v_mfma_f32_16x16x32_bf16 v[80:83], v[16:19], a[0:3], v[80:83]\n"
"ds_read_b128 a[28:31], v1 offset:33792\n"
"v_perm_b32 v26, v55, v51, s56\n"
"v_mfma_f32_16x16x32_bf16 v[84:87], v[20:23], a[0:3], v[84:87]\n"
"buffer_load_dwordx4 v[244:247], v14, s[36:39], s84 offen offset:1024\n"
"v_perm_b32 v27, v63, v59, s56\n"
"v_mfma_f32_16x16x32_bf16 v[112:115], v[16:19], a[4:7], v[112:115]\n"
"v_perm_b32 v28, v39, v35, s57\n"
"v_perm_b32 v29, v47, v43, s57\n"
"v_mfma_f32_16x16x32_bf16 v[116:119], v[20:23], a[4:7], v[116:119]\n"
"v_perm_b32 v30, v55, v51, s57\n"
"v_perm_b32 v31, v63, v59, s57\n"
"v_mfma_f32_16x16x32_bf16 v[144:147], v[16:19], a[8:11], v[144:147]\n"
"v_mfma_f32_16x16x32_bf16 v[148:151], v[20:23], a[8:11], v[148:151]\n"
"buffer_load_dwordx4 v[248:251], v14, s[36:39], s84 offen offset:2048\n"
"v_mfma_f32_16x16x32_bf16 v[176:179], v[16:19], a[12:15], v[176:179]\n"
"s_waitcnt vmcnt(7)\n"
"v_perm_b32 v16, v196, v192, s56\n"
"v_perm_b32 v17, v204, v200, s56\n"
"v_mfma_f32_16x16x32_bf16 v[180:183], v[20:23], a[12:15], v[180:183]\n"
"v_perm_b32 v18, v212, v208, s56\n"
"v_perm_b32 v19, v220, v216, s56\n"
"v_mfma_f32_16x16x32_bf16 v[88:91], v[24:27], a[0:3], v[88:91]\n"
"v_perm_b32 v20, v196, v192, s57\n"
"v_perm_b32 v21, v204, v200, s57\n"
"buffer_load_dwordx4 v[252:255], v14, s[36:39], s84 offen offset:3072\n"
"v_mfma_f32_16x16x32_bf16 v[92:95], v[28:31], a[0:3], v[92:95]\n"
"v_perm_b32 v22, v212, v208, s57\n"
"v_perm_b32 v23, v220, v216, s57\n"
"v_mfma_f32_16x16x32_bf16 v[120:123], v[24:27], a[4:7], v[120:123]\n"
"s_waitcnt lgkmcnt(0)\n"
"v_mfma_f32_16x16x32_bf16 v[124:127], v[28:31], a[4:7], v[124:127]\n"
"s_add_u32 s85, s51, 7\n"
"s_and_b32 s85, s85, 15\n"
"v_mfma_f32_16x16x32_bf16 v[152:155], v[24:27], a[8:11], v[152:155]\n"
"s_lshl_b32 s85, s85, 15\n"
"v_mfma_f32_16x16x32_bf16 v[156:159], v[28:31], a[8:11], v[156:159]\n"
"buffer_load_dwordx4 v[32:35], v13, s[36:39], s85 offen offset:0\n"
"v_mfma_f32_16x16x32_bf16 v[184:187], v[24:27], a[12:15], v[184:187]\n"
"v_perm_b32 v24, v197, v193, s56\n"
"v_perm_b32 v25, v205, v201, s56\n"
"v_mfma_f32_16x16x32_bf16 v[188:191], v[28:31], a[12:15], v[188:191]\n"
"v_perm_b32 v26, v213, v209, s56\n"
"v_perm_b32 v27, v221, v217, s56\n"
"v_mfma_f32_16x16x32_bf16 v[64:67], v[16:19], a[16:19], v[64:67]\n"
"v_perm_b32 v28, v197, v193, s57\n"
"v_perm_b32 v29, v205, v201, s57\n"
"v_mfma_f32_16x16x32_bf16 v[68:71], v[20:23], a[16:19], v[68:71]\n"
"buffer_load_dwordx4 v[36:39], v13, s[36:39], s85 offen offset:1024\n"
"v_perm_b32 v30, v213, v209, s57\n"
"v_mfma_f32_16x16x32_bf16 v[96:99], v[16:19], a[20:23], v[96:99]\n"
"v_perm_b32 v31, v221, v217, s57\n"
"v_mfma_f32_16x16x32_bf16 v[100:103], v[20:23], a[20:23], v[100:103]\n"
"v_mfma_f32_16x16x32_bf16 v[128:131], v[16:19], a[24:27], v[128:131]\n"
"v_mfma_f32_16x16x32_bf16 v[132:135], v[20:23], a[24:27], v[132:135]\n"
"buffer_load_dwordx4 v[40:43], v13, s[36:39], s85 offen offset:2048\n"
"v_mfma_f32_16x16x32_bf16 v[160:163], v[16:19], a[28:31], v[160:163]\n"
"v_perm_b32 v16, v198, v194, s56\n"
"v_perm_b32 v17, v206, v202, s56\n"
"v_mfma_f32_16x16x32_bf16 v[164:167], v[20:23], a[28:31], v[164:167]\n"
"v_perm_b32 v18, v214, v210, s56\n"
"v_perm_b32 v19, v222, v218, s56\n"
"v_mfma_f32_16x16x32_bf16 v[72:75], v[24:27], a[16:19], v[72:75]\n"
"v_perm_b32 v20, v198, v194, s57\n"
"v_perm_b32 v21, v206, v202, s57\n"
"v_mfma_f32_16x16x32_bf16 v[76:79], v[28:31], a[16:19], v[76:79]\n"
"buffer_load_dwordx4 v[44:47], v13, s[36:39], s85 offen offset:3072\n"
"v_perm_b32 v22, v214, v210, s57\n"
"v_mfma_f32_16x16x32_bf16 v[104:107], v[24:27], a[20:23], v[104:107]\n"
"v_perm_b32 v23, v222, v218, s57\n"
"s_add_u32 s83, s51, 6\n"
"v_mfma_f32_16x16x32_bf16 v[108:111], v[28:31], a[20:23], v[108:111]\n"
"s_and_b32 s83, s83, 15\n"
"s_lshl_b32 s83, s83, 11\n"
"v_mfma_f32_16x16x32_bf16 v[136:139], v[24:27], a[24:27], v[136:139]\n"
"v_add_u32 v0, s83, v12\n"
"v_mfma_f32_16x16x32_bf16 v[140:143], v[28:31], a[24:27], v[140:143]\n"
"ds_read_b128 a[0:3], v0 offset:0\n"
"buffer_load_dwordx4 v[48:51], v14, s[36:39], s85 offen offset:0\n"
"v_mfma_f32_16x16x32_bf16 v[168:171], v[24:27], a[28:31], v[168:171]\n"
"ds_read_b128 a[4:7], v0 offset:1024\n"
"v_perm_b32 v24, v199, v195, s56\n"
"v_mfma_f32_16x16x32_bf16 v[172:175], v[28:31], a[28:31], v[172:175]\n"
"ds_read_b128 a[8:11], v0 offset:32768\n"
"v_perm_b32 v25, v207, v203, s56\n"
"v_mfma_f32_16x16x32_bf16 v[80:83], v[16:19], a[16:19], v[80:83]\n"
"ds_read_b128 a[12:15], v0 offset:33792\n"
"v_perm_b32 v26, v215, v211, s56\n"
"v_mfma_f32_16x16x32_bf16 v[84:87], v[20:23], a[16:19], v[84:87]\n"
"buffer_load_dwordx4 v[52:55], v14, s[36:39], s85 offen offset:1024\n"
"v_perm_b32 v27, v223, v219, s56\n"
"v_mfma_f32_16x16x32_bf16 v[112:115], v[16:19], a[20:23], v[112:115]\n"
"v_perm_b32 v28, v199, v195, s57\n"
"v_perm_b32 v29, v207, v203, s57\n"
"v_mfma_f32_16x16x32_bf16 v[116:119], v[20:23], a[20:23], v[116:119]\n"
"v_perm_b32 v30, v215, v211, s57\n"
"v_perm_b32 v31, v223, v219, s57\n"
"v_mfma_f32_16x16x32_bf16 v[144:147], v[16:19], a[24:27], v[144:147]\n"
"v_mfma_f32_16x16x32_bf16 v[148:151], v[20:23], a[24:27], v[148:151]\n"
"buffer_load_dwordx4 v[56:59], v14, s[36:39], s85 offen offset:2048\n"
"v_mfma_f32_16x16x32_bf16 v[176:179], v[16:19], a[28:31], v[176:179]\n"
"s_waitcnt vmcnt(7)\n"
"v_perm_b32 v16, v228, v224, s56\n"
"v_perm_b32 v17, v236, v232, s56\n"
"v_mfma_f32_16x16x32_bf16 v[180:183], v[20:23], a[28:31], v[180:183]\n"
"v_perm_b32 v18, v244, v240, s56\n"
"v_perm_b32 v19, v252, v248, s56\n"
"v_mfma_f32_16x16x32_bf16 v[88:91], v[24:27], a[16:19], v[88:91]\n"
"v_perm_b32 v20, v228, v224, s57\n"
"v_perm_b32 v21, v236, v232, s57\n"
"buffer_load_dwordx4 v[60:63], v14, s[36:39], s85 offen offset:3072\n"
"v_mfma_f32_16x16x32_bf16 v[92:95], v[28:31], a[16:19], v[92:95]\n"
"v_perm_b32 v22, v244, v240, s57\n"
"v_perm_b32 v23, v252, v248, s57\n"
"v_mfma_f32_16x16x32_bf16 v[120:123], v[24:27], a[20:23], v[120:123]\n"
"s_waitcnt lgkmcnt(0)\n"
"v_mfma_f32_16x16x32_bf16 v[124:127], v[28:31], a[20:23], v[124:127]\n"
"s_add_u32 s84, s51, 8\n"
"s_and_b32 s84, s84, 15\n"
"v_mfma_f32_16x16x32_bf16 v[152:155], v[24:27], a[24:27], v[152:155]\n"
"s_lshl_b32 s84, s84, 15\n"
"v_mfma_f32_16x16x32_bf16 v[156:159], v[28:31], a[24:27], v[156:159]\n"
"buffer_load_dwordx4 v[192:195], v13, s[36:39], s84 offen offset:0\n"
"v_mfma_f32_16x16x32_bf16 v[184:187], v[24:27], a[28:31], v[184:187]\n"
"v_perm_b32 v24, v229, v225, s56\n"
"v_perm_b32 v25, v237, v233, s56\n"
"v_mfma_f32_16x16x32_bf16 v[188:191], v[28:31], a[28:31], v[188:191]\n"
"v_perm_b32 v26, v245, v241, s56\n"
"v_perm_b32 v27, v253, v249, s56\n"
"v_mfma_f32_16x16x32_bf16 v[64:67], v[16:19], a[0:3], v[64:67]\n"
"v_perm_b32 v28, v229, v225, s57\n"
"v_perm_b32 v29, v237, v233, s57\n"
"v_mfma_f32_16x16x32_bf16 v[68:71], v[20:23], a[0:3], v[68:71]\n"
"buffer_load_dwordx4 v[196:199], v13, s[36:39], s84 offen offset:1024\n"
"v_perm_b32 v30, v245, v241, s57\n"
"v_mfma_f32_16x16x32_bf16 v[96:99], v[16:19], a[4:7], v[96:99]\n"
"v_perm_b32 v31, v253, v249, s57\n"
"v_mfma_f32_16x16x32_bf16 v[100:103], v[20:23], a[4:7], v[100:103]\n"
"v_mfma_f32_16x16x32_bf16 v[128:131], v[16:19], a[8:11], v[128:131]\n"
"v_mfma_f32_16x16x32_bf16 v[132:135], v[20:23], a[8:11], v[132:135]\n"
"buffer_load_dwordx4 v[200:203], v13, s[36:39], s84 offen offset:2048\n"
"v_mfma_f32_16x16x32_bf16 v[160:163], v[16:19], a[12:15], v[160:163]\n"
"v_perm_b32 v16, v230, v226, s56\n"
"v_perm_b32 v17, v238, v234, s56\n"
"v_mfma_f32_16x16x32_bf16 v[164:167], v[20:23], a[12:15], v[164:167]\n"
"v_perm_b32 v18, v246, v242, s56\n"
"v_perm_b32 v19, v254, v250, s56\n"
"v_mfma_f32_16x16x32_bf16 v[72:75], v[24:27], a[0:3], v[72:75]\n"
"v_perm_b32 v20, v230, v226, s57\n"
"v_perm_b32 v21, v238, v234, s57\n"
"v_mfma_f32_16x16x32_bf16 v[76:79], v[28:31], a[0:3], v[76:79]\n"
"buffer_load_dwordx4 v[204:207], v13, s[36:39], s84 offen offset:3072\n"
"v_perm_b32 v22, v246, v242, s57\n"
"v_mfma_f32_16x16x32_bf16 v[104:107], v[24:27], a[4:7], v[104:107]\n"
"v_perm_b32 v23, v254, v250, s57\n"
"s_add_u32 s83, s51, 7\n"
"v_mfma_f32_16x16x32_bf16 v[108:111], v[28:31], a[4:7], v[108:111]\n"
"s_and_b32 s83, s83, 15\n"
"s_lshl_b32 s83, s83, 11\n"
"v_mfma_f32_16x16x32_bf16 v[136:139], v[24:27], a[8:11], v[136:139]\n"
"v_add_u32 v1, s83, v12\n"
"v_mfma_f32_16x16x32_bf16 v[140:143], v[28:31], a[8:11], v[140:143]\n"
"ds_read_b128 a[16:19], v1 offset:0\n"
"buffer_load_dwordx4 v[208:211], v14, s[36:39], s84 offen offset:0\n"
"v_mfma_f32_16x16x32_bf16 v[168:171], v[24:27], a[12:15], v[168:171]\n"
"ds_read_b128 a[20:23], v1 offset:1024\n"
"v_perm_b32 v24, v231, v227, s56\n"
"v_mfma_f32_16x16x32_bf16 v[172:175], v[28:31], a[12:15], v[172:175]\n"
"ds_read_b128 a[24:27], v1 offset:32768\n"
"v_perm_b32 v25, v239, v235, s56\n"
"v_mfma_f32_16x16x32_bf16 v[80:83], v[16:19], a[0:3], v[80:83]\n"
"ds_read_b128 a[28:31], v1 offset:33792\n"
"v_perm_b32 v26, v247, v243, s56\n"
"v_mfma_f32_16x16x32_bf16 v[84:87], v[20:23], a[0:3], v[84:87]\n"
"buffer_load_dwordx4 v[212:215], v14, s[36:39], s84 offen offset:1024\n"
"v_perm_b32 v27, v255, v251, s56\n"
"v_mfma_f32_16x16x32_bf16 v[112:115], v[16:19], a[4:7], v[112:115]\n"
"v_perm_b32 v28, v231, v227, s57\n"
"v_perm_b32 v29, v239, v235, s57\n"
"v_mfma_f32_16x16x32_bf16 v[116:119], v[20:23], a[4:7], v[116:119]\n"
"v_perm_b32 v30, v247, v243, s57\n"
"v_perm_b32 v31, v255, v251, s57\n"
"v_mfma_f32_16x16x32_bf16 v[144:147], v[16:19], a[8:11], v[144:147]\n"
"v_mfma_f32_16x16x32_bf16 v[148:151], v[20:23], a[8:11], v[148:151]\n"
"buffer_load_dwordx4 v[216:219], v14, s[36:39], s84 offen offset:2048\n"
"v_mfma_f32_16x16x32_bf16 v[176:179], v[16:19], a[12:15], v[176:179]\n"
"s_waitcnt vmcnt(7)\n"
"v_perm_b32 v16, v36, v32, s56\n"
"v_perm_b32 v17, v44, v40, s56\n"
"v_mfma_f32_16x16x32_bf16 v[180:183], v[20:23], a[12:15], v[180:183]\n"
"v_perm_b32 v18, v52, v48, s56\n"
"v_perm_b32 v19, v60, v56, s56\n"
"v_mfma_f32_16x16x32_bf16 v[88:91], v[24:27], a[0:3], v[88:91]\n"
"v_perm_b32 v20, v36, v32, s57\n"
"v_perm_b32 v21, v44, v40, s57\n"
"buffer_load_dwordx4 v[220:223], v14, s[36:39], s84 offen offset:3072\n"
"v_mfma_f32_16x16x32_bf16 v[92:95], v[28:31], a[0:3], v[92:95]\n"
"v_perm_b32 v22, v52, v48, s57\n"
"v_perm_b32 v23, v60, v56, s57\n"
"v_mfma_f32_16x16x32_bf16 v[120:123], v[24:27], a[4:7], v[120:123]\n"
"s_waitcnt lgkmcnt(0)\n"
"v_mfma_f32_16x16x32_bf16 v[124:127], v[28:31], a[4:7], v[124:127]\n"
"s_add_u32 s85, s51, 9\n"
"s_and_b32 s85, s85, 15\n"
"v_mfma_f32_16x16x32_bf16 v[152:155], v[24:27], a[8:11], v[152:155]\n"
"s_lshl_b32 s85, s85, 15\n"
"v_mfma_f32_16x16x32_bf16 v[156:159], v[28:31], a[8:11], v[156:159]\n"
"buffer_load_dwordx4 v[224:227], v13, s[36:39], s85 offen offset:0\n"
"v_mfma_f32_16x16x32_bf16 v[184:187], v[24:27], a[12:15], v[184:187]\n"
"v_perm_b32 v24, v37, v33, s56\n"
"v_perm_b32 v25, v45, v41, s56\n"
"v_mfma_f32_16x16x32_bf16 v[188:191], v[28:31], a[12:15], v[188:191]\n"
"v_perm_b32 v26, v53, v49, s56\n"
"v_perm_b32 v27, v61, v57, s56\n"
"v_mfma_f32_16x16x32_bf16 v[64:67], v[16:19], a[16:19], v[64:67]\n"
"v_perm_b32 v28, v37, v33, s57\n"
"v_perm_b32 v29, v45, v41, s57\n"
"v_mfma_f32_16x16x32_bf16 v[68:71], v[20:23], a[16:19], v[68:71]\n"
"buffer_load_dwordx4 v[228:231], v13, s[36:39], s85 offen offset:1024\n"
"v_perm_b32 v30, v53, v49, s57\n"
"v_mfma_f32_16x16x32_bf16 v[96:99], v[16:19], a[20:23], v[96:99]\n"
"v_perm_b32 v31, v61, v57, s57\n"
"v_mbcnt_lo_u32_b32 v10, -1, 0\n"
"v_mfma_f32_16x16x32_bf16 v[100:103], v[20:23], a[20:23], v[100:103]\n"
"v_mbcnt_hi_u32_b32 v10, -1, v10\n"
"v_and_b32 v11, 7, v10\n"
"v_mfma_f32_16x16x32_bf16 v[128:131], v[16:19], a[24:27], v[128:131]\n"
"s_lshl_b32 s80, s53, 2\n"
"s_add_u32 s80, s80, s48\n"
"v_mfma_f32_16x16x32_bf16 v[132:135], v[20:23], a[24:27], v[132:135]\n"
"buffer_load_dwordx4 v[232:235], v13, s[36:39], s85 offen offset:2048\n"
"s_lshl_b32 s80, s80, 7\n"
"v_mfma_f32_16x16x32_bf16 v[160:163], v[16:19], a[28:31], v[160:163]\n"
"v_perm_b32 v16, v38, v34, s56\n"
"v_perm_b32 v17, v46, v42, s56\n"
"v_mfma_f32_16x16x32_bf16 v[164:167], v[20:23], a[28:31], v[164:167]\n"
"v_perm_b32 v18, v54, v50, s56\n"
"v_perm_b32 v19, v62, v58, s56\n"
"v_mfma_f32_16x16x32_bf16 v[72:75], v[24:27], a[16:19], v[72:75]\n"
"v_perm_b32 v20, v38, v34, s57\n"
"v_perm_b32 v21, v46, v42, s57\n"
"v_mfma_f32_16x16x32_bf16 v[76:79], v[28:31], a[16:19], v[76:79]\n"
"buffer_load_dwordx4 v[236:239], v13, s[36:39], s85 offen offset:3072\n"
"v_perm_b32 v22, v54, v50, s57\n"
"v_mfma_f32_16x16x32_bf16 v[104:107], v[24:27], a[20:23], v[104:107]\n"
"v_perm_b32 v23, v62, v58, s57\n"
"s_add_u32 s83, s51, 8\n"
"v_mfma_f32_16x16x32_bf16 v[108:111], v[28:31], a[20:23], v[108:111]\n"
"s_and_b32 s83, s83, 15\n"
"s_lshl_b32 s83, s83, 11\n"
"v_mfma_f32_16x16x32_bf16 v[136:139], v[24:27], a[24:27], v[136:139]\n"
"v_add_u32 v0, s83, v12\n"
"s_add_u32 s80, s80, 524288\n"
"v_mfma_f32_16x16x32_bf16 v[140:143], v[28:31], a[24:27], v[140:143]\n"
"ds_read_b128 a[0:3], v0 offset:0\n"
"buffer_load_dwordx4 v[240:243], v14, s[36:39], s85 offen offset:0\n"
"v_mfma_f32_16x16x32_bf16 v[168:171], v[24:27], a[28:31], v[168:171]\n"
"ds_read_b128 a[4:7], v0 offset:1024\n"
"v_perm_b32 v24, v39, v35, s56\n"
"v_mfma_f32_16x16x32_bf16 v[172:175], v[28:31], a[28:31], v[172:175]\n"
"ds_read_b128 a[8:11], v0 offset:32768\n"
"v_perm_b32 v25, v47, v43, s56\n"
"v_mfma_f32_16x16x32_bf16 v[80:83], v[16:19], a[16:19], v[80:83]\n"
"ds_read_b128 a[12:15], v0 offset:33792\n"
"v_perm_b32 v26, v55, v51, s56\n"
"v_mfma_f32_16x16x32_bf16 v[84:87], v[20:23], a[16:19], v[84:87]\n"
"buffer_load_dwordx4 v[244:247], v14, s[36:39], s85 offen offset:1024\n"
"v_perm_b32 v27, v63, v59, s56\n"
"v_mfma_f32_16x16x32_bf16 v[112:115], v[16:19], a[20:23], v[112:115]\n"
"v_perm_b32 v28, v39, v35, s57\n"
"v_perm_b32 v29, v47, v43, s57\n"
"v_mfma_f32_16x16x32_bf16 v[116:119], v[20:23], a[20:23], v[116:119]\n"
"v_perm_b32 v30, v55, v51, s57\n"
"v_perm_b32 v31, v63, v59, s57\n"
"v_mfma_f32_16x16x32_bf16 v[144:147], v[16:19], a[24:27], v[144:147]\n"
"v_lshl_add_u32 v4, v11, 2, s80\n"
"s_lshl_b32 s80, s53, 2\n"
"v_mfma_f32_16x16x32_bf16 v[148:151], v[20:23], a[24:27], v[148:151]\n"
"buffer_load_dwordx4 v[248:251], v14, s[36:39], s85 offen offset:2048\n"
"s_add_u32 s80, s80, s48\n"
"v_mfma_f32_16x16x32_bf16 v[176:179], v[16:19], a[28:31], v[176:179]\n"
"s_waitcnt vmcnt(7)\n"
"v_perm_b32 v16, v196, v192, s56\n"
"v_perm_b32 v17, v204, v200, s56\n"
"v_mfma_f32_16x16x32_bf16 v[180:183], v[20:23], a[28:31], v[180:183]\n"
"v_perm_b32 v18, v212, v208, s56\n"
"v_perm_b32 v19, v220, v216, s56\n"
"v_mfma_f32_16x16x32_bf16 v[88:91], v[24:27], a[16:19], v[88:91]\n"
"v_perm_b32 v20, v196, v192, s57\n"
"v_perm_b32 v21, v204, v200, s57\n"
"buffer_load_dwordx4 v[252:255], v14, s[36:39], s85 offen offset:3072\n"
"v_mfma_f32_16x16x32_bf16 v[92:95], v[28:31], a[16:19], v[92:95]\n"
"v_perm_b32 v22, v212, v208, s57\n"
"v_perm_b32 v23, v220, v216, s57\n"
"v_mfma_f32_16x16x32_bf16 v[120:123], v[24:27], a[20:23], v[120:123]\n"
"s_waitcnt lgkmcnt(0)\n"
"s_lshl_b32 s80, s80, 7\n"
"s_add_u32 s80, s80, 524352\n"
"v_mfma_f32_16x16x32_bf16 v[124:127], v[28:31], a[20:23], v[124:127]\n"
"s_add_u32 s84, s51, 10\n"
"s_and_b32 s84, s84, 15\n"
"v_mfma_f32_16x16x32_bf16 v[152:155], v[24:27], a[24:27], v[152:155]\n"
"s_lshl_b32 s84, s84, 15\n"
"v_lshl_add_u32 v5, v11, 3, s80\n"
"v_mfma_f32_16x16x32_bf16 v[156:159], v[28:31], a[24:27], v[156:159]\n"
"buffer_load_dwordx4 v[32:35], v13, s[36:39], s84 offen offset:0\n"
"s_lshl_b32 s80, s48, 10\n"
"v_mfma_f32_16x16x32_bf16 v[184:187], v[24:27], a[28:31], v[184:187]\n"
"v_perm_b32 v24, v197, v193, s56\n"
"v_perm_b32 v25, v205, v201, s56\n"
"v_mfma_f32_16x16x32_bf16 v[188:191], v[28:31], a[28:31], v[188:191]\n"
"v_perm_b32 v26, v213, v209, s56\n"
"v_perm_b32 v27, v221, v217, s56\n"
"v_mfma_f32_16x16x32_bf16 v[64:67], v[16:19], a[0:3], v[64:67]\n"
"v_perm_b32 v28, v197, v193, s57\n"
"v_perm_b32 v29, v205, v201, s57\n"
"v_mfma_f32_16x16x32_bf16 v[68:71], v[20:23], a[0:3], v[68:71]\n"
"buffer_load_dwordx4 v[36:39], v13, s[36:39], s84 offen offset:1024\n"
"v_perm_b32 v30, v213, v209, s57\n"
"v_mfma_f32_16x16x32_bf16 v[96:99], v[16:19], a[4:7], v[96:99]\n"
"v_perm_b32 v31, v221, v217, s57\n"
"s_lshl_b32 s81, s53, 2\n"
"v_mfma_f32_16x16x32_bf16 v[100:103], v[20:23], a[4:7], v[100:103]\n"
"s_add_u32 s80, s80, s81\n"
"s_add_u32 s80, s80, 524288\n"
"v_mfma_f32_16x16x32_bf16 v[128:131], v[16:19], a[8:11], v[128:131]\n"
"v_lshl_add_u32 v6, v11, 7, s80\n"
"s_lshr_b32 s80, s53, 1\n"
"v_mfma_f32_16x16x32_bf16 v[132:135], v[20:23], a[8:11], v[132:135]\n"
"buffer_load_dwordx4 v[40:43], v13, s[36:39], s84 offen offset:2048\n"
"s_lshl_b32 s80, s80, 7\n"
"v_mfma_f32_16x16x32_bf16 v[160:163], v[16:19], a[12:15], v[160:163]\n"
"v_perm_b32 v16, v198, v194, s56\n"
"v_perm_b32 v17, v206, v202, s56\n"
"v_mfma_f32_16x16x32_bf16 v[164:167], v[20:23], a[12:15], v[164:167]\n"
"v_perm_b32 v18, v214, v210, s56\n"
"v_perm_b32 v19, v222, v218, s56\n"
"v_mfma_f32_16x16x32_bf16 v[72:75], v[24:27], a[0:3], v[72:75]\n"
"v_perm_b32 v20, v198, v194, s57\n"
"v_perm_b32 v21, v206, v202, s57\n"
"v_mfma_f32_16x16x32_bf16 v[76:79], v[28:31], a[0:3], v[76:79]\n"
"buffer_load_dwordx4 v[44:47], v13, s[36:39], s84 offen offset:3072\n"
"v_perm_b32 v22, v214, v210, s57\n"
"v_mfma_f32_16x16x32_bf16 v[104:107], v[24:27], a[4:7], v[104:107]\n"
"v_perm_b32 v23, v222, v218, s57\n"
"s_add_u32 s83, s51, 9\n"
"v_mfma_f32_16x16x32_bf16 v[108:111], v[28:31], a[4:7], v[108:111]\n"
"s_and_b32 s83, s83, 15\n"
"s_lshl_b32 s83, s83, 11\n"
"v_mfma_f32_16x16x32_bf16 v[136:139], v[24:27], a[8:11], v[136:139]\n"
"v_add_u32 v1, s83, v12\n"
"s_and_b32 s81, s53, 1\n"
"v_mfma_f32_16x16x32_bf16 v[140:143], v[28:31], a[8:11], v[140:143]\n"
"ds_read_b128 a[16:19], v1 offset:0\n"
"buffer_load_dwordx4 v[48:51], v14, s[36:39], s84 offen offset:0\n"
"v_mfma_f32_16x16x32_bf16 v[168:171], v[24:27], a[12:15], v[168:171]\n"
"ds_read_b128 a[20:23], v1 offset:1024\n"
"v_perm_b32 v24, v199, v195, s56\n"
"v_mfma_f32_16x16x32_bf16 v[172:175], v[28:31], a[12:15], v[172:175]\n"
"ds_read_b128 a[24:27], v1 offset:32768\n"
"v_perm_b32 v25, v207, v203, s56\n"
"v_mfma_f32_16x16x32_bf16 v[80:83], v[16:19], a[0:3], v[80:83]\n"
"ds_read_b128 a[28:31], v1 offset:33792\n"
"v_perm_b32 v26, v215, v211, s56\n"
"v_mfma_f32_16x16x32_bf16 v[84:87], v[20:23], a[0:3], v[84:87]\n"
"buffer_load_dwordx4 v[52:55], v14, s[36:39], s84 offen offset:1024\n"
"v_perm_b32 v27, v223, v219, s56\n"
"v_mfma_f32_16x16x32_bf16 v[112:115], v[16:19], a[4:7], v[112:115]\n"
"v_perm_b32 v28, v199, v195, s57\n"
"v_perm_b32 v29, v207, v203, s57\n"
"v_mfma_f32_16x16x32_bf16 v[116:119], v[20:23], a[4:7], v[116:119]\n"
"v_perm_b32 v30, v215, v211, s57\n"
"v_perm_b32 v31, v223, v219, s57\n"
"v_mfma_f32_16x16x32_bf16 v[144:147], v[16:19], a[8:11], v[144:147]\n"
"s_lshl_b32 s81, s81, 2\n"
"s_add_u32 s81, s81, s48\n"
"v_mfma_f32_16x16x32_bf16 v[148:151], v[20:23], a[8:11], v[148:151]\n"
"buffer_load_dwordx4 v[56:59], v14, s[36:39], s84 offen offset:2048\n"
"s_lshl_b32 s81, s81, 3\n"
"v_mfma_f32_16x16x32_bf16 v[176:179], v[16:19], a[12:15], v[176:179]\n"
"s_waitcnt vmcnt(7)\n"
"v_perm_b32 v16, v228, v224, s56\n"
"v_perm_b32 v17, v236, v232, s56\n"
"v_mfma_f32_16x16x32_bf16 v[180:183], v[20:23], a[12:15], v[180:183]\n"
"v_perm_b32 v18, v244, v240, s56\n"
"v_perm_b32 v19, v252, v248, s56\n"
"v_mfma_f32_16x16x32_bf16 v[88:91], v[24:27], a[0:3], v[88:91]\n"
"v_perm_b32 v20, v228, v224, s57\n"
"v_perm_b32 v21, v236, v232, s57\n"
"buffer_load_dwordx4 v[60:63], v14, s[36:39], s84 offen offset:3072\n"
"v_mfma_f32_16x16x32_bf16 v[92:95], v[28:31], a[0:3], v[92:95]\n"
"v_perm_b32 v22, v244, v240, s57\n"
"v_perm_b32 v23, v252, v248, s57\n"
"v_mfma_f32_16x16x32_bf16 v[120:123], v[24:27], a[4:7], v[120:123]\n"
"s_waitcnt lgkmcnt(0)\n"
"s_add_u32 s80, s80, s81\n"
"s_add_u32 s80, s80, 524352\n"
"v_mfma_f32_16x16x32_bf16 v[124:127], v[28:31], a[4:7], v[124:127]\n"
"s_add_u32 s85, s51, 11\n"
"s_and_b32 s85, s85, 15\n"
"v_mfma_f32_16x16x32_bf16 v[152:155], v[24:27], a[8:11], v[152:155]\n"
"s_lshl_b32 s85, s85, 15\n"
"v_lshl_add_u32 v7, v11, 9, s80\n"
"v_mfma_f32_16x16x32_bf16 v[156:159], v[28:31], a[8:11], v[156:159]\n"
"buffer_load_dwordx4 v[192:195], v13, s[36:39], s85 offen offset:0\n"
"s_lshr_b32 s80, s53, 1\n"
"v_mfma_f32_16x16x32_bf16 v[184:187], v[24:27], a[12:15], v[184:187]\n"
"v_perm_b32 v24, v229, v225, s56\n"
"v_perm_b32 v25, v237, v233, s56\n"
"v_mfma_f32_16x16x32_bf16 v[188:191], v[28:31], a[12:15], v[188:191]\n"
"v_perm_b32 v26, v245, v241, s56\n"
"v_perm_b32 v27, v253, v249, s56\n"
"v_mfma_f32_16x16x32_bf16 v[64:67], v[16:19], a[16:19], v[64:67]\n"
"v_perm_b32 v28, v229, v225, s57\n"
"v_perm_b32 v29, v237, v233, s57\n"
"v_mfma_f32_16x16x32_bf16 v[68:71], v[20:23], a[16:19], v[68:71]\n"
"buffer_load_dwordx4 v[196:199], v13, s[36:39], s85 offen offset:1024\n"
"v_perm_b32 v30, v245, v241, s57\n"
"v_mfma_f32_16x16x32_bf16 v[96:99], v[16:19], a[20:23], v[96:99]\n"
"v_perm_b32 v31, v253, v249, s57\n"
"s_lshl_b32 s80, s80, 14\n"
"v_mfma_f32_16x16x32_bf16 v[100:103], v[20:23], a[20:23], v[100:103]\n"
"s_and_b32 s81, s53, 1\n"
"s_lshl_b32 s81, s81, 13\n"
"v_mfma_f32_16x16x32_bf16 v[128:131], v[16:19], a[24:27], v[128:131]\n"
"s_add_u32 s80, s80, s81\n"
"s_lshl_b32 s81, s48, 11\n"
"v_mfma_f32_16x16x32_bf16 v[132:135], v[20:23], a[24:27], v[132:135]\n"
"buffer_load_dwordx4 v[200:203], v13, s[36:39], s85 offen offset:2048\n"
"s_add_u32 s80, s80, s81\n"
"v_mfma_f32_16x16x32_bf16 v[160:163], v[16:19], a[28:31], v[160:163]\n"
"v_perm_b32 v16, v230, v226, s56\n"
"v_perm_b32 v17, v238, v234, s56\n"
"v_mfma_f32_16x16x32_bf16 v[164:167], v[20:23], a[28:31], v[164:167]\n"
"v_perm_b32 v18, v246, v242, s56\n"
"v_perm_b32 v19, v254, v250, s56\n"
"v_mfma_f32_16x16x32_bf16 v[72:75], v[24:27], a[16:19], v[72:75]\n"
"v_perm_b32 v20, v230, v226, s57\n"
"v_perm_b32 v21, v238, v234, s57\n"
"v_mfma_f32_16x16x32_bf16 v[76:79], v[28:31], a[16:19], v[76:79]\n"
"buffer_load_dwordx4 v[204:207], v13, s[36:39], s85 offen offset:3072\n"
"v_perm_b32 v22, v246, v242, s57\n"
"v_mfma_f32_16x16x32_bf16 v[104:107], v[24:27], a[20:23], v[104:107]\n"
"v_perm_b32 v23, v254, v250, s57\n"
"s_add_u32 s83, s51, 10\n"
"v_mfma_f32_16x16x32_bf16 v[108:111], v[28:31], a[20:23], v[108:111]\n"
"s_and_b32 s83, s83, 15\n"
"s_lshl_b32 s83, s83, 11\n"
"v_mfma_f32_16x16x32_bf16 v[136:139], v[24:27], a[24:27], v[136:139]\n"
"v_add_u32 v0, s83, v12\n"
"v_add_u32 v8, s80, v12\n"
"v_mfma_f32_16x16x32_bf16 v[140:143], v[28:31], a[24:27], v[140:143]\n"
"ds_read_b128 a[0:3], v0 offset:0\n"
"buffer_load_dwordx4 v[208:211], v14, s[36:39], s85 offen offset:0\n"
"v_mfma_f32_16x16x32_bf16 v[168:171], v[24:27], a[28:31], v[168:171]\n"
"ds_read_b128 a[4:7], v0 offset:1024\n"
"v_perm_b32 v24, v231, v227, s56\n"
"v_mfma_f32_16x16x32_bf16 v[172:175], v[28:31], a[28:31], v[172:175]\n"
"ds_read_b128 a[8:11], v0 offset:32768\n"
"v_perm_b32 v25, v239, v235, s56\n"
"v_mfma_f32_16x16x32_bf16 v[80:83], v[16:19], a[16:19], v[80:83]\n"
"ds_read_b128 a[12:15], v0 offset:33792\n"
"v_perm_b32 v26, v247, v243, s56\n"
"v_mfma_f32_16x16x32_bf16 v[84:87], v[20:23], a[16:19], v[84:87]\n"
"buffer_load_dwordx4 v[212:215], v14, s[36:39], s85 offen offset:1024\n"
"v_perm_b32 v27, v255, v251, s56\n"
"v_mfma_f32_16x16x32_bf16 v[112:115], v[16:19], a[20:23], v[112:115]\n"
"v_perm_b32 v28, v231, v227, s57\n"
"v_perm_b32 v29, v239, v235, s57\n"
"v_mfma_f32_16x16x32_bf16 v[116:119], v[20:23], a[20:23], v[116:119]\n"
"v_perm_b32 v30, v247, v243, s57\n"
"v_perm_b32 v31, v255, v251, s57\n"
"v_mfma_f32_16x16x32_bf16 v[144:147], v[16:19], a[24:27], v[144:147]\n"
"s_mov_b32 s62, 0\n"
"s_mov_b32 s63, 65536\n"
"v_mfma_f32_16x16x32_bf16 v[148:151], v[20:23], a[24:27], v[148:151]\n"
"buffer_load_dwordx4 v[216:219], v14, s[36:39], s85 offen offset:2048\n"
"s_mov_b32 s64, 131072\n"
"v_mfma_f32_16x16x32_bf16 v[176:179], v[16:19], a[28:31], v[176:179]\n"
"s_waitcnt vmcnt(7)\n"
"v_perm_b32 v16, v36, v32, s56\n"
"v_perm_b32 v17, v44, v40, s56\n"
"v_mfma_f32_16x16x32_bf16 v[180:183], v[20:23], a[28:31], v[180:183]\n"
"v_perm_b32 v18, v52, v48, s56\n"
"v_perm_b32 v19, v60, v56, s56\n"
"v_mfma_f32_16x16x32_bf16 v[88:91], v[24:27], a[16:19], v[88:91]\n"
"v_perm_b32 v20, v36, v32, s57\n"
"v_perm_b32 v21, v44, v40, s57\n"
"buffer_load_dwordx4 v[220:223], v14, s[36:39], s85 offen offset:3072\n"
"v_mfma_f32_16x16x32_bf16 v[92:95], v[28:31], a[16:19], v[92:95]\n"
"v_perm_b32 v22, v52, v48, s57\n"
"v_perm_b32 v23, v60, v56, s57\n"
"v_mfma_f32_16x16x32_bf16 v[120:123], v[24:27], a[20:23], v[120:123]\n"
"s_waitcnt lgkmcnt(0)\n"
"s_mov_b32 s65, 196608\n"
"s_mov_b32 s66, 262144\n"
"v_mfma_f32_16x16x32_bf16 v[124:127], v[28:31], a[20:23], v[124:127]\n"
"s_add_u32 s84, s51, 12\n"
"s_and_b32 s84, s84, 15\n"
"v_mfma_f32_16x16x32_bf16 v[152:155], v[24:27], a[24:27], v[152:155]\n"
"s_lshl_b32 s84, s84, 15\n"
"s_mov_b32 s67, 327680\n"
"v_mfma_f32_16x16x32_bf16 v[156:159], v[28:31], a[24:27], v[156:159]\n"
"buffer_load_dwordx4 v[224:227], v13, s[36:39], s84 offen offset:0\n"
"s_mov_b32 s68, 393216\n"
"v_mfma_f32_16x16x32_bf16 v[184:187], v[24:27], a[28:31], v[184:187]\n"
"v_perm_b32 v24, v37, v33, s56\n"
"v_perm_b32 v25, v45, v41, s56\n"
"v_mfma_f32_16x16x32_bf16 v[188:191], v[28:31], a[28:31], v[188:191]\n"
"v_perm_b32 v26, v53, v49, s56\n"
"v_perm_b32 v27, v61, v57, s56\n"
"v_mfma_f32_16x16x32_bf16 v[64:67], v[16:19], a[0:3], v[64:67]\n"
"v_perm_b32 v28, v37, v33, s57\n"
"v_perm_b32 v29, v45, v41, s57\n"
"v_mfma_f32_16x16x32_bf16 v[68:71], v[20:23], a[0:3], v[68:71]\n"
"buffer_load_dwordx4 v[228:231], v13, s[36:39], s84 offen offset:1024\n"
"v_perm_b32 v30, v53, v49, s57\n"
"v_mfma_f32_16x16x32_bf16 v[96:99], v[16:19], a[4:7], v[96:99]\n"
"v_perm_b32 v31, v61, v57, s57\n"
"s_mov_b32 s69, 458752\n"
"v_mfma_f32_16x16x32_bf16 v[100:103], v[20:23], a[4:7], v[100:103]\n"
"s_and_b32 s80, s53, 1\n"
"s_lshl_b32 s80, s80, 1\n"
"v_mfma_f32_16x16x32_bf16 v[128:131], v[16:19], a[8:11], v[128:131]\n"
"s_lshr_b32 s81, s48, 1\n"
"s_add_u32 s80, s80, s81\n"
"v_mfma_f32_16x16x32_bf16 v[132:135], v[20:23], a[8:11], v[132:135]\n"
"buffer_load_dwordx4 v[232:235], v13, s[36:39], s84 offen offset:2048\n"
"s_lshl_b32 s80, s80, 15\n"
"v_mfma_f32_16x16x32_bf16 v[160:163], v[16:19], a[12:15], v[160:163]\n"
"v_perm_b32 v16, v38, v34, s56\n"
"v_perm_b32 v17, v46, v42, s56\n"
"v_mfma_f32_16x16x32_bf16 v[164:167], v[20:23], a[12:15], v[164:167]\n"
"v_perm_b32 v18, v54, v50, s56\n"
"v_perm_b32 v19, v62, v58, s56\n"
"v_mfma_f32_16x16x32_bf16 v[72:75], v[24:27], a[0:3], v[72:75]\n"
"v_perm_b32 v20, v38, v34, s57\n"
"v_perm_b32 v21, v46, v42, s57\n"
"v_mfma_f32_16x16x32_bf16 v[76:79], v[28:31], a[0:3], v[76:79]\n"
"buffer_load_dwordx4 v[236:239], v13, s[36:39], s84 offen offset:3072\n"
"v_perm_b32 v22, v54, v50, s57\n"
"v_mfma_f32_16x16x32_bf16 v[104:107], v[24:27], a[4:7], v[104:107]\n"
"v_perm_b32 v23, v62, v58, s57\n"
"s_add_u32 s83, s51, 11\n"
"v_mfma_f32_16x16x32_bf16 v[108:111], v[28:31], a[4:7], v[108:111]\n"
"s_and_b32 s83, s83, 15\n"
"s_lshl_b32 s83, s83, 11\n"
"v_mfma_f32_16x16x32_bf16 v[136:139], v[24:27], a[8:11], v[136:139]\n"
"v_add_u32 v1, s83, v12\n"
"s_lshr_b32 s81, s53, 1\n"
"v_mfma_f32_16x16x32_bf16 v[140:143], v[28:31], a[8:11], v[140:143]\n"
"ds_read_b128 a[16:19], v1 offset:0\n"
"buffer_load_dwordx4 v[240:243], v14, s[36:39], s84 offen offset:0\n"
"v_mfma_f32_16x16x32_bf16 v[168:171], v[24:27], a[12:15], v[168:171]\n"
"ds_read_b128 a[20:23], v1 offset:1024\n"
"v_perm_b32 v24, v39, v35, s56\n"
"v_mfma_f32_16x16x32_bf16 v[172:175], v[28:31], a[12:15], v[172:175]\n"
"ds_read_b128 a[24:27], v1 offset:32768\n"
"v_perm_b32 v25, v47, v43, s56\n"
"v_mfma_f32_16x16x32_bf16 v[80:83], v[16:19], a[0:3], v[80:83]\n"
"ds_read_b128 a[28:31], v1 offset:33792\n"
"v_perm_b32 v26, v55, v51, s56\n"
"v_mfma_f32_16x16x32_bf16 v[84:87], v[20:23], a[0:3], v[84:87]\n"
"buffer_load_dwordx4 v[244:247], v14, s[36:39], s84 offen offset:1024\n"
"v_perm_b32 v27, v63, v59, s56\n"
"v_mfma_f32_16x16x32_bf16 v[112:115], v[16:19], a[4:7], v[112:115]\n"
"v_perm_b32 v28, v39, v35, s57\n"
"v_perm_b32 v29, v47, v43, s57\n"
"v_mfma_f32_16x16x32_bf16 v[116:119], v[20:23], a[4:7], v[116:119]\n"
"v_perm_b32 v30, v55, v51, s57\n"
"v_perm_b32 v31, v63, v59, s57\n"
"v_mfma_f32_16x16x32_bf16 v[144:147], v[16:19], a[8:11], v[144:147]\n"
"s_lshl_b32 s81, s81, 8\n"
"s_add_u32 s80, s80, s81\n"
"v_mfma_f32_16x16x32_bf16 v[148:151], v[20:23], a[8:11], v[148:151]\n"
"buffer_load_dwordx4 v[248:251], v14, s[36:39], s84 offen offset:2048\n"
"s_and_b32 s81, s48, 1\n"
"v_mfma_f32_16x16x32_bf16 v[176:179], v[16:19], a[12:15], v[176:179]\n"
"s_waitcnt vmcnt(7)\n"
"v_perm_b32 v16, v196, v192, s56\n"
"v_perm_b32 v17, v204, v200, s56\n"
"v_mfma_f32_16x16x32_bf16 v[180:183], v[20:23], a[12:15], v[180:183]\n"
"v_perm_b32 v18, v212, v208, s56\n"
"v_perm_b32 v19, v220, v216, s56\n"
"v_mfma_f32_16x16x32_bf16 v[88:91], v[24:27], a[0:3], v[88:91]\n"
"v_perm_b32 v20, v196, v192, s57\n"
"v_perm_b32 v21, v204, v200, s57\n"
"buffer_load_dwordx4 v[252:255], v14, s[36:39], s84 offen offset:3072\n"
"v_mfma_f32_16x16x32_bf16 v[92:95], v[28:31], a[0:3], v[92:95]\n"
"v_perm_b32 v22, v212, v208, s57\n"
"v_perm_b32 v23, v220, v216, s57\n"
"v_mfma_f32_16x16x32_bf16 v[120:123], v[24:27], a[4:7], v[120:123]\n"
"s_waitcnt lgkmcnt(0)\n"
"s_lshl_b32 s81, s81, 5\n"
"s_add_u32 s80, s80, s81\n"
"v_mfma_f32_16x16x32_bf16 v[124:127], v[28:31], a[4:7], v[124:127]\n"
"s_add_u32 s85, s51, 13\n"
"s_and_b32 s85, s85, 15\n"
"v_mfma_f32_16x16x32_bf16 v[152:155], v[24:27], a[8:11], v[152:155]\n"
"s_lshl_b32 s85, s85, 15\n"
"v_and_b32 v11, 15, v10\n"
"v_mfma_f32_16x16x32_bf16 v[156:159], v[28:31], a[8:11], v[156:159]\n"
"buffer_load_dwordx4 v[32:35], v13, s[36:39], s85 offen offset:0\n"
"v_lshlrev_b32 v11, 11, v11\n"
"v_mfma_f32_16x16x32_bf16 v[184:187], v[24:27], a[12:15], v[184:187]\n"
"v_perm_b32 v24, v197, v193, s56\n"
"v_perm_b32 v25, v205, v201, s56\n"
"v_mfma_f32_16x16x32_bf16 v[188:191], v[28:31], a[12:15], v[188:191]\n"
"v_perm_b32 v26, v213, v209, s56\n"
"v_perm_b32 v27, v221, v217, s56\n"
"v_mfma_f32_16x16x32_bf16 v[64:67], v[16:19], a[16:19], v[64:67]\n"
"v_perm_b32 v28, v197, v193, s57\n"
"v_perm_b32 v29, v205, v201, s57\n"
"v_mfma_f32_16x16x32_bf16 v[68:71], v[20:23], a[16:19], v[68:71]\n"
"buffer_load_dwordx4 v[36:39], v13, s[36:39], s85 offen offset:1024\n"
"v_perm_b32 v30, v213, v209, s57\n"
"v_mfma_f32_16x16x32_bf16 v[96:99], v[16:19], a[20:23], v[96:99]\n"
"v_perm_b32 v31, v221, v217, s57\n"
"v_lshrrev_b32 v10, 4, v10\n"
"v_mfma_f32_16x16x32_bf16 v[100:103], v[20:23], a[20:23], v[100:103]\n"
"v_lshl_add_u32 v11, v10, 6, v11\n"
"v_add_u32 v9, s80, v11\n"
"v_mfma_f32_16x16x32_bf16 v[128:131], v[16:19], a[24:27], v[128:131]\n"
"v_and_b32 v15, 0xffff, v15\n"
"v_lshl_or_b32 v10, v3, 16, v15\n"
"v_mfma_f32_16x16x32_bf16 v[132:135], v[20:23], a[24:27], v[132:135]\n"
"buffer_load_dwordx4 v[40:43], v13, s[36:39], s85 offen offset:2048\n"
"s_lshl_b32 s77, 1, s53\n"
"v_mfma_f32_16x16x32_bf16 v[160:163], v[16:19], a[28:31], v[160:163]\n"
"v_perm_b32 v16, v198, v194, s56\n"
"v_perm_b32 v17, v206, v202, s56\n"
"v_mfma_f32_16x16x32_bf16 v[164:167], v[20:23], a[28:31], v[164:167]\n"
"v_perm_b32 v18, v214, v210, s56\n"
"v_perm_b32 v19, v222, v218, s56\n"
"v_mfma_f32_16x16x32_bf16 v[72:75], v[24:27], a[16:19], v[72:75]\n"
"v_perm_b32 v20, v198, v194, s57\n"
"v_perm_b32 v21, v206, v202, s57\n"
"v_mfma_f32_16x16x32_bf16 v[76:79], v[28:31], a[16:19], v[76:79]\n"
"buffer_load_dwordx4 v[44:47], v13, s[36:39], s85 offen offset:3072\n"
"v_perm_b32 v22, v214, v210, s57\n"
"v_mfma_f32_16x16x32_bf16 v[104:107], v[24:27], a[20:23], v[104:107]\n"
"v_perm_b32 v23, v222, v218, s57\n"
"s_add_u32 s83, s51, 12\n"
"v_mfma_f32_16x16x32_bf16 v[108:111], v[28:31], a[20:23], v[108:111]\n"
"s_and_b32 s83, s83, 15\n"
"s_lshl_b32 s83, s83, 11\n"
"v_mfma_f32_16x16x32_bf16 v[136:139], v[24:27], a[24:27], v[136:139]\n"
"v_add_u32 v0, s83, v12\n"
"s_and_b32 s80, s53, 1\n"
"v_mfma_f32_16x16x32_bf16 v[140:143], v[28:31], a[24:27], v[140:143]\n"
"ds_read_b128 a[0:3], v0 offset:0\n"
"buffer_load_dwordx4 v[48:51], v14, s[36:39], s85 offen offset:0\n"
"v_mfma_f32_16x16x32_bf16 v[168:171], v[24:27], a[28:31], v[168:171]\n"
"ds_read_b128 a[4:7], v0 offset:1024\n"
"v_perm_b32 v24, v199, v195, s56\n"
"v_mfma_f32_16x16x32_bf16 v[172:175], v[28:31], a[28:31], v[172:175]\n"
"ds_read_b128 a[8:11], v0 offset:32768\n"
"v_perm_b32 v25, v207, v203, s56\n"
"v_mfma_f32_16x16x32_bf16 v[80:83], v[16:19], a[16:19], v[80:83]\n"
"ds_read_b128 a[12:15], v0 offset:33792\n"
"v_perm_b32 v26, v215, v211, s56\n"
"v_mfma_f32_16x16x32_bf16 v[84:87], v[20:23], a[16:19], v[84:87]\n"
"buffer_load_dwordx4 v[52:55], v14, s[36:39], s85 offen offset:1024\n"
"v_perm_b32 v27, v223, v219, s56\n"
"v_mfma_f32_16x16x32_bf16 v[112:115], v[16:19], a[20:23], v[112:115]\n"
"v_perm_b32 v28, v199, v195, s57\n"
"v_perm_b32 v29, v207, v203, s57\n"
"v_mfma_f32_16x16x32_bf16 v[116:119], v[20:23], a[20:23], v[116:119]\n"
"v_perm_b32 v30, v215, v211, s57\n"
"v_perm_b32 v31, v223, v219, s57\n"
"v_mfma_f32_16x16x32_bf16 v[144:147], v[16:19], a[24:27], v[144:147]\n"
"s_lshl_b32 s80, s80, 2\n"
"s_lshl_b32 s80, 15, s80\n"
"v_mfma_f32_16x16x32_bf16 v[148:151], v[20:23], a[24:27], v[148:151]\n"
"buffer_load_dwordx4 v[56:59], v14, s[36:39], s85 offen offset:2048\n"
"s_lshr_b32 s81, s53, 1\n"
"v_mfma_f32_16x16x32_bf16 v[176:179], v[16:19], a[28:31], v[176:179]\n"
"s_waitcnt vmcnt(7)\n"
"v_perm_b32 v16, v228, v224, s56\n"
"v_perm_b32 v17, v236, v232, s56\n"
"v_mfma_f32_16x16x32_bf16 v[180:183], v[20:23], a[28:31], v[180:183]\n"
"v_perm_b32 v18, v244, v240, s56\n"
"v_perm_b32 v19, v252, v248, s56\n"
"v_mfma_f32_16x16x32_bf16 v[88:91], v[24:27], a[16:19], v[88:91]\n"
"v_perm_b32 v20, v228, v224, s57\n"
"v_perm_b32 v21, v236, v232, s57\n"
"buffer_load_dwordx4 v[60:63], v14, s[36:39], s85 offen offset:3072\n"
"v_mfma_f32_16x16x32_bf16 v[92:95], v[28:31], a[16:19], v[92:95]\n"
"v_perm_b32 v22, v244, v240, s57\n"
"v_perm_b32 v23, v252, v248, s57\n"
"v_mfma_f32_16x16x32_bf16 v[120:123], v[24:27], a[20:23], v[120:123]\n"
"s_waitcnt lgkmcnt(0)\n"
"s_cmp_eq_u32 s81, s48\n"
"s_cselect_b32 s80, s80, 0\n"
"s_andn2_b32 s55, 255, s80\n"
"v_mfma_f32_16x16x32_bf16 v[124:127], v[28:31], a[20:23], v[124:127]\n"
"s_add_u32 s84, s51, 14\n"
"s_and_b32 s84, s84, 15\n"
"v_mfma_f32_16x16x32_bf16 v[152:155], v[24:27], a[24:27], v[152:155]\n"
"s_lshl_b32 s84, s84, 15\n"
"s_mov_b32 s61, 21802\n"
"v_mfma_f32_16x16x32_bf16 v[156:159], v[28:31], a[24:27], v[156:159]\n"
"buffer_load_dwordx4 v[192:195], v13, s[36:39], s84 offen offset:0\n"
"s_lshl_b32 s76, s53, 16\n"
"v_mfma_f32_16x16x32_bf16 v[184:187], v[24:27], a[28:31], v[184:187]\n"
"v_perm_b32 v24, v229, v225, s56\n"
"v_perm_b32 v25, v237, v233, s56\n"
"v_mfma_f32_16x16x32_bf16 v[188:191], v[28:31], a[28:31], v[188:191]\n"
"v_perm_b32 v26, v245, v241, s56\n"
"v_perm_b32 v27, v253, v249, s56\n"
"v_mfma_f32_16x16x32_bf16 v[64:67], v[16:19], a[0:3], v[64:67]\n"
"v_perm_b32 v28, v229, v225, s57\n"
"v_perm_b32 v29, v237, v233, s57\n"
"v_mfma_f32_16x16x32_bf16 v[68:71], v[20:23], a[0:3], v[68:71]\n"
"buffer_load_dwordx4 v[196:199], v13, s[36:39], s84 offen offset:1024\n"
"v_perm_b32 v30, v245, v241, s57\n"
"v_mfma_f32_16x16x32_bf16 v[96:99], v[16:19], a[4:7], v[96:99]\n"
"v_perm_b32 v31, v253, v249, s57\n"
"s_lshl_b32 s81, s60, 2\n"
"v_mfma_f32_16x16x32_bf16 v[100:103], v[20:23], a[4:7], v[100:103]\n"
"s_add_u32 s76, s76, s81\n"
"v_mfma_f32_16x16x32_bf16 v[128:131], v[16:19], a[8:11], v[128:131]\n"
"v_mfma_f32_16x16x32_bf16 v[132:135], v[20:23], a[8:11], v[132:135]\n"
"buffer_load_dwordx4 v[200:203], v13, s[36:39], s84 offen offset:2048\n"
"v_mfma_f32_16x16x32_bf16 v[160:163], v[16:19], a[12:15], v[160:163]\n"
"v_perm_b32 v16, v230, v226, s56\n"
"v_perm_b32 v17, v238, v234, s56\n"
"v_mfma_f32_16x16x32_bf16 v[164:167], v[20:23], a[12:15], v[164:167]\n"
"v_perm_b32 v18, v246, v242, s56\n"
"v_perm_b32 v19, v254, v250, s56\n"
"v_mfma_f32_16x16x32_bf16 v[72:75], v[24:27], a[0:3], v[72:75]\n"
"v_perm_b32 v20, v230, v226, s57\n"
"v_perm_b32 v21, v238, v234, s57\n"
"v_mfma_f32_16x16x32_bf16 v[76:79], v[28:31], a[0:3], v[76:79]\n"
"buffer_load_dwordx4 v[204:207], v13, s[36:39], s84 offen offset:3072\n"
"v_perm_b32 v22, v246, v242, s57\n"
"v_mfma_f32_16x16x32_bf16 v[104:107], v[24:27], a[4:7], v[104:107]\n"
"v_perm_b32 v23, v254, v250, s57\n"
"s_add_u32 s83, s51, 13\n"
"v_mfma_f32_16x16x32_bf16 v[108:111], v[28:31], a[4:7], v[108:111]\n"
"s_and_b32 s83, s83, 15\n"
"s_lshl_b32 s83, s83, 11\n"
"v_mfma_f32_16x16x32_bf16 v[136:139], v[24:27], a[8:11], v[136:139]\n"
"v_add_u32 v1, s83, v12\n"
"v_mfma_f32_16x16x32_bf16 v[140:143], v[28:31], a[8:11], v[140:143]\n"
"ds_read_b128 a[16:19], v1 offset:0\n"
"buffer_load_dwordx4 v[208:211], v14, s[36:39], s84 offen offset:0\n"
"v_mfma_f32_16x16x32_bf16 v[168:171], v[24:27], a[12:15], v[168:171]\n"
"ds_read_b128 a[20:23], v1 offset:1024\n"
"v_perm_b32 v24, v231, v227, s56\n"
"v_mfma_f32_16x16x32_bf16 v[172:175], v[28:31], a[12:15], v[172:175]\n"
"ds_read_b128 a[24:27], v1 offset:32768\n"
"v_perm_b32 v25, v239, v235, s56\n"
"v_mfma_f32_16x16x32_bf16 v[80:83], v[16:19], a[0:3], v[80:83]\n"
"ds_read_b128 a[28:31], v1 offset:33792\n"
"v_perm_b32 v26, v247, v243, s56\n"
"v_mfma_f32_16x16x32_bf16 v[84:87], v[20:23], a[0:3], v[84:87]\n"
"buffer_load_dwordx4 v[212:215], v14, s[36:39], s84 offen offset:1024\n"
"v_perm_b32 v27, v255, v251, s56\n"
"v_mfma_f32_16x16x32_bf16 v[112:115], v[16:19], a[4:7], v[112:115]\n"
"v_perm_b32 v28, v231, v227, s57\n"
"v_perm_b32 v29, v239, v235, s57\n"
"v_mfma_f32_16x16x32_bf16 v[116:119], v[20:23], a[4:7], v[116:119]\n"
"v_perm_b32 v30, v247, v243, s57\n"
"v_perm_b32 v31, v255, v251, s57\n"
"v_mfma_f32_16x16x32_bf16 v[144:147], v[16:19], a[8:11], v[144:147]\n"
"v_mfma_f32_16x16x32_bf16 v[148:151], v[20:23], a[8:11], v[148:151]\n"
"buffer_load_dwordx4 v[216:219], v14, s[36:39], s84 offen offset:2048\n"
"v_mfma_f32_16x16x32_bf16 v[176:179], v[16:19], a[12:15], v[176:179]\n"
"s_waitcnt vmcnt(7)\n"
"v_perm_b32 v16, v36, v32, s56\n"
"v_perm_b32 v17, v44, v40, s56\n"
"v_mfma_f32_16x16x32_bf16 v[180:183], v[20:23], a[12:15], v[180:183]\n"
"v_perm_b32 v18, v52, v48, s56\n"
"v_perm_b32 v19, v60, v56, s56\n"
"v_mfma_f32_16x16x32_bf16 v[88:91], v[24:27], a[0:3], v[88:91]\n"
"v_perm_b32 v20, v36, v32, s57\n"
"v_perm_b32 v21, v44, v40, s57\n"
"buffer_load_dwordx4 v[220:223], v14, s[36:39], s84 offen offset:3072\n"
"v_mfma_f32_16x16x32_bf16 v[92:95], v[28:31], a[0:3], v[92:95]\n"
"v_perm_b32 v22, v52, v48, s57\n"
"v_perm_b32 v23, v60, v56, s57\n"
"v_mfma_f32_16x16x32_bf16 v[120:123], v[24:27], a[4:7], v[120:123]\n"
"s_waitcnt lgkmcnt(0)\n"
"v_mfma_f32_16x16x32_bf16 v[124:127], v[28:31], a[4:7], v[124:127]\n"
"s_add_u32 s85, s51, 15\n"
"s_and_b32 s85, s85, 15\n"
"v_mfma_f32_16x16x32_bf16 v[152:155], v[24:27], a[8:11], v[152:155]\n"
"s_lshl_b32 s85, s85, 15\n"
"v_mfma_f32_16x16x32_bf16 v[156:159], v[28:31], a[8:11], v[156:159]\n"
"buffer_load_dwordx4 v[224:227], v13, s[36:39], s85 offen offset:0\n"
"v_mfma_f32_16x16x32_bf16 v[184:187], v[24:27], a[12:15], v[184:187]\n"
"v_perm_b32 v24, v37, v33, s56\n"
"v_perm_b32 v25, v45, v41, s56\n"
"v_mfma_f32_16x16x32_bf16 v[188:191], v[28:31], a[12:15], v[188:191]\n"
"v_perm_b32 v26, v53, v49, s56\n"
"v_perm_b32 v27, v61, v57, s56\n"
"v_mfma_f32_16x16x32_bf16 v[64:67], v[16:19], a[16:19], v[64:67]\n"
"v_perm_b32 v28, v37, v33, s57\n"
"v_perm_b32 v29, v45, v41, s57\n"
"v_mfma_f32_16x16x32_bf16 v[68:71], v[20:23], a[16:19], v[68:71]\n"
"buffer_load_dwordx4 v[228:231], v13, s[36:39], s85 offen offset:1024\n"
"v_perm_b32 v30, v53, v49, s57\n"
"v_mfma_f32_16x16x32_bf16 v[96:99], v[16:19], a[20:23], v[96:99]\n"
"v_perm_b32 v31, v61, v57, s57\n"
"v_mfma_f32_16x16x32_bf16 v[100:103], v[20:23], a[20:23], v[100:103]\n"
"v_mfma_f32_16x16x32_bf16 v[128:131], v[16:19], a[24:27], v[128:131]\n"
"v_mfma_f32_16x16x32_bf16 v[132:135], v[20:23], a[24:27], v[132:135]\n"
"buffer_load_dwordx4 v[232:235], v13, s[36:39], s85 offen offset:2048\n"
"v_mfma_f32_16x16x32_bf16 v[160:163], v[16:19], a[28:31], v[160:163]\n"
"v_perm_b32 v16, v38, v34, s56\n"
"v_perm_b32 v17, v46, v42, s56\n"
"v_mfma_f32_16x16x32_bf16 v[164:167], v[20:23], a[28:31], v[164:167]\n"
"v_perm_b32 v18, v54, v50, s56\n"
"v_perm_b32 v19, v62, v58, s56\n"
"v_mfma_f32_16x16x32_bf16 v[72:75], v[24:27], a[16:19], v[72:75]\n"
"v_perm_b32 v20, v38, v34, s57\n"
"v_perm_b32 v21, v46, v42, s57\n"
"v_mfma_f32_16x16x32_bf16 v[76:79], v[28:31], a[16:19], v[76:79]\n"
"buffer_load_dwordx4 v[236:239], v13, s[36:39], s85 offen offset:3072\n"
"v_perm_b32 v22, v54, v50, s57\n"
"v_mfma_f32_16x16x32_bf16 v[104:107], v[24:27], a[20:23], v[104:107]\n"
"v_perm_b32 v23, v62, v58, s57\n"
"s_add_u32 s83, s51, 14\n"
"v_mfma_f32_16x16x32_bf16 v[108:111], v[28:31], a[20:23], v[108:111]\n"
"s_and_b32 s83, s83, 15\n"
"s_lshl_b32 s83, s83, 11\n"
"v_mfma_f32_16x16x32_bf16 v[136:139], v[24:27], a[24:27], v[136:139]\n"
"v_add_u32 v0, s83, v12\n"
"v_mfma_f32_16x16x32_bf16 v[140:143], v[28:31], a[24:27], v[140:143]\n"
"ds_read_b128 a[0:3], v0 offset:0\n"
"buffer_load_dwordx4 v[240:243], v14, s[36:39], s85 offen offset:0\n"
"v_mfma_f32_16x16x32_bf16 v[168:171], v[24:27], a[28:31], v[168:171]\n"
"ds_read_b128 a[4:7], v0 offset:1024\n"
"v_perm_b32 v24, v39, v35, s56\n"
"v_mfma_f32_16x16x32_bf16 v[172:175], v[28:31], a[28:31], v[172:175]\n"
"ds_read_b128 a[8:11], v0 offset:32768\n"
"v_perm_b32 v25, v47, v43, s56\n"
"v_mfma_f32_16x16x32_bf16 v[80:83], v[16:19], a[16:19], v[80:83]\n"
"ds_read_b128 a[12:15], v0 offset:33792\n"
"v_perm_b32 v26, v55, v51, s56\n"
"v_mfma_f32_16x16x32_bf16 v[84:87], v[20:23], a[16:19], v[84:87]\n"
"buffer_load_dwordx4 v[244:247], v14, s[36:39], s85 offen offset:1024\n"
"v_perm_b32 v27, v63, v59, s56\n"
"v_mfma_f32_16x16x32_bf16 v[112:115], v[16:19], a[20:23], v[112:115]\n"
"v_perm_b32 v28, v39, v35, s57\n"
"v_perm_b32 v29, v47, v43, s57\n"
"v_mfma_f32_16x16x32_bf16 v[116:119], v[20:23], a[20:23], v[116:119]\n"
"v_perm_b32 v30, v55, v51, s57\n"
"v_perm_b32 v31, v63, v59, s57\n"
"v_mfma_f32_16x16x32_bf16 v[144:147], v[16:19], a[24:27], v[144:147]\n"
"v_mfma_f32_16x16x32_bf16 v[148:151], v[20:23], a[24:27], v[148:151]\n"
"buffer_load_dwordx4 v[248:251], v14, s[36:39], s85 offen offset:2048\n"
"v_mfma_f32_16x16x32_bf16 v[176:179], v[16:19], a[28:31], v[176:179]\n"
"s_waitcnt vmcnt(7)\n"
"v_perm_b32 v16, v196, v192, s56\n"
"v_perm_b32 v17, v204, v200, s56\n"
"v_mfma_f32_16x16x32_bf16 v[180:183], v[20:23], a[28:31], v[180:183]\n"
"v_perm_b32 v18, v212, v208, s56\n"
"v_perm_b32 v19, v220, v216, s56\n"
"v_mfma_f32_16x16x32_bf16 v[88:91], v[24:27], a[16:19], v[88:91]\n"
"v_perm_b32 v20, v196, v192, s57\n"
"v_perm_b32 v21, v204, v200, s57\n"
"buffer_load_dwordx4 v[252:255], v14, s[36:39], s85 offen offset:3072\n"
"v_mfma_f32_16x16x32_bf16 v[92:95], v[28:31], a[16:19], v[92:95]\n"
"v_perm_b32 v22, v212, v208, s57\n"
"v_perm_b32 v23, v220, v216, s57\n"
"v_mfma_f32_16x16x32_bf16 v[120:123], v[24:27], a[20:23], v[120:123]\n"
"s_waitcnt lgkmcnt(0)\n"
"v_mfma_f32_16x16x32_bf16 v[124:127], v[28:31], a[20:23], v[124:127]\n"
"v_mfma_f32_16x16x32_bf16 v[152:155], v[24:27], a[24:27], v[152:155]\n"
"v_mfma_f32_16x16x32_bf16 v[156:159], v[28:31], a[24:27], v[156:159]\n"
"v_mfma_f32_16x16x32_bf16 v[184:187], v[24:27], a[28:31], v[184:187]\n"
"v_perm_b32 v24, v197, v193, s56\n"
"v_perm_b32 v25, v205, v201, s56\n"
"v_mfma_f32_16x16x32_bf16 v[188:191], v[28:31], a[28:31], v[188:191]\n"
"v_perm_b32 v26, v213, v209, s56\n"
"v_perm_b32 v27, v221, v217, s56\n"
"v_mfma_f32_16x16x32_bf16 v[64:67], v[16:19], a[0:3], v[64:67]\n"
"v_perm_b32 v28, v197, v193, s57\n"
"v_perm_b32 v29, v205, v201, s57\n"
"v_mfma_f32_16x16x32_bf16 v[68:71], v[20:23], a[0:3], v[68:71]\n"
"v_perm_b32 v30, v213, v209, s57\n"
"v_perm_b32 v31, v221, v217, s57\n"
"v_mfma_f32_16x16x32_bf16 v[96:99], v[16:19], a[4:7], v[96:99]\n"
"v_mfma_f32_16x16x32_bf16 v[100:103], v[20:23], a[4:7], v[100:103]\n"
"v_mfma_f32_16x16x32_bf16 v[128:131], v[16:19], a[8:11], v[128:131]\n"
"v_mfma_f32_16x16x32_bf16 v[132:135], v[20:23], a[8:11], v[132:135]\n"
"v_mfma_f32_16x16x32_bf16 v[160:163], v[16:19], a[12:15], v[160:163]\n"
"v_perm_b32 v16, v198, v194, s56\n"
"v_perm_b32 v17, v206, v202, s56\n"
"v_mfma_f32_16x16x32_bf16 v[164:167], v[20:23], a[12:15], v[164:167]\n"
"v_perm_b32 v18, v214, v210, s56\n"
"v_perm_b32 v19, v222, v218, s56\n"
"v_mfma_f32_16x16x32_bf16 v[72:75], v[24:27], a[0:3], v[72:75]\n"
"v_perm_b32 v20, v198, v194, s57\n"
"v_perm_b32 v21, v206, v202, s57\n"
"v_mfma_f32_16x16x32_bf16 v[76:79], v[28:31], a[0:3], v[76:79]\n"
"v_perm_b32 v22, v214, v210, s57\n"
"v_perm_b32 v23, v222, v218, s57\n"
"v_mfma_f32_16x16x32_bf16 v[104:107], v[24:27], a[4:7], v[104:107]\n"
"s_add_u32 s83, s51, 15\n"
"s_and_b32 s83, s83, 15\n"
"v_mfma_f32_16x16x32_bf16 v[108:111], v[28:31], a[4:7], v[108:111]\n"
"s_lshl_b32 s83, s83, 11\n"
"v_add_u32 v1, s83, v12\n"
"v_mfma_f32_16x16x32_bf16 v[136:139], v[24:27], a[8:11], v[136:139]\n"
"ds_read_b128 a[16:19], v1 offset:0\n"
"v_mfma_f32_16x16x32_bf16 v[140:143], v[28:31], a[8:11], v[140:143]\n"
"ds_read_b128 a[20:23], v1 offset:1024\n"
"v_mfma_f32_16x16x32_bf16 v[168:171], v[24:27], a[12:15], v[168:171]\n"
"ds_read_b128 a[24:27], v1 offset:32768\n"
"v_perm_b32 v24, v199, v195, s56\n"
"v_mfma_f32_16x16x32_bf16 v[172:175], v[28:31], a[12:15], v[172:175]\n"
"ds_read_b128 a[28:31], v1 offset:33792\n"
"v_perm_b32 v25, v207, v203, s56\n"
"v_mfma_f32_16x16x32_bf16 v[80:83], v[16:19], a[0:3], v[80:83]\n"
"v_perm_b32 v26, v215, v211, s56\n"
"v_perm_b32 v27, v223, v219, s56\n"
"v_mfma_f32_16x16x32_bf16 v[84:87], v[20:23], a[0:3], v[84:87]\n"
"v_perm_b32 v28, v199, v195, s57\n"
"v_perm_b32 v29, v207, v203, s57\n"
"v_mfma_f32_16x16x32_bf16 v[112:115], v[16:19], a[4:7], v[112:115]\n"
"v_perm_b32 v30, v215, v211, s57\n"
"v_perm_b32 v31, v223, v219, s57\n"
"v_mfma_f32_16x16x32_bf16 v[116:119], v[20:23], a[4:7], v[116:119]\n"
"v_mfma_f32_16x16x32_bf16 v[144:147], v[16:19], a[8:11], v[144:147]\n"
"v_mfma_f32_16x16x32_bf16 v[148:151], v[20:23], a[8:11], v[148:151]\n"
"v_mfma_f32_16x16x32_bf16 v[176:179], v[16:19], a[12:15], v[176:179]\n"
"s_waitcnt vmcnt(0)\n"
"v_perm_b32 v16, v228, v224, s56\n"
"v_perm_b32 v17, v236, v232, s56\n"
"v_mfma_f32_16x16x32_bf16 v[180:183], v[20:23], a[12:15], v[180:183]\n"
"v_perm_b32 v18, v244, v240, s56\n"
"v_perm_b32 v19, v252, v248, s56\n"
"v_mfma_f32_16x16x32_bf16 v[88:91], v[24:27], a[0:3], v[88:91]\n"
"v_perm_b32 v20, v228, v224, s57\n"
"v_perm_b32 v21, v236, v232, s57\n"
"v_mfma_f32_16x16x32_bf16 v[92:95], v[28:31], a[0:3], v[92:95]\n"
"v_perm_b32 v22, v244, v240, s57\n"
"v_perm_b32 v23, v252, v248, s57\n"
"v_mfma_f32_16x16x32_bf16 v[120:123], v[24:27], a[4:7], v[120:123]\n"
"s_waitcnt lgkmcnt(0)\n"
"v_mfma_f32_16x16x32_bf16 v[124:127], v[28:31], a[4:7], v[124:127]\n"
"v_mfma_f32_16x16x32_bf16 v[152:155], v[24:27], a[8:11], v[152:155]\n"
"v_mfma_f32_16x16x32_bf16 v[156:159], v[28:31], a[8:11], v[156:159]\n"
"v_mfma_f32_16x16x32_bf16 v[184:187], v[24:27], a[12:15], v[184:187]\n"
"v_perm_b32 v24, v229, v225, s56\n"
"v_perm_b32 v25, v237, v233, s56\n"
"v_mfma_f32_16x16x32_bf16 v[188:191], v[28:31], a[12:15], v[188:191]\n"
"v_perm_b32 v26, v245, v241, s56\n"
"v_perm_b32 v27, v253, v249, s56\n"
"v_mfma_f32_16x16x32_bf16 v[64:67], v[16:19], a[16:19], v[64:67]\n"
"v_perm_b32 v28, v229, v225, s57\n"
"v_perm_b32 v29, v237, v233, s57\n"
"v_mfma_f32_16x16x32_bf16 v[68:71], v[20:23], a[16:19], v[68:71]\n"
"v_perm_b32 v30, v245, v241, s57\n"
"v_perm_b32 v31, v253, v249, s57\n"
"v_mfma_f32_16x16x32_bf16 v[96:99], v[16:19], a[20:23], v[96:99]\n"
"v_mfma_f32_16x16x32_bf16 v[100:103], v[20:23], a[20:23], v[100:103]\n"
"v_mfma_f32_16x16x32_bf16 v[128:131], v[16:19], a[24:27], v[128:131]\n"
"v_mfma_f32_16x16x32_bf16 v[132:135], v[20:23], a[24:27], v[132:135]\n"
"v_mfma_f32_16x16x32_bf16 v[160:163], v[16:19], a[28:31], v[160:163]\n"
"v_perm_b32 v16, v230, v226, s56\n"
"v_perm_b32 v17, v238, v234, s56\n"
"v_mfma_f32_16x16x32_bf16 v[164:167], v[20:23], a[28:31], v[164:167]\n"
"v_perm_b32 v18, v246, v242, s56\n"
"v_perm_b32 v19, v254, v250, s56\n"
"v_mfma_f32_16x16x32_bf16 v[72:75], v[24:27], a[16:19], v[72:75]\n"
"buffer_load_dword v32, v4, s[40:43], 0 offen offset:0 sc1\n"
"v_perm_b32 v20, v230, v226, s57\n"
"v_mfma_f32_16x16x32_bf16 v[76:79], v[28:31], a[16:19], v[76:79]\n"
"v_perm_b32 v21, v238, v234, s57\n"
"v_perm_b32 v22, v246, v242, s57\n"
"v_mfma_f32_16x16x32_bf16 v[104:107], v[24:27], a[20:23], v[104:107]\n"
"v_perm_b32 v23, v254, v250, s57\n"
"v_mfma_f32_16x16x32_bf16 v[108:111], v[28:31], a[20:23], v[108:111]\n"
"v_mfma_f32_16x16x32_bf16 v[136:139], v[24:27], a[24:27], v[136:139]\n"
"buffer_load_dword v33, v5, s[40:43], 0 offen offset:0 sc1\n"
"v_mfma_f32_16x16x32_bf16 v[140:143], v[28:31], a[24:27], v[140:143]\n"
"v_mfma_f32_16x16x32_bf16 v[168:171], v[24:27], a[28:31], v[168:171]\n"
"v_perm_b32 v24, v231, v227, s56\n"
"v_perm_b32 v25, v239, v235, s56\n"
"v_mfma_f32_16x16x32_bf16 v[172:175], v[28:31], a[28:31], v[172:175]\n"
"v_perm_b32 v26, v247, v243, s56\n"
"v_perm_b32 v27, v255, v251, s56\n"
"v_mfma_f32_16x16x32_bf16 v[80:83], v[16:19], a[16:19], v[80:83]\n"
"v_perm_b32 v28, v231, v227, s57\n"
"v_perm_b32 v29, v239, v235, s57\n"
"v_mfma_f32_16x16x32_bf16 v[84:87], v[20:23], a[16:19], v[84:87]\n"
"v_perm_b32 v30, v247, v243, s57\n"
"v_perm_b32 v31, v255, v251, s57\n"
"v_mfma_f32_16x16x32_bf16 v[112:115], v[16:19], a[20:23], v[112:115]\n"
"v_mfma_f32_16x16x32_bf16 v[116:119], v[20:23], a[20:23], v[116:119]\n"
"v_mfma_f32_16x16x32_bf16 v[144:147], v[16:19], a[24:27], v[144:147]\n"
"v_mfma_f32_16x16x32_bf16 v[148:151], v[20:23], a[24:27], v[148:151]\n"
"v_mfma_f32_16x16x32_bf16 v[176:179], v[16:19], a[28:31], v[176:179]\n"
"v_mfma_f32_16x16x32_bf16 v[180:183], v[20:23], a[28:31], v[180:183]\n"
"v_mfma_f32_16x16x32_bf16 v[88:91], v[24:27], a[16:19], v[88:91]\n"
"v_mfma_f32_16x16x32_bf16 v[92:95], v[28:31], a[16:19], v[92:95]\n"
"v_mfma_f32_16x16x32_bf16 v[120:123], v[24:27], a[20:23], v[120:123]\n"
"v_mfma_f32_16x16x32_bf16 v[124:127], v[28:31], a[20:23], v[124:127]\n"
"v_mfma_f32_16x16x32_bf16 v[152:155], v[24:27], a[24:27], v[152:155]\n"
"v_mfma_f32_16x16x32_bf16 v[156:159], v[28:31], a[24:27], v[156:159]\n"
"v_mfma_f32_16x16x32_bf16 v[184:187], v[24:27], a[28:31], v[184:187]\n"
"v_mfma_f32_16x16x32_bf16 v[188:191], v[28:31], a[28:31], v[188:191]\n"
"s_add_u32 s84, s76, 0\n"
"v_cvt_pk_bf16_f32 v16, v64, v68\n"
"v_cvt_pk_bf16_f32 v17, v72, v76\n"
"v_cvt_pk_bf16_f32 v18, v80, v84\n"
"v_cvt_pk_bf16_f32 v19, v88, v92\n"
"buffer_store_dwordx4 v[16:19], v12, s[40:43], s84 offen offset:0 sc1\n"
"v_cvt_pk_bf16_f32 v20, v65, v69\n"
"v_cvt_pk_bf16_f32 v21, v73, v77\n"
"v_cvt_pk_bf16_f32 v22, v81, v85\n"
"v_cvt_pk_bf16_f32 v23, v89, v93\n"
"buffer_store_dwordx4 v[20:23], v12, s[40:43], s84 offen offset:1024 sc1\n"
"v_cvt_pk_bf16_f32 v24, v66, v70\n"
"v_cvt_pk_bf16_f32 v25, v74, v78\n"
"v_cvt_pk_bf16_f32 v26, v82, v86\n"
"v_cvt_pk_bf16_f32 v27, v90, v94\n"
"buffer_store_dwordx4 v[24:27], v12, s[40:43], s84 offen offset:2048 sc1\n"
"v_cvt_pk_bf16_f32 v28, v67, v71\n"
"v_cvt_pk_bf16_f32 v29, v75, v79\n"
"v_cvt_pk_bf16_f32 v30, v83, v87\n"
"v_cvt_pk_bf16_f32 v31, v91, v95\n"
"buffer_store_dwordx4 v[28:31], v12, s[40:43], s84 offen offset:3072 sc1\n"
"s_add_u32 s85, s76, 4096\n"
"v_cvt_pk_bf16_f32 v16, v96, v100\n"
"v_cvt_pk_bf16_f32 v17, v104, v108\n"
"v_cvt_pk_bf16_f32 v18, v112, v116\n"
"v_cvt_pk_bf16_f32 v19, v120, v124\n"
"buffer_store_dwordx4 v[16:19], v12, s[40:43], s85 offen offset:0 sc1\n"
"v_cvt_pk_bf16_f32 v20, v97, v101\n"
"v_cvt_pk_bf16_f32 v21, v105, v109\n"
"v_cvt_pk_bf16_f32 v22, v113, v117\n"
"v_cvt_pk_bf16_f32 v23, v121, v125\n"
"buffer_store_dwordx4 v[20:23], v12, s[40:43], s85 offen offset:1024 sc1\n"
"v_cvt_pk_bf16_f32 v24, v98, v102\n"
"v_cvt_pk_bf16_f32 v25, v106, v110\n"
"v_cvt_pk_bf16_f32 v26, v114, v118\n"
"v_cvt_pk_bf16_f32 v27, v122, v126\n"
"buffer_store_dwordx4 v[24:27], v12, s[40:43], s85 offen offset:2048 sc1\n"
"v_cvt_pk_bf16_f32 v28, v99, v103\n"
"v_cvt_pk_bf16_f32 v29, v107, v111\n"
"v_cvt_pk_bf16_f32 v30, v115, v119\n"
"v_cvt_pk_bf16_f32 v31, v123, v127\n"
"buffer_store_dwordx4 v[28:31], v12, s[40:43], s85 offen offset:3072 sc1\n"
"s_add_u32 s84, s76, 8192\n"
"v_cvt_pk_bf16_f32 v16, v128, v132\n"
"v_cvt_pk_bf16_f32 v17, v136, v140\n"
"v_cvt_pk_bf16_f32 v18, v144, v148\n"
"v_cvt_pk_bf16_f32 v19, v152, v156\n"
"buffer_store_dwordx4 v[16:19], v12, s[40:43], s84 offen offset:0 sc1\n"
"v_cvt_pk_bf16_f32 v20, v129, v133\n"
"v_cvt_pk_bf16_f32 v21, v137, v141\n"
"v_cvt_pk_bf16_f32 v22, v145, v149\n"
"v_cvt_pk_bf16_f32 v23, v153, v157\n"
"buffer_store_dwordx4 v[20:23], v12, s[40:43], s84 offen offset:1024 sc1\n"
"v_cvt_pk_bf16_f32 v24, v130, v134\n"
"v_cvt_pk_bf16_f32 v25, v138, v142\n"
"v_cvt_pk_bf16_f32 v26, v146, v150\n"
"v_cvt_pk_bf16_f32 v27, v154, v158\n"
"buffer_store_dwordx4 v[24:27], v12, s[40:43], s84 offen offset:2048 sc1\n"
"v_cvt_pk_bf16_f32 v28, v131, v135\n"
"v_cvt_pk_bf16_f32 v29, v139, v143\n"
"v_cvt_pk_bf16_f32 v30, v147, v151\n"
"v_cvt_pk_bf16_f32 v31, v155, v159\n"
"buffer_store_dwordx4 v[28:31], v12, s[40:43], s84 offen offset:3072 sc1\n"
"s_add_u32 s85, s76, 12288\n"
"v_cvt_pk_bf16_f32 v16, v160, v164\n"
"v_cvt_pk_bf16_f32 v17, v168, v172\n"
"v_cvt_pk_bf16_f32 v18, v176, v180\n"
"v_cvt_pk_bf16_f32 v19, v184, v188\n"
"buffer_store_dwordx4 v[16:19], v12, s[40:43], s85 offen offset:0 sc1\n"
"v_cvt_pk_bf16_f32 v20, v161, v165\n"
"v_cvt_pk_bf16_f32 v21, v169, v173\n"
"v_cvt_pk_bf16_f32 v22, v177, v181\n"
"v_cvt_pk_bf16_f32 v23, v185, v189\n"
"buffer_store_dwordx4 v[20:23], v12, s[40:43], s85 offen offset:1024 sc1\n"
"v_cvt_pk_bf16_f32 v24, v162, v166\n"
"v_cvt_pk_bf16_f32 v25, v170, v174\n"
"v_cvt_pk_bf16_f32 v26, v178, v182\n"
"v_cvt_pk_bf16_f32 v27, v186, v190\n"
"buffer_store_dwordx4 v[24:27], v12, s[40:43], s85 offen offset:2048 sc1\n"
"v_cvt_pk_bf16_f32 v28, v163, v167\n"
"v_cvt_pk_bf16_f32 v29, v171, v175\n"
"v_cvt_pk_bf16_f32 v30, v179, v183\n"
"v_cvt_pk_bf16_f32 v31, v187, v191\n"
"buffer_store_dwordx4 v[28:31], v12, s[40:43], s85 offen offset:3072 sc1\n"
"s_waitcnt vmcnt(0)\n"
"s_mov_b64 exec, 0xff\n"
"buffer_store_dword v10, v6, s[40:43], 0 offen sc1\n"
"s_mov_b32 s80, 0x40000\n"
"s_mov_b32 s78, 0\n"
"s_barrier\n"
"s_branch .LAchk1\n"
".LApoll1:\n"
"buffer_load_dword v32, v4, s[40:43], 0 offen sc1\n"
"buffer_load_dword v33, v5, s[40:43], 0 offen sc1\n"
".LAnor2:\n"
"s_waitcnt vmcnt(0)\n"
".LAchk1:\n"
"v_and_b32 v33, 0xffff, v33\n"
"v_cmp_ne_u32 vcc, v33, v15\n"
"s_nop 1\n"
"s_cbranch_vccz .LAsame1\n"
"v_mov_b32 v15, v33\n"
"v_lshl_or_b32 v10, v3, 16, v15\n"
"buffer_store_dword v10, v6, s[40:43], 0 offen sc1\n"
".LAsame1:\n"
"v_and_b32 v11, 0xffff, v32\n"
"v_cmp_eq_u32 vcc, v11, v2\n"
"s_nop 1\n"
"s_andn2_b32 s81, vcc_lo, s77\n"
"s_andn2_b32 s81, s81, s78\n"
"s_or_b32 s79, vcc_lo, s77\n"
"s_andn2_b32 s79, s79, s78\n"
"s_cbranch_scc0 .LAnone1\n"
"s_or_b32 s78, s78, s79\n"
"s_mov_b64 exec, -1\n"
"s_bitcmp0_b32 s79, 0\n"
"s_cbranch_scc1 .LAnf0\n"
"buffer_load_dwordx4 v[192:195], v8, s[40:43], s62 offen sc1\n"
"buffer_load_dwordx4 v[196:199], v8, s[40:43], s62 offen offset:1024 sc1\n"
".LAnf0:\n"
"s_bitcmp0_b32 s79, 1\n"
"s_cbranch_scc1 .LAnf1\n"
"buffer_load_dwordx4 v[200:203], v8, s[40:43], s63 offen sc1\n"
"buffer_load_dwordx4 v[204:207], v8, s[40:43], s63 offen offset:1024 sc1\n"
".LAnf1:\n"
"s_bitcmp0_b32 s79, 2\n"
"s_cbranch_scc1 .LAnf2\n"
"buffer_load_dwordx4 v[208:211], v8, s[40:43], s64 offen sc1\n"
"buffer_load_dwordx4 v[212:215], v8, s[40:43], s64 offen offset:1024 sc1\n"
".LAnf2:\n"
"s_bitcmp0_b32 s79, 3\n"
"s_cbranch_scc1 .LAnf3\n"
"buffer_load_dwordx4 v[216:219], v8, s[40:43], s65 offen sc1\n"
"buffer_load_dwordx4 v[220:223], v8, s[40:43], s65 offen offset:1024 sc1\n"
".LAnf3:\n"
"s_bitcmp0_b32 s79, 4\n"
"s_cbranch_scc1 .LAnf4\n"
"buffer_load_dwordx4 v[224:227], v8, s[40:43], s66 offen sc1\n"
"buffer_load_dwordx4 v[228:231], v8, s[40:43], s66 offen offset:1024 sc1\n"
".LAnf4:\n"
"s_bitcmp0_b32 s79, 5\n"
"s_cbranch_scc1 .LAnf5\n"
"buffer_load_dwordx4 v[232:235], v8, s[40:43], s67 offen sc1\n"
"buffer_load_dwordx4 v[236:239], v8, s[40:43], s67 offen offset:1024 sc1\n"
".LAnf5:\n"
"s_bitcmp0_b32 s79, 6\n"
"s_cbranch_scc1 .LAnf6\n"
"buffer_load_dwordx4 v[240:243], v8, s[40:43], s68 offen sc1\n"
"buffer_load_dwordx4 v[244:247], v8, s[40:43], s68 offen offset:1024 sc1\n"
".LAnf6:\n"
"s_bitcmp0_b32 s79, 7\n"
"s_cbranch_scc1 .LAnf7\n"
"buffer_load_dwordx4 v[248:251], v8, s[40:43], s69 offen sc1\n"
"buffer_load_dwordx4 v[252:255], v8, s[40:43], s69 offen offset:1024 sc1\n"
".LAnf7:\n"
"s_cmp_eq_u32 s81, 0\n"
"s_cbranch_scc1 .LAnoack\n"
"s_mov_b32 exec_hi, 0\n"
"s_mov_b32 exec_lo, s81\n"
"v_lshrrev_b32 v11, 16, v32\n"
"buffer_store_dword v11, v7, s[40:43], 0 offen offset:4 sc1\n"
".LAnoack:\n"
"s_mov_b64 exec, 0xff\n"
"s_cmp_eq_u32 s78, 0xff\n"
"s_cbranch_scc1 .LAgot1\n"
".LAnone1:\n"
"s_sub_u32 s80, s80, 1\n"
"s_cmp_lg_u32 s80, 0\n"
"s_cbranch_scc1 .LApoll1\n"
".LAgot1:\n"
"s_mov_b64 exec, -1\n"
"buffer_load_dwordx2 v[34:35], v5, s[40:43], 0 offen offset:0 sc1\n"
"s_waitcnt vmcnt(1)\n"
"v_lshlrev_b32 v64, 16, v192\n"
"v_and_b32 v65, 0xffff0000, v192\n"
"v_lshlrev_b32 v66, 16, v193\n"
"v_and_b32 v67, 0xffff0000, v193\n"
"v_lshlrev_b32 v68, 16, v194\n"
"v_and_b32 v69, 0xffff0000, v194\n"
"v_lshlrev_b32 v70, 16, v195\n"
"v_and_b32 v71, 0xffff0000, v195\n"
"v_lshlrev_b32 v72, 16, v196\n"
"v_and_b32 v73, 0xffff0000, v196\n"
"v_lshlrev_b32 v74, 16, v197\n"
"v_and_b32 v75, 0xffff0000, v197\n"
"v_lshlrev_b32 v76, 16, v198\n"
"v_and_b32 v77, 0xffff0000, v198\n"
"v_lshlrev_b32 v78, 16, v199\n"
"v_and_b32 v79, 0xffff0000, v199\n"
"v_mov_b32 v80, 0x3f80\n"
"v_mov_b32 v81, 0x3f800000\n"
"v_dot2c_f32_bf16 v64, v200, v80\n"
"v_dot2c_f32_bf16 v65, v200, v81\n"
"v_dot2c_f32_bf16 v66, v201, v80\n"
"v_dot2c_f32_bf16 v67, v201, v81\n"
"v_dot2c_f32_bf16 v68, v202, v80\n"
"v_dot2c_f32_bf16 v69, v202, v81\n"
"v_dot2c_f32_bf16 v70, v203, v80\n"
"v_dot2c_f32_bf16 v71, v203, v81\n"
"v_dot2c_f32_bf16 v72, v204, v80\n"
"v_dot2c_f32_bf16 v73, v204, v81\n"
"v_dot2c_f32_bf16 v74, v205, v80\n"
"v_dot2c_f32_bf16 v75, v205, v81\n"
"v_dot2c_f32_bf16 v76, v206, v80\n"
"v_dot2c_f32_bf16 v77, v206, v81\n"
"v_dot2c_f32_bf16 v78, v207, v80\n"
"v_dot2c_f32_bf16 v79, v207, v81\n"
"v_dot2c_f32_bf16 v64, v208, v80\n"
"v_dot2c_f32_bf16 v65, v208, v81\n"
"v_dot2c_f32_bf16 v66, v209, v80\n"
"v_dot2c_f32_bf16 v67, v209, v81\n"
"v_dot2c_f32_bf16 v68, v210, v80\n"
"v_dot2c_f32_bf16 v69, v210, v81\n"
"v_dot2c_f32_bf16 v70, v211, v80\n"
"v_dot2c_f32_bf16 v71, v211, v81\n"
"v_dot2c_f32_bf16 v72, v212, v80\n"
"v_dot2c_f32_bf16 v73, v212, v81\n"
"v_dot2c_f32_bf16 v74, v213, v80\n"
"v_dot2c_f32_bf16 v75, v213, v81\n"
"v_dot2c_f32_bf16 v76, v214, v80\n"
"v_dot2c_f32_bf16 v77, v214, v81\n"
"v_dot2c_f32_bf16 v78, v215, v80\n"
"v_dot2c_f32_bf16 v79, v215, v81\n"
"v_dot2c_f32_bf16 v64, v216, v80\n"
"v_dot2c_f32_bf16 v65, v216, v81\n"
"v_dot2c_f32_bf16 v66, v217, v80\n"
"v_dot2c_f32_bf16 v67, v217, v81\n"
"v_dot2c_f32_bf16 v68, v218, v80\n"
"v_dot2c_f32_bf16 v69, v218, v81\n"
"v_dot2c_f32_bf16 v70, v219, v80\n"
"v_dot2c_f32_bf16 v71, v219, v81\n"
"v_dot2c_f32_bf16 v72, v220, v80\n"
"v_dot2c_f32_bf16 v73, v220, v81\n"
"v_dot2c_f32_bf16 v74, v221, v80\n"
"v_dot2c_f32_bf16 v75, v221, v81\n"
"v_dot2c_f32_bf16 v76, v222, v80\n"
"v_dot2c_f32_bf16 v77, v222, v81\n"
"v_dot2c_f32_bf16 v78, v223, v80\n"
"v_dot2c_f32_bf16 v79, v223, v81\n"
"v_dot2c_f32_bf16 v64, v224, v80\n"
"v_dot2c_f32_bf16 v65, v224, v81\n"
"v_dot2c_f32_bf16 v66, v225, v80\n"
"v_dot2c_f32_bf16 v67, v225, v81\n"
"v_dot2c_f32_bf16 v68, v226, v80\n"
"v_dot2c_f32_bf16 v69, v226, v81\n"
"v_dot2c_f32_bf16 v70, v227, v80\n"
"v_dot2c_f32_bf16 v71, v227, v81\n"
"v_dot2c_f32_bf16 v72, v228, v80\n"
"v_dot2c_f32_bf16 v73, v228, v81\n"
"v_dot2c_f32_bf16 v74, v229, v80\n"
"v_dot2c_f32_bf16 v75, v229, v81\n"
"v_dot2c_f32_bf16 v76, v230, v80\n"
"v_dot2c_f32_bf16 v77, v230, v81\n"
"v_dot2c_f32_bf16 v78, v231, v80\n"
"v_dot2c_f32_bf16 v79, v231, v81\n"
"v_dot2c_f32_bf16 v64, v232, v80\n"
"v_dot2c_f32_bf16 v65, v232, v81\n"
"v_dot2c_f32_bf16 v66, v233, v80\n"
"v_dot2c_f32_bf16 v67, v233, v81\n"
"v_dot2c_f32_bf16 v68, v234, v80\n"
"v_dot2c_f32_bf16 v69, v234, v81\n"
"v_dot2c_f32_bf16 v70, v235, v80\n"
"v_dot2c_f32_bf16 v71, v235, v81\n"
"v_dot2c_f32_bf16 v72, v236, v80\n"
"v_dot2c_f32_bf16 v73, v236, v81\n"
"v_dot2c_f32_bf16 v74, v237, v80\n"
"v_dot2c_f32_bf16 v75, v237, v81\n"
"v_dot2c_f32_bf16 v76, v238, v80\n"
"v_dot2c_f32_bf16 v77, v238, v81\n"
"v_dot2c_f32_bf16 v78, v239, v80\n"
"v_dot2c_f32_bf16 v79, v239, v81\n"
"v_dot2c_f32_bf16 v64, v240, v80\n"
"v_dot2c_f32_bf16 v65, v240, v81\n"
"v_dot2c_f32_bf16 v66, v241, v80\n"
"v_dot2c_f32_bf16 v67, v241, v81\n"
"v_dot2c_f32_bf16 v68, v242, v80\n"
"v_dot2c_f32_bf16 v69, v242, v81\n"
"v_dot2c_f32_bf16 v70, v243, v80\n"
"v_dot2c_f32_bf16 v71, v243, v81\n"
"v_dot2c_f32_bf16 v72, v244, v80\n"
"v_dot2c_f32_bf16 v73, v244, v81\n"
"v_dot2c_f32_bf16 v74, v245, v80\n"
"v_dot2c_f32_bf16 v75, v245, v81\n"
"v_dot2c_f32_bf16 v76, v246, v80\n"
"v_dot2c_f32_bf16 v77, v246, v81\n"
"v_dot2c_f32_bf16 v78, v247, v80\n"
"v_dot2c_f32_bf16 v79, v247, v81\n"
"v_dot2c_f32_bf16 v64, v248, v80\n"
"v_dot2c_f32_bf16 v65, v248, v81\n"
"v_dot2c_f32_bf16 v66, v249, v80\n"
"v_dot2c_f32_bf16 v67, v249, v81\n"
"v_dot2c_f32_bf16 v68, v250, v80\n"
"v_dot2c_f32_bf16 v69, v250, v81\n"
"v_dot2c_f32_bf16 v70, v251, v80\n"
"v_dot2c_f32_bf16 v71, v251, v81\n"
"v_dot2c_f32_bf16 v72, v252, v80\n"
"v_dot2c_f32_bf16 v73, v252, v81\n"
"v_dot2c_f32_bf16 v74, v253, v80\n"
"v_dot2c_f32_bf16 v75, v253, v81\n"
"v_dot2c_f32_bf16 v76, v254, v80\n"
"v_dot2c_f32_bf16 v77, v254, v81\n"
"v_dot2c_f32_bf16 v78, v255, v80\n"
"v_dot2c_f32_bf16 v79, v255, v81\n"
"s_nop 2\n"
"v_cvt_pk_bf16_f32 v84, v64, v65\n"
"v_cvt_pk_bf16_f32 v85, v66, v67\n"
"v_cvt_pk_bf16_f32 v86, v68, v69\n"
"v_cvt_pk_bf16_f32 v87, v70, v71\n"
"buffer_store_dwordx4 v[84:87], v9, s[44:47], 0 offen offset:0\n"
"v_cvt_pk_bf16_f32 v88, v72, v73\n"
"v_cvt_pk_bf16_f32 v89, v74, v75\n"
"v_cvt_pk_bf16_f32 v90, v76, v77\n"
"v_cvt_pk_bf16_f32 v91, v78, v79\n"
"buffer_store_dwordx4 v[88:91], v9, s[44:47], 0 offen offset:16\n"
"s_waitcnt vmcnt(2)\n"
"s_mov_b32 exec_hi, 0\n"
"s_mov_b32 exec_lo, s55\n"
"s_mov_b32 s80, 0x40000\n"
"s_branch .LAchk2\n"
".LApoll2:\n"
"buffer_load_dwordx2 v[34:35], v5, s[40:43], 0 offen sc1\n"
"s_waitcnt vmcnt(0)\n"
".LAchk2:\n"
"v_mov_b32 v33, v34\n"
"v_and_b32 v33, 0xffff, v33\n"
"v_cmp_ne_u32 vcc, v33, v15\n"
"s_nop 1\n"
"s_cbranch_vccz .LAsame2\n"
"v_mov_b32 v15, v33\n"
"v_lshl_or_b32 v10, v3, 16, v15\n"
"buffer_store_dword v10, v6, s[40:43], 0 offen sc1\n"
".LAsame2:\n"
"v_and_b32 v11, 0xffff, v35\n"
"v_cmp_ne_u32 vcc, v11, v3\n"
"s_nop 1\n"
"s_cbranch_vccz .LAgot2\n"
"s_sleep 1\n"
"s_sub_u32 s80, s80, 1\n"
"s_cmp_lg_u32 s80, 0\n"
"s_cbranch_scc1 .LApoll2\n"
".LAgot2:\n"
"s_mov_b64 exec, -1\n"
"s_branch .Lend\n"
".Lrare0:\n"
"s_nop 15\n"
"v_max_f32 v188, v188, v91\n"
"s_nop 1\n"
"v_max_f32 v189, v189, v95\n"
"s_nop 1\n"
"s_nop 0\n"
"s_nop 1\n"
"v_permlane32_swap_b32 v188, v189\n"
"s_nop 1\n"
"v_max_f32 v188, v188, v189\n"
"s_nop 1\n"
"v_mov_b32 v189, v188\n"
"s_nop 1\n"
"s_nop 0\n"
"s_nop 1\n"
"v_permlane16_swap_b32 v188, v189\n"
"s_nop 1\n"
"v_max_f32 v188, v188, v189\n"
"s_nop 1\n"
"v_mov_b32 v190, v188\n"
"s_nop 1\n"
"s_nop 0\n"
"s_nop 1\n"
"v_permlane32_swap_b32 v188, v190\n"
"s_nop 1\n"
"v_max_f32 v188, 0, v188\n"
"v_exp_f32 v8, -v188\n"
"s_nop 1\n"
"v_sub_f32 v128, v128, v188\n"
"v_sub_f32 v129, v129, v188\n"
"v_sub_f32 v130, v130, v188\n"
"v_sub_f32 v131, v131, v188\n"
"v_sub_f32 v64, v64, v188\n"
"v_sub_f32 v65, v65, v188\n"
"v_sub_f32 v66, v66, v188\n"
"v_sub_f32 v67, v67, v188\n"
"v_sub_f32 v72, v72, v188\n"
"v_sub_f32 v73, v73, v188\n"
"v_sub_f32 v74, v74, v188\n"
"v_sub_f32 v75, v75, v188\n"
"v_sub_f32 v80, v80, v188\n"
"v_sub_f32 v81, v81, v188\n"
"v_sub_f32 v82, v82, v188\n"
"v_sub_f32 v83, v83, v188\n"
"v_sub_f32 v88, v88, v188\n"
"v_sub_f32 v89, v89, v188\n"
"v_sub_f32 v90, v90, v188\n"
"v_sub_f32 v91, v91, v188\n"
"v_max_f32 v190, 0, v190\n"
"v_exp_f32 v9, -v190\n"
"s_nop 1\n"
"v_sub_f32 v132, v132, v190\n"
"v_sub_f32 v133, v133, v190\n"
"v_sub_f32 v134, v134, v190\n"
"v_sub_f32 v135, v135, v190\n"
"v_sub_f32 v68, v68, v190\n"
"v_sub_f32 v69, v69, v190\n"
"v_sub_f32 v70, v70, v190\n"
"v_sub_f32 v71, v71, v190\n"
"v_sub_f32 v76, v76, v190\n"
"v_sub_f32 v77, v77, v190\n"
"v_sub_f32 v78, v78, v190\n"
"v_sub_f32 v79, v79, v190\n"
"v_sub_f32 v84, v84, v190\n"
"v_sub_f32 v85, v85, v190\n"
"v_sub_f32 v86, v86, v190\n"
"v_sub_f32 v87, v87, v190\n"
"v_sub_f32 v92, v92, v190\n"
"v_sub_f32 v93, v93, v190\n"
"v_sub_f32 v94, v94, v190\n"
"v_sub_f32 v95, v95, v190\n"
"s_cmp_eq_u32 s61, 0\n"
"s_cbranch_scc0 .Lrb1_0\n"
"v_accvgpr_read_b32 v189, a0\n"
"v_mul_f32 v189, v189, v8\n"
"v_accvgpr_write_b32 a0, v189\n"
"v_accvgpr_read_b32 v189, a1\n"
"v_mul_f32 v189, v189, v8\n"
"v_accvgpr_write_b32 a1, v189\n"
"v_accvgpr_read_b32 v189, a2\n"
"v_mul_f32 v189, v189, v8\n"
"v_accvgpr_write_b32 a2, v189\n"
"v_accvgpr_read_b32 v189, a3\n"
"v_mul_f32 v189, v189, v8\n"
"v_accvgpr_write_b32 a3, v189\n"
"v_accvgpr_read_b32 v189, a4\n"
"v_mul_f32 v189, v189, v9\n"
"v_accvgpr_write_b32 a4, v189\n"
"v_accvgpr_read_b32 v189, a5\n"
"v_mul_f32 v189, v189, v9\n"
"v_accvgpr_write_b32 a5, v189\n"
"v_accvgpr_read_b32 v189, a6\n"
"v_mul_f32 v189, v189, v9\n"
"v_accvgpr_write_b32 a6, v189\n"
"v_accvgpr_read_b32 v189, a7\n"
"v_mul_f32 v189, v189, v9\n"
"v_accvgpr_write_b32 a7, v189\n"
"v_accvgpr_read_b32 v189, a8\n"
"v_mul_f32 v189, v189, v8\n"
"v_accvgpr_write_b32 a8, v189\n"
"v_accvgpr_read_b32 v189, a9\n"
"v_mul_f32 v189, v189, v8\n"
"v_accvgpr_write_b32 a9, v189\n"
"v_accvgpr_read_b32 v189, a10\n"
"v_mul_f32 v189, v189, v8\n"
"v_accvgpr_write_b32 a10, v189\n"
"v_accvgpr_read_b32 v189, a11\n"
"v_mul_f32 v189, v189, v8\n"
"v_accvgpr_write_b32 a11, v189\n"
"v_accvgpr_read_b32 v189, a12\n"
"v_mul_f32 v189, v189, v9\n"
"v_accvgpr_write_b32 a12, v189\n"
"v_accvgpr_read_b32 v189, a13\n"
"v_mul_f32 v189, v189, v9\n"
"v_accvgpr_write_b32 a13, v189\n"
"v_accvgpr_read_b32 v189, a14\n"
"v_mul_f32 v189, v189, v9\n"
"v_accvgpr_write_b32 a14, v189\n"
"v_accvgpr_read_b32 v189, a15\n"
"v_mul_f32 v189, v189, v9\n"
"v_accvgpr_write_b32 a15, v189\n"
"v_accvgpr_read_b32 v189, a16\n"
"v_mul_f32 v189, v189, v8\n"
"v_accvgpr_write_b32 a16, v189\n"
"v_accvgpr_read_b32 v189, a17\n"
"v_mul_f32 v189, v189, v8\n"
"v_accvgpr_write_b32 a17, v189\n"
"v_accvgpr_read_b32 v189, a18\n"
"v_mul_f32 v189, v189, v8\n"
"v_accvgpr_write_b32 a18, v189\n"
"v_accvgpr_read_b32 v189, a19\n"
"v_mul_f32 v189, v189, v8\n"
"v_accvgpr_write_b32 a19, v189\n"
"v_accvgpr_read_b32 v189, a20\n"
"v_mul_f32 v189, v189, v9\n"
"v_accvgpr_write_b32 a20, v189\n"
"v_accvgpr_read_b32 v189, a21\n"
"v_mul_f32 v189, v189, v9\n"
"v_accvgpr_write_b32 a21, v189\n"
"v_accvgpr_read_b32 v189, a22\n"
"v_mul_f32 v189, v189, v9\n"
"v_accvgpr_write_b32 a22, v189\n"
"v_accvgpr_read_b32 v189, a23\n"
"v_mul_f32 v189, v189, v9\n"
"v_accvgpr_write_b32 a23, v189\n"
"v_accvgpr_read_b32 v189, a24\n"
"v_mul_f32 v189, v189, v8\n"
"v_accvgpr_write_b32 a24, v189\n"
"v_accvgpr_read_b32 v189, a25\n"
"v_mul_f32 v189, v189, v8\n"
"v_accvgpr_write_b32 a25, v189\n"
"v_accvgpr_read_b32 v189, a26\n"
"v_mul_f32 v189, v189, v8\n"
"v_accvgpr_write_b32 a26, v189\n"
"v_accvgpr_read_b32 v189, a27\n"
"v_mul_f32 v189, v189, v8\n"
"v_accvgpr_write_b32 a27, v189\n"
"v_accvgpr_read_b32 v189, a28\n"
"v_mul_f32 v189, v189, v9\n"
"v_accvgpr_write_b32 a28, v189\n"
"v_accvgpr_read_b32 v189, a29\n"
"v_mul_f32 v189, v189, v9\n"
"v_accvgpr_write_b32 a29, v189\n"
"v_accvgpr_read_b32 v189, a30\n"
"v_mul_f32 v189, v189, v9\n"
"v_accvgpr_write_b32 a30, v189\n"
"v_accvgpr_read_b32 v189, a31\n"
"v_mul_f32 v189, v189, v9\n"
"v_accvgpr_write_b32 a31, v189\n"
"v_accvgpr_read_b32 v189, a32\n"
"v_mul_f32 v189, v189, v8\n"
"v_accvgpr_write_b32 a32, v189\n"
"v_accvgpr_read_b32 v189, a33\n"
"v_mul_f32 v189, v189, v8\n"
"v_accvgpr_write_b32 a33, v189\n"
"v_accvgpr_read_b32 v189, a34\n"
"v_mul_f32 v189, v189, v8\n"
"v_accvgpr_write_b32 a34, v189\n"
"v_accvgpr_read_b32 v189, a35\n"
"v_mul_f32 v189, v189, v8\n"
"v_accvgpr_write_b32 a35, v189\n"
"v_accvgpr_read_b32 v189, a36\n"
"v_mul_f32 v189, v189, v9\n"
"v_accvgpr_write_b32 a36, v189\n"
"v_accvgpr_read_b32 v189, a37\n"
"v_mul_f32 v189, v189, v9\n"
"v_accvgpr_write_b32 a37, v189\n"
"v_accvgpr_read_b32 v189, a38\n"
"v_mul_f32 v189, v189, v9\n"
"v_accvgpr_write_b32 a38, v189\n"
"v_accvgpr_read_b32 v189, a39\n"
"v_mul_f32 v189, v189, v9\n"
"v_accvgpr_write_b32 a39, v189\n"
"v_accvgpr_read_b32 v189, a40\n"
"v_mul_f32 v189, v189, v8\n"
"v_accvgpr_write_b32 a40, v189\n"
"v_accvgpr_read_b32 v189, a41\n"
"v_mul_f32 v189, v189, v8\n"
"v_accvgpr_write_b32 a41, v189\n"
"v_accvgpr_read_b32 v189, a42\n"
"v_mul_f32 v189, v189, v8\n"
"v_accvgpr_write_b32 a42, v189\n"
"v_accvgpr_read_b32 v189, a43\n"
"v_mul_f32 v189, v189, v8\n"
"v_accvgpr_write_b32 a43, v189\n"
"v_accvgpr_read_b32 v189, a44\n"
"v_mul_f32 v189, v189, v9\n"
"v_accvgpr_write_b32 a44, v189\n"
"v_accvgpr_read_b32 v189, a45\n"
"v_mul_f32 v189, v189, v9\n"
"v_accvgpr_write_b32 a45, v189\n"
"v_accvgpr_read_b32 v189, a46\n"
"v_mul_f32 v189, v189, v9\n"
"v_accvgpr_write_b32 a46, v189\n"
"v_accvgpr_read_b32 v189, a47\n"
"v_mul_f32 v189, v189, v9\n"
"v_accvgpr_write_b32 a47, v189\n"
"v_accvgpr_read_b32 v189, a48\n"
"v_mul_f32 v189, v189, v8\n"
"v_accvgpr_write_b32 a48, v189\n"
"v_accvgpr_read_b32 v189, a49\n"
"v_mul_f32 v189, v189, v8\n"
"v_accvgpr_write_b32 a49, v189\n"
"v_accvgpr_read_b32 v189, a50\n"
"v_mul_f32 v189, v189, v8\n"
"v_accvgpr_write_b32 a50, v189\n"
"v_accvgpr_read_b32 v189, a51\n"
"v_mul_f32 v189, v189, v8\n"
"v_accvgpr_write_b32 a51, v189\n"
"v_accvgpr_read_b32 v189, a52\n"
"v_mul_f32 v189, v189, v9\n"
"v_accvgpr_write_b32 a52, v189\n"
"v_accvgpr_read_b32 v189, a53\n"
"v_mul_f32 v189, v189, v9\n"
"v_accvgpr_write_b32 a53, v189\n"
"v_accvgpr_read_b32 v189, a54\n"
"v_mul_f32 v189, v189, v9\n"
"v_accvgpr_write_b32 a54, v189\n"
"v_accvgpr_read_b32 v189, a55\n"
"v_mul_f32 v189, v189, v9\n"
"v_accvgpr_write_b32 a55, v189\n"
"v_accvgpr_read_b32 v189, a56\n"
"v_mul_f32 v189, v189, v8\n"
"v_accvgpr_write_b32 a56, v189\n"
"v_accvgpr_read_b32 v189, a57\n"
"v_mul_f32 v189, v189, v8\n"
"v_accvgpr_write_b32 a57, v189\n"
"v_accvgpr_read_b32 v189, a58\n"
"v_mul_f32 v189, v189, v8\n"
"v_accvgpr_write_b32 a58, v189\n"
"v_accvgpr_read_b32 v189, a59\n"
"v_mul_f32 v189, v189, v8\n"
"v_accvgpr_write_b32 a59, v189\n"
"v_accvgpr_read_b32 v189, a60\n"
"v_mul_f32 v189, v189, v9\n"
"v_accvgpr_write_b32 a60, v189\n"
"v_accvgpr_read_b32 v189, a61\n"
"v_mul_f32 v189, v189, v9\n"
"v_accvgpr_write_b32 a61, v189\n"
"v_accvgpr_read_b32 v189, a62\n"
"v_mul_f32 v189, v189, v9\n"
"v_accvgpr_write_b32 a62, v189\n"
"v_accvgpr_read_b32 v189, a63\n"
"v_mul_f32 v189, v189, v9\n"
"v_accvgpr_write_b32 a63, v189\n"
"v_mul_f32 v160, v160, v8\n"
"v_mul_f32 v161, v161, v8\n"
"v_mul_f32 v162, v162, v8\n"
"v_mul_f32 v163, v163, v8\n"
"v_mul_f32 v164, v164, v9\n"
"v_mul_f32 v165, v165, v9\n"
"v_mul_f32 v166, v166, v9\n"
"v_mul_f32 v167, v167, v9\n"
"s_branch .Lrd_0\n"
".Lrb1_0:\n"
"v_accvgpr_read_b32 v189, a160\n"
"v_mul_f32 v189, v189, v8\n"
"v_accvgpr_write_b32 a160, v189\n"
"v_accvgpr_read_b32 v189, a161\n"
"v_mul_f32 v189, v189, v8\n"
"v_accvgpr_write_b32 a161, v189\n"
"v_accvgpr_read_b32 v189, a162\n"
"v_mul_f32 v189, v189, v8\n"
"v_accvgpr_write_b32 a162, v189\n"
"v_accvgpr_read_b32 v189, a163\n"
"v_mul_f32 v189, v189, v8\n"
"v_accvgpr_write_b32 a163, v189\n"
"v_accvgpr_read_b32 v189, a164\n"
"v_mul_f32 v189, v189, v9\n"
"v_accvgpr_write_b32 a164, v189\n"
"v_accvgpr_read_b32 v189, a165\n"
"v_mul_f32 v189, v189, v9\n"
"v_accvgpr_write_b32 a165, v189\n"
"v_accvgpr_read_b32 v189, a166\n"
"v_mul_f32 v189, v189, v9\n"
"v_accvgpr_write_b32 a166, v189\n"
"v_accvgpr_read_b32 v189, a167\n"
"v_mul_f32 v189, v189, v9\n"
"v_accvgpr_write_b32 a167, v189\n"
"v_accvgpr_read_b32 v189, a168\n"
"v_mul_f32 v189, v189, v8\n"
"v_accvgpr_write_b32 a168, v189\n"
"v_accvgpr_read_b32 v189, a169\n"
"v_mul_f32 v189, v189, v8\n"
"v_accvgpr_write_b32 a169, v189\n"
"v_accvgpr_read_b32 v189, a170\n"
"v_mul_f32 v189, v189, v8\n"
"v_accvgpr_write_b32 a170, v189\n"
"v_accvgpr_read_b32 v189, a171\n"
"v_mul_f32 v189, v189, v8\n"
"v_accvgpr_write_b32 a171, v189\n"
"v_accvgpr_read_b32 v189, a172\n"
"v_mul_f32 v189, v189, v9\n"
"v_accvgpr_write_b32 a172, v189\n"
"v_accvgpr_read_b32 v189, a173\n"
"v_mul_f32 v189, v189, v9\n"
"v_accvgpr_write_b32 a173, v189\n"
"v_accvgpr_read_b32 v189, a174\n"
"v_mul_f32 v189, v189, v9\n"
"v_accvgpr_write_b32 a174, v189\n"
"v_accvgpr_read_b32 v189, a175\n"
"v_mul_f32 v189, v189, v9\n"
"v_accvgpr_write_b32 a175, v189\n"
"v_accvgpr_read_b32 v189, a176\n"
"v_mul_f32 v189, v189, v8\n"
"v_accvgpr_write_b32 a176, v189\n"
"v_accvgpr_read_b32 v189, a177\n"
"v_mul_f32 v189, v189, v8\n"
"v_accvgpr_write_b32 a177, v189\n"
"v_accvgpr_read_b32 v189, a178\n"
"v_mul_f32 v189, v189, v8\n"
"v_accvgpr_write_b32 a178, v189\n"
"v_accvgpr_read_b32 v189, a179\n"
"v_mul_f32 v189, v189, v8\n"
"v_accvgpr_write_b32 a179, v189\n"
"v_accvgpr_read_b32 v189, a180\n"
"v_mul_f32 v189, v189, v9\n"
"v_accvgpr_write_b32 a180, v189\n"
"v_accvgpr_read_b32 v189, a181\n"
"v_mul_f32 v189, v189, v9\n"
"v_accvgpr_write_b32 a181, v189\n"
"v_accvgpr_read_b32 v189, a182\n"
"v_mul_f32 v189, v189, v9\n"
"v_accvgpr_write_b32 a182, v189\n"
"v_accvgpr_read_b32 v189, a183\n"
"v_mul_f32 v189, v189, v9\n"
"v_accvgpr_write_b32 a183, v189\n"
"v_accvgpr_read_b32 v189, a184\n"
"v_mul_f32 v189, v189, v8\n"
"v_accvgpr_write_b32 a184, v189\n"
"v_accvgpr_read_b32 v189, a185\n"
"v_mul_f32 v189, v189, v8\n"
"v_accvgpr_write_b32 a185, v189\n"
"v_accvgpr_read_b32 v189, a186\n"
"v_mul_f32 v189, v189, v8\n"
"v_accvgpr_write_b32 a186, v189\n"
"v_accvgpr_read_b32 v189, a187\n"
"v_mul_f32 v189, v189, v8\n"
"v_accvgpr_write_b32 a187, v189\n"
"v_accvgpr_read_b32 v189, a188\n"
"v_mul_f32 v189, v189, v9\n"
"v_accvgpr_write_b32 a188, v189\n"
"v_accvgpr_read_b32 v189, a189\n"
"v_mul_f32 v189, v189, v9\n"
"v_accvgpr_write_b32 a189, v189\n"
"v_accvgpr_read_b32 v189, a190\n"
"v_mul_f32 v189, v189, v9\n"
"v_accvgpr_write_b32 a190, v189\n"
"v_accvgpr_read_b32 v189, a191\n"
"v_mul_f32 v189, v189, v9\n"
"v_accvgpr_write_b32 a191, v189\n"
"v_accvgpr_read_b32 v189, a192\n"
"v_mul_f32 v189, v189, v8\n"
"v_accvgpr_write_b32 a192, v189\n"
"v_accvgpr_read_b32 v189, a193\n"
"v_mul_f32 v189, v189, v8\n"
"v_accvgpr_write_b32 a193, v189\n"
"v_accvgpr_read_b32 v189, a194\n"
"v_mul_f32 v189, v189, v8\n"
"v_accvgpr_write_b32 a194, v189\n"
"v_accvgpr_read_b32 v189, a195\n"
"v_mul_f32 v189, v189, v8\n"
"v_accvgpr_write_b32 a195, v189\n"
"v_accvgpr_read_b32 v189, a196\n"
"v_mul_f32 v189, v189, v9\n"
"v_accvgpr_write_b32 a196, v189\n"
"v_accvgpr_read_b32 v189, a197\n"
"v_mul_f32 v189, v189, v9\n"
"v_accvgpr_write_b32 a197, v189\n"
"v_accvgpr_read_b32 v189, a198\n"
"v_mul_f32 v189, v189, v9\n"
"v_accvgpr_write_b32 a198, v189\n"
"v_accvgpr_read_b32 v189, a199\n"
"v_mul_f32 v189, v189, v9\n"
"v_accvgpr_write_b32 a199, v189\n"
"v_accvgpr_read_b32 v189, a200\n"
"v_mul_f32 v189, v189, v8\n"
"v_accvgpr_write_b32 a200, v189\n"
"v_accvgpr_read_b32 v189, a201\n"
"v_mul_f32 v189, v189, v8\n"
"v_accvgpr_write_b32 a201, v189\n"
"v_accvgpr_read_b32 v189, a202\n"
"v_mul_f32 v189, v189, v8\n"
"v_accvgpr_write_b32 a202, v189\n"
"v_accvgpr_read_b32 v189, a203\n"
"v_mul_f32 v189, v189, v8\n"
"v_accvgpr_write_b32 a203, v189\n"
"v_accvgpr_read_b32 v189, a204\n"
"v_mul_f32 v189, v189, v9\n"
"v_accvgpr_write_b32 a204, v189\n"
"v_accvgpr_read_b32 v189, a205\n"
"v_mul_f32 v189, v189, v9\n"
"v_accvgpr_write_b32 a205, v189\n"
"v_accvgpr_read_b32 v189, a206\n"
"v_mul_f32 v189, v189, v9\n"
"v_accvgpr_write_b32 a206, v189\n"
"v_accvgpr_read_b32 v189, a207\n"
"v_mul_f32 v189, v189, v9\n"
"v_accvgpr_write_b32 a207, v189\n"
"v_accvgpr_read_b32 v189, a208\n"
"v_mul_f32 v189, v189, v8\n"
"v_accvgpr_write_b32 a208, v189\n"
"v_accvgpr_read_b32 v189, a209\n"
"v_mul_f32 v189, v189, v8\n"
"v_accvgpr_write_b32 a209, v189\n"
"v_accvgpr_read_b32 v189, a210\n"
"v_mul_f32 v189, v189, v8\n"
"v_accvgpr_write_b32 a210, v189\n"
"v_accvgpr_read_b32 v189, a211\n"
"v_mul_f32 v189, v189, v8\n"
"v_accvgpr_write_b32 a211, v189\n"
"v_accvgpr_read_b32 v189, a212\n"
"v_mul_f32 v189, v189, v9\n"
"v_accvgpr_write_b32 a212, v189\n"
"v_accvgpr_read_b32 v189, a213\n"
"v_mul_f32 v189, v189, v9\n"
"v_accvgpr_write_b32 a213, v189\n"
"v_accvgpr_read_b32 v189, a214\n"
"v_mul_f32 v189, v189, v9\n"
"v_accvgpr_write_b32 a214, v189\n"
"v_accvgpr_read_b32 v189, a215\n"
"v_mul_f32 v189, v189, v9\n"
"v_accvgpr_write_b32 a215, v189\n"
"v_accvgpr_read_b32 v189, a216\n"
"v_mul_f32 v189, v189, v8\n"
"v_accvgpr_write_b32 a216, v189\n"
"v_accvgpr_read_b32 v189, a217\n"
"v_mul_f32 v189, v189, v8\n"
"v_accvgpr_write_b32 a217, v189\n"
"v_accvgpr_read_b32 v189, a218\n"
"v_mul_f32 v189, v189, v8\n"
"v_accvgpr_write_b32 a218, v189\n"
"v_accvgpr_read_b32 v189, a219\n"
"v_mul_f32 v189, v189, v8\n"
"v_accvgpr_write_b32 a219, v189\n"
"v_accvgpr_read_b32 v189, a220\n"
"v_mul_f32 v189, v189, v9\n"
"v_accvgpr_write_b32 a220, v189\n"
"v_accvgpr_read_b32 v189, a221\n"
"v_mul_f32 v189, v189, v9\n"
"v_accvgpr_write_b32 a221, v189\n"
"v_accvgpr_read_b32 v189, a222\n"
"v_mul_f32 v189, v189, v9\n"
"v_accvgpr_write_b32 a222, v189\n"
"v_accvgpr_read_b32 v189, a223\n"
"v_mul_f32 v189, v189, v9\n"
"v_accvgpr_write_b32 a223, v189\n"
"v_mul_f32 v136, v136, v8\n"
"v_mul_f32 v137, v137, v8\n"
"v_mul_f32 v138, v138, v8\n"
"v_mul_f32 v139, v139, v8\n"
"v_mul_f32 v140, v140, v9\n"
"v_mul_f32 v141, v141, v9\n"
"v_mul_f32 v142, v142, v9\n"
"v_mul_f32 v143, v143, v9\n"
".Lrd_0:\n"
"s_nop 3\n"
"s_setpc_b64 s[70:71]\n"
".Lrare1:\n"
"s_nop 15\n"
"v_max_f32 v188, v188, v123\n"
"s_nop 1\n"
"v_max_f32 v189, v189, v127\n"
"s_nop 1\n"
"s_nop 0\n"
"s_nop 1\n"
"v_permlane32_swap_b32 v188, v189\n"
"s_nop 1\n"
"v_max_f32 v188, v188, v189\n"
"s_nop 1\n"
"v_mov_b32 v189, v188\n"
"s_nop 1\n"
"s_nop 0\n"
"s_nop 1\n"
"v_permlane16_swap_b32 v188, v189\n"
"s_nop 1\n"
"v_max_f32 v188, v188, v189\n"
"s_nop 1\n"
"v_mov_b32 v190, v188\n"
"s_nop 1\n"
"s_nop 0\n"
"s_nop 1\n"
"v_permlane32_swap_b32 v188, v190\n"
"s_nop 1\n"
"v_max_f32 v188, 0, v188\n"
"v_exp_f32 v8, -v188\n"
"s_nop 1\n"
"v_sub_f32 v128, v128, v188\n"
"v_sub_f32 v129, v129, v188\n"
"v_sub_f32 v130, v130, v188\n"
"v_sub_f32 v131, v131, v188\n"
"v_sub_f32 v96, v96, v188\n"
"v_sub_f32 v97, v97, v188\n"
"v_sub_f32 v98, v98, v188\n"
"v_sub_f32 v99, v99, v188\n"
"v_sub_f32 v104, v104, v188\n"
"v_sub_f32 v105, v105, v188\n"
"v_sub_f32 v106, v106, v188\n"
"v_sub_f32 v107, v107, v188\n"
"v_sub_f32 v112, v112, v188\n"
"v_sub_f32 v113, v113, v188\n"
"v_sub_f32 v114, v114, v188\n"
"v_sub_f32 v115, v115, v188\n"
"v_sub_f32 v120, v120, v188\n"
"v_sub_f32 v121, v121, v188\n"
"v_sub_f32 v122, v122, v188\n"
"v_sub_f32 v123, v123, v188\n"
"v_max_f32 v190, 0, v190\n"
"v_exp_f32 v9, -v190\n"
"s_nop 1\n"
"v_sub_f32 v132, v132, v190\n"
"v_sub_f32 v133, v133, v190\n"
"v_sub_f32 v134, v134, v190\n"
"v_sub_f32 v135, v135, v190\n"
"v_sub_f32 v100, v100, v190\n"
"v_sub_f32 v101, v101, v190\n"
"v_sub_f32 v102, v102, v190\n"
"v_sub_f32 v103, v103, v190\n"
"v_sub_f32 v108, v108, v190\n"
"v_sub_f32 v109, v109, v190\n"
"v_sub_f32 v110, v110, v190\n"
"v_sub_f32 v111, v111, v190\n"
"v_sub_f32 v116, v116, v190\n"
"v_sub_f32 v117, v117, v190\n"
"v_sub_f32 v118, v118, v190\n"
"v_sub_f32 v119, v119, v190\n"
"v_sub_f32 v124, v124, v190\n"
"v_sub_f32 v125, v125, v190\n"
"v_sub_f32 v126, v126, v190\n"
"v_sub_f32 v127, v127, v190\n"
"s_cmp_eq_u32 s61, 0\n"
"s_cbranch_scc0 .Lrb1_1\n"
"v_accvgpr_read_b32 v189, a0\n"
"v_mul_f32 v189, v189, v8\n"
"v_accvgpr_write_b32 a0, v189\n"
"v_accvgpr_read_b32 v189, a1\n"
"v_mul_f32 v189, v189, v8\n"
"v_accvgpr_write_b32 a1, v189\n"
"v_accvgpr_read_b32 v189, a2\n"
"v_mul_f32 v189, v189, v8\n"
"v_accvgpr_write_b32 a2, v189\n"
"v_accvgpr_read_b32 v189, a3\n"
"v_mul_f32 v189, v189, v8\n"
"v_accvgpr_write_b32 a3, v189\n"
"v_accvgpr_read_b32 v189, a4\n"
"v_mul_f32 v189, v189, v9\n"
"v_accvgpr_write_b32 a4, v189\n"
"v_accvgpr_read_b32 v189, a5\n"
"v_mul_f32 v189, v189, v9\n"
"v_accvgpr_write_b32 a5, v189\n"
"v_accvgpr_read_b32 v189, a6\n"
"v_mul_f32 v189, v189, v9\n"
"v_accvgpr_write_b32 a6, v189\n"
"v_accvgpr_read_b32 v189, a7\n"
"v_mul_f32 v189, v189, v9\n"
"v_accvgpr_write_b32 a7, v189\n"
"v_accvgpr_read_b32 v189, a8\n"
"v_mul_f32 v189, v189, v8\n"
"v_accvgpr_write_b32 a8, v189\n"
"v_accvgpr_read_b32 v189, a9\n"
"v_mul_f32 v189, v189, v8\n"
"v_accvgpr_write_b32 a9, v189\n"
"v_accvgpr_read_b32 v189, a10\n"
"v_mul_f32 v189, v189, v8\n"
"v_accvgpr_write_b32 a10, v189\n"
"v_accvgpr_read_b32 v189, a11\n"
"v_mul_f32 v189, v189, v8\n"
"v_accvgpr_write_b32 a11, v189\n"
"v_accvgpr_read_b32 v189, a12\n"
"v_mul_f32 v189, v189, v9\n"
"v_accvgpr_write_b32 a12, v189\n"
"v_accvgpr_read_b32 v189, a13\n"
"v_mul_f32 v189, v189, v9\n"
"v_accvgpr_write_b32 a13, v189\n"
"v_accvgpr_read_b32 v189, a14\n"
"v_mul_f32 v189, v189, v9\n"
"v_accvgpr_write_b32 a14, v189\n"
"v_accvgpr_read_b32 v189, a15\n"
"v_mul_f32 v189, v189, v9\n"
"v_accvgpr_write_b32 a15, v189\n"
"v_accvgpr_read_b32 v189, a16\n"
"v_mul_f32 v189, v189, v8\n"
"v_accvgpr_write_b32 a16, v189\n"
"v_accvgpr_read_b32 v189, a17\n"
"v_mul_f32 v189, v189, v8\n"
"v_accvgpr_write_b32 a17, v189\n"
"v_accvgpr_read_b32 v189, a18\n"
"v_mul_f32 v189, v189, v8\n"
"v_accvgpr_write_b32 a18, v189\n"
"v_accvgpr_read_b32 v189, a19\n"
"v_mul_f32 v189, v189, v8\n"
"v_accvgpr_write_b32 a19, v189\n"
"v_accvgpr_read_b32 v189, a20\n"
"v_mul_f32 v189, v189, v9\n"
"v_accvgpr_write_b32 a20, v189\n"
"v_accvgpr_read_b32 v189, a21\n"
"v_mul_f32 v189, v189, v9\n"
"v_accvgpr_write_b32 a21, v189\n"
"v_accvgpr_read_b32 v189, a22\n"
"v_mul_f32 v189, v189, v9\n"
"v_accvgpr_write_b32 a22, v189\n"
"v_accvgpr_read_b32 v189, a23\n"
"v_mul_f32 v189, v189, v9\n"
"v_accvgpr_write_b32 a23, v189\n"
"v_accvgpr_read_b32 v189, a24\n"
"v_mul_f32 v189, v189, v8\n"
"v_accvgpr_write_b32 a24, v189\n"
"v_accvgpr_read_b32 v189, a25\n"
"v_mul_f32 v189, v189, v8\n"
"v_accvgpr_write_b32 a25, v189\n"
"v_accvgpr_read_b32 v189, a26\n"
"v_mul_f32 v189, v189, v8\n"
"v_accvgpr_write_b32 a26, v189\n"
"v_accvgpr_read_b32 v189, a27\n"
"v_mul_f32 v189, v189, v8\n"
"v_accvgpr_write_b32 a27, v189\n"
"v_accvgpr_read_b32 v189, a28\n"
"v_mul_f32 v189, v189, v9\n"
"v_accvgpr_write_b32 a28, v189\n"
"v_accvgpr_read_b32 v189, a29\n"
"v_mul_f32 v189, v189, v9\n"
"v_accvgpr_write_b32 a29, v189\n"
"v_accvgpr_read_b32 v189, a30\n"
"v_mul_f32 v189, v189, v9\n"
"v_accvgpr_write_b32 a30, v189\n"
"v_accvgpr_read_b32 v189, a31\n"
"v_mul_f32 v189, v189, v9\n"
"v_accvgpr_write_b32 a31, v189\n"
"v_accvgpr_read_b32 v189, a32\n"
"v_mul_f32 v189, v189, v8\n"
"v_accvgpr_write_b32 a32, v189\n"
"v_accvgpr_read_b32 v189, a33\n"
"v_mul_f32 v189, v189, v8\n"
"v_accvgpr_write_b32 a33, v189\n"
"v_accvgpr_read_b32 v189, a34\n"
"v_mul_f32 v189, v189, v8\n"
"v_accvgpr_write_b32 a34, v189\n"
"v_accvgpr_read_b32 v189, a35\n"
"v_mul_f32 v189, v189, v8\n"
"v_accvgpr_write_b32 a35, v189\n"
"v_accvgpr_read_b32 v189, a36\n"
"v_mul_f32 v189, v189, v9\n"
"v_accvgpr_write_b32 a36, v189\n"
"v_accvgpr_read_b32 v189, a37\n"
"v_mul_f32 v189, v189, v9\n"
"v_accvgpr_write_b32 a37, v189\n"
"v_accvgpr_read_b32 v189, a38\n"
"v_mul_f32 v189, v189, v9\n"
"v_accvgpr_write_b32 a38, v189\n"
"v_accvgpr_read_b32 v189, a39\n"
"v_mul_f32 v189, v189, v9\n"
"v_accvgpr_write_b32 a39, v189\n"
"v_accvgpr_read_b32 v189, a40\n"
"v_mul_f32 v189, v189, v8\n"
"v_accvgpr_write_b32 a40, v189\n"
"v_accvgpr_read_b32 v189, a41\n"
"v_mul_f32 v189, v189, v8\n"
"v_accvgpr_write_b32 a41, v189\n"
"v_accvgpr_read_b32 v189, a42\n"
"v_mul_f32 v189, v189, v8\n"
"v_accvgpr_write_b32 a42, v189\n"
"v_accvgpr_read_b32 v189, a43\n"
"v_mul_f32 v189, v189, v8\n"
"v_accvgpr_write_b32 a43, v189\n"
"v_accvgpr_read_b32 v189, a44\n"
"v_mul_f32 v189, v189, v9\n"
"v_accvgpr_write_b32 a44, v189\n"
"v_accvgpr_read_b32 v189, a45\n"
"v_mul_f32 v189, v189, v9\n"
"v_accvgpr_write_b32 a45, v189\n"
"v_accvgpr_read_b32 v189, a46\n"
"v_mul_f32 v189, v189, v9\n"
"v_accvgpr_write_b32 a46, v189\n"
"v_accvgpr_read_b32 v189, a47\n"
"v_mul_f32 v189, v189, v9\n"
"v_accvgpr_write_b32 a47, v189\n"
"v_accvgpr_read_b32 v189, a48\n"
"v_mul_f32 v189, v189, v8\n"
"v_accvgpr_write_b32 a48, v189\n"
"v_accvgpr_read_b32 v189, a49\n"
"v_mul_f32 v189, v189, v8\n"
"v_accvgpr_write_b32 a49, v189\n"
"v_accvgpr_read_b32 v189, a50\n"
"v_mul_f32 v189, v189, v8\n"
"v_accvgpr_write_b32 a50, v189\n"
"v_accvgpr_read_b32 v189, a51\n"
"v_mul_f32 v189, v189, v8\n"
"v_accvgpr_write_b32 a51, v189\n"
"v_accvgpr_read_b32 v189, a52\n"
"v_mul_f32 v189, v189, v9\n"
"v_accvgpr_write_b32 a52, v189\n"
"v_accvgpr_read_b32 v189, a53\n"
"v_mul_f32 v189, v189, v9\n"
"v_accvgpr_write_b32 a53, v189\n"
"v_accvgpr_read_b32 v189, a54\n"
"v_mul_f32 v189, v189, v9\n"
"v_accvgpr_write_b32 a54, v189\n"
"v_accvgpr_read_b32 v189, a55\n"
"v_mul_f32 v189, v189, v9\n"
"v_accvgpr_write_b32 a55, v189\n"
"v_accvgpr_read_b32 v189, a56\n"
"v_mul_f32 v189, v189, v8\n"
"v_accvgpr_write_b32 a56, v189\n"
"v_accvgpr_read_b32 v189, a57\n"
"v_mul_f32 v189, v189, v8\n"
"v_accvgpr_write_b32 a57, v189\n"
"v_accvgpr_read_b32 v189, a58\n"
"v_mul_f32 v189, v189, v8\n"
"v_accvgpr_write_b32 a58, v189\n"
"v_accvgpr_read_b32 v189, a59\n"
"v_mul_f32 v189, v189, v8\n"
"v_accvgpr_write_b32 a59, v189\n"
"v_accvgpr_read_b32 v189, a60\n"
"v_mul_f32 v189, v189, v9\n"
"v_accvgpr_write_b32 a60, v189\n"
"v_accvgpr_read_b32 v189, a61\n"
"v_mul_f32 v189, v189, v9\n"
"v_accvgpr_write_b32 a61, v189\n"
"v_accvgpr_read_b32 v189, a62\n"
"v_mul_f32 v189, v189, v9\n"
"v_accvgpr_write_b32 a62, v189\n"
"v_accvgpr_read_b32 v189, a63\n"
"v_mul_f32 v189, v189, v9\n"
"v_accvgpr_write_b32 a63, v189\n"
"v_mul_f32 v160, v160, v8\n"
"v_mul_f32 v161, v161, v8\n"
"v_mul_f32 v162, v162, v8\n"
"v_mul_f32 v163, v163, v8\n"
"v_mul_f32 v164, v164, v9\n"
"v_mul_f32 v165, v165, v9\n"
"v_mul_f32 v166, v166, v9\n"
"v_mul_f32 v167, v167, v9\n"
"s_branch .Lrd_1\n"
".Lrb1_1:\n"
"v_accvgpr_read_b32 v189, a160\n"
"v_mul_f32 v189, v189, v8\n"
"v_accvgpr_write_b32 a160, v189\n"
"v_accvgpr_read_b32 v189, a161\n"
"v_mul_f32 v189, v189, v8\n"
"v_accvgpr_write_b32 a161, v189\n"
"v_accvgpr_read_b32 v189, a162\n"
"v_mul_f32 v189, v189, v8\n"
"v_accvgpr_write_b32 a162, v189\n"
"v_accvgpr_read_b32 v189, a163\n"
"v_mul_f32 v189, v189, v8\n"
"v_accvgpr_write_b32 a163, v189\n"
"v_accvgpr_read_b32 v189, a164\n"
"v_mul_f32 v189, v189, v9\n"
"v_accvgpr_write_b32 a164, v189\n"
"v_accvgpr_read_b32 v189, a165\n"
"v_mul_f32 v189, v189, v9\n"
"v_accvgpr_write_b32 a165, v189\n"
"v_accvgpr_read_b32 v189, a166\n"
"v_mul_f32 v189, v189, v9\n"
"v_accvgpr_write_b32 a166, v189\n"
"v_accvgpr_read_b32 v189, a167\n"
"v_mul_f32 v189, v189, v9\n"
"v_accvgpr_write_b32 a167, v189\n"
"v_accvgpr_read_b32 v189, a168\n"
"v_mul_f32 v189, v189, v8\n"
"v_accvgpr_write_b32 a168, v189\n"
"v_accvgpr_read_b32 v189, a169\n"
"v_mul_f32 v189, v189, v8\n"
"v_accvgpr_write_b32 a169, v189\n"
"v_accvgpr_read_b32 v189, a170\n"
"v_mul_f32 v189, v189, v8\n"
"v_accvgpr_write_b32 a170, v189\n"
"v_accvgpr_read_b32 v189, a171\n"
"v_mul_f32 v189, v189, v8\n"
"v_accvgpr_write_b32 a171, v189\n"
"v_accvgpr_read_b32 v189, a172\n"
"v_mul_f32 v189, v189, v9\n"
"v_accvgpr_write_b32 a172, v189\n"
"v_accvgpr_read_b32 v189, a173\n"
"v_mul_f32 v189, v189, v9\n"
"v_accvgpr_write_b32 a173, v189\n"
"v_accvgpr_read_b32 v189, a174\n"
"v_mul_f32 v189, v189, v9\n"
"v_accvgpr_write_b32 a174, v189\n"
"v_accvgpr_read_b32 v189, a175\n"
"v_mul_f32 v189, v189, v9\n"
"v_accvgpr_write_b32 a175, v189\n"
"v_accvgpr_read_b32 v189, a176\n"
"v_mul_f32 v189, v189, v8\n"
"v_accvgpr_write_b32 a176, v189\n"
"v_accvgpr_read_b32 v189, a177\n"
"v_mul_f32 v189, v189, v8\n"
"v_accvgpr_write_b32 a177, v189\n"
"v_accvgpr_read_b32 v189, a178\n"
"v_mul_f32 v189, v189, v8\n"
"v_accvgpr_write_b32 a178, v189\n"
"v_accvgpr_read_b32 v189, a179\n"
"v_mul_f32 v189, v189, v8\n"
"v_accvgpr_write_b32 a179, v189\n"
"v_accvgpr_read_b32 v189, a180\n"
"v_mul_f32 v189, v189, v9\n"
"v_accvgpr_write_b32 a180, v189\n"
"v_accvgpr_read_b32 v189, a181\n"
"v_mul_f32 v189, v189, v9\n"
"v_accvgpr_write_b32 a181, v189\n"
"v_accvgpr_read_b32 v189, a182\n"
"v_mul_f32 v189, v189, v9\n"
"v_accvgpr_write_b32 a182, v189\n"
"v_accvgpr_read_b32 v189, a183\n"
"v_mul_f32 v189, v189, v9\n"
"v_accvgpr_write_b32 a183, v189\n"
"v_accvgpr_read_b32 v189, a184\n"
"v_mul_f32 v189, v189, v8\n"
"v_accvgpr_write_b32 a184, v189\n"
"v_accvgpr_read_b32 v189, a185\n"
"v_mul_f32 v189, v189, v8\n"
"v_accvgpr_write_b32 a185, v189\n"
"v_accvgpr_read_b32 v189, a186\n"
"v_mul_f32 v189, v189, v8\n"
"v_accvgpr_write_b32 a186, v189\n"
"v_accvgpr_read_b32 v189, a187\n"
"v_mul_f32 v189, v189, v8\n"
"v_accvgpr_write_b32 a187, v189\n"
"v_accvgpr_read_b32 v189, a188\n"
"v_mul_f32 v189, v189, v9\n"
"v_accvgpr_write_b32 a188, v189\n"
"v_accvgpr_read_b32 v189, a189\n"
"v_mul_f32 v189, v189, v9\n"
"v_accvgpr_write_b32 a189, v189\n"
"v_accvgpr_read_b32 v189, a190\n"
"v_mul_f32 v189, v189, v9\n"
"v_accvgpr_write_b32 a190, v189\n"
"v_accvgpr_read_b32 v189, a191\n"
"v_mul_f32 v189, v189, v9\n"
"v_accvgpr_write_b32 a191, v189\n"
"v_accvgpr_read_b32 v189, a192\n"
"v_mul_f32 v189, v189, v8\n"
"v_accvgpr_write_b32 a192, v189\n"
"v_accvgpr_read_b32 v189, a193\n"
"v_mul_f32 v189, v189, v8\n"
"v_accvgpr_write_b32 a193, v189\n"
"v_accvgpr_read_b32 v189, a194\n"
"v_mul_f32 v189, v189, v8\n"
"v_accvgpr_write_b32 a194, v189\n"
"v_accvgpr_read_b32 v189, a195\n"
"v_mul_f32 v189, v189, v8\n"
"v_accvgpr_write_b32 a195, v189\n"
"v_accvgpr_read_b32 v189, a196\n"
"v_mul_f32 v189, v189, v9\n"
"v_accvgpr_write_b32 a196, v189\n"
"v_accvgpr_read_b32 v189, a197\n"
"v_mul_f32 v189, v189, v9\n"
"v_accvgpr_write_b32 a197, v189\n"
"v_accvgpr_read_b32 v189, a198\n"
"v_mul_f32 v189, v189, v9\n"
"v_accvgpr_write_b32 a198, v189\n"
"v_accvgpr_read_b32 v189, a199\n"
"v_mul_f32 v189, v189, v9\n"
"v_accvgpr_write_b32 a199, v189\n"
"v_accvgpr_read_b32 v189, a200\n"
"v_mul_f32 v189, v189, v8\n"
"v_accvgpr_write_b32 a200, v189\n"
"v_accvgpr_read_b32 v189, a201\n"
"v_mul_f32 v189, v189, v8\n"
"v_accvgpr_write_b32 a201, v189\n"
"v_accvgpr_read_b32 v189, a202\n"
"v_mul_f32 v189, v189, v8\n"
"v_accvgpr_write_b32 a202, v189\n"
"v_accvgpr_read_b32 v189, a203\n"
"v_mul_f32 v189, v189, v8\n"
"v_accvgpr_write_b32 a203, v189\n"
"v_accvgpr_read_b32 v189, a204\n"
"v_mul_f32 v189, v189, v9\n"
"v_accvgpr_write_b32 a204, v189\n"
"v_accvgpr_read_b32 v189, a205\n"
"v_mul_f32 v189, v189, v9\n"
"v_accvgpr_write_b32 a205, v189\n"
"v_accvgpr_read_b32 v189, a206\n"
"v_mul_f32 v189, v189, v9\n"
"v_accvgpr_write_b32 a206, v189\n"
"v_accvgpr_read_b32 v189, a207\n"
"v_mul_f32 v189, v189, v9\n"
"v_accvgpr_write_b32 a207, v189\n"
"v_accvgpr_read_b32 v189, a208\n"
"v_mul_f32 v189, v189, v8\n"
"v_accvgpr_write_b32 a208, v189\n"
"v_accvgpr_read_b32 v189, a209\n"
"v_mul_f32 v189, v189, v8\n"
"v_accvgpr_write_b32 a209, v189\n"
"v_accvgpr_read_b32 v189, a210\n"
"v_mul_f32 v189, v189, v8\n"
"v_accvgpr_write_b32 a210, v189\n"
"v_accvgpr_read_b32 v189, a211\n"
"v_mul_f32 v189, v189, v8\n"
"v_accvgpr_write_b32 a211, v189\n"
"v_accvgpr_read_b32 v189, a212\n"
"v_mul_f32 v189, v189, v9\n"
"v_accvgpr_write_b32 a212, v189\n"
"v_accvgpr_read_b32 v189, a213\n"
"v_mul_f32 v189, v189, v9\n"
"v_accvgpr_write_b32 a213, v189\n"
"v_accvgpr_read_b32 v189, a214\n"
"v_mul_f32 v189, v189, v9\n"
"v_accvgpr_write_b32 a214, v189\n"
"v_accvgpr_read_b32 v189, a215\n"
"v_mul_f32 v189, v189, v9\n"
"v_accvgpr_write_b32 a215, v189\n"
"v_accvgpr_read_b32 v189, a216\n"
"v_mul_f32 v189, v189, v8\n"
"v_accvgpr_write_b32 a216, v189\n"
"v_accvgpr_read_b32 v189, a217\n"
"v_mul_f32 v189, v189, v8\n"
"v_accvgpr_write_b32 a217, v189\n"
"v_accvgpr_read_b32 v189, a218\n"
"v_mul_f32 v189, v189, v8\n"
"v_accvgpr_write_b32 a218, v189\n"
"v_accvgpr_read_b32 v189, a219\n"
"v_mul_f32 v189, v189, v8\n"
"v_accvgpr_write_b32 a219, v189\n"
"v_accvgpr_read_b32 v189, a220\n"
"v_mul_f32 v189, v189, v9\n"
"v_accvgpr_write_b32 a220, v189\n"
"v_accvgpr_read_b32 v189, a221\n"
"v_mul_f32 v189, v189, v9\n"
"v_accvgpr_write_b32 a221, v189\n"
"v_accvgpr_read_b32 v189, a222\n"
"v_mul_f32 v189, v189, v9\n"
"v_accvgpr_write_b32 a222, v189\n"
"v_accvgpr_read_b32 v189, a223\n"
"v_mul_f32 v189, v189, v9\n"
"v_accvgpr_write_b32 a223, v189\n"
"v_mul_f32 v136, v136, v8\n"
"v_mul_f32 v137, v137, v8\n"
"v_mul_f32 v138, v138, v8\n"
"v_mul_f32 v139, v139, v8\n"
"v_mul_f32 v140, v140, v9\n"
"v_mul_f32 v141, v141, v9\n"
"v_mul_f32 v142, v142, v9\n"
"v_mul_f32 v143, v143, v9\n"
".Lrd_1:\n"
"s_nop 3\n"
"s_setpc_b64 s[70:71]\n"
".Lend:\n"
      :
      : "s"(ka), "s"(blockIdx.x), "s"(w)
      : "memory", "vcc", "scc", "v0", "v1", "v2", "v3", "v4", "v5", "v6", "v7", "v8", "v9", "v10", "v11", "v12", "v13", "v14", "v15", "v16", "v17", "v18", "v19", "v20", "v21", "v22", "v23", "v24", "v25", "v26", "v27", "v28", "v29", "v30", "v31", "v32", "v33", "v34", "v35", "v36", "v37", "v38", "v39", "v40", "v41", "v42", "v43", "v44", "v45", "v46", "v47", "v48", "v49", "v50", "v51", "v52", "v53", "v54", "v55", "v56", "v57", "v58", "v59", "v60", "v61", "v62", "v63", "v64", "v65", "v66", "v67", "v68", "v69", "v70", "v71", "v72", "v73", "v74", "v75", "v76", "v77", "v78", "v79", "v80", "v81", "v82", "v83", "v84", "v85", "v86", "v87", "v88", "v89", "v90", "v91", "v92", "v93", "v94", "v95", "v96", "v97", "v98", "v99", "v100", "v101", "v102", "v103", "v104", "v105", "v106", "v107", "v108", "v109", "v110", "v111", "v112", "v113", "v114", "v115", "v116", "v117", "v118", "v119", "v120", "v121", "v122", "v123", "v124", "v125", "v126", "v127", "v128", "v129", "v130", "v131", "v132", "v133", "v134", "v135", "v136", "v137", "v138", "v139", "v140", "v141", "v142", "v143", "v144", "v145", "v146", "v147", "v148", "v149", "v150", "v151", "v152", "v153", "v154", "v155", "v156", "v157", "v158", "v159", "v160", "v161", "v162", "v163", "v164", "v165", "v166", "v167", "v168", "v169", "v170", "v171", "v172", "v173", "v174", "v175", "v176", "v177", "v178", "v179", "v180", "v181", "v182", "v183", "v184", "v185", "v186", "v187", "v188", "v189", "v190", "v191", "v192", "v193", "v194", "v195", "v196", "v197", "v198", "v199", "v200", "v201", "v202", "v203", "v204", "v205", "v206", "v207", "v208", "v209", "v210", "v211", "v212", "v213", "v214", "v215", "v216", "v217", "v218", "v219", "v220", "v221", "v222", "v223", "v224", "v225", "v226", "v227", "v228", "v229", "v230", "v231", "v232", "v233", "v234", "v235", "v236", "v237", "v238", "v239", "v240", "v241", "v242", "v243", "v244", "v245", "v246", "v247", "v248", "v249", "v250", "v251", "v252", "v253", "v254", "v255", "a0", "a1", "a2", "a3", "a4", "a5", "a6", "a7", "a8", "a9", "a10", "a11", "a12", "a13", "a14", "a15", "a16", "a17", "a18", "a19", "a20", "a21", "a22", "a23", "a24", "a25", "a26", "a27", "a28", "a29", "a30", "a31", "a32", "a33", "a34", "a35", "a36", "a37", "a38", "a39", "a40", "a41", "a42", "a43", "a44", "a45", "a46", "a47", "a48", "a49", "a50", "a51", "a52", "a53", "a54", "a55", "a56", "a57", "a58", "a59", "a60", "a61", "a62", "a63", "a64", "a65", "a66", "a67", "a68", "a69", "a70", "a71", "a72", "a73", "a74", "a75", "a76", "a77", "a78", "a79", "a80", "a81", "a82", "a83", "a84", "a85", "a86", "a87", "a88", "a89", "a90", "a91", "a92", "a93", "a94", "a95", "a96", "a97", "a98", "a99", "a100", "a101", "a102", "a103", "a104", "a105", "a106", "a107", "a108", "a109", "a110", "a111", "a112", "a113", "a114", "a115", "a116", "a117", "a118", "a119", "a120", "a121", "a122", "a123", "a124", "a125", "a126", "a127", "a128", "a129", "a130", "a131", "a132", "a133", "a134", "a135", "a136", "a137", "a138", "a139", "a140", "a141", "a142", "a143", "a144", "a145", "a146", "a147", "a148", "a149", "a150", "a151", "a152", "a153", "a154", "a155", "a156", "a157", "a158", "a159", "a160", "a161", "a162", "a163", "a164", "a165", "a166", "a167", "a168", "a169", "a170", "a171", "a172", "a173", "a174", "a175", "a176", "a177", "a178", "a179", "a180", "a181", "a182", "a183", "a184", "a185", "a186", "a187", "a188", "a189", "a190", "a191", "a192", "a193", "a194", "a195", "a196", "a197", "a198", "a199", "a200", "a201", "a202", "a203", "a204", "a205", "a206", "a207", "a208", "a209", "a210", "a211", "a212", "a213", "a214", "a215", "a216", "a217", "a218", "a219", "a220", "a221", "a222", "a223", "a224", "a225", "a226", "a227", "a228", "a229", "a230", "a231", "a232", "a233", "a234", "a235", "a236", "a237", "a238", "a239", "a240", "a241", "a242", "a243", "a244", "a245", "a246", "a247", "a248", "a249", "a250", "a251", "a252", "a253", "a254", "a255", "s4", "s5", "s6", "s7", "s8", "s9", "s10", "s11", "s12", "s13", "s14", "s15", "s16", "s17", "s18", "s19", "s20", "s21", "s22", "s23", "s24", "s25", "s26", "s27", "s28", "s29", "s30", "s31", "s32", "s33", "s34", "s35", "s36", "s37", "s38", "s39", "s40", "s41", "s42", "s43", "s44", "s45", "s46", "s47", "s48", "s49", "s50", "s51", "s52", "s53", "s54", "s55", "s56", "s57", "s58", "s59", "s60", "s61", "s62", "s63", "s64", "s65", "s66", "s67", "s68", "s69", "s70", "s71", "s72", "s73", "s74", "s75", "s76", "s77", "s78", "s79", "s80", "s81", "s82", "s83", "s84", "s85", "s86", "s87", "s88", "s89", "s90", "s91", "s92", "s93", "s94", "s95", "s96", "s97", "s98", "s99", "s100", "s101");
}

extern "C" void kernel_launch(void* const* d_in, const int* in_sizes, int n_in, void* d_out, int out_size, void* d_ws, size_t ws_size, hipStream_t stream) {
  (void)hipFuncSetAttribute((const void*)fused_k, hipFuncAttributeMaxDynamicSharedMemorySize, 163840);
  fused_k<<<256, 256, 163840, stream>>>((const char*)d_in[0], (const char*)d_in[1], (const char*)d_in[2], (const char*)d_in[3], (const char*)d_in[4],
                                        (const char*)d_in[5], (const char*)d_in[6], (const char*)d_in[7], (const char*)d_in[8], (const char*)d_in[9], (char*)d_ws, (char*)d_out);
}
```
